```python
import torch

def _softmax(x, axis=-1):
    return torch.softmax(x, dim=axis)
def _gelu(x):
    return torch.nn.functional.gelu(x, approximate="tanh")
def _silu(x):
    return torch.nn.functional.silu(x)
def _sigmoid(x):
    return torch.sigmoid(x)
def _layernorm(x, axis=-1, eps=1e-5):
    m = x.mean(dim=axis, keepdim=True)
    v = x.var(dim=axis, keepdim=True, unbiased=False)
    return (x - m) / torch.sqrt(v + eps)
def _scatter_add(tgt, idx, src, axis=0):
    return tgt.clone().index_add_(axis, idx, src)
def _segment_sum(src, seg, n):
    out = torch.zeros((n,) + src.shape[1:], dtype=src.dtype)
    return out.index_add_(0, seg, src)
def _pad(x, pw):
    flat = tuple(v for pair in reversed(pw) for v in pair)
    return torch.nn.functional.pad(x, flat)

kernel_name = 'standalone__weight_only_int_squeezellm__prompt'

def setup_inputs():

    _w = (lambda a: (lambda t: t.to(torch.bfloat16) if t.is_floating_point() else t)(torch.as_tensor(a)))
    B, S, D, F = 1, 2048, 1024, 3584
    bits = 8 if (2 <= 8 <= 4) else 4
    L = 2 ** bits
    qw = torch.randint(0, L, (D, F)).to(torch.int64)
    lut = (lambda *s: torch.randn(*s, dtype=torch.bfloat16) * 0.05)(L, F)
    return {
        "x": _w((lambda *s: torch.randn(*s, dtype=torch.bfloat16) * 0.05)(B, S, D)),
        "qw": _w(qw),
        "lut": _w(lut),
    }

def reference(x, qw, lut):
    w = (lambda x, i, axis: torch.take_along_dim(x, i, dim=axis))(lut, qw, axis=0)
    w = (lambda x, dt: x.to(dt))(w, x.dtype)
    return torch.einsum('bsd,df->bsf', x, w)
```

```cpp
#include <hip/hip_runtime.h>
#include <hip/hip_bf16.h>

typedef unsigned short u16;
typedef unsigned int u32;
using bf16x8 = __attribute__((ext_vector_type(8))) short;
using f32x4  = __attribute__((ext_vector_type(4))) float;
using i32x4  = __attribute__((ext_vector_type(4))) int;
using u32x2  = __attribute__((ext_vector_type(2))) unsigned int;
using u32x4  = __attribute__((ext_vector_type(4))) unsigned int;

constexpr int M_ = 2048, K_ = 1024, N_ = 3584;
constexpr int BM = 256, BN = 128;
constexpr int NPF = 32;
#define XSLEEP 4
#define SP0 3
#define NQ1 4
#define CPF 144
#define CPFB 32
#define PACE 0
#define VORD 1
constexpr int XS = BM * 64 * 2;
constexpr int BS = BN * 32 * 2;
constexpr int OFF_B = 3 * XS;
constexpr int WSL = 2 * BS;
constexpr int OFF_L = OFF_B + 3 * WSL;
constexpr int OFF_D = OFF_L + 16 * BN * 2;
constexpr int WS_F = 28 * 256, WS_W = 65536;
constexpr unsigned TW = 28 * 16384;
constexpr int SPIN_MAX = 1 << 16;
constexpr int LDS_BYTES = OFF_D + 1024;

typedef __bf16 bf16x2_t __attribute__((ext_vector_type(2)));
typedef float f32x2_t __attribute__((ext_vector_type(2)));
__device__ __forceinline__ u32 pack_bf16(float lo, float hi) {
    f32x2_t f = {lo, hi};
    bf16x2_t b = __builtin_convertvector(f, bf16x2_t);
    return __builtin_bit_cast(u32, b);
}

#define TSEQ(i) (0xFEDCBA9876532140ul ^ (unsigned long)((u32)(i) * 0x11111111u))
#define BAR() __builtin_amdgcn_s_barrier()
#define SB() __builtin_amdgcn_sched_barrier(0)
#define WAITQ(n, Qa) asm volatile("s_waitcnt vmcnt(" #n ") lgkmcnt(0)" : "+v"(Qa[0]), "+v"(Qa[1]), "+v"(Qa[2]), "+v"(Qa[3]), \
                                  "+v"(Qa[4]), "+v"(Qa[5]), "+v"(Qa[6]), "+v"(Qa[7]) :: "memory")
#define PBAR() BAR()
#define PAD(id, nx) asm volatile(".Lwp%c0:\n\ts_cbranch_execz .Lwp%c1" :: "i"(id), "i"(nx))

__global__ __launch_bounds__(512, 2) void fused_kernel(const u16* __restrict__ x_, const int* __restrict__ qw_,
                                                       const u16* __restrict__ lut_, u16* __restrict__ out_, char* ws_) {
    extern __shared__ __attribute__((aligned(16))) char smem[];
    typedef unsigned long u64x4 __attribute__((ext_vector_type(4)));
    u64x4 ka;
    unsigned long kp[5], kw;
    asm volatile("s_load_dwordx8 %0, %2, 0x0\n\ts_load_dwordx2 %1, %2, 0x20" : "=&s"(ka), "=&s"(kw) : "s"(__builtin_amdgcn_kernarg_segment_ptr()));
    {
        unsigned long pc0, ex;
        u32 co;
        asm volatile("s_getpc_b64 %0\n\t"
                     "s_cmp_lt_u32 %4, %5\n\t"
                     "s_cselect_b32 m0, %6, 0\n\t"
                     "v_cmp_gt_u32 vcc, m0, %3\n\t"
                     "s_and_b32 m0, %4, 24\n\t"
                     "s_lshl_b32 m0, m0, 4\n\t"
                     "v_lshl_add_u32 %2, %3, 9, m0\n\t"
                     "s_mov_b32 m0, %7\n\t"
                     "s_and_saveexec_b64 %1, vcc\n\t"
                     "global_load_lds_dword %2, %0\n\t"
                     "s_mov_b64 exec, %1"
                     : "=&s"(pc0), "=&s"(ex), "=&v"(co) : "v"(threadIdx.x), "s"(blockIdx.x), "i"(CPFB), "i"(CPF / 4), "i"(OFF_D) : "vcc", "scc", "memory");
    }
#define KWAIT() do { SB(); asm volatile("s_waitcnt lgkmcnt(0)\n\ts_mov_b64 %0, %5\n\ts_mov_b64 %1, %6\n\ts_mov_b64 %2, %7\n\ts_mov_b64 %3, %8\n\ts_mov_b64 %4, %9" \
                     : "=&s"(kp[0]), "=&s"(kp[1]), "=&s"(kp[2]), "=&s"(kp[3]), "=&s"(kp[4]) : "s"(ka[0]), "s"(ka[1]), "s"(ka[2]), "s"(ka[3]), "s"(kw) : "memory"); SB(); } while (0)
#define KPTR(i) (kp[i])
    const int tid = threadIdx.x;
    const int w = __builtin_amdgcn_readfirstlane(tid >> 6);
    const int lane = tid & 63;
    const int bid = (int)blockIdx.x - NPF;
    const int xcd = blockIdx.x & 7, idx = bid >> 3;
    if (bid < 0) {
        const int pw = (blockIdx.x >> 3) * 8 + w;
        KWAIT();
        const char* qb = (const char*)KPTR(1) + (xcd >> 1) * 896 * 4 + (long)((xcd & 1) * 2) * (64 * N_ * 4);
        const char* xb = (const char*)KPTR(0) + (long)(xcd & 1) * 1024 * 2048;
        const u32 dst = (u32)(size_t)(smem) + w * 256;
        {
            const int m = (xcd & 1) * 4 + (blockIdx.x >> 3);
            const u32 off = (u32)((m * 256 + (w & 3) * 64 + lane) * 2048 + (int)((TSEQ(m >> 1) >> (4 * (w >> 2))) & 15) * 128);
            asm volatile("s_mov_b32 m0, %2\n\ts_nop 0\n\tglobal_load_lds_dword %0, %1" :: "v"(off), "s"(KPTR(0)), "s"(dst) : "memory");
        }
#pragma unroll 1
        for (int G = pw; G < 168; G += 32) {
            int kind = 0, arg = 0, e = 0;
            if (G >= 0) { kind = 0; arg = 2; e = G - 0 + (xcd >> 1) * 4; }
            if (G >= 4) { kind = 0; arg = 3; e = G - 4 + (xcd >> 1) * 4; }
            if (G >= 8) { kind = 0; arg = 4; e = G - 8 + (xcd >> 1) * 4; }
            if (G >= 12) { kind = 1; arg = 8; e = G - 12; }
            if (G >= 32) { kind = 0; arg = 5; e = G - 32 + (xcd >> 1) * 4; }
            if (G >= 36) { kind = 1; arg = 8; e = G - 16; }
            if (G >= 64) { kind = 1; arg = 8; e = G - 16; }
            if (G >= 72) { kind = 0; arg = 6; e = G - 72 + (xcd >> 1) * 4; }
            if (G >= 76) { kind = 0; arg = 7; e = G - 76 + (xcd >> 1) * 4; }
            if (G >= 80) { kind = 1; arg = 12; e = G - 80; }
            if (G >= 96) { kind = 0; arg = 8; e = G - 96 + (xcd >> 1) * 4; }
            if (G >= 100) { kind = 0; arg = 9; e = G - 100 + (xcd >> 1) * 4; }
            if (G >= 104) { kind = 1; arg = 12; e = G - 88; }
            if (G >= 128) { kind = 0; arg = 10; e = G - 128 + (xcd >> 1) * 4; }
            if (G >= 132) { kind = 1; arg = 12; e = G - 92; }
            if (G >= 148) { kind = 0; arg = 11; e = G - 148 + (xcd >> 1) * 4; }
            if (G >= 152) { kind = 0; arg = 12; e = G - 152 + (xcd >> 1) * 4; }
            if (G >= 156) { kind = 0; arg = 13; e = G - 156 + (xcd >> 1) * 4; }
            if (G >= 160) { kind = 0; arg = 14; e = G - 160 + (xcd >> 1) * 4; }
            if (G >= 164) { kind = 0; arg = 15; e = G - 164 + (xcd >> 1) * 4; }
            asm volatile("s_waitcnt vmcnt(%0)" :: "i"(PACE) : "memory");
            if (kind) {
                const int L = e * 64 + lane;
                const int r = L / 28, c = L - r * 28;
                const u32 off = (u32)((arg * 64 + r) * (N_ * 4) + c * 128);
                asm volatile("s_mov_b32 m0, %2\n\ts_nop 0\n\tglobal_load_lds_dword %0, %1" :: "v"(off), "s"(qb), "s"(dst) : "memory");
            } else {
                const int t = (int)((TSEQ((xcd & 1) * 2 + (e >> 3)) >> (4 * arg)) & 15);
                const u32 off = (u32)((e * 64 + lane) * 2048 + t * 128);
                asm volatile("s_mov_b32 m0, %2\n\ts_nop 0\n\tglobal_load_lds_dword %0, %1" :: "v"(off), "s"(xb), "s"(dst) : "memory");
            }
        }
        asm volatile("s_waitcnt vmcnt(0)" ::: "memory");
        return;
    }
    const int mt = (xcd & 1) * 4 + (idx & 3);
    const int nt = (xcd >> 1) * 7 + (idx >> 2);
    const int row0 = mt * BM, col0 = nt * BN;

    if (__builtin_expect(w >= 4, 0)) {
        const int p = w - 4;
        const int l = lane & 31, h = lane >> 5;
        const int g = lane >> 4;
        u32 xoff[4];
#pragma unroll
        for (int i = 0; i < 4; ++i)
            xoff[i] = (u32)(((row0 + p * 64 + i * 8 + (lane >> 3)) * K_ + (((lane & 7) ^ ((i & 1) * 4 + g)) * 8)) * 2 - i * 1024);
        const u32 ldsx = (u32)(size_t)(smem) + p * 8192;
        auto issue_x1 = [&](int tile, int hh, int slotoff, int i) {
            const u16* base = (const u16*)KPTR(0) + (long)hh * 32 * K_ + tile * 64;
            const u32 dst = ldsx + slotoff + hh * 4096;
            if (i == 0) asm volatile("s_mov_b32 m0, %2\n\ts_nop 0\n\tglobal_load_lds_dwordx4 %0, %1" :: "v"(xoff[0]), "s"(base), "s"(dst) : "memory");
            else asm volatile("global_load_lds_dwordx4 %0, %1 offset:%2" :: "v"(xoff[i]), "s"(base), "i"(i * 1024) : "memory");
        };
        u32 qoff[8];
#pragma unroll
        for (int r = 0; r < 8; ++r) qoff[r] = (u32)(((p * 16 + h * 8 + r) * N_ + col0 + l * 4) * 4);
        auto issue_q1 = [&](int t, i32x4 (&Q)[8], int r) {
            const int* base = (const int*)KPTR(1) + (long)t * 64 * N_;
            asm volatile("global_load_dwordx4 %0, %1, %2 " : "=v"(Q[r]) : "v"(qoff[r]), "s"(base) : "memory");
        };
        u32 lutl = (u32)(size_t)(smem) + OFF_L + l * 4;
        u32 bdst = (u32)(size_t)(smem) + OFF_B + (p >> 1) * BS + ((2 * p + h) & 3) * 2048 + ((l >> 4) * 64 + (l & 15)) * 16;
        u32 V[4][8];
#define LUT1(Q, j, r) asm volatile("v_lshl_add_u32 %0, %1, 8, %2\n\tds_read_u16 %0, %0 offset:%3" : "=&v"(V[j][r]) \
                                   : "v"(Q[r][j]), "v"(lutl), "i"(((j) >> 1) * 128 + ((j) & 1) * 2) : "memory")
#define LUT4(Q, e) do { LUT1(Q, (e) >> 1, ((e) & 1) * 4); LUT1(Q, (e) >> 1, ((e) & 1) * 4 + 1); \
                        LUT1(Q, (e) >> 1, ((e) & 1) * 4 + 2); LUT1(Q, (e) >> 1, ((e) & 1) * 4 + 3); SB(); } while (0)
#define WRB(j, n) do { u32x4 b_; \
                       asm volatile("s_waitcnt lgkmcnt(" #n ")\n\t" \
                                    "v_lshl_or_b32 %0, %5, 16, %4\n\tv_lshl_or_b32 %1, %7, 16, %6\n\t" \
                                    "v_lshl_or_b32 %2, %9, 16, %8\n\tv_lshl_or_b32 %3, %11, 16, %10" \
                                    : "=&v"(b_[0]), "=&v"(b_[1]), "=&v"(b_[2]), "=&v"(b_[3]) \
                                    : "v"(V[j][0]), "v"(V[j][1]), "v"(V[j][2]), "v"(V[j][3]), "v"(V[j][4]), "v"(V[j][5]), "v"(V[j][6]), "v"(V[j][7]) : "memory"); \
                       asm volatile("ds_write_b128 %0, %1 offset:%2" :: "v"(bdst), "v"(b_), "i"((j) * 256) : "memory"); SB(); } while (0)
        const int i2 = mt >> 1, h2 = mt & 1;
        const u32 tseq = (u32)TSEQ(i2);
        const u32 ldsw = (u32)(size_t)(smem) + OFF_B + p * 4096;
        u32 wv = (u32)(nt * 16384 + p * 4096 + lane * 16);
        u32 wo = (u32)(nt * 16384 + p * 4096 + h * 2048 + (l >> 4) * 1024 + (l & 15) * 16);
        u32 po = (u32)(WS_F + (nt * 4 + p) * 256 + lane * 4);
        u32 fo0 = po + (u32)(i2 * 16 + h2 * 8);
        u32 no = (u32)(nt * 256 + (lane & 1) * 4);
        u32 nvl = no;
        KWAIT();
        BAR();
        const char* st1 = (const char*)KPTR(4) + WS_W + (long)(i2 + 4) * TW + h2 * 512;
        const char* qa1 = (const char*)KPTR(1);
        const int tb = 8 + (p >> 1) * 4 + i2, qd = h2 * 2 + (p & 1);
        const char* qbg = (const char*)KPTR(1) + (long)(tb * 64 + (qd - p) * 16) * (N_ * 4);
        const char* stb = (const char*)KPTR(4) + WS_W + (long)tb * TW + (qd - p) * 4096;
        u32 fob = po + (u32)((qd - p) * 256 + tb * 16);
        asm volatile("" : "+v"(wo), "+v"(fo0), "+v"(fob), "+v"(wv), "+v"(po), "+v"(bdst), "+v"(lutl), "+v"(qoff[0]));
        asm volatile("" : "+v"(xoff[0]), "+v"(xoff[1]), "+v"(xoff[2]), "+v"(xoff[3]));
        asm volatile("" : "+s"(st1), "+s"(qa1), "+s"(qbg), "+s"(stb));
        {
            unsigned long ex_;
            asm volatile("s_mov_b64 %0, exec\n\ts_mov_b64 exec, 0\n\t"
                         "s_cmp_eq_u32 %1, 0\n\ts_cbranch_scc1 .Lwp30\n\t"
                         "s_cmp_eq_u32 %1, 1\n\ts_cbranch_scc1 .Lwp31\n\t"
                         "s_cmp_eq_u32 %1, 2\n\ts_cbranch_scc1 .Lwp32\n\t"
                         "s_branch .Lwp33\n\t"
                         ".Lwp98:\n\ts_mov_b64 exec, %0" : "=&s"(ex_) : "s"(p) : "scc", "memory");
        }
        asm volatile(
            "s_mov_b32 s73, 65536\n\t"
            "s_mov_b64 s[84:85], %[ws]\n\t"
            "s_add_u32 s84, s84, 65536\n\t"
            "s_addc_u32 s85, s85, 0\n\t"
            "s_bfe_u32 s56, %[tseq], 262144\n\t"
            "s_bfe_u32 s57, %[tseq], 262148\n\t"
            "s_bfe_u32 s58, %[tseq], 262152\n\t"
            "s_bfe_u32 s59, %[tseq], 262156\n\t"
            "s_bfe_u32 s60, %[tseq], 262160\n\t"
            "s_bfe_u32 s61, %[tseq], 262164\n\t"
            "s_bfe_u32 s62, %[tseq], 262168\n\t"
            "s_bfe_u32 s63, %[tseq], 262172\n\t"
            "s_barrier\n\t"
            ".Lwp30:\n\t"
            "s_cbranch_execz .Lwp98\n\t"
            "s_add_u32 m0, %[ldsx], 0\n\t"
            "s_lshl_b32 s76, s56, 7\n\t"
            "s_add_u32 s68, %[xpl], s76\n\t"
            "s_addc_u32 s69, %[xph], 0\n\t"
            "global_load_lds_dwordx4 %[x0], s[68:69] offset:0\n\t"
            "global_load_lds_dwordx4 %[x1], s[68:69] offset:1024\n\t"
            "global_load_lds_dwordx4 %[x2], s[68:69] offset:2048\n\t"
            "global_load_lds_dwordx4 %[x3], s[68:69] offset:3072\n\t"
            "s_add_u32 m0, %[ldsx], 4096\n\t"
            "s_lshl_b32 s76, s56, 7\n\t"
            "s_add_u32 s76, s76, 65536\n\t"
            "s_add_u32 s68, %[xpl], s76\n\t"
            "s_addc_u32 s69, %[xph], 0\n\t"
            "global_load_lds_dwordx4 %[x0], s[68:69] offset:0\n\t"
            "global_load_lds_dwordx4 %[x1], s[68:69] offset:1024\n\t"
            "global_load_lds_dwordx4 %[x2], s[68:69] offset:2048\n\t"
            "global_load_lds_dwordx4 %[x3], s[68:69] offset:3072\n\t"
            "global_load_dword %[nvl], %[nvl], %[ws] sc1\n\t"
            "s_mul_i32 s76, s57, 917504\n\t"
            "s_mov_b64 s[88:89], %[qa1]\n\t"
            "s_add_u32 s88, s88, s76\n\t"
            "s_addc_u32 s89, s89, 0\n\t"
            "global_load_dwordx4 v[128:131], %[qo], s[88:89] \n\t"
            "s_add_u32 s88, s88, 14336\n\t"
            "s_addc_u32 s89, s89, 0\n\t"
            "global_load_dwordx4 v[132:135], %[qo], s[88:89] \n\t"
            "s_add_u32 s88, s88, 14336\n\t"
            "s_addc_u32 s89, s89, 0\n\t"
            "global_load_dwordx4 v[136:139], %[qo], s[88:89] \n\t"
            "s_add_u32 s88, s88, 14336\n\t"
            "s_addc_u32 s89, s89, 0\n\t"
            "global_load_dwordx4 v[140:143], %[qo], s[88:89] \n\t"
            "s_add_u32 s88, s88, 14336\n\t"
            "s_addc_u32 s89, s89, 0\n\t"
            "global_load_dwordx4 v[144:147], %[qo], s[88:89] \n\t"
            "s_add_u32 s88, s88, 14336\n\t"
            "s_addc_u32 s89, s89, 0\n\t"
            "global_load_dwordx4 v[148:151], %[qo], s[88:89] \n\t"
            "s_add_u32 s88, s88, 14336\n\t"
            "s_addc_u32 s89, s89, 0\n\t"
            "global_load_dwordx4 v[152:155], %[qo], s[88:89] \n\t"
            "s_add_u32 s88, s88, 14336\n\t"
            "s_addc_u32 s89, s89, 0\n\t"
            "global_load_dwordx4 v[156:159], %[qo], s[88:89] \n\t"
            "s_waitcnt vmcnt(9)\n\t"
            "s_barrier\n\t"
            ".Lwp31:\n\t"
            "s_cbranch_execz .Lwp98\n\t"
            "s_lshl_b32 s76, %[h2], 9\n\t"
            "v_add_u32 v193, s76, %[bd]\n\t"
            "ds_read_b128 v[196:199], v193\n\t"
            "ds_read_b128 v[200:203], v193 offset:256\n\t"
            "s_sub_u32 s70, %[st1l], 1835008\n\t"
            "s_subb_u32 s71, %[st1h], 0\n\t"
            "s_waitcnt lgkmcnt(0)\n\t"
            "global_store_dwordx4 %[wo], v[196:199], s[70:71] sc1\n\t"
            "global_store_dwordx4 %[wo], v[200:203], s[70:71] offset:256 sc1\n\t"
            "s_waitcnt vmcnt(10)\n\t"
            "s_mov_b32 s76, 1664525\n\t"
            "v_mul_lo_u32 %[nvl], %[nvl], s76\n\t"
            "v_add_u32 %[nvl], 1013904223, %[nvl]\n\t"
            "s_setprio 1\n\t"
            "s_waitcnt vmcnt(9)\n\t"
            "v_lshl_add_u32 v160, v128, 8, %[lutl]\n\t"
            "ds_read_u16 v160, v160 offset:0\n\t"
            "v_lshl_add_u32 v168, v129, 8, %[lutl]\n\t"
            "ds_read_u16 v168, v168 offset:2\n\t"
            "v_lshl_add_u32 v176, v130, 8, %[lutl]\n\t"
            "ds_read_u16 v176, v176 offset:128\n\t"
            "v_lshl_add_u32 v184, v131, 8, %[lutl]\n\t"
            "ds_read_u16 v184, v184 offset:130\n\t"
            "s_add_u32 m0, %[ldsx], 32768\n\t"
            "s_lshl_b32 s76, s57, 7\n\t"
            "s_add_u32 s68, %[xpl], s76\n\t"
            "s_addc_u32 s69, %[xph], 0\n\t"
            "global_load_lds_dwordx4 %[x0], s[68:69] offset:0\n\t"
            "s_waitcnt vmcnt(9)\n\t"
            "v_lshl_add_u32 v161, v132, 8, %[lutl]\n\t"
            "ds_read_u16 v161, v161 offset:0\n\t"
            "v_lshl_add_u32 v169, v133, 8, %[lutl]\n\t"
            "ds_read_u16 v169, v169 offset:2\n\t"
            "v_lshl_add_u32 v177, v134, 8, %[lutl]\n\t"
            "ds_read_u16 v177, v177 offset:128\n\t"
            "v_lshl_add_u32 v185, v135, 8, %[lutl]\n\t"
            "ds_read_u16 v185, v185 offset:130\n\t"
            "global_load_lds_dwordx4 %[x1], s[68:69] offset:1024\n\t"
            "s_waitcnt vmcnt(9)\n\t"
            "v_lshl_add_u32 v162, v136, 8, %[lutl]\n\t"
            "ds_read_u16 v162, v162 offset:0\n\t"
            "v_lshl_add_u32 v170, v137, 8, %[lutl]\n\t"
            "ds_read_u16 v170, v170 offset:2\n\t"
            "v_lshl_add_u32 v178, v138, 8, %[lutl]\n\t"
            "ds_read_u16 v178, v178 offset:128\n\t"
            "v_lshl_add_u32 v186, v139, 8, %[lutl]\n\t"
            "ds_read_u16 v186, v186 offset:130\n\t"
            "global_load_lds_dwordx4 %[x2], s[68:69] offset:2048\n\t"
            "s_waitcnt vmcnt(9)\n\t"
            "v_lshl_add_u32 v163, v140, 8, %[lutl]\n\t"
            "ds_read_u16 v163, v163 offset:0\n\t"
            "v_lshl_add_u32 v171, v141, 8, %[lutl]\n\t"
            "ds_read_u16 v171, v171 offset:2\n\t"
            "v_lshl_add_u32 v179, v142, 8, %[lutl]\n\t"
            "ds_read_u16 v179, v179 offset:128\n\t"
            "v_lshl_add_u32 v187, v143, 8, %[lutl]\n\t"
            "ds_read_u16 v187, v187 offset:130\n\t"
            "global_load_lds_dwordx4 %[x3], s[68:69] offset:3072\n\t"
            "s_waitcnt vmcnt(9)\n\t"
            "v_lshl_add_u32 v164, v144, 8, %[lutl]\n\t"
            "ds_read_u16 v164, v164 offset:0\n\t"
            "v_lshl_add_u32 v172, v145, 8, %[lutl]\n\t"
            "ds_read_u16 v172, v172 offset:2\n\t"
            "v_lshl_add_u32 v180, v146, 8, %[lutl]\n\t"
            "ds_read_u16 v180, v180 offset:128\n\t"
            "v_lshl_add_u32 v188, v147, 8, %[lutl]\n\t"
            "ds_read_u16 v188, v188 offset:130\n\t"
            "s_add_u32 m0, %[ldsx], 36864\n\t"
            "s_lshl_b32 s76, s57, 7\n\t"
            "s_add_u32 s76, s76, 65536\n\t"
            "s_add_u32 s68, %[xpl], s76\n\t"
            "s_addc_u32 s69, %[xph], 0\n\t"
            "global_load_lds_dwordx4 %[x0], s[68:69] offset:0\n\t"
            "s_waitcnt vmcnt(9)\n\t"
            "v_lshl_add_u32 v165, v148, 8, %[lutl]\n\t"
            "ds_read_u16 v165, v165 offset:0\n\t"
            "v_lshl_add_u32 v173, v149, 8, %[lutl]\n\t"
            "ds_read_u16 v173, v173 offset:2\n\t"
            "v_lshl_add_u32 v181, v150, 8, %[lutl]\n\t"
            "ds_read_u16 v181, v181 offset:128\n\t"
            "v_lshl_add_u32 v189, v151, 8, %[lutl]\n\t"
            "ds_read_u16 v189, v189 offset:130\n\t"
            "global_load_lds_dwordx4 %[x1], s[68:69] offset:1024\n\t"
            "s_waitcnt vmcnt(9)\n\t"
            "v_lshl_add_u32 v166, v152, 8, %[lutl]\n\t"
            "ds_read_u16 v166, v166 offset:0\n\t"
            "v_lshl_add_u32 v174, v153, 8, %[lutl]\n\t"
            "ds_read_u16 v174, v174 offset:2\n\t"
            "v_lshl_add_u32 v182, v154, 8, %[lutl]\n\t"
            "ds_read_u16 v182, v182 offset:128\n\t"
            "v_lshl_add_u32 v190, v155, 8, %[lutl]\n\t"
            "ds_read_u16 v190, v190 offset:130\n\t"
            "global_load_lds_dwordx4 %[x2], s[68:69] offset:2048\n\t"
            "s_waitcnt vmcnt(9)\n\t"
            "v_lshl_add_u32 v167, v156, 8, %[lutl]\n\t"
            "ds_read_u16 v167, v167 offset:0\n\t"
            "v_lshl_add_u32 v175, v157, 8, %[lutl]\n\t"
            "ds_read_u16 v175, v175 offset:2\n\t"
            "v_lshl_add_u32 v183, v158, 8, %[lutl]\n\t"
            "ds_read_u16 v183, v183 offset:128\n\t"
            "v_lshl_add_u32 v191, v159, 8, %[lutl]\n\t"
            "ds_read_u16 v191, v191 offset:130\n\t"
            "global_load_lds_dwordx4 %[x3], s[68:69] offset:3072\n\t"
            "s_setprio 0\n\t"
            "s_waitcnt vmcnt(8)\n\t"
            "s_mov_b64 exec, 3\n\t"
            "global_store_dword %[fo0], %[nvl], %[ws] offset:0 sc1\n\t"
            "s_mov_b64 exec, -1\n\t"
            "s_waitcnt lgkmcnt(3)\n\t"
            "v_lshl_or_b32 v128, v161, 16, v160\n\t"
            "v_lshl_or_b32 v129, v163, 16, v162\n\t"
            "v_lshl_or_b32 v130, v165, 16, v164\n\t"
            "v_lshl_or_b32 v131, v167, 16, v166\n\t"
            "ds_write_b128 %[bd], v[128:131] offset:16384\n\t"
            "s_waitcnt lgkmcnt(3)\n\t"
            "v_lshl_or_b32 v132, v169, 16, v168\n\t"
            "v_lshl_or_b32 v133, v171, 16, v170\n\t"
            "v_lshl_or_b32 v134, v173, 16, v172\n\t"
            "v_lshl_or_b32 v135, v175, 16, v174\n\t"
            "ds_write_b128 %[bd], v[132:135] offset:16640\n\t"
            "s_waitcnt lgkmcnt(3)\n\t"
            "v_lshl_or_b32 v136, v177, 16, v176\n\t"
            "v_lshl_or_b32 v137, v179, 16, v178\n\t"
            "v_lshl_or_b32 v138, v181, 16, v180\n\t"
            "v_lshl_or_b32 v139, v183, 16, v182\n\t"
            "ds_write_b128 %[bd], v[136:139] offset:16896\n\t"
            "s_waitcnt lgkmcnt(3)\n\t"
            "v_lshl_or_b32 v140, v185, 16, v184\n\t"
            "v_lshl_or_b32 v141, v187, 16, v186\n\t"
            "v_lshl_or_b32 v142, v189, 16, v188\n\t"
            "v_lshl_or_b32 v143, v191, 16, v190\n\t"
            "ds_write_b128 %[bd], v[140:143] offset:17152\n\t"
            "s_add_u32 m0, %[ldsx], 65536\n\t"
            "s_lshl_b32 s76, s58, 7\n\t"
            "s_add_u32 s68, %[xpl], s76\n\t"
            "s_addc_u32 s69, %[xph], 0\n\t"
            "global_load_lds_dwordx4 %[x0], s[68:69] offset:0\n\t"
            "global_load_lds_dwordx4 %[x1], s[68:69] offset:1024\n\t"
            "global_load_lds_dwordx4 %[x2], s[68:69] offset:2048\n\t"
            "global_load_lds_dwordx4 %[x3], s[68:69] offset:3072\n\t"
            "s_add_u32 m0, %[ldsx], 69632\n\t"
            "s_lshl_b32 s76, s58, 7\n\t"
            "s_add_u32 s76, s76, 65536\n\t"
            "s_add_u32 s68, %[xpl], s76\n\t"
            "s_addc_u32 s69, %[xph], 0\n\t"
            "global_load_lds_dwordx4 %[x0], s[68:69] offset:0\n\t"
            "global_load_lds_dwordx4 %[x1], s[68:69] offset:1024\n\t"
            "global_load_lds_dwordx4 %[x2], s[68:69] offset:2048\n\t"
            "global_load_lds_dwordx4 %[x3], s[68:69] offset:3072\n\t"
            "s_waitcnt vmcnt(9) lgkmcnt(0)\n\t"
            "s_barrier\n\t"
            ".Lwp32:\n\t"
            "s_cbranch_execz .Lwp98\n\t"
            "global_load_dword v192, %[po], %[ws] sc1\n\t"
            "s_add_u32 m0, %[ldsx], 0\n\t"
            "s_lshl_b32 s76, s59, 7\n\t"
            "s_add_u32 s68, %[xpl], s76\n\t"
            "s_addc_u32 s69, %[xph], 0\n\t"
            "global_load_lds_dwordx4 %[x0], s[68:69] offset:0\n\t"
            "global_load_lds_dwordx4 %[x1], s[68:69] offset:1024\n\t"
            "global_load_lds_dwordx4 %[x2], s[68:69] offset:2048\n\t"
            "global_load_lds_dwordx4 %[x3], s[68:69] offset:3072\n\t"
            "s_add_u32 m0, %[ldsx], 4096\n\t"
            "s_lshl_b32 s76, s59, 7\n\t"
            "s_add_u32 s76, s76, 65536\n\t"
            "s_add_u32 s68, %[xpl], s76\n\t"
            "s_addc_u32 s69, %[xph], 0\n\t"
            "global_load_lds_dwordx4 %[x0], s[68:69] offset:0\n\t"
            "global_load_lds_dwordx4 %[x1], s[68:69] offset:1024\n\t"
            "global_load_lds_dwordx4 %[x2], s[68:69] offset:2048\n\t"
            "global_load_lds_dwordx4 %[x3], s[68:69] offset:3072\n\t"
            "s_waitcnt vmcnt(8)\n\t"
            "s_lshl_b32 s76, s58, 2\n\t"
            ".Lzc0:\n\t"
            "v_cmp_eq_u32 vcc, v192, %[nvl]\n\t"
            "s_lshr_b64 s[74:75], vcc, s76\n\t"
            "s_and_b32 s74, s74, 15\n\t"
            "s_cmp_eq_u32 s74, 15\n\t"
            "s_cbranch_scc1 .Lzd0\n\t"
            "s_cmp_eq_u32 s73, 0\n\t"
            "s_cbranch_scc1 .Lzd0\n\t"
            "s_sub_u32 s73, s73, 1\n\t"
            "global_load_dword v192, %[po], %[ws] sc1\n\t"
            "s_waitcnt vmcnt(0)\n\t"
            "s_branch .Lzc0\n\t"
            ".Lzd0:\n\t"
            "s_add_u32 m0, %[ldsw], 32768\n\t"
            "s_mul_i32 s76, s58, 458752\n\t"
            "s_add_u32 s70, s84, s76\n\t"
            "s_addc_u32 s71, s85, 0\n\t"
            "global_load_lds_dwordx4 %[wv], s[70:71] offset:0 sc1\n\t"
            "global_load_lds_dwordx4 %[wv], s[70:71] offset:1024 sc1\n\t"
            "global_load_lds_dwordx4 %[wv], s[70:71] offset:2048 sc1\n\t"
            "global_load_lds_dwordx4 %[wv], s[70:71] offset:3072 sc1\n\t"
            "s_cmp_lg_u32 %[h2], 0\n\t"
            "s_cbranch_scc1 .Lzs1\n\t"
            "global_store_dwordx4 %[wo], v[128:131], %[st1] sc1\n\t"
            "global_store_dwordx4 %[wo], v[132:135], %[st1] offset:256 sc1\n\t"
            "s_branch .Lzt1\n\t"
            ".Lzs1:\n\t"
            "global_store_dwordx4 %[wo], v[136:139], %[st1] sc1\n\t"
            "global_store_dwordx4 %[wo], v[140:143], %[st1] offset:256 sc1\n\t"
            ".Lzt1:\n\t"
            "s_lshl_b32 s76, s59, 2\n\t"
            ".Lzc2:\n\t"
            "v_cmp_eq_u32 vcc, v192, %[nvl]\n\t"
            "s_lshr_b64 s[74:75], vcc, s76\n\t"
            "s_and_b32 s74, s74, 15\n\t"
            "s_cmp_eq_u32 s74, 15\n\t"
            "s_cbranch_scc1 .Lzd2\n\t"
            "s_cmp_eq_u32 s73, 0\n\t"
            "s_cbranch_scc1 .Lzd2\n\t"
            "s_sub_u32 s73, s73, 1\n\t"
            "global_load_dword v192, %[po], %[ws] sc1\n\t"
            "s_waitcnt vmcnt(0)\n\t"
            "s_branch .Lzc2\n\t"
            ".Lzd2:\n\t"
            "s_add_u32 m0, %[ldsw], 0\n\t"
            "s_mul_i32 s76, s59, 458752\n\t"
            "s_add_u32 s70, s84, s76\n\t"
            "s_addc_u32 s71, s85, 0\n\t"
            "global_load_lds_dwordx4 %[wv], s[70:71] offset:0 sc1\n\t"
            "global_load_lds_dwordx4 %[wv], s[70:71] offset:1024 sc1\n\t"
            "global_load_lds_dwordx4 %[wv], s[70:71] offset:2048 sc1\n\t"
            "global_load_lds_dwordx4 %[wv], s[70:71] offset:3072 sc1\n\t"
            "s_waitcnt vmcnt(6)\n\t"
            "s_barrier\n\t"
            "s_cmp_ge_u32 %[p], 2\n\t"
            "s_cbranch_scc1 .Lzcls1\n\t"
            ".Lwp33:\n\t"
            "s_cbranch_execz .Lwp98\n\t"
            "s_lshl_b32 s76, s60, 2\n\t"
            ".Lzc3:\n\t"
            "v_cmp_eq_u32 vcc, v192, %[nvl]\n\t"
            "s_lshr_b64 s[74:75], vcc, s76\n\t"
            "s_and_b32 s74, s74, 15\n\t"
            "s_cmp_eq_u32 s74, 15\n\t"
            "s_cbranch_scc1 .Lzd3\n\t"
            "s_cmp_eq_u32 s73, 0\n\t"
            "s_cbranch_scc1 .Lzd3\n\t"
            "s_sub_u32 s73, s73, 1\n\t"
            "global_load_dword v192, %[po], %[ws] sc1\n\t"
            "s_waitcnt vmcnt(0)\n\t"
            "s_branch .Lzc3\n\t"
            ".Lzd3:\n\t"
            "s_add_u32 m0, %[ldsw], 16384\n\t"
            "s_mul_i32 s76, s60, 458752\n\t"
            "s_add_u32 s70, s84, s76\n\t"
            "s_addc_u32 s71, s85, 0\n\t"
            "global_load_lds_dwordx4 %[wv], s[70:71] offset:0 sc1\n\t"
            "global_load_lds_dwordx4 %[wv], s[70:71] offset:1024 sc1\n\t"
            "global_load_lds_dwordx4 %[wv], s[70:71] offset:2048 sc1\n\t"
            "global_load_lds_dwordx4 %[wv], s[70:71] offset:3072 sc1\n\t"
            "s_waitcnt vmcnt(8)\n\t"
            "s_mov_b64 exec, 3\n\t"
            "global_store_dword %[fo0], %[nvl], %[ws] offset:64 sc1\n\t"
            "s_mov_b64 exec, -1\n\t"
            "s_mov_b64 s[88:89], %[qbg]\n\t"
            "global_load_dwordx4 v[128:131], %[qo], s[88:89] \n\t"
            "s_add_u32 s88, s88, 14336\n\t"
            "s_addc_u32 s89, s89, 0\n\t"
            "global_load_dwordx4 v[132:135], %[qo], s[88:89] \n\t"
            "s_add_u32 s88, s88, 14336\n\t"
            "s_addc_u32 s89, s89, 0\n\t"
            "global_load_dwordx4 v[136:139], %[qo], s[88:89] \n\t"
            "s_add_u32 s88, s88, 14336\n\t"
            "s_addc_u32 s89, s89, 0\n\t"
            "global_load_dwordx4 v[140:143], %[qo], s[88:89] \n\t"
            "s_add_u32 s88, s88, 14336\n\t"
            "s_addc_u32 s89, s89, 0\n\t"
            "global_load_dwordx4 v[144:147], %[qo], s[88:89] \n\t"
            "s_add_u32 s88, s88, 14336\n\t"
            "s_addc_u32 s89, s89, 0\n\t"
            "global_load_dwordx4 v[148:151], %[qo], s[88:89] \n\t"
            "s_add_u32 s88, s88, 14336\n\t"
            "s_addc_u32 s89, s89, 0\n\t"
            "global_load_dwordx4 v[152:155], %[qo], s[88:89] \n\t"
            "s_add_u32 s88, s88, 14336\n\t"
            "s_addc_u32 s89, s89, 0\n\t"
            "global_load_dwordx4 v[156:159], %[qo], s[88:89] \n\t"
            "s_add_u32 m0, %[ldsx], 32768\n\t"
            "s_lshl_b32 s76, s60, 7\n\t"
            "s_add_u32 s68, %[xpl], s76\n\t"
            "s_addc_u32 s69, %[xph], 0\n\t"
            "global_load_lds_dwordx4 %[x0], s[68:69] offset:0\n\t"
            "global_load_lds_dwordx4 %[x1], s[68:69] offset:1024\n\t"
            "global_load_lds_dwordx4 %[x2], s[68:69] offset:2048\n\t"
            "global_load_lds_dwordx4 %[x3], s[68:69] offset:3072\n\t"
            "s_add_u32 m0, %[ldsx], 36864\n\t"
            "s_lshl_b32 s76, s60, 7\n\t"
            "s_add_u32 s76, s76, 65536\n\t"
            "s_add_u32 s68, %[xpl], s76\n\t"
            "s_addc_u32 s69, %[xph], 0\n\t"
            "global_load_lds_dwordx4 %[x0], s[68:69] offset:0\n\t"
            "global_load_lds_dwordx4 %[x1], s[68:69] offset:1024\n\t"
            "global_load_lds_dwordx4 %[x2], s[68:69] offset:2048\n\t"
            "global_load_lds_dwordx4 %[x3], s[68:69] offset:3072\n\t"
            "s_waitcnt vmcnt(21)\n\t"
            "s_barrier\n\t"
            ".Lwp34:\n\t"
            "s_cbranch_execz .Lwp35\n\t"
            "global_load_dword v192, %[po], %[ws] sc1\n\t"
            "s_waitcnt vmcnt(9)\n\t"
            "s_setprio 0\n\t"
            "s_add_u32 m0, %[ldsx], 65536\n\t"
            "s_lshl_b32 s76, s61, 7\n\t"
            "s_add_u32 s68, %[xpl], s76\n\t"
            "s_addc_u32 s69, %[xph], 0\n\t"
            "global_load_lds_dwordx4 %[x0], s[68:69] offset:0\n\t"
            "v_lshl_add_u32 v160, v128, 8, %[lutl]\n\t"
            "ds_read_u16 v160, v160 offset:0\n\t"
            "v_lshl_add_u32 v161, v132, 8, %[lutl]\n\t"
            "ds_read_u16 v161, v161 offset:0\n\t"
            "v_lshl_add_u32 v162, v136, 8, %[lutl]\n\t"
            "ds_read_u16 v162, v162 offset:0\n\t"
            "v_lshl_add_u32 v163, v140, 8, %[lutl]\n\t"
            "ds_read_u16 v163, v163 offset:0\n\t"
            "global_load_lds_dwordx4 %[x1], s[68:69] offset:1024\n\t"
            "v_lshl_add_u32 v164, v144, 8, %[lutl]\n\t"
            "ds_read_u16 v164, v164 offset:0\n\t"
            "v_lshl_add_u32 v165, v148, 8, %[lutl]\n\t"
            "ds_read_u16 v165, v165 offset:0\n\t"
            "v_lshl_add_u32 v166, v152, 8, %[lutl]\n\t"
            "ds_read_u16 v166, v166 offset:0\n\t"
            "v_lshl_add_u32 v167, v156, 8, %[lutl]\n\t"
            "ds_read_u16 v167, v167 offset:0\n\t"
            "global_load_lds_dwordx4 %[x2], s[68:69] offset:2048\n\t"
            "v_lshl_add_u32 v168, v129, 8, %[lutl]\n\t"
            "ds_read_u16 v168, v168 offset:2\n\t"
            "v_lshl_add_u32 v169, v133, 8, %[lutl]\n\t"
            "ds_read_u16 v169, v169 offset:2\n\t"
            "v_lshl_add_u32 v170, v137, 8, %[lutl]\n\t"
            "ds_read_u16 v170, v170 offset:2\n\t"
            "v_lshl_add_u32 v171, v141, 8, %[lutl]\n\t"
            "ds_read_u16 v171, v171 offset:2\n\t"
            "global_load_lds_dwordx4 %[x3], s[68:69] offset:3072\n\t"
            "v_lshl_add_u32 v172, v145, 8, %[lutl]\n\t"
            "ds_read_u16 v172, v172 offset:2\n\t"
            "v_lshl_add_u32 v173, v149, 8, %[lutl]\n\t"
            "ds_read_u16 v173, v173 offset:2\n\t"
            "v_lshl_add_u32 v174, v153, 8, %[lutl]\n\t"
            "ds_read_u16 v174, v174 offset:2\n\t"
            "v_lshl_add_u32 v175, v157, 8, %[lutl]\n\t"
            "ds_read_u16 v175, v175 offset:2\n\t"
            "s_add_u32 m0, %[ldsx], 69632\n\t"
            "s_lshl_b32 s76, s61, 7\n\t"
            "s_add_u32 s76, s76, 65536\n\t"
            "s_add_u32 s68, %[xpl], s76\n\t"
            "s_addc_u32 s69, %[xph], 0\n\t"
            "global_load_lds_dwordx4 %[x0], s[68:69] offset:0\n\t"
            "v_lshl_add_u32 v176, v130, 8, %[lutl]\n\t"
            "ds_read_u16 v176, v176 offset:128\n\t"
            "v_lshl_add_u32 v177, v134, 8, %[lutl]\n\t"
            "ds_read_u16 v177, v177 offset:128\n\t"
            "v_lshl_add_u32 v178, v138, 8, %[lutl]\n\t"
            "ds_read_u16 v178, v178 offset:128\n\t"
            "v_lshl_add_u32 v179, v142, 8, %[lutl]\n\t"
            "ds_read_u16 v179, v179 offset:128\n\t"
            "global_load_lds_dwordx4 %[x1], s[68:69] offset:1024\n\t"
            "v_lshl_add_u32 v180, v146, 8, %[lutl]\n\t"
            "ds_read_u16 v180, v180 offset:128\n\t"
            "v_lshl_add_u32 v181, v150, 8, %[lutl]\n\t"
            "ds_read_u16 v181, v181 offset:128\n\t"
            "v_lshl_add_u32 v182, v154, 8, %[lutl]\n\t"
            "ds_read_u16 v182, v182 offset:128\n\t"
            "v_lshl_add_u32 v183, v158, 8, %[lutl]\n\t"
            "ds_read_u16 v183, v183 offset:128\n\t"
            "global_load_lds_dwordx4 %[x2], s[68:69] offset:2048\n\t"
            "v_lshl_add_u32 v184, v131, 8, %[lutl]\n\t"
            "ds_read_u16 v184, v184 offset:130\n\t"
            "v_lshl_add_u32 v185, v135, 8, %[lutl]\n\t"
            "ds_read_u16 v185, v185 offset:130\n\t"
            "v_lshl_add_u32 v186, v139, 8, %[lutl]\n\t"
            "ds_read_u16 v186, v186 offset:130\n\t"
            "v_lshl_add_u32 v187, v143, 8, %[lutl]\n\t"
            "ds_read_u16 v187, v187 offset:130\n\t"
            "global_load_lds_dwordx4 %[x3], s[68:69] offset:3072\n\t"
            "v_lshl_add_u32 v188, v147, 8, %[lutl]\n\t"
            "ds_read_u16 v188, v188 offset:130\n\t"
            "v_lshl_add_u32 v189, v151, 8, %[lutl]\n\t"
            "ds_read_u16 v189, v189 offset:130\n\t"
            "v_lshl_add_u32 v190, v155, 8, %[lutl]\n\t"
            "ds_read_u16 v190, v190 offset:130\n\t"
            "v_lshl_add_u32 v191, v159, 8, %[lutl]\n\t"
            "ds_read_u16 v191, v191 offset:130\n\t"
            "s_setprio 0\n\t"
            "s_waitcnt vmcnt(8)\n\t"
            "s_lshl_b32 s76, s61, 2\n\t"
            ".Lzc4:\n\t"
            "v_cmp_eq_u32 vcc, v192, %[nvl]\n\t"
            "s_lshr_b64 s[74:75], vcc, s76\n\t"
            "s_and_b32 s74, s74, 15\n\t"
            "s_cmp_eq_u32 s74, 15\n\t"
            "s_cbranch_scc1 .Lzd4\n\t"
            "s_cmp_eq_u32 s73, 0\n\t"
            "s_cbranch_scc1 .Lzd4\n\t"
            "s_sub_u32 s73, s73, 1\n\t"
            "global_load_dword v192, %[po], %[ws] sc1\n\t"
            "s_waitcnt vmcnt(0)\n\t"
            "s_branch .Lzc4\n\t"
            ".Lzd4:\n\t"
            "s_add_u32 m0, %[ldsw], 32768\n\t"
            "s_mul_i32 s76, s61, 458752\n\t"
            "s_add_u32 s70, s84, s76\n\t"
            "s_addc_u32 s71, s85, 0\n\t"
            "global_load_lds_dwordx4 %[wv], s[70:71] offset:0 sc1\n\t"
            "global_load_lds_dwordx4 %[wv], s[70:71] offset:1024 sc1\n\t"
            "global_load_lds_dwordx4 %[wv], s[70:71] offset:2048 sc1\n\t"
            "global_load_lds_dwordx4 %[wv], s[70:71] offset:3072 sc1\n\t"
            "s_waitcnt lgkmcnt(15)\n\t"
            "v_lshl_or_b32 v128, v161, 16, v160\n\t"
            "v_lshl_or_b32 v129, v163, 16, v162\n\t"
            "v_lshl_or_b32 v130, v165, 16, v164\n\t"
            "v_lshl_or_b32 v131, v167, 16, v166\n\t"
            "s_waitcnt lgkmcnt(15)\n\t"
            "v_lshl_or_b32 v132, v169, 16, v168\n\t"
            "v_lshl_or_b32 v133, v171, 16, v170\n\t"
            "v_lshl_or_b32 v134, v173, 16, v172\n\t"
            "v_lshl_or_b32 v135, v175, 16, v174\n\t"
            "s_waitcnt lgkmcnt(8)\n\t"
            "v_lshl_or_b32 v136, v177, 16, v176\n\t"
            "v_lshl_or_b32 v137, v179, 16, v178\n\t"
            "v_lshl_or_b32 v138, v181, 16, v180\n\t"
            "v_lshl_or_b32 v139, v183, 16, v182\n\t"
            "s_waitcnt lgkmcnt(0)\n\t"
            "v_lshl_or_b32 v140, v185, 16, v184\n\t"
            "v_lshl_or_b32 v141, v187, 16, v186\n\t"
            "v_lshl_or_b32 v142, v189, 16, v188\n\t"
            "v_lshl_or_b32 v143, v191, 16, v190\n\t"
            "global_store_dwordx4 %[wo], v[128:131], %[stb] offset:0 sc1\n\t"
            "global_store_dwordx4 %[wo], v[132:135], %[stb] offset:256 sc1\n\t"
            "global_store_dwordx4 %[wo], v[136:139], %[stb] offset:512 sc1\n\t"
            "global_store_dwordx4 %[wo], v[140:143], %[stb] offset:768 sc1\n\t"
            "s_barrier\n\t"
            ".Lwp35:\n\t"
            "s_cbranch_execz .Lwp36\n\t"
            "s_lshl_b32 s76, s62, 2\n\t"
            ".Lzc5:\n\t"
            "v_cmp_eq_u32 vcc, v192, %[nvl]\n\t"
            "s_lshr_b64 s[74:75], vcc, s76\n\t"
            "s_and_b32 s74, s74, 15\n\t"
            "s_cmp_eq_u32 s74, 15\n\t"
            "s_cbranch_scc1 .Lzd5\n\t"
            "s_cmp_eq_u32 s73, 0\n\t"
            "s_cbranch_scc1 .Lzd5\n\t"
            "s_sub_u32 s73, s73, 1\n\t"
            "global_load_dword v192, %[po], %[ws] sc1\n\t"
            "s_waitcnt vmcnt(0)\n\t"
            "s_branch .Lzc5\n\t"
            ".Lzd5:\n\t"
            "s_add_u32 m0, %[ldsw], 0\n\t"
            "s_mul_i32 s76, s62, 458752\n\t"
            "s_add_u32 s70, s84, s76\n\t"
            "s_addc_u32 s71, s85, 0\n\t"
            "global_load_lds_dwordx4 %[wv], s[70:71] offset:0 sc1\n\t"
            "global_load_lds_dwordx4 %[wv], s[70:71] offset:1024 sc1\n\t"
            "global_load_lds_dwordx4 %[wv], s[70:71] offset:2048 sc1\n\t"
            "global_load_lds_dwordx4 %[wv], s[70:71] offset:3072 sc1\n\t"
            "s_add_u32 m0, %[ldsx], 0\n\t"
            "s_lshl_b32 s76, s62, 7\n\t"
            "s_add_u32 s68, %[xpl], s76\n\t"
            "s_addc_u32 s69, %[xph], 0\n\t"
            "global_load_lds_dwordx4 %[x0], s[68:69] offset:0\n\t"
            "global_load_lds_dwordx4 %[x1], s[68:69] offset:1024\n\t"
            "global_load_lds_dwordx4 %[x2], s[68:69] offset:2048\n\t"
            "global_load_lds_dwordx4 %[x3], s[68:69] offset:3072\n\t"
            "s_add_u32 m0, %[ldsx], 4096\n\t"
            "s_lshl_b32 s76, s62, 7\n\t"
            "s_add_u32 s76, s76, 65536\n\t"
            "s_add_u32 s68, %[xpl], s76\n\t"
            "s_addc_u32 s69, %[xph], 0\n\t"
            "global_load_lds_dwordx4 %[x0], s[68:69] offset:0\n\t"
            "global_load_lds_dwordx4 %[x1], s[68:69] offset:1024\n\t"
            "global_load_lds_dwordx4 %[x2], s[68:69] offset:2048\n\t"
            "global_load_lds_dwordx4 %[x3], s[68:69] offset:3072\n\t"
            "s_waitcnt vmcnt(12)\n\t"
            "s_mov_b64 exec, 15\n\t"
            "global_store_dword %[fob], %[nvl], %[ws] offset:0 sc1\n\t"
            "s_mov_b64 exec, -1\n\t"
            "s_barrier\n\t"
            "s_lshl_b32 s76, s63, 2\n\t"
            ".Lzc6:\n\t"
            "v_cmp_eq_u32 vcc, v192, %[nvl]\n\t"
            "s_lshr_b64 s[74:75], vcc, s76\n\t"
            "s_and_b32 s74, s74, 15\n\t"
            "s_cmp_eq_u32 s74, 15\n\t"
            "s_cbranch_scc1 .Lzd6\n\t"
            "s_cmp_eq_u32 s73, 0\n\t"
            "s_cbranch_scc1 .Lzd6\n\t"
            "s_sub_u32 s73, s73, 1\n\t"
            "global_load_dword v192, %[po], %[ws] sc1\n\t"
            "s_waitcnt vmcnt(0)\n\t"
            "s_branch .Lzc6\n\t"
            ".Lzd6:\n\t"
            "s_add_u32 m0, %[ldsw], 16384\n\t"
            "s_mul_i32 s76, s63, 458752\n\t"
            "s_add_u32 s70, s84, s76\n\t"
            "s_addc_u32 s71, s85, 0\n\t"
            "global_load_lds_dwordx4 %[wv], s[70:71] offset:0 sc1\n\t"
            "global_load_lds_dwordx4 %[wv], s[70:71] offset:1024 sc1\n\t"
            "global_load_lds_dwordx4 %[wv], s[70:71] offset:2048 sc1\n\t"
            "global_load_lds_dwordx4 %[wv], s[70:71] offset:3072 sc1\n\t"
            "s_add_u32 m0, %[ldsx], 32768\n\t"
            "s_lshl_b32 s76, s63, 7\n\t"
            "s_add_u32 s68, %[xpl], s76\n\t"
            "s_addc_u32 s69, %[xph], 0\n\t"
            "global_load_lds_dwordx4 %[x0], s[68:69] offset:0\n\t"
            "global_load_lds_dwordx4 %[x1], s[68:69] offset:1024\n\t"
            "global_load_lds_dwordx4 %[x2], s[68:69] offset:2048\n\t"
            "global_load_lds_dwordx4 %[x3], s[68:69] offset:3072\n\t"
            "s_add_u32 m0, %[ldsx], 36864\n\t"
            "s_lshl_b32 s76, s63, 7\n\t"
            "s_add_u32 s76, s76, 65536\n\t"
            "s_add_u32 s68, %[xpl], s76\n\t"
            "s_addc_u32 s69, %[xph], 0\n\t"
            "global_load_lds_dwordx4 %[x0], s[68:69] offset:0\n\t"
            "global_load_lds_dwordx4 %[x1], s[68:69] offset:1024\n\t"
            "global_load_lds_dwordx4 %[x2], s[68:69] offset:2048\n\t"
            "global_load_lds_dwordx4 %[x3], s[68:69] offset:3072\n\t"
            "s_waitcnt vmcnt(13)\n\t"
            "s_barrier\n\t"
            "global_load_dword v192, %[po], %[ws] sc1\n\t"
            "s_add_u32 m0, %[ldsx], 65536\n\t"
            "s_add_u32 s68, %[xpl], 1024\n\t"
            "s_addc_u32 s69, %[xph], 0\n\t"
            "global_load_lds_dwordx4 %[x0], s[68:69] offset:0\n\t"
            "global_load_lds_dwordx4 %[x1], s[68:69] offset:1024\n\t"
            "global_load_lds_dwordx4 %[x2], s[68:69] offset:2048\n\t"
            "global_load_lds_dwordx4 %[x3], s[68:69] offset:3072\n\t"
            "s_add_u32 m0, %[ldsx], 69632\n\t"
            "s_add_u32 s68, %[xpl], 66560\n\t"
            "s_addc_u32 s69, %[xph], 0\n\t"
            "global_load_lds_dwordx4 %[x0], s[68:69] offset:0\n\t"
            "global_load_lds_dwordx4 %[x1], s[68:69] offset:1024\n\t"
            "global_load_lds_dwordx4 %[x2], s[68:69] offset:2048\n\t"
            "global_load_lds_dwordx4 %[x3], s[68:69] offset:3072\n\t"
            "s_waitcnt vmcnt(8)\n\t"
            "s_mov_b32 s76, 32\n\t"
            ".Lzc7:\n\t"
            "v_cmp_eq_u32 vcc, v192, %[nvl]\n\t"
            "s_lshr_b64 s[74:75], vcc, s76\n\t"
            "s_and_b32 s74, s74, 15\n\t"
            "s_cmp_eq_u32 s74, 15\n\t"
            "s_cbranch_scc1 .Lzd7\n\t"
            "s_cmp_eq_u32 s73, 0\n\t"
            "s_cbranch_scc1 .Lzd7\n\t"
            "s_sub_u32 s73, s73, 1\n\t"
            "global_load_dword v192, %[po], %[ws] sc1\n\t"
            "s_waitcnt vmcnt(0)\n\t"
            "s_branch .Lzc7\n\t"
            ".Lzd7:\n\t"
            "s_add_u32 m0, %[ldsw], 32768\n\t"
            "s_add_u32 s70, s84, 3670016\n\t"
            "s_addc_u32 s71, s85, 0\n\t"
            "global_load_lds_dwordx4 %[wv], s[70:71] offset:0 sc1\n\t"
            "global_load_lds_dwordx4 %[wv], s[70:71] offset:1024 sc1\n\t"
            "global_load_lds_dwordx4 %[wv], s[70:71] offset:2048 sc1\n\t"
            "global_load_lds_dwordx4 %[wv], s[70:71] offset:3072 sc1\n\t"
            "s_barrier\n\t"
            ".Lwp36:\n\t"
            "s_cbranch_execz .Lwp37\n\t"
            "s_mov_b32 s76, 36\n\t"
            ".Lzc8:\n\t"
            "v_cmp_eq_u32 vcc, v192, %[nvl]\n\t"
            "s_lshr_b64 s[74:75], vcc, s76\n\t"
            "s_and_b32 s74, s74, 15\n\t"
            "s_cmp_eq_u32 s74, 15\n\t"
            "s_cbranch_scc1 .Lzd8\n\t"
            "s_cmp_eq_u32 s73, 0\n\t"
            "s_cbranch_scc1 .Lzd8\n\t"
            "s_sub_u32 s73, s73, 1\n\t"
            "global_load_dword v192, %[po], %[ws] sc1\n\t"
            "s_waitcnt vmcnt(0)\n\t"
            "s_branch .Lzc8\n\t"
            ".Lzd8:\n\t"
            "s_add_u32 m0, %[ldsw], 0\n\t"
            "s_add_u32 s70, s84, 4128768\n\t"
            "s_addc_u32 s71, s85, 0\n\t"
            "global_load_lds_dwordx4 %[wv], s[70:71] offset:0 sc1\n\t"
            "global_load_lds_dwordx4 %[wv], s[70:71] offset:1024 sc1\n\t"
            "global_load_lds_dwordx4 %[wv], s[70:71] offset:2048 sc1\n\t"
            "global_load_lds_dwordx4 %[wv], s[70:71] offset:3072 sc1\n\t"
            "s_add_u32 m0, %[ldsx], 0\n\t"
            "s_add_u32 s68, %[xpl], 1152\n\t"
            "s_addc_u32 s69, %[xph], 0\n\t"
            "global_load_lds_dwordx4 %[x0], s[68:69] offset:0\n\t"
            "global_load_lds_dwordx4 %[x1], s[68:69] offset:1024\n\t"
            "global_load_lds_dwordx4 %[x2], s[68:69] offset:2048\n\t"
            "global_load_lds_dwordx4 %[x3], s[68:69] offset:3072\n\t"
            "s_add_u32 m0, %[ldsx], 4096\n\t"
            "s_add_u32 s68, %[xpl], 66688\n\t"
            "s_addc_u32 s69, %[xph], 0\n\t"
            "global_load_lds_dwordx4 %[x0], s[68:69] offset:0\n\t"
            "global_load_lds_dwordx4 %[x1], s[68:69] offset:1024\n\t"
            "global_load_lds_dwordx4 %[x2], s[68:69] offset:2048\n\t"
            "global_load_lds_dwordx4 %[x3], s[68:69] offset:3072\n\t"
            "s_waitcnt vmcnt(12)\n\t"
            "s_barrier\n\t"
            "s_mov_b32 s76, 40\n\t"
            ".Lzc9:\n\t"
            "v_cmp_eq_u32 vcc, v192, %[nvl]\n\t"
            "s_lshr_b64 s[74:75], vcc, s76\n\t"
            "s_and_b32 s74, s74, 15\n\t"
            "s_cmp_eq_u32 s74, 15\n\t"
            "s_cbranch_scc1 .Lzd9\n\t"
            "s_cmp_eq_u32 s73, 0\n\t"
            "s_cbranch_scc1 .Lzd9\n\t"
            "s_sub_u32 s73, s73, 1\n\t"
            "global_load_dword v192, %[po], %[ws] sc1\n\t"
            "s_waitcnt vmcnt(0)\n\t"
            "s_branch .Lzc9\n\t"
            ".Lzd9:\n\t"
            "s_add_u32 m0, %[ldsw], 16384\n\t"
            "s_add_u32 s70, s84, 4587520\n\t"
            "s_addc_u32 s71, s85, 0\n\t"
            "global_load_lds_dwordx4 %[wv], s[70:71] offset:0 sc1\n\t"
            "global_load_lds_dwordx4 %[wv], s[70:71] offset:1024 sc1\n\t"
            "global_load_lds_dwordx4 %[wv], s[70:71] offset:2048 sc1\n\t"
            "global_load_lds_dwordx4 %[wv], s[70:71] offset:3072 sc1\n\t"
            "s_add_u32 m0, %[ldsx], 32768\n\t"
            "s_add_u32 s68, %[xpl], 1280\n\t"
            "s_addc_u32 s69, %[xph], 0\n\t"
            "global_load_lds_dwordx4 %[x0], s[68:69] offset:0\n\t"
            "global_load_lds_dwordx4 %[x1], s[68:69] offset:1024\n\t"
            "global_load_lds_dwordx4 %[x2], s[68:69] offset:2048\n\t"
            "global_load_lds_dwordx4 %[x3], s[68:69] offset:3072\n\t"
            "s_add_u32 m0, %[ldsx], 36864\n\t"
            "s_add_u32 s68, %[xpl], 66816\n\t"
            "s_addc_u32 s69, %[xph], 0\n\t"
            "global_load_lds_dwordx4 %[x0], s[68:69] offset:0\n\t"
            "global_load_lds_dwordx4 %[x1], s[68:69] offset:1024\n\t"
            "global_load_lds_dwordx4 %[x2], s[68:69] offset:2048\n\t"
            "global_load_lds_dwordx4 %[x3], s[68:69] offset:3072\n\t"
            "s_waitcnt vmcnt(12)\n\t"
            "s_barrier\n\t"
            "s_mov_b32 s76, 44\n\t"
            ".Lzc10:\n\t"
            "v_cmp_eq_u32 vcc, v192, %[nvl]\n\t"
            "s_lshr_b64 s[74:75], vcc, s76\n\t"
            "s_and_b32 s74, s74, 15\n\t"
            "s_cmp_eq_u32 s74, 15\n\t"
            "s_cbranch_scc1 .Lzd10\n\t"
            "s_cmp_eq_u32 s73, 0\n\t"
            "s_cbranch_scc1 .Lzd10\n\t"
            "s_sub_u32 s73, s73, 1\n\t"
            "global_load_dword v192, %[po], %[ws] sc1\n\t"
            "s_waitcnt vmcnt(0)\n\t"
            "s_branch .Lzc10\n\t"
            ".Lzd10:\n\t"
            "s_add_u32 m0, %[ldsw], 32768\n\t"
            "s_add_u32 s70, s84, 5046272\n\t"
            "s_addc_u32 s71, s85, 0\n\t"
            "global_load_lds_dwordx4 %[wv], s[70:71] offset:0 sc1\n\t"
            "global_load_lds_dwordx4 %[wv], s[70:71] offset:1024 sc1\n\t"
            "global_load_lds_dwordx4 %[wv], s[70:71] offset:2048 sc1\n\t"
            "global_load_lds_dwordx4 %[wv], s[70:71] offset:3072 sc1\n\t"
            "s_add_u32 m0, %[ldsx], 65536\n\t"
            "s_add_u32 s68, %[xpl], 1408\n\t"
            "s_addc_u32 s69, %[xph], 0\n\t"
            "global_load_lds_dwordx4 %[x0], s[68:69] offset:0\n\t"
            "global_load_lds_dwordx4 %[x1], s[68:69] offset:1024\n\t"
            "global_load_lds_dwordx4 %[x2], s[68:69] offset:2048\n\t"
            "global_load_lds_dwordx4 %[x3], s[68:69] offset:3072\n\t"
            "s_add_u32 m0, %[ldsx], 69632\n\t"
            "s_add_u32 s68, %[xpl], 66944\n\t"
            "s_addc_u32 s69, %[xph], 0\n\t"
            "global_load_lds_dwordx4 %[x0], s[68:69] offset:0\n\t"
            "global_load_lds_dwordx4 %[x1], s[68:69] offset:1024\n\t"
            "global_load_lds_dwordx4 %[x2], s[68:69] offset:2048\n\t"
            "global_load_lds_dwordx4 %[x3], s[68:69] offset:3072\n\t"
            "s_waitcnt vmcnt(12)\n\t"
            "s_barrier\n\t"
            ".Lwp37:\n\t"
            "s_cbranch_execz .Lwp97\n\t"
            "global_load_dword v192, %[po], %[ws] sc1\n\t"
            "s_add_u32 m0, %[ldsx], 0\n\t"
            "s_add_u32 s68, %[xpl], 1536\n\t"
            "s_addc_u32 s69, %[xph], 0\n\t"
            "global_load_lds_dwordx4 %[x0], s[68:69] offset:0\n\t"
            "global_load_lds_dwordx4 %[x1], s[68:69] offset:1024\n\t"
            "global_load_lds_dwordx4 %[x2], s[68:69] offset:2048\n\t"
            "global_load_lds_dwordx4 %[x3], s[68:69] offset:3072\n\t"
            "s_add_u32 m0, %[ldsx], 4096\n\t"
            "s_add_u32 s68, %[xpl], 67072\n\t"
            "s_addc_u32 s69, %[xph], 0\n\t"
            "global_load_lds_dwordx4 %[x0], s[68:69] offset:0\n\t"
            "global_load_lds_dwordx4 %[x1], s[68:69] offset:1024\n\t"
            "global_load_lds_dwordx4 %[x2], s[68:69] offset:2048\n\t"
            "global_load_lds_dwordx4 %[x3], s[68:69] offset:3072\n\t"
            "s_waitcnt vmcnt(8)\n\t"
            "s_mov_b32 s76, 48\n\t"
            ".Lzc11:\n\t"
            "v_cmp_eq_u32 vcc, v192, %[nvl]\n\t"
            "s_lshr_b64 s[74:75], vcc, s76\n\t"
            "s_and_b32 s74, s74, 15\n\t"
            "s_cmp_eq_u32 s74, 15\n\t"
            "s_cbranch_scc1 .Lzd11\n\t"
            "s_cmp_eq_u32 s73, 0\n\t"
            "s_cbranch_scc1 .Lzd11\n\t"
            "s_sub_u32 s73, s73, 1\n\t"
            "global_load_dword v192, %[po], %[ws] sc1\n\t"
            "s_waitcnt vmcnt(0)\n\t"
            "s_branch .Lzc11\n\t"
            ".Lzd11:\n\t"
            "s_add_u32 m0, %[ldsw], 0\n\t"
            "s_add_u32 s70, s84, 5505024\n\t"
            "s_addc_u32 s71, s85, 0\n\t"
            "global_load_lds_dwordx4 %[wv], s[70:71] offset:0 sc1\n\t"
            "global_load_lds_dwordx4 %[wv], s[70:71] offset:1024 sc1\n\t"
            "global_load_lds_dwordx4 %[wv], s[70:71] offset:2048 sc1\n\t"
            "global_load_lds_dwordx4 %[wv], s[70:71] offset:3072 sc1\n\t"
            "s_barrier\n\t"
            "s_mov_b32 s76, 52\n\t"
            ".Lzc12:\n\t"
            "v_cmp_eq_u32 vcc, v192, %[nvl]\n\t"
            "s_lshr_b64 s[74:75], vcc, s76\n\t"
            "s_and_b32 s74, s74, 4095\n\t"
            "s_cmp_eq_u32 s74, 4095\n\t"
            "s_cbranch_scc1 .Lzd12\n\t"
            "s_cmp_eq_u32 s73, 0\n\t"
            "s_cbranch_scc1 .Lzd12\n\t"
            "s_sub_u32 s73, s73, 1\n\t"
            "global_load_dword v192, %[po], %[ws] sc1\n\t"
            "s_waitcnt vmcnt(0)\n\t"
            "s_branch .Lzc12\n\t"
            ".Lzd12:\n\t"
            "s_add_u32 m0, %[ldsw], 16384\n\t"
            "s_add_u32 s70, s84, 5963776\n\t"
            "s_addc_u32 s71, s85, 0\n\t"
            "s_add_u32 s86, s84, 6421504\n\t"
            "s_addc_u32 s87, s85, 0\n\t"
            "global_load_lds_dwordx4 %[wv], s[70:71] offset:0 sc1\n\t"
            "global_load_lds_dwordx4 %[wv], s[86:87] offset:1024 sc1\n\t"
            "global_load_lds_dwordx4 %[wv], s[70:71] offset:2048 sc1\n\t"
            "global_load_lds_dwordx4 %[wv], s[86:87] offset:3072 sc1\n\t"
            "s_add_u32 m0, %[ldsx], 32768\n\t"
            "s_add_u32 s68, %[xpl], 1664\n\t"
            "s_addc_u32 s69, %[xph], 0\n\t"
            "global_load_lds_dwordx4 %[x0], s[68:69] offset:0\n\t"
            "global_load_lds_dwordx4 %[x1], s[68:69] offset:1024\n\t"
            "global_load_lds_dwordx4 %[x2], s[68:69] offset:2048\n\t"
            "global_load_lds_dwordx4 %[x3], s[68:69] offset:3072\n\t"
            "s_add_u32 m0, %[ldsx], 36864\n\t"
            "s_add_u32 s68, %[xpl], 67200\n\t"
            "s_addc_u32 s69, %[xph], 0\n\t"
            "global_load_lds_dwordx4 %[x0], s[68:69] offset:0\n\t"
            "global_load_lds_dwordx4 %[x1], s[68:69] offset:1024\n\t"
            "global_load_lds_dwordx4 %[x2], s[68:69] offset:2048\n\t"
            "global_load_lds_dwordx4 %[x3], s[68:69] offset:3072\n\t"
            "s_waitcnt vmcnt(12)\n\t"
            "s_barrier\n\t"
            ".Lwp38:\n\t"
            "s_cbranch_execz .Lwp39\n\t"
            "s_add_u32 m0, %[ldsx], 65536\n\t"
            "s_add_u32 s68, %[xpl], 1792\n\t"
            "s_addc_u32 s69, %[xph], 0\n\t"
            "global_load_lds_dwordx4 %[x0], s[68:69] offset:0\n\t"
            "global_load_lds_dwordx4 %[x1], s[68:69] offset:1024\n\t"
            "global_load_lds_dwordx4 %[x2], s[68:69] offset:2048\n\t"
            "global_load_lds_dwordx4 %[x3], s[68:69] offset:3072\n\t"
            "s_add_u32 m0, %[ldsx], 69632\n\t"
            "s_add_u32 s68, %[xpl], 67328\n\t"
            "s_addc_u32 s69, %[xph], 0\n\t"
            "global_load_lds_dwordx4 %[x0], s[68:69] offset:0\n\t"
            "global_load_lds_dwordx4 %[x1], s[68:69] offset:1024\n\t"
            "global_load_lds_dwordx4 %[x2], s[68:69] offset:2048\n\t"
            "global_load_lds_dwordx4 %[x3], s[68:69] offset:3072\n\t"
            "s_add_u32 m0, %[ldsw], 32768\n\t"
            "s_add_u32 s70, s84, 6881280\n\t"
            "s_addc_u32 s71, s85, 0\n\t"
            "s_add_u32 s86, s84, 5963776\n\t"
            "s_addc_u32 s87, s85, 0\n\t"
            "global_load_lds_dwordx4 %[wv], s[70:71] offset:0 sc1\n\t"
            "global_load_lds_dwordx4 %[wv], s[86:87] offset:1024 sc1\n\t"
            "global_load_lds_dwordx4 %[wv], s[70:71] offset:2048 sc1\n\t"
            "global_load_lds_dwordx4 %[wv], s[86:87] offset:3072 sc1\n\t"
            "s_waitcnt vmcnt(4)\n\t"
            "s_barrier\n\t"
            "s_add_u32 m0, %[ldsx], 0\n\t"
            "s_add_u32 s68, %[xpl], 1920\n\t"
            "s_addc_u32 s69, %[xph], 0\n\t"
            "global_load_lds_dwordx4 %[x0], s[68:69] offset:0\n\t"
            "global_load_lds_dwordx4 %[x1], s[68:69] offset:1024\n\t"
            "global_load_lds_dwordx4 %[x2], s[68:69] offset:2048\n\t"
            "global_load_lds_dwordx4 %[x3], s[68:69] offset:3072\n\t"
            "s_add_u32 m0, %[ldsx], 4096\n\t"
            "s_add_u32 s68, %[xpl], 67456\n\t"
            "s_addc_u32 s69, %[xph], 0\n\t"
            "global_load_lds_dwordx4 %[x0], s[68:69] offset:0\n\t"
            "global_load_lds_dwordx4 %[x1], s[68:69] offset:1024\n\t"
            "global_load_lds_dwordx4 %[x2], s[68:69] offset:2048\n\t"
            "global_load_lds_dwordx4 %[x3], s[68:69] offset:3072\n\t"
            "s_add_u32 m0, %[ldsw], 0\n\t"
            "s_add_u32 s70, s84, 6423552\n\t"
            "s_addc_u32 s71, s85, 0\n\t"
            "s_add_u32 s86, s84, 6881280\n\t"
            "s_addc_u32 s87, s85, 0\n\t"
            "global_load_lds_dwordx4 %[wv], s[70:71] offset:0 sc1\n\t"
            "global_load_lds_dwordx4 %[wv], s[86:87] offset:1024 sc1\n\t"
            "global_load_lds_dwordx4 %[wv], s[70:71] offset:2048 sc1\n\t"
            "global_load_lds_dwordx4 %[wv], s[86:87] offset:3072 sc1\n\t"
            "s_waitcnt vmcnt(4)\n\t"
            "s_barrier\n\t"
            "s_mov_b32 s76, 16\n\t"
            ".Lzc13:\n\t"
            "v_cmp_eq_u32 vcc, v192, %[nvl]\n\t"
            "s_lshr_b64 s[74:75], vcc, s76\n\t"
            "s_and_b32 s74, s74, 65535\n\t"
            "s_cmp_eq_u32 s74, 65535\n\t"
            "s_cbranch_scc1 .Lzd13\n\t"
            "s_cmp_eq_u32 s73, 0\n\t"
            "s_cbranch_scc1 .Lzd13\n\t"
            "s_sub_u32 s73, s73, 1\n\t"
            "global_load_dword v192, %[po], %[ws] sc1\n\t"
            "s_waitcnt vmcnt(0)\n\t"
            "s_branch .Lzc13\n\t"
            ".Lzd13:\n\t"
            "s_waitcnt vmcnt(0)\n\t"
            "s_barrier\n\t"
            "s_branch .Lzend\n\t"
            ".Lzcls1:\n\t"
            ".Lwp39:\n\t"
            "s_cbranch_execz .Lwp40\n\t"
            "s_lshl_b32 s76, s60, 2\n\t"
            ".Lzc14:\n\t"
            "v_cmp_eq_u32 vcc, v192, %[nvl]\n\t"
            "s_lshr_b64 s[74:75], vcc, s76\n\t"
            "s_and_b32 s74, s74, 15\n\t"
            "s_cmp_eq_u32 s74, 15\n\t"
            "s_cbranch_scc1 .Lzd14\n\t"
            "s_cmp_eq_u32 s73, 0\n\t"
            "s_cbranch_scc1 .Lzd14\n\t"
            "s_sub_u32 s73, s73, 1\n\t"
            "global_load_dword v192, %[po], %[ws] sc1\n\t"
            "s_waitcnt vmcnt(0)\n\t"
            "s_branch .Lzc14\n\t"
            ".Lzd14:\n\t"
            "s_add_u32 m0, %[ldsw], 16384\n\t"
            "s_mul_i32 s76, s60, 458752\n\t"
            "s_add_u32 s70, s84, s76\n\t"
            "s_addc_u32 s71, s85, 0\n\t"
            "global_load_lds_dwordx4 %[wv], s[70:71] offset:0 sc1\n\t"
            "global_load_lds_dwordx4 %[wv], s[70:71] offset:1024 sc1\n\t"
            "global_load_lds_dwordx4 %[wv], s[70:71] offset:2048 sc1\n\t"
            "global_load_lds_dwordx4 %[wv], s[70:71] offset:3072 sc1\n\t"
            "s_waitcnt vmcnt(8)\n\t"
            "s_mov_b64 exec, 3\n\t"
            "global_store_dword %[fo0], %[nvl], %[ws] offset:64 sc1\n\t"
            "s_mov_b64 exec, -1\n\t"
            "s_add_u32 m0, %[ldsx], 32768\n\t"
            "s_lshl_b32 s76, s60, 7\n\t"
            "s_add_u32 s68, %[xpl], s76\n\t"
            "s_addc_u32 s69, %[xph], 0\n\t"
            "global_load_lds_dwordx4 %[x0], s[68:69] offset:0\n\t"
            "global_load_lds_dwordx4 %[x1], s[68:69] offset:1024\n\t"
            "global_load_lds_dwordx4 %[x2], s[68:69] offset:2048\n\t"
            "global_load_lds_dwordx4 %[x3], s[68:69] offset:3072\n\t"
            "s_add_u32 m0, %[ldsx], 36864\n\t"
            "s_lshl_b32 s76, s60, 7\n\t"
            "s_add_u32 s76, s76, 65536\n\t"
            "s_add_u32 s68, %[xpl], s76\n\t"
            "s_addc_u32 s69, %[xph], 0\n\t"
            "global_load_lds_dwordx4 %[x0], s[68:69] offset:0\n\t"
            "global_load_lds_dwordx4 %[x1], s[68:69] offset:1024\n\t"
            "global_load_lds_dwordx4 %[x2], s[68:69] offset:2048\n\t"
            "global_load_lds_dwordx4 %[x3], s[68:69] offset:3072\n\t"
            "s_waitcnt vmcnt(13)\n\t"
            "s_barrier\n\t"
            "global_load_dword v192, %[po], %[ws] sc1\n\t"
            "s_add_u32 m0, %[ldsx], 65536\n\t"
            "s_lshl_b32 s76, s61, 7\n\t"
            "s_add_u32 s68, %[xpl], s76\n\t"
            "s_addc_u32 s69, %[xph], 0\n\t"
            "global_load_lds_dwordx4 %[x0], s[68:69] offset:0\n\t"
            "global_load_lds_dwordx4 %[x1], s[68:69] offset:1024\n\t"
            "global_load_lds_dwordx4 %[x2], s[68:69] offset:2048\n\t"
            "global_load_lds_dwordx4 %[x3], s[68:69] offset:3072\n\t"
            "s_add_u32 m0, %[ldsx], 69632\n\t"
            "s_lshl_b32 s76, s61, 7\n\t"
            "s_add_u32 s76, s76, 65536\n\t"
            "s_add_u32 s68, %[xpl], s76\n\t"
            "s_addc_u32 s69, %[xph], 0\n\t"
            "global_load_lds_dwordx4 %[x0], s[68:69] offset:0\n\t"
            "global_load_lds_dwordx4 %[x1], s[68:69] offset:1024\n\t"
            "global_load_lds_dwordx4 %[x2], s[68:69] offset:2048\n\t"
            "global_load_lds_dwordx4 %[x3], s[68:69] offset:3072\n\t"
            "s_waitcnt vmcnt(8)\n\t"
            "s_lshl_b32 s76, s61, 2\n\t"
            ".Lzc15:\n\t"
            "v_cmp_eq_u32 vcc, v192, %[nvl]\n\t"
            "s_lshr_b64 s[74:75], vcc, s76\n\t"
            "s_and_b32 s74, s74, 15\n\t"
            "s_cmp_eq_u32 s74, 15\n\t"
            "s_cbranch_scc1 .Lzd15\n\t"
            "s_cmp_eq_u32 s73, 0\n\t"
            "s_cbranch_scc1 .Lzd15\n\t"
            "s_sub_u32 s73, s73, 1\n\t"
            "global_load_dword v192, %[po], %[ws] sc1\n\t"
            "s_waitcnt vmcnt(0)\n\t"
            "s_branch .Lzc15\n\t"
            ".Lzd15:\n\t"
            "s_add_u32 m0, %[ldsw], 32768\n\t"
            "s_mul_i32 s76, s61, 458752\n\t"
            "s_add_u32 s70, s84, s76\n\t"
            "s_addc_u32 s71, s85, 0\n\t"
            "global_load_lds_dwordx4 %[wv], s[70:71] offset:0 sc1\n\t"
            "global_load_lds_dwordx4 %[wv], s[70:71] offset:1024 sc1\n\t"
            "global_load_lds_dwordx4 %[wv], s[70:71] offset:2048 sc1\n\t"
            "global_load_lds_dwordx4 %[wv], s[70:71] offset:3072 sc1\n\t"
            "s_barrier\n\t"
            ".Lwp40:\n\t"
            "s_cbranch_execz .Lwp41\n\t"
            "s_lshl_b32 s76, s62, 2\n\t"
            ".Lzc16:\n\t"
            "v_cmp_eq_u32 vcc, v192, %[nvl]\n\t"
            "s_lshr_b64 s[74:75], vcc, s76\n\t"
            "s_and_b32 s74, s74, 15\n\t"
            "s_cmp_eq_u32 s74, 15\n\t"
            "s_cbranch_scc1 .Lzd16\n\t"
            "s_cmp_eq_u32 s73, 0\n\t"
            "s_cbranch_scc1 .Lzd16\n\t"
            "s_sub_u32 s73, s73, 1\n\t"
            "global_load_dword v192, %[po], %[ws] sc1\n\t"
            "s_waitcnt vmcnt(0)\n\t"
            "s_branch .Lzc16\n\t"
            ".Lzd16:\n\t"
            "s_add_u32 m0, %[ldsw], 0\n\t"
            "s_mul_i32 s76, s62, 458752\n\t"
            "s_add_u32 s70, s84, s76\n\t"
            "s_addc_u32 s71, s85, 0\n\t"
            "global_load_lds_dwordx4 %[wv], s[70:71] offset:0 sc1\n\t"
            "global_load_lds_dwordx4 %[wv], s[70:71] offset:1024 sc1\n\t"
            "global_load_lds_dwordx4 %[wv], s[70:71] offset:2048 sc1\n\t"
            "global_load_lds_dwordx4 %[wv], s[70:71] offset:3072 sc1\n\t"
            "s_add_u32 m0, %[ldsx], 0\n\t"
            "s_lshl_b32 s76, s62, 7\n\t"
            "s_add_u32 s68, %[xpl], s76\n\t"
            "s_addc_u32 s69, %[xph], 0\n\t"
            "global_load_lds_dwordx4 %[x0], s[68:69] offset:0\n\t"
            "global_load_lds_dwordx4 %[x1], s[68:69] offset:1024\n\t"
            "global_load_lds_dwordx4 %[x2], s[68:69] offset:2048\n\t"
            "global_load_lds_dwordx4 %[x3], s[68:69] offset:3072\n\t"
            "s_add_u32 m0, %[ldsx], 4096\n\t"
            "s_lshl_b32 s76, s62, 7\n\t"
            "s_add_u32 s76, s76, 65536\n\t"
            "s_add_u32 s68, %[xpl], s76\n\t"
            "s_addc_u32 s69, %[xph], 0\n\t"
            "global_load_lds_dwordx4 %[x0], s[68:69] offset:0\n\t"
            "global_load_lds_dwordx4 %[x1], s[68:69] offset:1024\n\t"
            "global_load_lds_dwordx4 %[x2], s[68:69] offset:2048\n\t"
            "global_load_lds_dwordx4 %[x3], s[68:69] offset:3072\n\t"
            "s_waitcnt vmcnt(12)\n\t"
            "s_barrier\n\t"
            "s_lshl_b32 s76, s63, 2\n\t"
            ".Lzc17:\n\t"
            "v_cmp_eq_u32 vcc, v192, %[nvl]\n\t"
            "s_lshr_b64 s[74:75], vcc, s76\n\t"
            "s_and_b32 s74, s74, 15\n\t"
            "s_cmp_eq_u32 s74, 15\n\t"
            "s_cbranch_scc1 .Lzd17\n\t"
            "s_cmp_eq_u32 s73, 0\n\t"
            "s_cbranch_scc1 .Lzd17\n\t"
            "s_sub_u32 s73, s73, 1\n\t"
            "global_load_dword v192, %[po], %[ws] sc1\n\t"
            "s_waitcnt vmcnt(0)\n\t"
            "s_branch .Lzc17\n\t"
            ".Lzd17:\n\t"
            "s_add_u32 m0, %[ldsw], 16384\n\t"
            "s_mul_i32 s76, s63, 458752\n\t"
            "s_add_u32 s70, s84, s76\n\t"
            "s_addc_u32 s71, s85, 0\n\t"
            "global_load_lds_dwordx4 %[wv], s[70:71] offset:0 sc1\n\t"
            "global_load_lds_dwordx4 %[wv], s[70:71] offset:1024 sc1\n\t"
            "global_load_lds_dwordx4 %[wv], s[70:71] offset:2048 sc1\n\t"
            "global_load_lds_dwordx4 %[wv], s[70:71] offset:3072 sc1\n\t"
            "s_add_u32 m0, %[ldsx], 32768\n\t"
            "s_lshl_b32 s76, s63, 7\n\t"
            "s_add_u32 s68, %[xpl], s76\n\t"
            "s_addc_u32 s69, %[xph], 0\n\t"
            "global_load_lds_dwordx4 %[x0], s[68:69] offset:0\n\t"
            "global_load_lds_dwordx4 %[x1], s[68:69] offset:1024\n\t"
            "global_load_lds_dwordx4 %[x2], s[68:69] offset:2048\n\t"
            "global_load_lds_dwordx4 %[x3], s[68:69] offset:3072\n\t"
            "s_add_u32 m0, %[ldsx], 36864\n\t"
            "s_lshl_b32 s76, s63, 7\n\t"
            "s_add_u32 s76, s76, 65536\n\t"
            "s_add_u32 s68, %[xpl], s76\n\t"
            "s_addc_u32 s69, %[xph], 0\n\t"
            "global_load_lds_dwordx4 %[x0], s[68:69] offset:0\n\t"
            "global_load_lds_dwordx4 %[x1], s[68:69] offset:1024\n\t"
            "global_load_lds_dwordx4 %[x2], s[68:69] offset:2048\n\t"
            "global_load_lds_dwordx4 %[x3], s[68:69] offset:3072\n\t"
            "s_mov_b64 s[88:89], %[qbg]\n\t"
            "global_load_dwordx4 v[128:131], %[qo], s[88:89] \n\t"
            "s_add_u32 s88, s88, 14336\n\t"
            "s_addc_u32 s89, s89, 0\n\t"
            "global_load_dwordx4 v[132:135], %[qo], s[88:89] \n\t"
            "s_add_u32 s88, s88, 14336\n\t"
            "s_addc_u32 s89, s89, 0\n\t"
            "global_load_dwordx4 v[136:139], %[qo], s[88:89] \n\t"
            "s_add_u32 s88, s88, 14336\n\t"
            "s_addc_u32 s89, s89, 0\n\t"
            "global_load_dwordx4 v[140:143], %[qo], s[88:89] \n\t"
            "s_add_u32 s88, s88, 14336\n\t"
            "s_addc_u32 s89, s89, 0\n\t"
            "global_load_dwordx4 v[144:147], %[qo], s[88:89] \n\t"
            "s_add_u32 s88, s88, 14336\n\t"
            "s_addc_u32 s89, s89, 0\n\t"
            "global_load_dwordx4 v[148:151], %[qo], s[88:89] \n\t"
            "s_add_u32 s88, s88, 14336\n\t"
            "s_addc_u32 s89, s89, 0\n\t"
            "global_load_dwordx4 v[152:155], %[qo], s[88:89] \n\t"
            "s_add_u32 s88, s88, 14336\n\t"
            "s_addc_u32 s89, s89, 0\n\t"
            "global_load_dwordx4 v[156:159], %[qo], s[88:89] \n\t"
            "s_waitcnt vmcnt(20)\n\t"
            "s_barrier\n\t"
            ".Lwp41:\n\t"
            "s_cbranch_execz .Lwp97\n\t"
            "global_load_dword v192, %[po], %[ws] sc1\n\t"
            "s_add_u32 m0, %[ldsx], 65536\n\t"
            "s_add_u32 s68, %[xpl], 1024\n\t"
            "s_addc_u32 s69, %[xph], 0\n\t"
            "global_load_lds_dwordx4 %[x0], s[68:69] offset:0\n\t"
            "global_load_lds_dwordx4 %[x1], s[68:69] offset:1024\n\t"
            "global_load_lds_dwordx4 %[x2], s[68:69] offset:2048\n\t"
            "global_load_lds_dwordx4 %[x3], s[68:69] offset:3072\n\t"
            "s_add_u32 m0, %[ldsx], 69632\n\t"
            "s_add_u32 s68, %[xpl], 66560\n\t"
            "s_addc_u32 s69, %[xph], 0\n\t"
            "global_load_lds_dwordx4 %[x0], s[68:69] offset:0\n\t"
            "global_load_lds_dwordx4 %[x1], s[68:69] offset:1024\n\t"
            "global_load_lds_dwordx4 %[x2], s[68:69] offset:2048\n\t"
            "global_load_lds_dwordx4 %[x3], s[68:69] offset:3072\n\t"
            "s_waitcnt vmcnt(8)\n\t"
            "s_mov_b32 s76, 32\n\t"
            ".Lzc18:\n\t"
            "v_cmp_eq_u32 vcc, v192, %[nvl]\n\t"
            "s_lshr_b64 s[74:75], vcc, s76\n\t"
            "s_and_b32 s74, s74, 15\n\t"
            "s_cmp_eq_u32 s74, 15\n\t"
            "s_cbranch_scc1 .Lzd18\n\t"
            "s_cmp_eq_u32 s73, 0\n\t"
            "s_cbranch_scc1 .Lzd18\n\t"
            "s_sub_u32 s73, s73, 1\n\t"
            "global_load_dword v192, %[po], %[ws] sc1\n\t"
            "s_waitcnt vmcnt(0)\n\t"
            "s_branch .Lzc18\n\t"
            ".Lzd18:\n\t"
            "s_add_u32 m0, %[ldsw], 32768\n\t"
            "s_add_u32 s70, s84, 3670016\n\t"
            "s_addc_u32 s71, s85, 0\n\t"
            "global_load_lds_dwordx4 %[wv], s[70:71] offset:0 sc1\n\t"
            "global_load_lds_dwordx4 %[wv], s[70:71] offset:1024 sc1\n\t"
            "global_load_lds_dwordx4 %[wv], s[70:71] offset:2048 sc1\n\t"
            "global_load_lds_dwordx4 %[wv], s[70:71] offset:3072 sc1\n\t"
            "s_setprio 0\n\t"
            "v_lshl_add_u32 v160, v128, 8, %[lutl]\n\t"
            "ds_read_u16 v160, v160 offset:0\n\t"
            "v_lshl_add_u32 v161, v132, 8, %[lutl]\n\t"
            "ds_read_u16 v161, v161 offset:0\n\t"
            "v_lshl_add_u32 v162, v136, 8, %[lutl]\n\t"
            "ds_read_u16 v162, v162 offset:0\n\t"
            "v_lshl_add_u32 v163, v140, 8, %[lutl]\n\t"
            "ds_read_u16 v163, v163 offset:0\n\t"
            "v_lshl_add_u32 v164, v144, 8, %[lutl]\n\t"
            "ds_read_u16 v164, v164 offset:0\n\t"
            "v_lshl_add_u32 v165, v148, 8, %[lutl]\n\t"
            "ds_read_u16 v165, v165 offset:0\n\t"
            "v_lshl_add_u32 v166, v152, 8, %[lutl]\n\t"
            "ds_read_u16 v166, v166 offset:0\n\t"
            "v_lshl_add_u32 v167, v156, 8, %[lutl]\n\t"
            "ds_read_u16 v167, v167 offset:0\n\t"
            "v_lshl_add_u32 v168, v129, 8, %[lutl]\n\t"
            "ds_read_u16 v168, v168 offset:2\n\t"
            "v_lshl_add_u32 v169, v133, 8, %[lutl]\n\t"
            "ds_read_u16 v169, v169 offset:2\n\t"
            "v_lshl_add_u32 v170, v137, 8, %[lutl]\n\t"
            "ds_read_u16 v170, v170 offset:2\n\t"
            "v_lshl_add_u32 v171, v141, 8, %[lutl]\n\t"
            "ds_read_u16 v171, v171 offset:2\n\t"
            "v_lshl_add_u32 v172, v145, 8, %[lutl]\n\t"
            "ds_read_u16 v172, v172 offset:2\n\t"
            "v_lshl_add_u32 v173, v149, 8, %[lutl]\n\t"
            "ds_read_u16 v173, v173 offset:2\n\t"
            "v_lshl_add_u32 v174, v153, 8, %[lutl]\n\t"
            "ds_read_u16 v174, v174 offset:2\n\t"
            "v_lshl_add_u32 v175, v157, 8, %[lutl]\n\t"
            "ds_read_u16 v175, v175 offset:2\n\t"
            "s_setprio 0\n\t"
            "s_barrier\n\t"
            ".Lwp42:\n\t"
            "s_cbranch_execz .Lwp43\n\t"
            "s_mov_b32 s76, 36\n\t"
            ".Lzc19:\n\t"
            "v_cmp_eq_u32 vcc, v192, %[nvl]\n\t"
            "s_lshr_b64 s[74:75], vcc, s76\n\t"
            "s_and_b32 s74, s74, 15\n\t"
            "s_cmp_eq_u32 s74, 15\n\t"
            "s_cbranch_scc1 .Lzd19\n\t"
            "s_cmp_eq_u32 s73, 0\n\t"
            "s_cbranch_scc1 .Lzd19\n\t"
            "s_sub_u32 s73, s73, 1\n\t"
            "global_load_dword v192, %[po], %[ws] sc1\n\t"
            "s_waitcnt vmcnt(0)\n\t"
            "s_branch .Lzc19\n\t"
            ".Lzd19:\n\t"
            "s_add_u32 m0, %[ldsw], 0\n\t"
            "s_add_u32 s70, s84, 4128768\n\t"
            "s_addc_u32 s71, s85, 0\n\t"
            "global_load_lds_dwordx4 %[wv], s[70:71] offset:0 sc1\n\t"
            "global_load_lds_dwordx4 %[wv], s[70:71] offset:1024 sc1\n\t"
            "global_load_lds_dwordx4 %[wv], s[70:71] offset:2048 sc1\n\t"
            "global_load_lds_dwordx4 %[wv], s[70:71] offset:3072 sc1\n\t"
            "s_setprio 0\n\t"
            "s_add_u32 m0, %[ldsx], 0\n\t"
            "s_add_u32 s68, %[xpl], 1152\n\t"
            "s_addc_u32 s69, %[xph], 0\n\t"
            "global_load_lds_dwordx4 %[x0], s[68:69] offset:0\n\t"
            "v_lshl_add_u32 v176, v130, 8, %[lutl]\n\t"
            "ds_read_u16 v176, v176 offset:128\n\t"
            "v_lshl_add_u32 v177, v134, 8, %[lutl]\n\t"
            "ds_read_u16 v177, v177 offset:128\n\t"
            "global_load_lds_dwordx4 %[x1], s[68:69] offset:1024\n\t"
            "v_lshl_add_u32 v178, v138, 8, %[lutl]\n\t"
            "ds_read_u16 v178, v178 offset:128\n\t"
            "v_lshl_add_u32 v179, v142, 8, %[lutl]\n\t"
            "ds_read_u16 v179, v179 offset:128\n\t"
            "global_load_lds_dwordx4 %[x2], s[68:69] offset:2048\n\t"
            "v_lshl_add_u32 v180, v146, 8, %[lutl]\n\t"
            "ds_read_u16 v180, v180 offset:128\n\t"
            "v_lshl_add_u32 v181, v150, 8, %[lutl]\n\t"
            "ds_read_u16 v181, v181 offset:128\n\t"
            "global_load_lds_dwordx4 %[x3], s[68:69] offset:3072\n\t"
            "v_lshl_add_u32 v182, v154, 8, %[lutl]\n\t"
            "ds_read_u16 v182, v182 offset:128\n\t"
            "v_lshl_add_u32 v183, v158, 8, %[lutl]\n\t"
            "ds_read_u16 v183, v183 offset:128\n\t"
            "s_add_u32 m0, %[ldsx], 4096\n\t"
            "s_add_u32 s68, %[xpl], 66688\n\t"
            "s_addc_u32 s69, %[xph], 0\n\t"
            "global_load_lds_dwordx4 %[x0], s[68:69] offset:0\n\t"
            "v_lshl_add_u32 v184, v131, 8, %[lutl]\n\t"
            "ds_read_u16 v184, v184 offset:130\n\t"
            "v_lshl_add_u32 v185, v135, 8, %[lutl]\n\t"
            "ds_read_u16 v185, v185 offset:130\n\t"
            "global_load_lds_dwordx4 %[x1], s[68:69] offset:1024\n\t"
            "v_lshl_add_u32 v186, v139, 8, %[lutl]\n\t"
            "ds_read_u16 v186, v186 offset:130\n\t"
            "v_lshl_add_u32 v187, v143, 8, %[lutl]\n\t"
            "ds_read_u16 v187, v187 offset:130\n\t"
            "global_load_lds_dwordx4 %[x2], s[68:69] offset:2048\n\t"
            "v_lshl_add_u32 v188, v147, 8, %[lutl]\n\t"
            "ds_read_u16 v188, v188 offset:130\n\t"
            "v_lshl_add_u32 v189, v151, 8, %[lutl]\n\t"
            "ds_read_u16 v189, v189 offset:130\n\t"
            "global_load_lds_dwordx4 %[x3], s[68:69] offset:3072\n\t"
            "v_lshl_add_u32 v190, v155, 8, %[lutl]\n\t"
            "ds_read_u16 v190, v190 offset:130\n\t"
            "v_lshl_add_u32 v191, v159, 8, %[lutl]\n\t"
            "ds_read_u16 v191, v191 offset:130\n\t"
            "s_setprio 0\n\t"
            "s_waitcnt lgkmcnt(15)\n\t"
            "v_lshl_or_b32 v128, v161, 16, v160\n\t"
            "v_lshl_or_b32 v129, v163, 16, v162\n\t"
            "v_lshl_or_b32 v130, v165, 16, v164\n\t"
            "v_lshl_or_b32 v131, v167, 16, v166\n\t"
            "s_waitcnt lgkmcnt(15)\n\t"
            "v_lshl_or_b32 v132, v169, 16, v168\n\t"
            "v_lshl_or_b32 v133, v171, 16, v170\n\t"
            "v_lshl_or_b32 v134, v173, 16, v172\n\t"
            "v_lshl_or_b32 v135, v175, 16, v174\n\t"
            "s_waitcnt lgkmcnt(8)\n\t"
            "v_lshl_or_b32 v136, v177, 16, v176\n\t"
            "v_lshl_or_b32 v137, v179, 16, v178\n\t"
            "v_lshl_or_b32 v138, v181, 16, v180\n\t"
            "v_lshl_or_b32 v139, v183, 16, v182\n\t"
            "s_waitcnt lgkmcnt(0)\n\t"
            "v_lshl_or_b32 v140, v185, 16, v184\n\t"
            "v_lshl_or_b32 v141, v187, 16, v186\n\t"
            "v_lshl_or_b32 v142, v189, 16, v188\n\t"
            "v_lshl_or_b32 v143, v191, 16, v190\n\t"
            "global_store_dwordx4 %[wo], v[128:131], %[stb] offset:0 sc1\n\t"
            "global_store_dwordx4 %[wo], v[132:135], %[stb] offset:256 sc1\n\t"
            "global_store_dwordx4 %[wo], v[136:139], %[stb] offset:512 sc1\n\t"
            "global_store_dwordx4 %[wo], v[140:143], %[stb] offset:768 sc1\n\t"
            "s_waitcnt vmcnt(16)\n\t"
            "s_barrier\n\t"
            "s_mov_b32 s76, 40\n\t"
            ".Lzc20:\n\t"
            "v_cmp_eq_u32 vcc, v192, %[nvl]\n\t"
            "s_lshr_b64 s[74:75], vcc, s76\n\t"
            "s_and_b32 s74, s74, 15\n\t"
            "s_cmp_eq_u32 s74, 15\n\t"
            "s_cbranch_scc1 .Lzd20\n\t"
            "s_cmp_eq_u32 s73, 0\n\t"
            "s_cbranch_scc1 .Lzd20\n\t"
            "s_sub_u32 s73, s73, 1\n\t"
            "global_load_dword v192, %[po], %[ws] sc1\n\t"
            "s_waitcnt vmcnt(0)\n\t"
            "s_branch .Lzc20\n\t"
            ".Lzd20:\n\t"
            "s_add_u32 m0, %[ldsw], 16384\n\t"
            "s_add_u32 s70, s84, 4587520\n\t"
            "s_addc_u32 s71, s85, 0\n\t"
            "global_load_lds_dwordx4 %[wv], s[70:71] offset:0 sc1\n\t"
            "global_load_lds_dwordx4 %[wv], s[70:71] offset:1024 sc1\n\t"
            "global_load_lds_dwordx4 %[wv], s[70:71] offset:2048 sc1\n\t"
            "global_load_lds_dwordx4 %[wv], s[70:71] offset:3072 sc1\n\t"
            "s_add_u32 m0, %[ldsx], 32768\n\t"
            "s_add_u32 s68, %[xpl], 1280\n\t"
            "s_addc_u32 s69, %[xph], 0\n\t"
            "global_load_lds_dwordx4 %[x0], s[68:69] offset:0\n\t"
            "global_load_lds_dwordx4 %[x1], s[68:69] offset:1024\n\t"
            "global_load_lds_dwordx4 %[x2], s[68:69] offset:2048\n\t"
            "global_load_lds_dwordx4 %[x3], s[68:69] offset:3072\n\t"
            "s_add_u32 m0, %[ldsx], 36864\n\t"
            "s_add_u32 s68, %[xpl], 66816\n\t"
            "s_addc_u32 s69, %[xph], 0\n\t"
            "global_load_lds_dwordx4 %[x0], s[68:69] offset:0\n\t"
            "global_load_lds_dwordx4 %[x1], s[68:69] offset:1024\n\t"
            "global_load_lds_dwordx4 %[x2], s[68:69] offset:2048\n\t"
            "global_load_lds_dwordx4 %[x3], s[68:69] offset:3072\n\t"
            "s_waitcnt vmcnt(12)\n\t"
            "s_mov_b64 exec, 15\n\t"
            "global_store_dword %[fob], %[nvl], %[ws] offset:0 sc1\n\t"
            "s_mov_b64 exec, -1\n\t"
            "s_barrier\n\t"
            "s_mov_b32 s76, 44\n\t"
            ".Lzc21:\n\t"
            "v_cmp_eq_u32 vcc, v192, %[nvl]\n\t"
            "s_lshr_b64 s[74:75], vcc, s76\n\t"
            "s_and_b32 s74, s74, 15\n\t"
            "s_cmp_eq_u32 s74, 15\n\t"
            "s_cbranch_scc1 .Lzd21\n\t"
            "s_cmp_eq_u32 s73, 0\n\t"
            "s_cbranch_scc1 .Lzd21\n\t"
            "s_sub_u32 s73, s73, 1\n\t"
            "global_load_dword v192, %[po], %[ws] sc1\n\t"
            "s_waitcnt vmcnt(0)\n\t"
            "s_branch .Lzc21\n\t"
            ".Lzd21:\n\t"
            "s_add_u32 m0, %[ldsw], 32768\n\t"
            "s_add_u32 s70, s84, 5046272\n\t"
            "s_addc_u32 s71, s85, 0\n\t"
            "global_load_lds_dwordx4 %[wv], s[70:71] offset:0 sc1\n\t"
            "global_load_lds_dwordx4 %[wv], s[70:71] offset:1024 sc1\n\t"
            "global_load_lds_dwordx4 %[wv], s[70:71] offset:2048 sc1\n\t"
            "global_load_lds_dwordx4 %[wv], s[70:71] offset:3072 sc1\n\t"
            "s_add_u32 m0, %[ldsx], 65536\n\t"
            "s_add_u32 s68, %[xpl], 1408\n\t"
            "s_addc_u32 s69, %[xph], 0\n\t"
            "global_load_lds_dwordx4 %[x0], s[68:69] offset:0\n\t"
            "global_load_lds_dwordx4 %[x1], s[68:69] offset:1024\n\t"
            "global_load_lds_dwordx4 %[x2], s[68:69] offset:2048\n\t"
            "global_load_lds_dwordx4 %[x3], s[68:69] offset:3072\n\t"
            "s_add_u32 m0, %[ldsx], 69632\n\t"
            "s_add_u32 s68, %[xpl], 66944\n\t"
            "s_addc_u32 s69, %[xph], 0\n\t"
            "global_load_lds_dwordx4 %[x0], s[68:69] offset:0\n\t"
            "global_load_lds_dwordx4 %[x1], s[68:69] offset:1024\n\t"
            "global_load_lds_dwordx4 %[x2], s[68:69] offset:2048\n\t"
            "global_load_lds_dwordx4 %[x3], s[68:69] offset:3072\n\t"
            "s_waitcnt vmcnt(13)\n\t"
            "s_barrier\n\t"
            ".Lwp43:\n\t"
            "s_cbranch_execz .Lwp44\n\t"
            "global_load_dword v192, %[po], %[ws] sc1\n\t"
            "s_add_u32 m0, %[ldsx], 0\n\t"
            "s_add_u32 s68, %[xpl], 1536\n\t"
            "s_addc_u32 s69, %[xph], 0\n\t"
            "global_load_lds_dwordx4 %[x0], s[68:69] offset:0\n\t"
            "global_load_lds_dwordx4 %[x1], s[68:69] offset:1024\n\t"
            "global_load_lds_dwordx4 %[x2], s[68:69] offset:2048\n\t"
            "global_load_lds_dwordx4 %[x3], s[68:69] offset:3072\n\t"
            "s_add_u32 m0, %[ldsx], 4096\n\t"
            "s_add_u32 s68, %[xpl], 67072\n\t"
            "s_addc_u32 s69, %[xph], 0\n\t"
            "global_load_lds_dwordx4 %[x0], s[68:69] offset:0\n\t"
            "global_load_lds_dwordx4 %[x1], s[68:69] offset:1024\n\t"
            "global_load_lds_dwordx4 %[x2], s[68:69] offset:2048\n\t"
            "global_load_lds_dwordx4 %[x3], s[68:69] offset:3072\n\t"
            "s_waitcnt vmcnt(8)\n\t"
            "s_mov_b32 s76, 48\n\t"
            ".Lzc22:\n\t"
            "v_cmp_eq_u32 vcc, v192, %[nvl]\n\t"
            "s_lshr_b64 s[74:75], vcc, s76\n\t"
            "s_and_b32 s74, s74, 15\n\t"
            "s_cmp_eq_u32 s74, 15\n\t"
            "s_cbranch_scc1 .Lzd22\n\t"
            "s_cmp_eq_u32 s73, 0\n\t"
            "s_cbranch_scc1 .Lzd22\n\t"
            "s_sub_u32 s73, s73, 1\n\t"
            "global_load_dword v192, %[po], %[ws] sc1\n\t"
            "s_waitcnt vmcnt(0)\n\t"
            "s_branch .Lzc22\n\t"
            ".Lzd22:\n\t"
            "s_add_u32 m0, %[ldsw], 0\n\t"
            "s_add_u32 s70, s84, 5505024\n\t"
            "s_addc_u32 s71, s85, 0\n\t"
            "global_load_lds_dwordx4 %[wv], s[70:71] offset:0 sc1\n\t"
            "global_load_lds_dwordx4 %[wv], s[70:71] offset:1024 sc1\n\t"
            "global_load_lds_dwordx4 %[wv], s[70:71] offset:2048 sc1\n\t"
            "global_load_lds_dwordx4 %[wv], s[70:71] offset:3072 sc1\n\t"
            "s_barrier\n\t"
            "s_mov_b32 s76, 52\n\t"
            ".Lzc23:\n\t"
            "v_cmp_eq_u32 vcc, v192, %[nvl]\n\t"
            "s_lshr_b64 s[74:75], vcc, s76\n\t"
            "s_and_b32 s74, s74, 4095\n\t"
            "s_cmp_eq_u32 s74, 4095\n\t"
            "s_cbranch_scc1 .Lzd23\n\t"
            "s_cmp_eq_u32 s73, 0\n\t"
            "s_cbranch_scc1 .Lzd23\n\t"
            "s_sub_u32 s73, s73, 1\n\t"
            "global_load_dword v192, %[po], %[ws] sc1\n\t"
            "s_waitcnt vmcnt(0)\n\t"
            "s_branch .Lzc23\n\t"
            ".Lzd23:\n\t"
            "s_add_u32 m0, %[ldsw], 16384\n\t"
            "s_add_u32 s70, s84, 5963776\n\t"
            "s_addc_u32 s71, s85, 0\n\t"
            "s_add_u32 s86, s84, 6421504\n\t"
            "s_addc_u32 s87, s85, 0\n\t"
            "global_load_lds_dwordx4 %[wv], s[70:71] offset:0 sc1\n\t"
            "global_load_lds_dwordx4 %[wv], s[86:87] offset:1024 sc1\n\t"
            "global_load_lds_dwordx4 %[wv], s[70:71] offset:2048 sc1\n\t"
            "global_load_lds_dwordx4 %[wv], s[86:87] offset:3072 sc1\n\t"
            "s_add_u32 m0, %[ldsx], 32768\n\t"
            "s_add_u32 s68, %[xpl], 1664\n\t"
            "s_addc_u32 s69, %[xph], 0\n\t"
            "global_load_lds_dwordx4 %[x0], s[68:69] offset:0\n\t"
            "global_load_lds_dwordx4 %[x1], s[68:69] offset:1024\n\t"
            "global_load_lds_dwordx4 %[x2], s[68:69] offset:2048\n\t"
            "global_load_lds_dwordx4 %[x3], s[68:69] offset:3072\n\t"
            "s_add_u32 m0, %[ldsx], 36864\n\t"
            "s_add_u32 s68, %[xpl], 67200\n\t"
            "s_addc_u32 s69, %[xph], 0\n\t"
            "global_load_lds_dwordx4 %[x0], s[68:69] offset:0\n\t"
            "global_load_lds_dwordx4 %[x1], s[68:69] offset:1024\n\t"
            "global_load_lds_dwordx4 %[x2], s[68:69] offset:2048\n\t"
            "global_load_lds_dwordx4 %[x3], s[68:69] offset:3072\n\t"
            "s_waitcnt vmcnt(12)\n\t"
            "s_barrier\n\t"
            ".Lwp44:\n\t"
            "s_cbranch_execz .Lwp14\n\t"
            "s_add_u32 m0, %[ldsx], 65536\n\t"
            "s_add_u32 s68, %[xpl], 1792\n\t"
            "s_addc_u32 s69, %[xph], 0\n\t"
            "global_load_lds_dwordx4 %[x0], s[68:69] offset:0\n\t"
            "global_load_lds_dwordx4 %[x1], s[68:69] offset:1024\n\t"
            "global_load_lds_dwordx4 %[x2], s[68:69] offset:2048\n\t"
            "global_load_lds_dwordx4 %[x3], s[68:69] offset:3072\n\t"
            "s_add_u32 m0, %[ldsx], 69632\n\t"
            "s_add_u32 s68, %[xpl], 67328\n\t"
            "s_addc_u32 s69, %[xph], 0\n\t"
            "global_load_lds_dwordx4 %[x0], s[68:69] offset:0\n\t"
            "global_load_lds_dwordx4 %[x1], s[68:69] offset:1024\n\t"
            "global_load_lds_dwordx4 %[x2], s[68:69] offset:2048\n\t"
            "global_load_lds_dwordx4 %[x3], s[68:69] offset:3072\n\t"
            "s_add_u32 m0, %[ldsw], 32768\n\t"
            "s_add_u32 s70, s84, 6881280\n\t"
            "s_addc_u32 s71, s85, 0\n\t"
            "s_add_u32 s86, s84, 5963776\n\t"
            "s_addc_u32 s87, s85, 0\n\t"
            "global_load_lds_dwordx4 %[wv], s[70:71] offset:0 sc1\n\t"
            "global_load_lds_dwordx4 %[wv], s[86:87] offset:1024 sc1\n\t"
            "global_load_lds_dwordx4 %[wv], s[70:71] offset:2048 sc1\n\t"
            "global_load_lds_dwordx4 %[wv], s[86:87] offset:3072 sc1\n\t"
            "s_waitcnt vmcnt(4)\n\t"
            "s_barrier\n\t"
            "s_add_u32 m0, %[ldsx], 0\n\t"
            "s_add_u32 s68, %[xpl], 1920\n\t"
            "s_addc_u32 s69, %[xph], 0\n\t"
            "global_load_lds_dwordx4 %[x0], s[68:69] offset:0\n\t"
            "global_load_lds_dwordx4 %[x1], s[68:69] offset:1024\n\t"
            "global_load_lds_dwordx4 %[x2], s[68:69] offset:2048\n\t"
            "global_load_lds_dwordx4 %[x3], s[68:69] offset:3072\n\t"
            "s_add_u32 m0, %[ldsx], 4096\n\t"
            "s_add_u32 s68, %[xpl], 67456\n\t"
            "s_addc_u32 s69, %[xph], 0\n\t"
            "global_load_lds_dwordx4 %[x0], s[68:69] offset:0\n\t"
            "global_load_lds_dwordx4 %[x1], s[68:69] offset:1024\n\t"
            "global_load_lds_dwordx4 %[x2], s[68:69] offset:2048\n\t"
            "global_load_lds_dwordx4 %[x3], s[68:69] offset:3072\n\t"
            "s_add_u32 m0, %[ldsw], 0\n\t"
            "s_add_u32 s70, s84, 6423552\n\t"
            "s_addc_u32 s71, s85, 0\n\t"
            "s_add_u32 s86, s84, 6881280\n\t"
            "s_addc_u32 s87, s85, 0\n\t"
            "global_load_lds_dwordx4 %[wv], s[70:71] offset:0 sc1\n\t"
            "global_load_lds_dwordx4 %[wv], s[86:87] offset:1024 sc1\n\t"
            "global_load_lds_dwordx4 %[wv], s[70:71] offset:2048 sc1\n\t"
            "global_load_lds_dwordx4 %[wv], s[86:87] offset:3072 sc1\n\t"
            "s_waitcnt vmcnt(4)\n\t"
            "s_barrier\n\t"
            "s_mov_b32 s76, 16\n\t"
            ".Lzc24:\n\t"
            "v_cmp_eq_u32 vcc, v192, %[nvl]\n\t"
            "s_lshr_b64 s[74:75], vcc, s76\n\t"
            "s_and_b32 s74, s74, 65535\n\t"
            "s_cmp_eq_u32 s74, 65535\n\t"
            "s_cbranch_scc1 .Lzd24\n\t"
            "s_cmp_eq_u32 s73, 0\n\t"
            "s_cbranch_scc1 .Lzd24\n\t"
            "s_sub_u32 s73, s73, 1\n\t"
            "global_load_dword v192, %[po], %[ws] sc1\n\t"
            "s_waitcnt vmcnt(0)\n\t"
            "s_branch .Lzc24\n\t"
            ".Lzd24:\n\t"
            "s_waitcnt vmcnt(0)\n\t"
            "s_barrier\n\t"
            ".Lzend:\n\t"
            : [nvl] "+v"(nvl)
            : [x0] "v"(xoff[0]), [x1] "v"(xoff[1]), [x2] "v"(xoff[2]), [x3] "v"(xoff[3]), [wv] "v"(wv), [po] "v"(po), [qo] "v"(qoff[0]),
              [lutl] "v"(lutl), [wo] "v"(wo), [fo0] "v"(fo0), [fob] "v"(fob), [bd] "v"(bdst),
              [xpl] "s"((u32)KPTR(0)), [xph] "s"((u32)(KPTR(0) >> 32)), [ws] "s"(KPTR(4)),
              [qbg] "s"(qbg), [qa1] "s"(qa1), [tseq] "s"(tseq), [p] "s"(p), [h2] "s"(h2), [ldsx] "s"(ldsx), [ldsw] "s"(ldsw), [st1] "s"(st1), [st1l] "s"((u32)(unsigned long)st1), [st1h] "s"((u32)((unsigned long)st1 >> 32)), [stb] "s"(stb)
            : "vcc", "scc", "memory", "s56", "s57", "s58", "s59", "s60", "s61", "s62", "s63", "s64", "s65", "s66", "s67", "s68", "s69", "s70", "s71", "s72", "s73", "s74", "s75", "s76", "s77", "s78", "s79", "s80", "s81", "s82", "s83", "s84", "s85", "s86", "s87", "s88", "s89", "v128", "v129", "v130", "v131", "v132", "v133", "v134", "v135", "v136", "v137", "v138", "v139", "v140", "v141", "v142", "v143", "v144", "v145", "v146", "v147", "v148", "v149", "v150", "v151", "v152", "v153", "v154", "v155", "v156", "v157", "v158", "v159", "v160", "v161", "v162", "v163", "v164", "v165", "v166", "v167", "v168", "v169", "v170", "v171", "v172", "v173", "v174", "v175", "v176", "v177", "v178", "v179", "v180", "v181", "v182", "v183", "v184", "v185", "v186", "v187", "v188", "v189", "v190", "v191", "v192", "v193", "v194", "v195", "v196", "v197", "v198", "v199", "v200", "v201", "v202", "v203", "v204", "v205", "v206", "v207", "v208", "v209", "v210", "v211", "v212", "v213", "v214", "v215", "v216", "v217", "v218", "v219", "v220", "v221", "v222", "v223", "v224", "v225", "v226", "v227", "v228", "v229", "v230", "v231", "v232", "v233", "v234", "v235");
        if (mt == 0 && p == 0 && lane < 2) asm volatile("global_store_dword %0, %1, %2 sc1" :: "v"(no), "v"(nvl), "s"(KPTR(4)) : "memory");
        return;
    }

    const int c = lane & 15, g = lane >> 4;
    f32x4 acc[4][8];

    const int xb0 = (w * 64 + c) * 128 + ((g ^ (c >> 1)) * 16);
    const int xb1 = (w * 64 + c) * 128 + (((4 + g) ^ (c >> 1)) * 16);
    int ba = OFF_B + g * 2048 + c * 16;
    asm volatile("" : "+v"(ba));
    bf16x8 XP[2][4], XQ[2][4], WC[2][2];
#define LDX(a) (*(const bf16x8*)(smem + (a)))
#define MP(i, j, x0, w0, x1, w1) do { asm volatile("v_mfma_f32_16x16x32_bf16 %0, %1, %2, %0\n\tv_mfma_f32_16x16x32_bf16 %0, %3, %4, %0" \
                                      : "+v"(acc[i][j]) : "v"(x0), "v"(w0), "v"(x1), "v"(w1)); SB(); } while (0)
#define LD(d, a) do { d = LDX(a); SB(); } while (0)
#define CBAR() do { asm volatile("s_waitcnt lgkmcnt(0)" ::: "memory"); BAR(); asm volatile("" ::: "memory"); } while (0)

    u32 fo0, nvl;
    {
        const int p = w, l = lane & 31, h = lane >> 5;
        const int i2 = mt >> 1, h2 = mt & 1;
        u32 qoff[8];
#pragma unroll
        for (int r = 0; r < 8; ++r) qoff[r] = (u32)(((i2 * 64 + p * 16 + h * 8 + r) * N_ + col0 + l * 4) * 4);
        u32 lutl = (u32)(size_t)(smem) + OFF_L + l * 4;
        u32 bdst = (u32)(size_t)(smem) + OFF_B + p * 4096 + h * 2048 + ((l >> 4) * 64 + (l & 15)) * 16;
        u32 wo = (u32)(nt * 16384 + p * 4096 + h * 2048 + (l >> 4) * 1024 + (l & 15) * 16);
        u32 no = (u32)(nt * 256 + (lane & 1) * 4);
        u32 V[4][8];
        i32x4 Q0[8];
#define PK(b_, j, n) asm volatile("s_waitcnt lgkmcnt(" #n ")\n\t" \
                                    "v_lshl_or_b32 %0, %5, 16, %4\n\tv_lshl_or_b32 %1, %7, 16, %6\n\t" \
                                    "v_lshl_or_b32 %2, %9, 16, %8\n\tv_lshl_or_b32 %3, %11, 16, %10" \
                                    : "=&v"(b_[0]), "=&v"(b_[1]), "=&v"(b_[2]), "=&v"(b_[3]) \
                                    : "v"(V[j][0]), "v"(V[j][1]), "v"(V[j][2]), "v"(V[j][3]), "v"(V[j][4]), "v"(V[j][5]), "v"(V[j][6]), "v"(V[j][7]) : "memory")
#define WR(b_, j) asm volatile("ds_write_b128 %0, %1 offset:%2" :: "v"(bdst), "v"(b_), "i"((j) * 256) : "memory")
        {
            u32 lo = (u32)((p * 4 * N_ + col0 + (l * 2 + h) * 2) * 2);
            asm volatile("" : "+v"(lo), "+v"(no), "+v"(qoff[0]), "+v"(qoff[1]), "+v"(qoff[2]), "+v"(qoff[3]), "+v"(qoff[4]), "+v"(qoff[5]), "+v"(qoff[6]), "+v"(qoff[7]));
            KWAIT();
#pragma unroll
            for (int r = 0; r < 4; ++r) {
                const u16* base = (const u16*)KPTR(2) + r * N_;
                const u32 dst = (u32)(size_t)(smem) + OFF_L + (p * 4 + r) * 256;
                asm volatile("s_mov_b32 m0, %2\n\ts_nop 0\n\tglobal_load_lds_dword %0, %1" :: "v"(lo), "s"(base), "s"(dst) : "memory");
            }
        }
#pragma unroll
        for (int r = 0; r < 8; ++r) asm volatile("global_load_dwordx4 %0, %1, %2 " : "=v"(Q0[r]) : "v"(qoff[r]), "s"(KPTR(1)) : "memory");
        SB();
        BAR();
#pragma unroll
    for (int i = 0; i < 4; ++i)
#pragma unroll
        for (int j = 0; j < 8; ++j) { acc[i][j] = f32x4{0.f, 0.f, 0.f, 0.f}; asm volatile("" : "+v"(acc[i][j])); }
        const char* st0 = (const char*)KPTR(4) + WS_W + (long)i2 * TW + h2 * 512;
        fo0 = (u32)(WS_F + (nt * 4 + p) * 256 + lane * 4 + i2 * 16 + h2 * 8);
        asm volatile("" : "+v"(wo), "+v"(fo0), "+v"(bdst), "+v"(lutl));
        asm volatile("" : "+s"(st0));
        {
            unsigned long ex_;
            asm volatile("s_mov_b64 %0, exec\n\ts_mov_b64 exec, 0\n\t"
                         "s_cmp_eq_u32 %1, 0\n\ts_cbranch_scc1 .Lwp20\n\t"
                         "s_cmp_eq_u32 %1, 1\n\ts_cbranch_scc1 .Lwp11\n\t"
                         "s_cmp_eq_u32 %1, 2\n\ts_cbranch_scc1 .Lwp12\n\t"
                         "s_branch .Lwp13\n\t"
                         ".Lwp99:\n\ts_mov_b64 exec, %0" : "=&s"(ex_) : "s"(w) : "scc", "memory");
        }
        asm volatile("s_waitcnt vmcnt(8)" ::: "memory");
        BAR();
        PAD(20, 10);
        asm volatile("s_waitcnt vmcnt(7)" : "+v"(Q0[0]) :: "memory"); LUT1(Q0, 0, 0); LUT1(Q0, 1, 0); LUT1(Q0, 2, 0); LUT1(Q0, 3, 0); SB();
        asm volatile("s_waitcnt vmcnt(6)" : "+v"(Q0[1]) :: "memory"); LUT1(Q0, 0, 1); LUT1(Q0, 1, 1); LUT1(Q0, 2, 1); LUT1(Q0, 3, 1); SB();
        asm volatile("s_waitcnt vmcnt(5)" : "+v"(Q0[2]) :: "memory"); LUT1(Q0, 0, 2); LUT1(Q0, 1, 2); LUT1(Q0, 2, 2); LUT1(Q0, 3, 2); SB();
        asm volatile("s_waitcnt vmcnt(4)" : "+v"(Q0[3]) :: "memory"); LUT1(Q0, 0, 3); LUT1(Q0, 1, 3); LUT1(Q0, 2, 3); LUT1(Q0, 3, 3); SB();
        asm volatile("s_waitcnt vmcnt(3)" : "+v"(Q0[4]) :: "memory"); LUT1(Q0, 0, 4); LUT1(Q0, 1, 4); LUT1(Q0, 2, 4); LUT1(Q0, 3, 4); SB();
        asm volatile("s_waitcnt vmcnt(2)" : "+v"(Q0[5]) :: "memory"); LUT1(Q0, 0, 5); LUT1(Q0, 1, 5); LUT1(Q0, 2, 5); LUT1(Q0, 3, 5); SB();
        asm volatile("s_waitcnt vmcnt(1)" : "+v"(Q0[6]) :: "memory"); LUT1(Q0, 0, 6); LUT1(Q0, 1, 6); LUT1(Q0, 2, 6); LUT1(Q0, 3, 6); SB();
        asm volatile("s_waitcnt vmcnt(0)" : "+v"(Q0[7]) :: "memory"); LUT1(Q0, 0, 7); LUT1(Q0, 1, 7); LUT1(Q0, 2, 7); LUT1(Q0, 3, 7); SB();
        {   u32x4 b0, b1, b2, b3;


            PK(b0, 0, 3); WR(b0, 0); PK(b1, 1, 3); WR(b1, 1); PK(b2, 2, 3); WR(b2, 2); PK(b3, 3, 3); WR(b3, 3);

            SB();
        }
        asm volatile("s_waitcnt lgkmcnt(0)" ::: "memory");
        BAR();
    }
    PAD(10, 99);
    {   bf16x8 W0[2][2], W1[2][2], W2[2][2];
    LD(W0[0][0], ba + 0 * BS + 0);
    LD(XP[0][0], xb0 + 0);
    LD(W0[0][1], ba + 1 * BS + 0);
    LD(XP[1][0], xb1 + 0);
    LD(W0[1][1], ba + 1 * BS + 256);
    LD(W0[1][0], ba + 0 * BS + 256);
    LD(XP[0][1], xb0 + 2048);
    LD(XP[1][1], xb1 + 2048);
    MP(0, 0, XP[0][0], W0[0][0], XP[1][0], W0[0][1]);
    LD(XP[0][2], xb0 + 4096);
    MP(0, 1, XP[1][0], W0[1][1], XP[0][0], W0[1][0]);
    LD(XP[1][2], xb1 + 4096);
    MP(1, 1, XP[0][1], W0[1][0], XP[1][1], W0[1][1]);
    LD(XP[0][3], xb0 + 6144);
    MP(1, 0, XP[1][1], W0[0][1], XP[0][1], W0[0][0]);
    LD(XP[1][3], xb1 + 6144);
    MP(2, 0, XP[0][2], W0[0][0], XP[1][2], W0[0][1]);
    LD(W1[0][0], ba + 0 * BS + 512);
    MP(2, 1, XP[1][2], W0[1][1], XP[0][2], W0[1][0]);
    LD(W1[0][1], ba + 1 * BS + 512);
    MP(3, 1, XP[0][3], W0[1][0], XP[1][3], W0[1][1]);
    LD(W1[1][1], ba + 1 * BS + 768);
    MP(3, 0, XP[1][3], W0[0][1], XP[0][3], W0[0][0]);
    LD(W1[1][0], ba + 0 * BS + 768);
    MP(0, 2, XP[0][0], W1[0][0], XP[1][0], W1[0][1]);
    LD(W2[0][0], ba + 0 * BS + 1024);
    MP(0, 3, XP[1][0], W1[1][1], XP[0][0], W1[1][0]);
    LD(W2[0][1], ba + 1 * BS + 1024);
    MP(1, 3, XP[0][1], W1[1][0], XP[1][1], W1[1][1]);
    LD(W2[1][1], ba + 1 * BS + 1280);
    MP(1, 2, XP[1][1], W1[0][1], XP[0][1], W1[0][0]);
    LD(W2[1][0], ba + 0 * BS + 1280);
    MP(2, 2, XP[0][2], W1[0][0], XP[1][2], W1[0][1]);
    MP(2, 3, XP[1][2], W1[1][1], XP[0][2], W1[1][0]);
    MP(3, 3, XP[0][3], W1[1][0], XP[1][3], W1[1][1]);
    MP(3, 2, XP[1][3], W1[0][1], XP[0][3], W1[0][0]);
    MP(0, 4, XP[0][0], W2[0][0], XP[1][0], W2[0][1]);
    LD(WC[0][0], ba + 0 * BS + 1536);
    MP(0, 5, XP[1][0], W2[1][1], XP[0][0], W2[1][0]);
    LD(WC[0][1], ba + 1 * BS + 1536);
    MP(1, 5, XP[0][1], W2[1][0], XP[1][1], W2[1][1]);
    LD(WC[1][1], ba + 1 * BS + 1792);
    MP(1, 4, XP[1][1], W2[0][1], XP[0][1], W2[0][0]);
    LD(WC[1][0], ba + 0 * BS + 1792);
    MP(2, 4, XP[0][2], W2[0][0], XP[1][2], W2[0][1]);
    MP(2, 5, XP[1][2], W2[1][1], XP[0][2], W2[1][0]);
    MP(3, 5, XP[0][3], W2[1][0], XP[1][3], W2[1][1]);
    MP(3, 4, XP[1][3], W2[0][1], XP[0][3], W2[0][0]);
    }
    {
            unsigned long ex_;
            asm volatile("s_mov_b64 %0, exec\n\ts_mov_b64 exec, 0\n\t"
                         "s_cmp_eq_u32 %1, 0\n\ts_cbranch_scc1 .Lwp34\n\t"
                         "s_cmp_eq_u32 %1, 1\n\ts_cbranch_scc1 .Lwp38\n\t"
                         "s_cmp_eq_u32 %1, 2\n\ts_cbranch_scc1 .Lwp42\n\t"
                         "s_branch .Lwp15\n\t"
                         ".Lwp97:\n\ts_mov_b64 exec, %0" : "=&s"(ex_) : "s"(w) : "scc", "memory");
        }
    CBAR();
    int ln;
    asm volatile("v_mbcnt_lo_u32_b32 %0, -1, 0\n\tv_mbcnt_hi_u32_b32 %0, -1, %0" : "=v"(ln));
    u32 oo[4];
    unsigned long ob[4];
#pragma unroll
    for (int r = 0; r < 4; ++r) {
        oo[r] = (u32)(((row0 + w * 64 + (ln >> 4) * 4 + r) * N_ + col0 + (ln & 15) * 4) * 2);
        ob[r] = KPTR(3) + (unsigned long)(r * 16 * N_ * 2);
        asm volatile("" : "+v"(oo[r]), "+s"(ob[r]));
    }
    SB();
    int xo = XS;
#pragma unroll 1
    for (int k = 0; k < 6; ++k) {
        PAD(11, 99);
        int xa0 = xb0 + xo, xa1 = xb1 + xo, wa = ba + (xo >> 1);
        {   bf16x8 W0[2][2], W1[2][2], W2[2][2];
        MP(0, 6, XP[0][0], WC[0][0], XP[1][0], WC[0][1]);
        LD(W0[0][0], wa + 0 * BS + 0);
        LD(XQ[0][0], xa0 + 0);
        MP(0, 7, XP[1][0], WC[1][1], XP[0][0], WC[1][0]);
        LD(W0[0][1], wa + 1 * BS + 0);
        LD(XQ[1][0], xa1 + 0);
        MP(1, 7, XP[0][1], WC[1][0], XP[1][1], WC[1][1]);
        LD(W0[1][1], wa + 1 * BS + 256);
        MP(1, 6, XP[1][1], WC[0][1], XP[0][1], WC[0][0]);
        LD(W0[1][0], wa + 0 * BS + 256);
        MP(2, 6, XP[0][2], WC[0][0], XP[1][2], WC[0][1]);
        LD(XQ[0][1], xa0 + 2048);
        MP(2, 7, XP[1][2], WC[1][1], XP[0][2], WC[1][0]);
        LD(XQ[1][1], xa1 + 2048);
        MP(3, 7, XP[0][3], WC[1][0], XP[1][3], WC[1][1]);
        LD(XQ[0][2], xa0 + 4096);
        MP(3, 6, XP[1][3], WC[0][1], XP[0][3], WC[0][0]);
        LD(XQ[1][2], xa1 + 4096);
        MP(0, 0, XQ[0][0], W0[0][0], XQ[1][0], W0[0][1]);
        LD(XQ[0][3], xa0 + 6144);
        LD(W1[0][0], wa + 0 * BS + 512);
        MP(0, 1, XQ[1][0], W0[1][1], XQ[0][0], W0[1][0]);
        LD(XQ[1][3], xa1 + 6144);
        LD(W1[0][1], wa + 1 * BS + 512);
        MP(1, 1, XQ[0][1], W0[1][0], XQ[1][1], W0[1][1]);
        LD(W1[1][1], wa + 1 * BS + 768);
        MP(1, 0, XQ[1][1], W0[0][1], XQ[0][1], W0[0][0]);
        LD(W1[1][0], wa + 0 * BS + 768);
        MP(2, 0, XQ[0][2], W0[0][0], XQ[1][2], W0[0][1]);
        MP(2, 1, XQ[1][2], W0[1][1], XQ[0][2], W0[1][0]);
        MP(3, 1, XQ[0][3], W0[1][0], XQ[1][3], W0[1][1]);
        MP(3, 0, XQ[1][3], W0[0][1], XQ[0][3], W0[0][0]);
        MP(0, 2, XQ[0][0], W1[0][0], XQ[1][0], W1[0][1]);
        LD(W2[0][0], wa + 0 * BS + 1024);
        MP(0, 3, XQ[1][0], W1[1][1], XQ[0][0], W1[1][0]);
        LD(W2[0][1], wa + 1 * BS + 1024);
        PAD(12, 99);
        MP(1, 3, XQ[0][1], W1[1][0], XQ[1][1], W1[1][1]);
        LD(W2[1][1], wa + 1 * BS + 1280);
        MP(1, 2, XQ[1][1], W1[0][1], XQ[0][1], W1[0][0]);
        LD(W2[1][0], wa + 0 * BS + 1280);
        MP(2, 2, XQ[0][2], W1[0][0], XQ[1][2], W1[0][1]);
        MP(2, 3, XQ[1][2], W1[1][1], XQ[0][2], W1[1][0]);
        MP(3, 3, XQ[0][3], W1[1][0], XQ[1][3], W1[1][1]);
        MP(3, 2, XQ[1][3], W1[0][1], XQ[0][3], W1[0][0]);
        MP(0, 4, XQ[0][0], W2[0][0], XQ[1][0], W2[0][1]);
        LD(WC[0][0], wa + 0 * BS + 1536);
        MP(0, 5, XQ[1][0], W2[1][1], XQ[0][0], W2[1][0]);
        LD(WC[0][1], wa + 1 * BS + 1536);
        MP(1, 5, XQ[0][1], W2[1][0], XQ[1][1], W2[1][1]);
        LD(WC[1][1], wa + 1 * BS + 1792);
        MP(1, 4, XQ[1][1], W2[0][1], XQ[0][1], W2[0][0]);
        LD(WC[1][0], wa + 0 * BS + 1792);
        MP(2, 4, XQ[0][2], W2[0][0], XQ[1][2], W2[0][1]);
        MP(2, 5, XQ[1][2], W2[1][1], XQ[0][2], W2[1][0]);
        MP(3, 5, XQ[0][3], W2[1][0], XQ[1][3], W2[1][1]);
        MP(3, 4, XQ[1][3], W2[0][1], XQ[0][3], W2[0][0]);
        }
        CBAR();
        xo = (xo == 2 * XS) ? 0 : xo + XS;
        xa0 = xb0 + xo; xa1 = xb1 + xo; wa = ba + (xo >> 1);
        {   bf16x8 W0[2][2], W1[2][2], W2[2][2];
        MP(0, 6, XQ[0][0], WC[0][0], XQ[1][0], WC[0][1]);
        LD(W0[0][0], wa + 0 * BS + 0);
        LD(XP[0][0], xa0 + 0);
        MP(0, 7, XQ[1][0], WC[1][1], XQ[0][0], WC[1][0]);
        LD(W0[0][1], wa + 1 * BS + 0);
        LD(XP[1][0], xa1 + 0);
        MP(1, 7, XQ[0][1], WC[1][0], XQ[1][1], WC[1][1]);
        LD(W0[1][1], wa + 1 * BS + 256);
        MP(1, 6, XQ[1][1], WC[0][1], XQ[0][1], WC[0][0]);
        LD(W0[1][0], wa + 0 * BS + 256);
        MP(2, 6, XQ[0][2], WC[0][0], XQ[1][2], WC[0][1]);
        LD(XP[0][1], xa0 + 2048);
        MP(2, 7, XQ[1][2], WC[1][1], XQ[0][2], WC[1][0]);
        LD(XP[1][1], xa1 + 2048);
        MP(3, 7, XQ[0][3], WC[1][0], XQ[1][3], WC[1][1]);
        LD(XP[0][2], xa0 + 4096);
        MP(3, 6, XQ[1][3], WC[0][1], XQ[0][3], WC[0][0]);
        LD(XP[1][2], xa1 + 4096);
        PAD(13, 99);
        MP(0, 0, XP[0][0], W0[0][0], XP[1][0], W0[0][1]);
        LD(XP[0][3], xa0 + 6144);
        LD(W1[0][0], wa + 0 * BS + 512);
        MP(0, 1, XP[1][0], W0[1][1], XP[0][0], W0[1][0]);
        LD(XP[1][3], xa1 + 6144);
        LD(W1[0][1], wa + 1 * BS + 512);
        MP(1, 1, XP[0][1], W0[1][0], XP[1][1], W0[1][1]);
        LD(W1[1][1], wa + 1 * BS + 768);
        MP(1, 0, XP[1][1], W0[0][1], XP[0][1], W0[0][0]);
        LD(W1[1][0], wa + 0 * BS + 768);
        MP(2, 0, XP[0][2], W0[0][0], XP[1][2], W0[0][1]);
        MP(2, 1, XP[1][2], W0[1][1], XP[0][2], W0[1][0]);
        MP(3, 1, XP[0][3], W0[1][0], XP[1][3], W0[1][1]);
        MP(3, 0, XP[1][3], W0[0][1], XP[0][3], W0[0][0]);
        MP(0, 2, XP[0][0], W1[0][0], XP[1][0], W1[0][1]);
        LD(W2[0][0], wa + 0 * BS + 1024);
        MP(0, 3, XP[1][0], W1[1][1], XP[0][0], W1[1][0]);
        LD(W2[0][1], wa + 1 * BS + 1024);
        MP(1, 3, XP[0][1], W1[1][0], XP[1][1], W1[1][1]);
        LD(W2[1][1], wa + 1 * BS + 1280);
        MP(1, 2, XP[1][1], W1[0][1], XP[0][1], W1[0][0]);
        LD(W2[1][0], wa + 0 * BS + 1280);
        MP(2, 2, XP[0][2], W1[0][0], XP[1][2], W1[0][1]);
        MP(2, 3, XP[1][2], W1[1][1], XP[0][2], W1[1][0]);
        MP(3, 3, XP[0][3], W1[1][0], XP[1][3], W1[1][1]);
        MP(3, 2, XP[1][3], W1[0][1], XP[0][3], W1[0][0]);
        MP(0, 4, XP[0][0], W2[0][0], XP[1][0], W2[0][1]);
        LD(WC[0][0], wa + 0 * BS + 1536);
        MP(0, 5, XP[1][0], W2[1][1], XP[0][0], W2[1][0]);
        LD(WC[0][1], wa + 1 * BS + 1536);
        MP(1, 5, XP[0][1], W2[1][0], XP[1][1], W2[1][1]);
        LD(WC[1][1], wa + 1 * BS + 1792);
        MP(1, 4, XP[1][1], W2[0][1], XP[0][1], W2[0][0]);
        LD(WC[1][0], wa + 0 * BS + 1792);
        MP(2, 4, XP[0][2], W2[0][0], XP[1][2], W2[0][1]);
        MP(2, 5, XP[1][2], W2[1][1], XP[0][2], W2[1][0]);
        MP(3, 5, XP[0][3], W2[1][0], XP[1][3], W2[1][1]);
        MP(3, 4, XP[1][3], W2[0][1], XP[0][3], W2[0][0]);
        }
        CBAR();
        xo = (xo == 2 * XS) ? 0 : xo + XS;
        if (k == 0) {   }
        if (k == 4) {   }
    }
    constexpr int X13 = XS, X15 = 0;
    int xe0 = xb0 + 2 * XS, xe1 = xb1 + 2 * XS;
    asm volatile("" : "+v"(xe0), "+v"(xe1));
    __builtin_amdgcn_s_setprio(2);
#define ST2(v, i, hf, r) asm volatile("global_store_dwordx2 %0, %1, %2 offset:%3 nt" :: "v"(oo[r]), "v"(v), "s"(ob[i]), "i"((hf) * 128) : "memory")
    {
        bf16x8 W13_0_0, X13_0_0_0, W13_0_1, X13_0_1_0, W13_1_1, W13_1_0, X13_1_0_0, X13_1_1_0, X13_2_0_0, X13_2_1_0, X13_3_0_0, X13_3_1_0;
        bf16x8 W13_2_0, W13_2_1, W13_3_1, W13_3_0, W14_0_0, X14_0_0_0, W14_0_1, X14_0_1_0, W14_1_1, W14_1_0, W14_2_0, W14_2_1;
        bf16x8 W14_3_1, W14_3_0, X14_1_0_0, X14_1_1_0, X14_2_0_0, X14_2_1_0, W15_0_0, X15_0_0_0, W15_0_1, X15_0_1_0, W15_1_1, W15_1_0;
        bf16x8 W15_2_0, W15_2_1, W15_3_1, W15_3_0, X14_3_0_0, X14_3_1_0, X15_1_0_0, X15_1_1_0, X15_2_0_0, X15_2_1_0, X15_3_0_0, X15_3_1_0;
        bf16x8 W13_4_0, X13_0_0_1, W13_4_1, X13_0_1_1, W13_5_1, W13_5_0, W13_6_0, W13_6_1, W13_7_1, W13_7_0, X13_1_0_1, X13_1_1_1;
        bf16x8 X13_2_0_1, X13_2_1_1, X13_3_0_1, X13_3_1_1, W14_4_0, X14_0_0_1, W14_4_1, X14_0_1_1, W15_4_0, X15_0_0_1, W15_4_1, X15_0_1_1;
        bf16x8 W15_5_1, W15_5_0, W14_5_1, W14_5_0, W14_6_0, W14_6_1, W15_6_0, W15_6_1, W15_7_1, W15_7_0, W14_7_1, W14_7_0;
        bf16x8 X14_1_0_1, X14_1_1_1, X15_1_0_1, X15_1_1_1, X14_2_0_1, X14_2_1_1, X15_2_0_1, X15_2_1_1, X14_3_0_1, X14_3_1_1, X15_3_0_1, X15_3_1_1;
        u32x2 S0, S1, S2, S3, S4, S5, S6, S7, S8, S9, S10, S11, S12, S13, S14, S15, S16, S17, S18, S19, S20, S21, S22, S23, S24, S25, S26, S27, S28, S29, S30, S31;
        PAD(14, 97);
        MP(0, 6, XP[0][0], WC[0][0], XP[1][0], WC[0][1]);
        LD(W13_0_0, ba + 1 * WSL + 0 * BS + 0);
        LD(X13_0_0_0, xb0 + X13 + 0);
        MP(0, 7, XP[1][0], WC[1][1], XP[0][0], WC[1][0]);
        LD(W13_0_1, ba + 1 * WSL + 1 * BS + 0);
        LD(X13_0_1_0, xb1 + X13 + 0);
        MP(1, 7, XP[0][1], WC[1][0], XP[1][1], WC[1][1]);
        LD(W13_1_1, ba + 1 * WSL + 1 * BS + 256);
        LD(W13_1_0, ba + 1 * WSL + 0 * BS + 256);
        LD(X13_1_0_0, xb0 + X13 + 2048);
        MP(1, 6, XP[1][1], WC[0][1], XP[0][1], WC[0][0]);
        LD(X13_1_1_0, xb1 + X13 + 2048);
        MP(2, 6, XP[0][2], WC[0][0], XP[1][2], WC[0][1]);
        LD(X13_2_0_0, xb0 + X13 + 4096);
        MP(2, 7, XP[1][2], WC[1][1], XP[0][2], WC[1][0]);
        LD(X13_2_1_0, xb1 + X13 + 4096);
        MP(3, 7, XP[0][3], WC[1][0], XP[1][3], WC[1][1]);
        LD(X13_3_0_0, xb0 + X13 + 6144);
        MP(3, 6, XP[1][3], WC[0][1], XP[0][3], WC[0][0]);
        LD(X13_3_1_0, xb1 + X13 + 6144);
        MP(0, 0, X13_0_0_0, W13_0_0, X13_0_1_0, W13_0_1);
        LD(W13_2_0, ba + 1 * WSL + 0 * BS + 512);
        MP(0, 1, X13_0_1_0, W13_1_1, X13_0_0_0, W13_1_0);
        LD(W13_2_1, ba + 1 * WSL + 1 * BS + 512);
        MP(1, 1, X13_1_0_0, W13_1_0, X13_1_1_0, W13_1_1);
        LD(W13_3_1, ba + 1 * WSL + 1 * BS + 768);
        MP(1, 0, X13_1_1_0, W13_0_1, X13_1_0_0, W13_0_0);
        LD(W13_3_0, ba + 1 * WSL + 0 * BS + 768);
        MP(2, 0, X13_2_0_0, W13_0_0, X13_2_1_0, W13_0_1);
        LD(W14_0_0, ba + 1 * WSL + 0 * BS + 1024);
        MP(2, 1, X13_2_1_0, W13_1_1, X13_2_0_0, W13_1_0);
        LD(X14_0_0_0, xe0 + 0);
        MP(3, 1, X13_3_0_0, W13_1_0, X13_3_1_0, W13_1_1);
        LD(W14_0_1, ba + 1 * WSL + 1 * BS + 1024);
        MP(3, 0, X13_3_1_0, W13_0_1, X13_3_0_0, W13_0_0);
        LD(X14_0_1_0, xe1 + 0);
        MP(0, 2, X13_0_0_0, W13_2_0, X13_0_1_0, W13_2_1);
        LD(W14_1_1, ba + 1 * WSL + 1 * BS + 1280);
        MP(0, 3, X13_0_1_0, W13_3_1, X13_0_0_0, W13_3_0);
        LD(W14_1_0, ba + 1 * WSL + 0 * BS + 1280);
        MP(1, 3, X13_1_0_0, W13_3_0, X13_1_1_0, W13_3_1);
        LD(W14_2_0, ba + 1 * WSL + 0 * BS + 1536);
        LD(W14_2_1, ba + 1 * WSL + 1 * BS + 1536);
        MP(1, 2, X13_1_1_0, W13_2_1, X13_1_0_0, W13_2_0);
        LD(W14_3_1, ba + 1 * WSL + 1 * BS + 1792);
        MP(2, 2, X13_2_0_0, W13_2_0, X13_2_1_0, W13_2_1);
        LD(W14_3_0, ba + 1 * WSL + 0 * BS + 1792);
        MP(2, 3, X13_2_1_0, W13_3_1, X13_2_0_0, W13_3_0);
        LD(X14_1_0_0, xe0 + 2048);
        MP(3, 3, X13_3_0_0, W13_3_0, X13_3_1_0, W13_3_1);
        LD(X14_1_1_0, xe1 + 2048);
        MP(3, 2, X13_3_1_0, W13_2_1, X13_3_0_0, W13_2_0);
        MP(0, 0, X14_0_0_0, W14_0_0, X14_0_1_0, W14_0_1);
        MP(0, 1, X14_0_1_0, W14_1_1, X14_0_0_0, W14_1_0);
        LD(X14_2_0_0, xe0 + 4096);
        MP(0, 2, X14_0_0_0, W14_2_0, X14_0_1_0, W14_2_1);
        LD(X14_2_1_0, xe1 + 4096);
        MP(0, 3, X14_0_1_0, W14_3_1, X14_0_0_0, W14_3_0);
        MP(1, 0, X14_1_0_0, W14_0_0, X14_1_1_0, W14_0_1);
        LD(X14_3_0_0, xe0 + 6144);
        MP(1, 1, X14_1_1_0, W14_1_1, X14_1_0_0, W14_1_0);
        LD(X14_3_1_0, xe1 + 6144);
        MP(1, 2, X14_1_0_0, W14_2_0, X14_1_1_0, W14_2_1);
        MP(1, 3, X14_1_1_0, W14_3_1, X14_1_0_0, W14_3_0);
        CBAR();
        SB();
        PAD(15, 16);
        MP(2, 0, X14_2_0_0, W14_0_0, X14_2_1_0, W14_0_1);
        LD(W15_0_0, ba + 2 * WSL + 0 * BS + 0);
        LD(X15_0_0_0, xb0 + X15 + 0);
        MP(2, 1, X14_2_1_0, W14_1_1, X14_2_0_0, W14_1_0);
        LD(W15_0_1, ba + 2 * WSL + 1 * BS + 0);
        LD(X15_0_1_0, xb1 + X15 + 0);
        MP(2, 2, X14_2_0_0, W14_2_0, X14_2_1_0, W14_2_1);
        LD(W15_1_1, ba + 2 * WSL + 1 * BS + 256);
        LD(W15_1_0, ba + 2 * WSL + 0 * BS + 256);
        MP(2, 3, X14_2_1_0, W14_3_1, X14_2_0_0, W14_3_0);
        LD(W15_2_0, ba + 2 * WSL + 0 * BS + 512);
        LD(W15_2_1, ba + 2 * WSL + 1 * BS + 512);
        MP(0, 0, X15_0_0_0, W15_0_0, X15_0_1_0, W15_0_1);
        LD(W15_3_1, ba + 2 * WSL + 1 * BS + 768);
        LD(W15_3_0, ba + 2 * WSL + 0 * BS + 768);
        MP(0, 1, X15_0_1_0, W15_1_1, X15_0_0_0, W15_1_0);
        LD(X15_1_0_0, xb0 + X15 + 2048);
        MP(0, 2, X15_0_0_0, W15_2_0, X15_0_1_0, W15_2_1);
        LD(X15_1_1_0, xb1 + X15 + 2048);
        MP(0, 3, X15_0_1_0, W15_3_1, X15_0_0_0, W15_3_0);
        MP(3, 0, X14_3_0_0, W14_0_0, X14_3_1_0, W14_0_1);
        LD(X15_2_0_0, xb0 + X15 + 4096);
        S0[0] = pack_bf16(acc[0][0][0], acc[0][1][0]); asm volatile("" : "+v"(S0[0])); SB();
        MP(3, 1, X14_3_1_0, W14_1_1, X14_3_0_0, W14_1_0);
        LD(X15_2_1_0, xb1 + X15 + 4096);
        S0[1] = pack_bf16(acc[0][2][0], acc[0][3][0]); asm volatile("" : "+v"(S0[1])); SB();
        ST2(S0, 0, 0, 0); SB();
        MP(3, 2, X14_3_0_0, W14_2_0, X14_3_1_0, W14_2_1);
        LD(X15_3_0_0, xb0 + X15 + 6144);
        S1[1] = pack_bf16(acc[0][2][1], acc[0][3][1]); asm volatile("" : "+v"(S1[1])); SB();
        S1[0] = pack_bf16(acc[0][0][1], acc[0][1][1]); asm volatile("" : "+v"(S1[0])); SB();
        MP(3, 3, X14_3_1_0, W14_3_1, X14_3_0_0, W14_3_0);
        LD(X15_3_1_0, xb1 + X15 + 6144);
        ST2(S1, 0, 0, 1); SB();
        MP(1, 0, X15_1_0_0, W15_0_0, X15_1_1_0, W15_0_1);
        LD(X13_0_0_1, xb0 + X13 + 0);
        S2[1] = pack_bf16(acc[0][2][2], acc[0][3][2]); asm volatile("" : "+v"(S2[1])); SB();
        S2[0] = pack_bf16(acc[0][0][2], acc[0][1][2]); asm volatile("" : "+v"(S2[0])); SB();
        MP(1, 1, X15_1_1_0, W15_1_1, X15_1_0_0, W15_1_0);
        LD(W13_4_0, ba + 2 * WSL + 0 * BS + 1024);
        ST2(S2, 0, 0, 2); SB();
        MP(1, 2, X15_1_0_0, W15_2_0, X15_1_1_0, W15_2_1);
        LD(X13_0_1_1, xb1 + X13 + 0);
        S3[1] = pack_bf16(acc[0][2][3], acc[0][3][3]); asm volatile("" : "+v"(S3[1])); SB();
        S3[0] = pack_bf16(acc[0][0][3], acc[0][1][3]); asm volatile("" : "+v"(S3[0])); SB();
        MP(1, 3, X15_1_1_0, W15_3_1, X15_1_0_0, W15_3_0);
        LD(W13_4_1, ba + 2 * WSL + 1 * BS + 1024);
        ST2(S3, 0, 0, 3); SB();
        MP(2, 0, X15_2_0_0, W15_0_0, X15_2_1_0, W15_0_1);
        LD(W13_5_1, ba + 2 * WSL + 1 * BS + 1280);
        S4[0] = pack_bf16(acc[1][0][0], acc[1][1][0]); asm volatile("" : "+v"(S4[0])); SB();
        MP(2, 1, X15_2_1_0, W15_1_1, X15_2_0_0, W15_1_0);
        LD(W13_5_0, ba + 2 * WSL + 0 * BS + 1280);
        S4[1] = pack_bf16(acc[1][2][0], acc[1][3][0]); asm volatile("" : "+v"(S4[1])); SB();
        ST2(S4, 1, 0, 0); SB();
        MP(2, 2, X15_2_0_0, W15_2_0, X15_2_1_0, W15_2_1);
        LD(W13_6_0, ba + 2 * WSL + 0 * BS + 1536);
        LD(W13_6_1, ba + 2 * WSL + 1 * BS + 1536);
        S5[1] = pack_bf16(acc[1][2][1], acc[1][3][1]); asm volatile("" : "+v"(S5[1])); SB();
        S5[0] = pack_bf16(acc[1][0][1], acc[1][1][1]); asm volatile("" : "+v"(S5[0])); SB();
        MP(2, 3, X15_2_1_0, W15_3_1, X15_2_0_0, W15_3_0);
        LD(W13_7_1, ba + 2 * WSL + 1 * BS + 1792);
        ST2(S5, 1, 0, 1); SB();
        PAD(16, 17);
        MP(3, 0, X15_3_0_0, W15_0_0, X15_3_1_0, W15_0_1);
        LD(W13_7_0, ba + 2 * WSL + 0 * BS + 1792);
        S6[1] = pack_bf16(acc[1][2][2], acc[1][3][2]); asm volatile("" : "+v"(S6[1])); SB();
        S6[0] = pack_bf16(acc[1][0][2], acc[1][1][2]); asm volatile("" : "+v"(S6[0])); SB();
        MP(3, 1, X15_3_1_0, W15_1_1, X15_3_0_0, W15_1_0);
        LD(X13_1_0_1, xb0 + X13 + 2048);
        ST2(S6, 1, 0, 2); SB();
        MP(3, 2, X15_3_0_0, W15_2_0, X15_3_1_0, W15_2_1);
        LD(X13_1_1_1, xb1 + X13 + 2048);
        S7[1] = pack_bf16(acc[1][2][3], acc[1][3][3]); asm volatile("" : "+v"(S7[1])); SB();
        S7[0] = pack_bf16(acc[1][0][3], acc[1][1][3]); asm volatile("" : "+v"(S7[0])); SB();
        MP(3, 3, X15_3_1_0, W15_3_1, X15_3_0_0, W15_3_0);
        ST2(S7, 1, 0, 3); SB();
        MP(0, 4, X13_0_0_1, W13_4_0, X13_0_1_1, W13_4_1);
        LD(X13_2_0_1, xb0 + X13 + 4096);
        S8[1] = pack_bf16(acc[2][2][0], acc[2][3][0]); asm volatile("" : "+v"(S8[1])); SB();
        S8[0] = pack_bf16(acc[2][0][0], acc[2][1][0]); asm volatile("" : "+v"(S8[0])); SB();
        MP(0, 5, X13_0_1_1, W13_5_1, X13_0_0_1, W13_5_0);
        LD(X13_2_1_1, xb1 + X13 + 4096);
        ST2(S8, 2, 0, 0); SB();
        MP(0, 6, X13_0_0_1, W13_6_0, X13_0_1_1, W13_6_1);
        LD(X13_3_0_1, xb0 + X13 + 6144);
        S9[1] = pack_bf16(acc[2][2][1], acc[2][3][1]); asm volatile("" : "+v"(S9[1])); SB();
        S9[0] = pack_bf16(acc[2][0][1], acc[2][1][1]); asm volatile("" : "+v"(S9[0])); SB();
        MP(0, 7, X13_0_1_1, W13_7_1, X13_0_0_1, W13_7_0);
        LD(X13_3_1_1, xb1 + X13 + 6144);
        ST2(S9, 2, 0, 1); SB();
        MP(1, 4, X13_1_0_1, W13_4_0, X13_1_1_1, W13_4_1);
        LD(X14_0_0_1, xe0 + 0);
        S10[1] = pack_bf16(acc[2][2][2], acc[2][3][2]); asm volatile("" : "+v"(S10[1])); SB();
        S10[0] = pack_bf16(acc[2][0][2], acc[2][1][2]); asm volatile("" : "+v"(S10[0])); SB();
        MP(1, 5, X13_1_1_1, W13_5_1, X13_1_0_1, W13_5_0);
        LD(X14_0_1_1, xe1 + 0);
        LD(X15_0_0_1, xb0 + X15 + 0);
        ST2(S10, 2, 0, 2); SB();
        MP(1, 6, X13_1_0_1, W13_6_0, X13_1_1_1, W13_6_1);
        S11[1] = pack_bf16(acc[2][2][3], acc[2][3][3]); asm volatile("" : "+v"(S11[1])); SB();
        S11[0] = pack_bf16(acc[2][0][3], acc[2][1][3]); asm volatile("" : "+v"(S11[0])); SB();
        MP(1, 7, X13_1_1_1, W13_7_1, X13_1_0_1, W13_7_0);
        ST2(S11, 2, 0, 3); SB();
        asm volatile("" ::: "memory");
        BAR();
        asm volatile("" ::: "memory");
        SB();
        MP(2, 4, X13_2_0_1, W13_4_0, X13_2_1_1, W13_4_1);
        LD(W14_4_0, ba + 0 * BS + 0);
        LD(X15_0_1_1, xb1 + X15 + 0);
        S12[1] = pack_bf16(acc[3][2][0], acc[3][3][0]); asm volatile("" : "+v"(S12[1])); SB();
        S12[0] = pack_bf16(acc[3][0][0], acc[3][1][0]); asm volatile("" : "+v"(S12[0])); SB();
        PAD(17, 18);
        MP(2, 5, X13_2_1_1, W13_5_1, X13_2_0_1, W13_5_0);
        LD(W14_4_1, ba + 1 * BS + 0);
        LD(W15_4_0, ba + 0 * BS + 1024);
        LD(W15_4_1, ba + 1 * BS + 1024);
        ST2(S12, 3, 0, 0); SB();
        MP(2, 6, X13_2_0_1, W13_6_0, X13_2_1_1, W13_6_1);
        LD(W15_5_1, ba + 1 * BS + 1280);
        LD(W15_5_0, ba + 0 * BS + 1280);
        LD(W14_5_1, ba + 1 * BS + 256);
        S13[1] = pack_bf16(acc[3][2][1], acc[3][3][1]); asm volatile("" : "+v"(S13[1])); SB();
        S13[0] = pack_bf16(acc[3][0][1], acc[3][1][1]); asm volatile("" : "+v"(S13[0])); SB();
        MP(2, 7, X13_2_1_1, W13_7_1, X13_2_0_1, W13_7_0);
        LD(W14_5_0, ba + 0 * BS + 256);
        LD(W14_6_0, ba + 0 * BS + 512);
        LD(W14_6_1, ba + 1 * BS + 512);
        ST2(S13, 3, 0, 1); SB();
        MP(3, 4, X13_3_0_1, W13_4_0, X13_3_1_1, W13_4_1);
        LD(W15_6_0, ba + 0 * BS + 1536);
        LD(W15_6_1, ba + 1 * BS + 1536);
        S14[1] = pack_bf16(acc[3][2][2], acc[3][3][2]); asm volatile("" : "+v"(S14[1])); SB();
        S14[0] = pack_bf16(acc[3][0][2], acc[3][1][2]); asm volatile("" : "+v"(S14[0])); SB();
        MP(3, 5, X13_3_1_1, W13_5_1, X13_3_0_1, W13_5_0);
        LD(W15_7_1, ba + 1 * BS + 1792);
        LD(W15_7_0, ba + 0 * BS + 1792);
        ST2(S14, 3, 0, 2); SB();
        MP(3, 6, X13_3_0_1, W13_6_0, X13_3_1_1, W13_6_1);
        LD(W14_7_1, ba + 1 * BS + 768);
        S15[1] = pack_bf16(acc[3][2][3], acc[3][3][3]); asm volatile("" : "+v"(S15[1])); SB();
        S15[0] = pack_bf16(acc[3][0][3], acc[3][1][3]); asm volatile("" : "+v"(S15[0])); SB();
        MP(3, 7, X13_3_1_1, W13_7_1, X13_3_0_1, W13_7_0);
        LD(W14_7_0, ba + 0 * BS + 768);
        ST2(S15, 3, 0, 3); SB();
        MP(0, 4, X14_0_0_1, W14_4_0, X14_0_1_1, W14_4_1);
        LD(X14_1_0_1, xe0 + 2048);
        MP(0, 4, X15_0_0_1, W15_4_0, X15_0_1_1, W15_4_1);
        LD(X14_1_1_1, xe1 + 2048);
        MP(0, 5, X15_0_1_1, W15_5_1, X15_0_0_1, W15_5_0);
        LD(X15_1_0_1, xb0 + X15 + 2048);
        MP(0, 5, X14_0_1_1, W14_5_1, X14_0_0_1, W14_5_0);
        LD(X15_1_1_1, xb1 + X15 + 2048);
        MP(0, 6, X14_0_0_1, W14_6_0, X14_0_1_1, W14_6_1);
        MP(0, 6, X15_0_0_1, W15_6_0, X15_0_1_1, W15_6_1);
        MP(0, 7, X15_0_1_1, W15_7_1, X15_0_0_1, W15_7_0);
        MP(0, 7, X14_0_1_1, W14_7_1, X14_0_0_1, W14_7_0);
        MP(1, 4, X14_1_0_1, W14_4_0, X14_1_1_1, W14_4_1);
        LD(X14_2_0_1, xe0 + 4096);
        S16[0] = pack_bf16(acc[0][4][0], acc[0][5][0]); asm volatile("" : "+v"(S16[0])); SB();
        MP(1, 4, X15_1_0_1, W15_4_0, X15_1_1_1, W15_4_1);
        LD(X14_2_1_1, xe1 + 4096);
        S16[1] = pack_bf16(acc[0][6][0], acc[0][7][0]); asm volatile("" : "+v"(S16[1])); SB();
        ST2(S16, 0, 1, 0); SB();
        MP(1, 5, X15_1_1_1, W15_5_1, X15_1_0_1, W15_5_0);
        LD(X15_2_0_1, xb0 + X15 + 4096);
        S17[1] = pack_bf16(acc[0][6][1], acc[0][7][1]); asm volatile("" : "+v"(S17[1])); SB();
        S17[0] = pack_bf16(acc[0][4][1], acc[0][5][1]); asm volatile("" : "+v"(S17[0])); SB();
        MP(1, 5, X14_1_1_1, W14_5_1, X14_1_0_1, W14_5_0);
        LD(X15_2_1_1, xb1 + X15 + 4096);
        ST2(S17, 0, 1, 1); SB();
        MP(1, 6, X14_1_0_1, W14_6_0, X14_1_1_1, W14_6_1);
        S18[1] = pack_bf16(acc[0][6][2], acc[0][7][2]); asm volatile("" : "+v"(S18[1])); SB();
        S18[0] = pack_bf16(acc[0][4][2], acc[0][5][2]); asm volatile("" : "+v"(S18[0])); SB();
        MP(1, 6, X15_1_0_1, W15_6_0, X15_1_1_1, W15_6_1);
        ST2(S18, 0, 1, 2); SB();
        MP(1, 7, X15_1_1_1, W15_7_1, X15_1_0_1, W15_7_0);
        S19[1] = pack_bf16(acc[0][6][3], acc[0][7][3]); asm volatile("" : "+v"(S19[1])); SB();
        S19[0] = pack_bf16(acc[0][4][3], acc[0][5][3]); asm volatile("" : "+v"(S19[0])); SB();
        PAD(18, 97);
        MP(1, 7, X14_1_1_1, W14_7_1, X14_1_0_1, W14_7_0);
        ST2(S19, 0, 1, 3); SB();
        MP(2, 4, X14_2_0_1, W14_4_0, X14_2_1_1, W14_4_1);
        LD(X14_3_0_1, xe0 + 6144);
        S20[0] = pack_bf16(acc[1][4][0], acc[1][5][0]); asm volatile("" : "+v"(S20[0])); SB();
        MP(2, 4, X15_2_0_1, W15_4_0, X15_2_1_1, W15_4_1);
        LD(X14_3_1_1, xe1 + 6144);
        S20[1] = pack_bf16(acc[1][6][0], acc[1][7][0]); asm volatile("" : "+v"(S20[1])); SB();
        ST2(S20, 1, 1, 0); SB();
        MP(2, 5, X15_2_1_1, W15_5_1, X15_2_0_1, W15_5_0);
        LD(X15_3_0_1, xb0 + X15 + 6144);
        S21[1] = pack_bf16(acc[1][6][1], acc[1][7][1]); asm volatile("" : "+v"(S21[1])); SB();
        S21[0] = pack_bf16(acc[1][4][1], acc[1][5][1]); asm volatile("" : "+v"(S21[0])); SB();
        MP(2, 5, X14_2_1_1, W14_5_1, X14_2_0_1, W14_5_0);
        LD(X15_3_1_1, xb1 + X15 + 6144);
        ST2(S21, 1, 1, 1); SB();
        MP(2, 6, X14_2_0_1, W14_6_0, X14_2_1_1, W14_6_1);
        S22[1] = pack_bf16(acc[1][6][2], acc[1][7][2]); asm volatile("" : "+v"(S22[1])); SB();
        S22[0] = pack_bf16(acc[1][4][2], acc[1][5][2]); asm volatile("" : "+v"(S22[0])); SB();
        MP(2, 6, X15_2_0_1, W15_6_0, X15_2_1_1, W15_6_1);
        ST2(S22, 1, 1, 2); SB();
        MP(2, 7, X15_2_1_1, W15_7_1, X15_2_0_1, W15_7_0);
        S23[1] = pack_bf16(acc[1][6][3], acc[1][7][3]); asm volatile("" : "+v"(S23[1])); SB();
        S23[0] = pack_bf16(acc[1][4][3], acc[1][5][3]); asm volatile("" : "+v"(S23[0])); SB();
        MP(2, 7, X14_2_1_1, W14_7_1, X14_2_0_1, W14_7_0);
        ST2(S23, 1, 1, 3); SB();
        MP(3, 4, X14_3_0_1, W14_4_0, X14_3_1_1, W14_4_1);
        S24[0] = pack_bf16(acc[2][4][0], acc[2][5][0]); asm volatile("" : "+v"(S24[0])); SB();
        MP(3, 4, X15_3_0_1, W15_4_0, X15_3_1_1, W15_4_1);
        S24[1] = pack_bf16(acc[2][6][0], acc[2][7][0]); asm volatile("" : "+v"(S24[1])); SB();
        ST2(S24, 2, 1, 0); SB();
        MP(3, 5, X15_3_1_1, W15_5_1, X15_3_0_1, W15_5_0);
        S25[1] = pack_bf16(acc[2][6][1], acc[2][7][1]); asm volatile("" : "+v"(S25[1])); SB();
        S25[0] = pack_bf16(acc[2][4][1], acc[2][5][1]); asm volatile("" : "+v"(S25[0])); SB();
        MP(3, 5, X14_3_1_1, W14_5_1, X14_3_0_1, W14_5_0);
        ST2(S25, 2, 1, 1); SB();
        MP(3, 6, X14_3_0_1, W14_6_0, X14_3_1_1, W14_6_1);
        S26[1] = pack_bf16(acc[2][6][2], acc[2][7][2]); asm volatile("" : "+v"(S26[1])); SB();
        S26[0] = pack_bf16(acc[2][4][2], acc[2][5][2]); asm volatile("" : "+v"(S26[0])); SB();
        MP(3, 6, X15_3_0_1, W15_6_0, X15_3_1_1, W15_6_1);
        ST2(S26, 2, 1, 2); SB();
        MP(3, 7, X15_3_1_1, W15_7_1, X15_3_0_1, W15_7_0);
        S29[0] = pack_bf16(acc[3][4][1], acc[3][5][1]); asm volatile("" : "+v"(S29[0])); SB();
        S28[0] = pack_bf16(acc[3][4][0], acc[3][5][0]); asm volatile("" : "+v"(S28[0])); SB();
        S27[1] = pack_bf16(acc[2][6][3], acc[2][7][3]); asm volatile("" : "+v"(S27[1])); SB();
        S27[0] = pack_bf16(acc[2][4][3], acc[2][5][3]); asm volatile("" : "+v"(S27[0])); SB();
        MP(3, 7, X14_3_1_1, W14_7_1, X14_3_0_1, W14_7_0);
        S31[0] = pack_bf16(acc[3][4][3], acc[3][5][3]); asm volatile("" : "+v"(S31[0])); SB();
        S30[0] = pack_bf16(acc[3][4][2], acc[3][5][2]); asm volatile("" : "+v"(S30[0])); SB();
        ST2(S27, 2, 1, 3); SB();
        asm volatile("s_nop 5" ::: "memory"); SB();
        S28[1] = pack_bf16(acc[3][6][0], acc[3][7][0]); asm volatile("" : "+v"(S28[1])); SB();
        ST2(S28, 3, 1, 0); SB();
        S29[1] = pack_bf16(acc[3][6][1], acc[3][7][1]); asm volatile("" : "+v"(S29[1])); SB();
        ST2(S29, 3, 1, 1); SB();
        S30[1] = pack_bf16(acc[3][6][2], acc[3][7][2]); asm volatile("" : "+v"(S30[1])); SB();
        ST2(S30, 3, 1, 2); SB();
        S31[1] = pack_bf16(acc[3][6][3], acc[3][7][3]); asm volatile("" : "+v"(S31[1])); SB();
        ST2(S31, 3, 1, 3); SB();
    }
}

extern "C" void kernel_launch(void* const* d_in, const int* in_sizes, int n_in,
                              void* d_out, int out_size, void* d_ws, size_t ws_size,
                              hipStream_t stream) {
    const u16* x = (const u16*)d_in[0];
    const int* qw = (const int*)d_in[1];
    const u16* lut = (const u16*)d_in[2];
    u16* out = (u16*)d_out;
    (void)hipFuncSetAttribute((const void*)fused_kernel, hipFuncAttributeMaxDynamicSharedMemorySize, LDS_BYTES);
    fused_kernel<<<(M_ / BM) * (N_ / BN) + NPF, 512, LDS_BYTES, stream>>>(x, qw, lut, out, (char*)d_ws);
}
```

```cpp
#include <hip/hip_runtime.h>
#include <hip/hip_bf16.h>

typedef unsigned short u16;
typedef unsigned int u32;
using bf16x8 = __attribute__((ext_vector_type(8))) short;
using f32x4  = __attribute__((ext_vector_type(4))) float;
using i32x4  = __attribute__((ext_vector_type(4))) int;
using u32x2  = __attribute__((ext_vector_type(2))) unsigned int;
using u32x4  = __attribute__((ext_vector_type(4))) unsigned int;

constexpr int M_ = 2048, K_ = 1024, N_ = 3584;
constexpr int BM = 256, BN = 128;
constexpr int NPF = 32;
#define XSLEEP 4
#define SP0 3
#define NQ1 4
#define CPF 144
#define CPFB 32
#define PACE 0
#define VORD 1
constexpr int XS = BM * 64 * 2;
constexpr int BS = BN * 32 * 2;
constexpr int OFF_B = 3 * XS;
constexpr int WSL = 2 * BS;
constexpr int OFF_L = OFF_B + 3 * WSL;
constexpr int OFF_D = OFF_L + 16 * BN * 2;
constexpr int WS_F = 28 * 256, WS_W = 65536;
constexpr unsigned TW = 28 * 16384;
constexpr int SPIN_MAX = 1 << 16;
constexpr int LDS_BYTES = OFF_D + 1024;

typedef __bf16 bf16x2_t __attribute__((ext_vector_type(2)));
typedef float f32x2_t __attribute__((ext_vector_type(2)));
__device__ __forceinline__ u32 pack_bf16(float lo, float hi) {
    f32x2_t f = {lo, hi};
    bf16x2_t b = __builtin_convertvector(f, bf16x2_t);
    return __builtin_bit_cast(u32, b);
}

#define TSEQ(i) (0xFEDCBA9876532140ul ^ (unsigned long)((u32)(i) * 0x11111111u))
#define BAR() __builtin_amdgcn_s_barrier()
#define SB() __builtin_amdgcn_sched_barrier(0)
#define WAITQ(n, Qa) asm volatile("s_waitcnt vmcnt(" #n ") lgkmcnt(0)" : "+v"(Qa[0]), "+v"(Qa[1]), "+v"(Qa[2]), "+v"(Qa[3]), \
                                  "+v"(Qa[4]), "+v"(Qa[5]), "+v"(Qa[6]), "+v"(Qa[7]) :: "memory")
#define PBAR() BAR()
#define PAD(id, nx) asm volatile(".Lwp%c0:\n\ts_cbranch_execz .Lwp%c1" :: "i"(id), "i"(nx))

__global__ __launch_bounds__(512, 2) void fused_kernel(const u16* __restrict__ x_, const int* __restrict__ qw_,
                                                       const u16* __restrict__ lut_, u16* __restrict__ out_, char* ws_) {
    extern __shared__ __attribute__((aligned(16))) char smem[];
    typedef unsigned long u64x4 __attribute__((ext_vector_type(4)));
    u64x4 ka;
    unsigned long kp[5], kw;
    asm volatile("s_load_dwordx8 %0, %2, 0x0\n\ts_load_dwordx2 %1, %2, 0x20" : "=&s"(ka), "=&s"(kw) : "s"(__builtin_amdgcn_kernarg_segment_ptr()));
    {
        unsigned long pc0, ex;
        u32 co;
        asm volatile("s_getpc_b64 %0\n\t"
                     "s_cmp_lt_u32 %4, %5\n\t"
                     "s_cselect_b32 m0, %6, 0\n\t"
                     "v_cmp_gt_u32 vcc, m0, %3\n\t"
                     "s_and_b32 m0, %4, 24\n\t"
                     "s_lshl_b32 m0, m0, 4\n\t"
                     "v_lshl_add_u32 %2, %3, 9, m0\n\t"
                     "s_mov_b32 m0, %7\n\t"
                     "s_and_saveexec_b64 %1, vcc\n\t"
                     "global_load_lds_dword %2, %0\n\t"
                     "s_mov_b64 exec, %1"
                     : "=&s"(pc0), "=&s"(ex), "=&v"(co) : "v"(threadIdx.x), "s"(blockIdx.x), "i"(CPFB), "i"(CPF / 4), "i"(OFF_D) : "vcc", "scc", "memory");
    }
#define KWAIT() do { SB(); asm volatile("s_waitcnt lgkmcnt(0)\n\ts_mov_b64 %0, %5\n\ts_mov_b64 %1, %6\n\ts_mov_b64 %2, %7\n\ts_mov_b64 %3, %8\n\ts_mov_b64 %4, %9" \
                     : "=&s"(kp[0]), "=&s"(kp[1]), "=&s"(kp[2]), "=&s"(kp[3]), "=&s"(kp[4]) : "s"(ka[0]), "s"(ka[1]), "s"(ka[2]), "s"(ka[3]), "s"(kw) : "memory"); SB(); } while (0)
#define KPTR(i) (kp[i])
    const int tid = threadIdx.x;
    const int w = __builtin_amdgcn_readfirstlane(tid >> 6);
    const int lane = tid & 63;
    const int bid = (int)blockIdx.x - NPF;
    const int xcd = blockIdx.x & 7, idx = bid >> 3;
    if (bid < 0) {
        const int pw = (blockIdx.x >> 3) * 8 + w;
        KWAIT();
        const char* qb = (const char*)KPTR(1) + (xcd >> 1) * 896 * 4 + (long)((xcd & 1) * 2) * (64 * N_ * 4);
        const char* xb = (const char*)KPTR(0) + (long)(xcd & 1) * 1024 * 2048;
        const u32 dst = (u32)(size_t)(smem) + w * 256;
        {
            const int m = (xcd & 1) * 4 + (blockIdx.x >> 3);
            const u32 off = (u32)((m * 256 + (w & 3) * 64 + lane) * 2048 + (int)((TSEQ(m >> 1) >> (4 * (w >> 2))) & 15) * 128);
            asm volatile("s_mov_b32 m0, %2\n\ts_nop 0\n\tglobal_load_lds_dword %0, %1" :: "v"(off), "s"(KPTR(0)), "s"(dst) : "memory");
        }
#pragma unroll 1
        for (int G = pw; G < 168; G += 32) {
            int kind = 0, arg = 0, e = 0;
            if (G >= 0) { kind = 0; arg = 2; e = G - 0 + (xcd >> 1) * 4; }
            if (G >= 4) { kind = 0; arg = 3; e = G - 4 + (xcd >> 1) * 4; }
            if (G >= 8) { kind = 0; arg = 4; e = G - 8 + (xcd >> 1) * 4; }
            if (G >= 12) { kind = 1; arg = 8; e = G - 12; }
            if (G >= 32) { kind = 0; arg = 5; e = G - 32 + (xcd >> 1) * 4; }
            if (G >= 36) { kind = 1; arg = 8; e = G - 16; }
            if (G >= 64) { kind = 1; arg = 8; e = G - 16; }
            if (G >= 72) { kind = 0; arg = 6; e = G - 72 + (xcd >> 1) * 4; }
            if (G >= 76) { kind = 0; arg = 7; e = G - 76 + (xcd >> 1) * 4; }
            if (G >= 80) { kind = 1; arg = 12; e = G - 80; }
            if (G >= 96) { kind = 0; arg = 8; e = G - 96 + (xcd >> 1) * 4; }
            if (G >= 100) { kind = 0; arg = 9; e = G - 100 + (xcd >> 1) * 4; }
            if (G >= 104) { kind = 1; arg = 12; e = G - 88; }
            if (G >= 128) { kind = 0; arg = 10; e = G - 128 + (xcd >> 1) * 4; }
            if (G >= 132) { kind = 1; arg = 12; e = G - 92; }
            if (G >= 148) { kind = 0; arg = 11; e = G - 148 + (xcd >> 1) * 4; }
            if (G >= 152) { kind = 0; arg = 12; e = G - 152 + (xcd >> 1) * 4; }
            if (G >= 156) { kind = 0; arg = 13; e = G - 156 + (xcd >> 1) * 4; }
            if (G >= 160) { kind = 0; arg = 14; e = G - 160 + (xcd >> 1) * 4; }
            if (G >= 164) { kind = 0; arg = 15; e = G - 164 + (xcd >> 1) * 4; }
            asm volatile("s_waitcnt vmcnt(%0)" :: "i"(PACE) : "memory");
            if (kind) {
                const int L = e * 64 + lane;
                const int r = L / 28, c = L - r * 28;
                const u32 off = (u32)((arg * 64 + r) * (N_ * 4) + c * 128);
                asm volatile("s_mov_b32 m0, %2\n\ts_nop 0\n\tglobal_load_lds_dword %0, %1" :: "v"(off), "s"(qb), "s"(dst) : "memory");
            } else {
                const int t = (int)((TSEQ((xcd & 1) * 2 + (e >> 3)) >> (4 * arg)) & 15);
                const u32 off = (u32)((e * 64 + lane) * 2048 + t * 128);
                asm volatile("s_mov_b32 m0, %2\n\ts_nop 0\n\tglobal_load_lds_dword %0, %1" :: "v"(off), "s"(xb), "s"(dst) : "memory");
            }
        }
        asm volatile("s_waitcnt vmcnt(0)" ::: "memory");
        return;
    }
    const int mt = (xcd & 1) * 4 + (idx & 3);
    const int nt = (xcd >> 1) * 7 + (idx >> 2);
    const int row0 = mt * BM, col0 = nt * BN;

    if (__builtin_expect(w >= 4, 0)) {
        const int p = w - 4;
        const int l = lane & 31, h = lane >> 5;
        const int g = lane >> 4;
        u32 xoff[4];
#pragma unroll
        for (int i = 0; i < 4; ++i)
            xoff[i] = (u32)(((row0 + p * 64 + i * 8 + (lane >> 3)) * K_ + (((lane & 7) ^ ((i & 1) * 4 + g)) * 8)) * 2 - i * 1024);
        const u32 ldsx = (u32)(size_t)(smem) + p * 8192;
        auto issue_x1 = [&](int tile, int hh, int slotoff, int i) {
            const u16* base = (const u16*)KPTR(0) + (long)hh * 32 * K_ + tile * 64;
            const u32 dst = ldsx + slotoff + hh * 4096;
            if (i == 0) asm volatile("s_mov_b32 m0, %2\n\ts_nop 0\n\tglobal_load_lds_dwordx4 %0, %1" :: "v"(xoff[0]), "s"(base), "s"(dst) : "memory");
            else asm volatile("global_load_lds_dwordx4 %0, %1 offset:%2" :: "v"(xoff[i]), "s"(base), "i"(i * 1024) : "memory");
        };
        u32 qoff[8];
#pragma unroll
        for (int r = 0; r < 8; ++r) qoff[r] = (u32)(((p * 16 + h * 8 + r) * N_ + col0 + l * 4) * 4);
        auto issue_q1 = [&](int t, i32x4 (&Q)[8], int r) {
            const int* base = (const int*)KPTR(1) + (long)t * 64 * N_;
            asm volatile("global_load_dwordx4 %0, %1, %2 " : "=v"(Q[r]) : "v"(qoff[r]), "s"(base) : "memory");
        };
        u32 lutl = (u32)(size_t)(smem) + OFF_L + l * 4;
        u32 bdst = (u32)(size_t)(smem) + OFF_B + (p >> 1) * BS + ((2 * p + h) & 3) * 2048 + ((l >> 4) * 64 + (l & 15)) * 16;
        u32 V[4][8];
#define LUT1(Q, j, r) asm volatile("v_lshl_add_u32 %0, %1, 8, %2\n\tds_read_u16 %0, %0 offset:%3" : "=&v"(V[j][r]) \
                                   : "v"(Q[r][j]), "v"(lutl), "i"(((j) >> 1) * 128 + ((j) & 1) * 2) : "memory")
#define LUT4(Q, e) do { LUT1(Q, (e) >> 1, ((e) & 1) * 4); LUT1(Q, (e) >> 1, ((e) & 1) * 4 + 1); \
                        LUT1(Q, (e) >> 1, ((e) & 1) * 4 + 2); LUT1(Q, (e) >> 1, ((e) & 1) * 4 + 3); SB(); } while (0)
#define WRB(j, n) do { u32x4 b_; \
                       asm volatile("s_waitcnt lgkmcnt(" #n ")\n\t" \
                                    "v_lshl_or_b32 %0, %5, 16, %4\n\tv_lshl_or_b32 %1, %7, 16, %6\n\t" \
                                    "v_lshl_or_b32 %2, %9, 16, %8\n\tv_lshl_or_b32 %3, %11, 16, %10" \
                                    : "=&v"(b_[0]), "=&v"(b_[1]), "=&v"(b_[2]), "=&v"(b_[3]) \
                                    : "v"(V[j][0]), "v"(V[j][1]), "v"(V[j][2]), "v"(V[j][3]), "v"(V[j][4]), "v"(V[j][5]), "v"(V[j][6]), "v"(V[j][7]) : "memory"); \
                       asm volatile("ds_write_b128 %0, %1 offset:%2" :: "v"(bdst), "v"(b_), "i"((j) * 256) : "memory"); SB(); } while (0)
        const int i2 = mt >> 1, h2 = mt & 1;
        const u32 tseq = (u32)TSEQ(i2);
        const u32 ldsw = (u32)(size_t)(smem) + OFF_B + p * 4096;
        u32 wv = (u32)(nt * 16384 + p * 4096 + lane * 16);
        u32 wo = (u32)(nt * 16384 + p * 4096 + h * 2048 + (l >> 4) * 1024 + (l & 15) * 16);
        u32 po = (u32)(WS_F + (nt * 4 + p) * 256 + lane * 4);
        u32 fo0 = po + (u32)(i2 * 16 + h2 * 8);
        u32 no = (u32)(nt * 256 + (lane & 1) * 4);
        u32 nvl = no;
        KWAIT();
        BAR();
        const char* st1 = (const char*)KPTR(4) + WS_W + (long)(i2 + 4) * TW + h2 * 512;
        const char* qa1 = (const char*)KPTR(1);
        const int tb = 8 + (p >> 1) * 4 + i2, qd = h2 * 2 + (p & 1);
        const char* qbg = (const char*)KPTR(1) + (long)(tb * 64 + (qd - p) * 16) * (N_ * 4);
        const char* stb = (const char*)KPTR(4) + WS_W + (long)tb * TW + (qd - p) * 4096;
        u32 fob = po + (u32)((qd - p) * 256 + tb * 16);
        asm volatile("" : "+v"(wo), "+v"(fo0), "+v"(fob), "+v"(wv), "+v"(po), "+v"(bdst), "+v"(lutl), "+v"(qoff[0]));
        asm volatile("" : "+v"(xoff[0]), "+v"(xoff[1]), "+v"(xoff[2]), "+v"(xoff[3]));
        asm volatile("" : "+s"(st1), "+s"(qa1), "+s"(qbg), "+s"(stb));
        {
            unsigned long ex_;
            asm volatile("s_mov_b64 %0, exec\n\ts_mov_b64 exec, 0\n\t"
                         "s_cmp_eq_u32 %1, 0\n\ts_cbranch_scc1 .Lwp30\n\t"
                         "s_cmp_eq_u32 %1, 1\n\ts_cbranch_scc1 .Lwp31\n\t"
                         "s_cmp_eq_u32 %1, 2\n\ts_cbranch_scc1 .Lwp32\n\t"
                         "s_branch .Lwp33\n\t"
                         ".Lwp98:\n\ts_mov_b64 exec, %0" : "=&s"(ex_) : "s"(p) : "scc", "memory");
        }
        asm volatile(
            "s_mov_b32 s73, 65536\n\t"
            "s_mov_b64 s[84:85], %[ws]\n\t"
            "s_add_u32 s84, s84, 65536\n\t"
            "s_addc_u32 s85, s85, 0\n\t"
            "s_bfe_u32 s56, %[tseq], 262144\n\t"
            "s_bfe_u32 s57, %[tseq], 262148\n\t"
            "s_bfe_u32 s58, %[tseq], 262152\n\t"
            "s_bfe_u32 s59, %[tseq], 262156\n\t"
            "s_bfe_u32 s60, %[tseq], 262160\n\t"
            "s_bfe_u32 s61, %[tseq], 262164\n\t"
            "s_bfe_u32 s62, %[tseq], 262168\n\t"
            "s_bfe_u32 s63, %[tseq], 262172\n\t"
            "s_barrier\n\t"
            ".Lwp30:\n\t"
            "s_cbranch_execz .Lwp98\n\t"
            "s_add_u32 m0, %[ldsx], 0\n\t"
            "s_lshl_b32 s76, s56, 7\n\t"
            "s_add_u32 s68, %[xpl], s76\n\t"
            "s_addc_u32 s69, %[xph], 0\n\t"
            "global_load_lds_dwordx4 %[x0], s[68:69] offset:0\n\t"
            "global_load_lds_dwordx4 %[x1], s[68:69] offset:1024\n\t"
            "global_load_lds_dwordx4 %[x2], s[68:69] offset:2048\n\t"
            "global_load_lds_dwordx4 %[x3], s[68:69] offset:3072\n\t"
            "s_add_u32 m0, %[ldsx], 4096\n\t"
            "s_lshl_b32 s76, s56, 7\n\t"
            "s_add_u32 s76, s76, 65536\n\t"
            "s_add_u32 s68, %[xpl], s76\n\t"
            "s_addc_u32 s69, %[xph], 0\n\t"
            "global_load_lds_dwordx4 %[x0], s[68:69] offset:0\n\t"
            "global_load_lds_dwordx4 %[x1], s[68:69] offset:1024\n\t"
            "global_load_lds_dwordx4 %[x2], s[68:69] offset:2048\n\t"
            "global_load_lds_dwordx4 %[x3], s[68:69] offset:3072\n\t"
            "global_load_dword %[nvl], %[nvl], %[ws] sc1\n\t"
            "s_mul_i32 s76, s57, 917504\n\t"
            "s_mov_b64 s[88:89], %[qa1]\n\t"
            "s_add_u32 s88, s88, s76\n\t"
            "s_addc_u32 s89, s89, 0\n\t"
            "global_load_dwordx4 v[128:131], %[qo], s[88:89] \n\t"
            "s_add_u32 s88, s88, 14336\n\t"
            "s_addc_u32 s89, s89, 0\n\t"
            "global_load_dwordx4 v[132:135], %[qo], s[88:89] \n\t"
            "s_add_u32 s88, s88, 14336\n\t"
            "s_addc_u32 s89, s89, 0\n\t"
            "global_load_dwordx4 v[136:139], %[qo], s[88:89] \n\t"
            "s_add_u32 s88, s88, 14336\n\t"
            "s_addc_u32 s89, s89, 0\n\t"
            "global_load_dwordx4 v[140:143], %[qo], s[88:89] \n\t"
            "s_add_u32 s88, s88, 14336\n\t"
            "s_addc_u32 s89, s89, 0\n\t"
            "global_load_dwordx4 v[144:147], %[qo], s[88:89] \n\t"
            "s_add_u32 s88, s88, 14336\n\t"
            "s_addc_u32 s89, s89, 0\n\t"
            "global_load_dwordx4 v[148:151], %[qo], s[88:89] \n\t"
            "s_add_u32 s88, s88, 14336\n\t"
            "s_addc_u32 s89, s89, 0\n\t"
            "global_load_dwordx4 v[152:155], %[qo], s[88:89] \n\t"
            "s_add_u32 s88, s88, 14336\n\t"
            "s_addc_u32 s89, s89, 0\n\t"
            "global_load_dwordx4 v[156:159], %[qo], s[88:89] \n\t"
            "s_waitcnt vmcnt(9)\n\t"
            "s_barrier\n\t"
            ".Lwp31:\n\t"
            "s_cbranch_execz .Lwp98\n\t"
            "s_lshl_b32 s76, %[h2], 9\n\t"
            "v_add_u32 v193, s76, %[bd]\n\t"
            "ds_read_b128 v[196:199], v193\n\t"
            "ds_read_b128 v[200:203], v193 offset:256\n\t"
            "s_sub_u32 s70, %[st1l], 1835008\n\t"
            "s_subb_u32 s71, %[st1h], 0\n\t"
            "s_waitcnt lgkmcnt(0)\n\t"
            "global_store_dwordx4 %[wo], v[196:199], s[70:71] sc1\n\t"
            "global_store_dwordx4 %[wo], v[200:203], s[70:71] offset:256 sc1\n\t"
            "s_waitcnt vmcnt(10)\n\t"
            "s_mov_b32 s76, 1664525\n\t"
            "v_mul_lo_u32 %[nvl], %[nvl], s76\n\t"
            "v_add_u32 %[nvl], 1013904223, %[nvl]\n\t"
            "s_setprio 1\n\t"
            "s_waitcnt vmcnt(9)\n\t"
            "v_lshl_add_u32 v160, v128, 8, %[lutl]\n\t"
            "ds_read_u16 v160, v160 offset:0\n\t"
            "v_lshl_add_u32 v168, v129, 8, %[lutl]\n\t"
            "ds_read_u16 v168, v168 offset:2\n\t"
            "v_lshl_add_u32 v176, v130, 8, %[lutl]\n\t"
            "ds_read_u16 v176, v176 offset:128\n\t"
            "v_lshl_add_u32 v184, v131, 8, %[lutl]\n\t"
            "ds_read_u16 v184, v184 offset:130\n\t"
            "s_add_u32 m0, %[ldsx], 32768\n\t"
            "s_lshl_b32 s76, s57, 7\n\t"
            "s_add_u32 s68, %[xpl], s76\n\t"
            "s_addc_u32 s69, %[xph], 0\n\t"
            "global_load_lds_dwordx4 %[x0], s[68:69] offset:0\n\t"
            "s_waitcnt vmcnt(9)\n\t"
            "v_lshl_add_u32 v161, v132, 8, %[lutl]\n\t"
            "ds_read_u16 v161, v161 offset:0\n\t"
            "v_lshl_add_u32 v169, v133, 8, %[lutl]\n\t"
            "ds_read_u16 v169, v169 offset:2\n\t"
            "v_lshl_add_u32 v177, v134, 8, %[lutl]\n\t"
            "ds_read_u16 v177, v177 offset:128\n\t"
            "v_lshl_add_u32 v185, v135, 8, %[lutl]\n\t"
            "ds_read_u16 v185, v185 offset:130\n\t"
            "global_load_lds_dwordx4 %[x1], s[68:69] offset:1024\n\t"
            "s_waitcnt vmcnt(9)\n\t"
            "v_lshl_add_u32 v162, v136, 8, %[lutl]\n\t"
            "ds_read_u16 v162, v162 offset:0\n\t"
            "v_lshl_add_u32 v170, v137, 8, %[lutl]\n\t"
            "ds_read_u16 v170, v170 offset:2\n\t"
            "v_lshl_add_u32 v178, v138, 8, %[lutl]\n\t"
            "ds_read_u16 v178, v178 offset:128\n\t"
            "v_lshl_add_u32 v186, v139, 8, %[lutl]\n\t"
            "ds_read_u16 v186, v186 offset:130\n\t"
            "global_load_lds_dwordx4 %[x2], s[68:69] offset:2048\n\t"
            "s_waitcnt vmcnt(9)\n\t"
            "v_lshl_add_u32 v163, v140, 8, %[lutl]\n\t"
            "ds_read_u16 v163, v163 offset:0\n\t"
            "v_lshl_add_u32 v171, v141, 8, %[lutl]\n\t"
            "ds_read_u16 v171, v171 offset:2\n\t"
            "v_lshl_add_u32 v179, v142, 8, %[lutl]\n\t"
            "ds_read_u16 v179, v179 offset:128\n\t"
            "v_lshl_add_u32 v187, v143, 8, %[lutl]\n\t"
            "ds_read_u16 v187, v187 offset:130\n\t"
            "global_load_lds_dwordx4 %[x3], s[68:69] offset:3072\n\t"
            "s_waitcnt vmcnt(9)\n\t"
            "v_lshl_add_u32 v164, v144, 8, %[lutl]\n\t"
            "ds_read_u16 v164, v164 offset:0\n\t"
            "v_lshl_add_u32 v172, v145, 8, %[lutl]\n\t"
            "ds_read_u16 v172, v172 offset:2\n\t"
            "v_lshl_add_u32 v180, v146, 8, %[lutl]\n\t"
            "ds_read_u16 v180, v180 offset:128\n\t"
            "v_lshl_add_u32 v188, v147, 8, %[lutl]\n\t"
            "ds_read_u16 v188, v188 offset:130\n\t"
            "s_add_u32 m0, %[ldsx], 36864\n\t"
            "s_lshl_b32 s76, s57, 7\n\t"
            "s_add_u32 s76, s76, 65536\n\t"
            "s_add_u32 s68, %[xpl], s76\n\t"
            "s_addc_u32 s69, %[xph], 0\n\t"
            "global_load_lds_dwordx4 %[x0], s[68:69] offset:0\n\t"
            "s_waitcnt vmcnt(9)\n\t"
            "v_lshl_add_u32 v165, v148, 8, %[lutl]\n\t"
            "ds_read_u16 v165, v165 offset:0\n\t"
            "v_lshl_add_u32 v173, v149, 8, %[lutl]\n\t"
            "ds_read_u16 v173, v173 offset:2\n\t"
            "v_lshl_add_u32 v181, v150, 8, %[lutl]\n\t"
            "ds_read_u16 v181, v181 offset:128\n\t"
            "v_lshl_add_u32 v189, v151, 8, %[lutl]\n\t"
            "ds_read_u16 v189, v189 offset:130\n\t"
            "global_load_lds_dwordx4 %[x1], s[68:69] offset:1024\n\t"
            "s_waitcnt vmcnt(9)\n\t"
            "v_lshl_add_u32 v166, v152, 8, %[lutl]\n\t"
            "ds_read_u16 v166, v166 offset:0\n\t"
            "v_lshl_add_u32 v174, v153, 8, %[lutl]\n\t"
            "ds_read_u16 v174, v174 offset:2\n\t"
            "v_lshl_add_u32 v182, v154, 8, %[lutl]\n\t"
            "ds_read_u16 v182, v182 offset:128\n\t"
            "v_lshl_add_u32 v190, v155, 8, %[lutl]\n\t"
            "ds_read_u16 v190, v190 offset:130\n\t"
            "global_load_lds_dwordx4 %[x2], s[68:69] offset:2048\n\t"
            "s_waitcnt vmcnt(9)\n\t"
            "v_lshl_add_u32 v167, v156, 8, %[lutl]\n\t"
            "ds_read_u16 v167, v167 offset:0\n\t"
            "v_lshl_add_u32 v175, v157, 8, %[lutl]\n\t"
            "ds_read_u16 v175, v175 offset:2\n\t"
            "v_lshl_add_u32 v183, v158, 8, %[lutl]\n\t"
            "ds_read_u16 v183, v183 offset:128\n\t"
            "v_lshl_add_u32 v191, v159, 8, %[lutl]\n\t"
            "ds_read_u16 v191, v191 offset:130\n\t"
            "global_load_lds_dwordx4 %[x3], s[68:69] offset:3072\n\t"
            "s_setprio 0\n\t"
            "s_waitcnt vmcnt(8)\n\t"
            "s_mov_b64 exec, 3\n\t"
            "global_store_dword %[fo0], %[nvl], %[ws] offset:0 sc1\n\t"
            "s_mov_b64 exec, -1\n\t"
            "s_waitcnt lgkmcnt(3)\n\t"
            "v_lshl_or_b32 v128, v161, 16, v160\n\t"
            "v_lshl_or_b32 v129, v163, 16, v162\n\t"
            "v_lshl_or_b32 v130, v165, 16, v164\n\t"
            "v_lshl_or_b32 v131, v167, 16, v166\n\t"
            "ds_write_b128 %[bd], v[128:131] offset:16384\n\t"
            "s_waitcnt lgkmcnt(3)\n\t"
            "v_lshl_or_b32 v132, v169, 16, v168\n\t"
            "v_lshl_or_b32 v133, v171, 16, v170\n\t"
            "v_lshl_or_b32 v134, v173, 16, v172\n\t"
            "v_lshl_or_b32 v135, v175, 16, v174\n\t"
            "ds_write_b128 %[bd], v[132:135] offset:16640\n\t"
            "s_waitcnt lgkmcnt(3)\n\t"
            "v_lshl_or_b32 v136, v177, 16, v176\n\t"
            "v_lshl_or_b32 v137, v179, 16, v178\n\t"
            "v_lshl_or_b32 v138, v181, 16, v180\n\t"
            "v_lshl_or_b32 v139, v183, 16, v182\n\t"
            "ds_write_b128 %[bd], v[136:139] offset:16896\n\t"
            "s_waitcnt lgkmcnt(3)\n\t"
            "v_lshl_or_b32 v140, v185, 16, v184\n\t"
            "v_lshl_or_b32 v141, v187, 16, v186\n\t"
            "v_lshl_or_b32 v142, v189, 16, v188\n\t"
            "v_lshl_or_b32 v143, v191, 16, v190\n\t"
            "ds_write_b128 %[bd], v[140:143] offset:17152\n\t"
            "s_add_u32 m0, %[ldsx], 65536\n\t"
            "s_lshl_b32 s76, s58, 7\n\t"
            "s_add_u32 s68, %[xpl], s76\n\t"
            "s_addc_u32 s69, %[xph], 0\n\t"
            "global_load_lds_dwordx4 %[x0], s[68:69] offset:0\n\t"
            "global_load_lds_dwordx4 %[x1], s[68:69] offset:1024\n\t"
            "global_load_lds_dwordx4 %[x2], s[68:69] offset:2048\n\t"
            "global_load_lds_dwordx4 %[x3], s[68:69] offset:3072\n\t"
            "s_add_u32 m0, %[ldsx], 69632\n\t"
            "s_lshl_b32 s76, s58, 7\n\t"
            "s_add_u32 s76, s76, 65536\n\t"
            "s_add_u32 s68, %[xpl], s76\n\t"
            "s_addc_u32 s69, %[xph], 0\n\t"
            "global_load_lds_dwordx4 %[x0], s[68:69] offset:0\n\t"
            "global_load_lds_dwordx4 %[x1], s[68:69] offset:1024\n\t"
            "global_load_lds_dwordx4 %[x2], s[68:69] offset:2048\n\t"
            "global_load_lds_dwordx4 %[x3], s[68:69] offset:3072\n\t"
            "s_waitcnt vmcnt(9) lgkmcnt(0)\n\t"
            "s_barrier\n\t"
            ".Lwp32:\n\t"
            "s_cbranch_execz .Lwp98\n\t"
            "global_load_dword v192, %[po], %[ws] sc1\n\t"
            "s_add_u32 m0, %[ldsx], 0\n\t"
            "s_lshl_b32 s76, s59, 7\n\t"
            "s_add_u32 s68, %[xpl], s76\n\t"
            "s_addc_u32 s69, %[xph], 0\n\t"
            "global_load_lds_dwordx4 %[x0], s[68:69] offset:0\n\t"
            "global_load_lds_dwordx4 %[x1], s[68:69] offset:1024\n\t"
            "global_load_lds_dwordx4 %[x2], s[68:69] offset:2048\n\t"
            "global_load_lds_dwordx4 %[x3], s[68:69] offset:3072\n\t"
            "s_add_u32 m0, %[ldsx], 4096\n\t"
            "s_lshl_b32 s76, s59, 7\n\t"
            "s_add_u32 s76, s76, 65536\n\t"
            "s_add_u32 s68, %[xpl], s76\n\t"
            "s_addc_u32 s69, %[xph], 0\n\t"
            "global_load_lds_dwordx4 %[x0], s[68:69] offset:0\n\t"
            "global_load_lds_dwordx4 %[x1], s[68:69] offset:1024\n\t"
            "global_load_lds_dwordx4 %[x2], s[68:69] offset:2048\n\t"
            "global_load_lds_dwordx4 %[x3], s[68:69] offset:3072\n\t"
            "s_waitcnt vmcnt(8)\n\t"
            "s_lshl_b32 s76, s58, 2\n\t"
            ".Lzc0:\n\t"
            "v_cmp_eq_u32 vcc, v192, %[nvl]\n\t"
            "s_lshr_b64 s[74:75], vcc, s76\n\t"
            "s_and_b32 s74, s74, 15\n\t"
            "s_cmp_eq_u32 s74, 15\n\t"
            "s_cbranch_scc1 .Lzd0\n\t"
            "s_cmp_eq_u32 s73, 0\n\t"
            "s_cbranch_scc1 .Lzd0\n\t"
            "s_sub_u32 s73, s73, 1\n\t"
            "global_load_dword v192, %[po], %[ws] sc1\n\t"
            "s_waitcnt vmcnt(0)\n\t"
            "s_branch .Lzc0\n\t"
            ".Lzd0:\n\t"
            "s_add_u32 m0, %[ldsw], 32768\n\t"
            "s_mul_i32 s76, s58, 458752\n\t"
            "s_add_u32 s70, s84, s76\n\t"
            "s_addc_u32 s71, s85, 0\n\t"
            "global_load_lds_dwordx4 %[wv], s[70:71] offset:0 sc1\n\t"
            "global_load_lds_dwordx4 %[wv], s[70:71] offset:1024 sc1\n\t"
            "global_load_lds_dwordx4 %[wv], s[70:71] offset:2048 sc1\n\t"
            "global_load_lds_dwordx4 %[wv], s[70:71] offset:3072 sc1\n\t"
            "s_cmp_lg_u32 %[h2], 0\n\t"
            "s_cbranch_scc1 .Lzs1\n\t"
            "global_store_dwordx4 %[wo], v[128:131], %[st1] sc1\n\t"
            "global_store_dwordx4 %[wo], v[132:135], %[st1] offset:256 sc1\n\t"
            "s_branch .Lzt1\n\t"
            ".Lzs1:\n\t"
            "global_store_dwordx4 %[wo], v[136:139], %[st1] sc1\n\t"
            "global_store_dwordx4 %[wo], v[140:143], %[st1] offset:256 sc1\n\t"
            ".Lzt1:\n\t"
            "s_lshl_b32 s76, s59, 2\n\t"
            ".Lzc2:\n\t"
            "v_cmp_eq_u32 vcc, v192, %[nvl]\n\t"
            "s_lshr_b64 s[74:75], vcc, s76\n\t"
            "s_and_b32 s74, s74, 15\n\t"
            "s_cmp_eq_u32 s74, 15\n\t"
            "s_cbranch_scc1 .Lzd2\n\t"
            "s_cmp_eq_u32 s73, 0\n\t"
            "s_cbranch_scc1 .Lzd2\n\t"
            "s_sub_u32 s73, s73, 1\n\t"
            "global_load_dword v192, %[po], %[ws] sc1\n\t"
            "s_waitcnt vmcnt(0)\n\t"
            "s_branch .Lzc2\n\t"
            ".Lzd2:\n\t"
            "s_add_u32 m0, %[ldsw], 0\n\t"
            "s_mul_i32 s76, s59, 458752\n\t"
            "s_add_u32 s70, s84, s76\n\t"
            "s_addc_u32 s71, s85, 0\n\t"
            "global_load_lds_dwordx4 %[wv], s[70:71] offset:0 sc1\n\t"
            "global_load_lds_dwordx4 %[wv], s[70:71] offset:1024 sc1\n\t"
            "global_load_lds_dwordx4 %[wv], s[70:71] offset:2048 sc1\n\t"
            "global_load_lds_dwordx4 %[wv], s[70:71] offset:3072 sc1\n\t"
            "s_waitcnt vmcnt(6)\n\t"
            "s_barrier\n\t"
            "s_cmp_ge_u32 %[p], 2\n\t"
            "s_cbranch_scc1 .Lzcls1\n\t"
            ".Lwp33:\n\t"
            "s_cbranch_execz .Lwp98\n\t"
            "s_lshl_b32 s76, s60, 2\n\t"
            ".Lzc3:\n\t"
            "v_cmp_eq_u32 vcc, v192, %[nvl]\n\t"
            "s_lshr_b64 s[74:75], vcc, s76\n\t"
            "s_and_b32 s74, s74, 15\n\t"
            "s_cmp_eq_u32 s74, 15\n\t"
            "s_cbranch_scc1 .Lzd3\n\t"
            "s_cmp_eq_u32 s73, 0\n\t"
            "s_cbranch_scc1 .Lzd3\n\t"
            "s_sub_u32 s73, s73, 1\n\t"
            "global_load_dword v192, %[po], %[ws] sc1\n\t"
            "s_waitcnt vmcnt(0)\n\t"
            "s_branch .Lzc3\n\t"
            ".Lzd3:\n\t"
            "s_add_u32 m0, %[ldsw], 16384\n\t"
            "s_mul_i32 s76, s60, 458752\n\t"
            "s_add_u32 s70, s84, s76\n\t"
            "s_addc_u32 s71, s85, 0\n\t"
            "global_load_lds_dwordx4 %[wv], s[70:71] offset:0 sc1\n\t"
            "global_load_lds_dwordx4 %[wv], s[70:71] offset:1024 sc1\n\t"
            "global_load_lds_dwordx4 %[wv], s[70:71] offset:2048 sc1\n\t"
            "global_load_lds_dwordx4 %[wv], s[70:71] offset:3072 sc1\n\t"
            "s_waitcnt vmcnt(8)\n\t"
            "s_mov_b64 exec, 3\n\t"
            "global_store_dword %[fo0], %[nvl], %[ws] offset:64 sc1\n\t"
            "s_mov_b64 exec, -1\n\t"
            "s_mov_b64 s[88:89], %[qbg]\n\t"
            "global_load_dwordx4 v[128:131], %[qo], s[88:89] \n\t"
            "s_add_u32 s88, s88, 14336\n\t"
            "s_addc_u32 s89, s89, 0\n\t"
            "global_load_dwordx4 v[132:135], %[qo], s[88:89] \n\t"
            "s_add_u32 s88, s88, 14336\n\t"
            "s_addc_u32 s89, s89, 0\n\t"
            "global_load_dwordx4 v[136:139], %[qo], s[88:89] \n\t"
            "s_add_u32 s88, s88, 14336\n\t"
            "s_addc_u32 s89, s89, 0\n\t"
            "global_load_dwordx4 v[140:143], %[qo], s[88:89] \n\t"
            "s_add_u32 s88, s88, 14336\n\t"
            "s_addc_u32 s89, s89, 0\n\t"
            "global_load_dwordx4 v[144:147], %[qo], s[88:89] \n\t"
            "s_add_u32 s88, s88, 14336\n\t"
            "s_addc_u32 s89, s89, 0\n\t"
            "global_load_dwordx4 v[148:151], %[qo], s[88:89] \n\t"
            "s_add_u32 s88, s88, 14336\n\t"
            "s_addc_u32 s89, s89, 0\n\t"
            "global_load_dwordx4 v[152:155], %[qo], s[88:89] \n\t"
            "s_add_u32 s88, s88, 14336\n\t"
            "s_addc_u32 s89, s89, 0\n\t"
            "global_load_dwordx4 v[156:159], %[qo], s[88:89] \n\t"
            "s_add_u32 m0, %[ldsx], 32768\n\t"
            "s_lshl_b32 s76, s60, 7\n\t"
            "s_add_u32 s68, %[xpl], s76\n\t"
            "s_addc_u32 s69, %[xph], 0\n\t"
            "global_load_lds_dwordx4 %[x0], s[68:69] offset:0\n\t"
            "global_load_lds_dwordx4 %[x1], s[68:69] offset:1024\n\t"
            "global_load_lds_dwordx4 %[x2], s[68:69] offset:2048\n\t"
            "global_load_lds_dwordx4 %[x3], s[68:69] offset:3072\n\t"
            "s_add_u32 m0, %[ldsx], 36864\n\t"
            "s_lshl_b32 s76, s60, 7\n\t"
            "s_add_u32 s76, s76, 65536\n\t"
            "s_add_u32 s68, %[xpl], s76\n\t"
            "s_addc_u32 s69, %[xph], 0\n\t"
            "global_load_lds_dwordx4 %[x0], s[68:69] offset:0\n\t"
            "global_load_lds_dwordx4 %[x1], s[68:69] offset:1024\n\t"
            "global_load_lds_dwordx4 %[x2], s[68:69] offset:2048\n\t"
            "global_load_lds_dwordx4 %[x3], s[68:69] offset:3072\n\t"
            "s_waitcnt vmcnt(21)\n\t"
            "s_barrier\n\t"
            ".Lwp34:\n\t"
            "s_cbranch_execz .Lwp35\n\t"
            "global_load_dword v192, %[po], %[ws] sc1\n\t"
            "s_waitcnt vmcnt(9)\n\t"
            "s_setprio 0\n\t"
            "s_add_u32 m0, %[ldsx], 65536\n\t"
            "s_lshl_b32 s76, s61, 7\n\t"
            "s_add_u32 s68, %[xpl], s76\n\t"
            "s_addc_u32 s69, %[xph], 0\n\t"
            "global_load_lds_dwordx4 %[x0], s[68:69] offset:0\n\t"
            "v_lshl_add_u32 v160, v128, 8, %[lutl]\n\t"
            "ds_read_u16 v160, v160 offset:0\n\t"
            "v_lshl_add_u32 v161, v132, 8, %[lutl]\n\t"
            "ds_read_u16 v161, v161 offset:0\n\t"
            "v_lshl_add_u32 v162, v136, 8, %[lutl]\n\t"
            "ds_read_u16 v162, v162 offset:0\n\t"
            "v_lshl_add_u32 v163, v140, 8, %[lutl]\n\t"
            "ds_read_u16 v163, v163 offset:0\n\t"
            "global_load_lds_dwordx4 %[x1], s[68:69] offset:1024\n\t"
            "v_lshl_add_u32 v164, v144, 8, %[lutl]\n\t"
            "ds_read_u16 v164, v164 offset:0\n\t"
            "v_lshl_add_u32 v165, v148, 8, %[lutl]\n\t"
            "ds_read_u16 v165, v165 offset:0\n\t"
            "v_lshl_add_u32 v166, v152, 8, %[lutl]\n\t"
            "ds_read_u16 v166, v166 offset:0\n\t"
            "v_lshl_add_u32 v167, v156, 8, %[lutl]\n\t"
            "ds_read_u16 v167, v167 offset:0\n\t"
            "global_load_lds_dwordx4 %[x2], s[68:69] offset:2048\n\t"
            "v_lshl_add_u32 v168, v129, 8, %[lutl]\n\t"
            "ds_read_u16 v168, v168 offset:2\n\t"
            "v_lshl_add_u32 v169, v133, 8, %[lutl]\n\t"
            "ds_read_u16 v169, v169 offset:2\n\t"
            "v_lshl_add_u32 v170, v137, 8, %[lutl]\n\t"
            "ds_read_u16 v170, v170 offset:2\n\t"
            "v_lshl_add_u32 v171, v141, 8, %[lutl]\n\t"
            "ds_read_u16 v171, v171 offset:2\n\t"
            "global_load_lds_dwordx4 %[x3], s[68:69] offset:3072\n\t"
            "v_lshl_add_u32 v172, v145, 8, %[lutl]\n\t"
            "ds_read_u16 v172, v172 offset:2\n\t"
            "v_lshl_add_u32 v173, v149, 8, %[lutl]\n\t"
            "ds_read_u16 v173, v173 offset:2\n\t"
            "v_lshl_add_u32 v174, v153, 8, %[lutl]\n\t"
            "ds_read_u16 v174, v174 offset:2\n\t"
            "v_lshl_add_u32 v175, v157, 8, %[lutl]\n\t"
            "ds_read_u16 v175, v175 offset:2\n\t"
            "s_add_u32 m0, %[ldsx], 69632\n\t"
            "s_lshl_b32 s76, s61, 7\n\t"
            "s_add_u32 s76, s76, 65536\n\t"
            "s_add_u32 s68, %[xpl], s76\n\t"
            "s_addc_u32 s69, %[xph], 0\n\t"
            "global_load_lds_dwordx4 %[x0], s[68:69] offset:0\n\t"
            "v_lshl_add_u32 v176, v130, 8, %[lutl]\n\t"
            "ds_read_u16 v176, v176 offset:128\n\t"
            "v_lshl_add_u32 v177, v134, 8, %[lutl]\n\t"
            "ds_read_u16 v177, v177 offset:128\n\t"
            "v_lshl_add_u32 v178, v138, 8, %[lutl]\n\t"
            "ds_read_u16 v178, v178 offset:128\n\t"
            "v_lshl_add_u32 v179, v142, 8, %[lutl]\n\t"
            "ds_read_u16 v179, v179 offset:128\n\t"
            "global_load_lds_dwordx4 %[x1], s[68:69] offset:1024\n\t"
            "v_lshl_add_u32 v180, v146, 8, %[lutl]\n\t"
            "ds_read_u16 v180, v180 offset:128\n\t"
            "v_lshl_add_u32 v181, v150, 8, %[lutl]\n\t"
            "ds_read_u16 v181, v181 offset:128\n\t"
            "v_lshl_add_u32 v182, v154, 8, %[lutl]\n\t"
            "ds_read_u16 v182, v182 offset:128\n\t"
            "v_lshl_add_u32 v183, v158, 8, %[lutl]\n\t"
            "ds_read_u16 v183, v183 offset:128\n\t"
            "global_load_lds_dwordx4 %[x2], s[68:69] offset:2048\n\t"
            "v_lshl_add_u32 v184, v131, 8, %[lutl]\n\t"
            "ds_read_u16 v184, v184 offset:130\n\t"
            "v_lshl_add_u32 v185, v135, 8, %[lutl]\n\t"
            "ds_read_u16 v185, v185 offset:130\n\t"
            "v_lshl_add_u32 v186, v139, 8, %[lutl]\n\t"
            "ds_read_u16 v186, v186 offset:130\n\t"
            "v_lshl_add_u32 v187, v143, 8, %[lutl]\n\t"
            "ds_read_u16 v187, v187 offset:130\n\t"
            "global_load_lds_dwordx4 %[x3], s[68:69] offset:3072\n\t"
            "v_lshl_add_u32 v188, v147, 8, %[lutl]\n\t"
            "ds_read_u16 v188, v188 offset:130\n\t"
            "v_lshl_add_u32 v189, v151, 8, %[lutl]\n\t"
            "ds_read_u16 v189, v189 offset:130\n\t"
            "v_lshl_add_u32 v190, v155, 8, %[lutl]\n\t"
            "ds_read_u16 v190, v190 offset:130\n\t"
            "v_lshl_add_u32 v191, v159, 8, %[lutl]\n\t"
            "ds_read_u16 v191, v191 offset:130\n\t"
            "s_setprio 0\n\t"
            "s_waitcnt vmcnt(8)\n\t"
            "s_lshl_b32 s76, s61, 2\n\t"
            ".Lzc4:\n\t"
            "v_cmp_eq_u32 vcc, v192, %[nvl]\n\t"
            "s_lshr_b64 s[74:75], vcc, s76\n\t"
            "s_and_b32 s74, s74, 15\n\t"
            "s_cmp_eq_u32 s74, 15\n\t"
            "s_cbranch_scc1 .Lzd4\n\t"
            "s_cmp_eq_u32 s73, 0\n\t"
            "s_cbranch_scc1 .Lzd4\n\t"
            "s_sub_u32 s73, s73, 1\n\t"
            "global_load_dword v192, %[po], %[ws] sc1\n\t"
            "s_waitcnt vmcnt(0)\n\t"
            "s_branch .Lzc4\n\t"
            ".Lzd4:\n\t"
            "s_add_u32 m0, %[ldsw], 32768\n\t"
            "s_mul_i32 s76, s61, 458752\n\t"
            "s_add_u32 s70, s84, s76\n\t"
            "s_addc_u32 s71, s85, 0\n\t"
            "global_load_lds_dwordx4 %[wv], s[70:71] offset:0 sc1\n\t"
            "global_load_lds_dwordx4 %[wv], s[70:71] offset:1024 sc1\n\t"
            "global_load_lds_dwordx4 %[wv], s[70:71] offset:2048 sc1\n\t"
            "global_load_lds_dwordx4 %[wv], s[70:71] offset:3072 sc1\n\t"
            "s_waitcnt lgkmcnt(15)\n\t"
            "v_lshl_or_b32 v128, v161, 16, v160\n\t"
            "v_lshl_or_b32 v129, v163, 16, v162\n\t"
            "v_lshl_or_b32 v130, v165, 16, v164\n\t"
            "v_lshl_or_b32 v131, v167, 16, v166\n\t"
            "s_waitcnt lgkmcnt(15)\n\t"
            "v_lshl_or_b32 v132, v169, 16, v168\n\t"
            "v_lshl_or_b32 v133, v171, 16, v170\n\t"
            "v_lshl_or_b32 v134, v173, 16, v172\n\t"
            "v_lshl_or_b32 v135, v175, 16, v174\n\t"
            "s_waitcnt lgkmcnt(8)\n\t"
            "v_lshl_or_b32 v136, v177, 16, v176\n\t"
            "v_lshl_or_b32 v137, v179, 16, v178\n\t"
            "v_lshl_or_b32 v138, v181, 16, v180\n\t"
            "v_lshl_or_b32 v139, v183, 16, v182\n\t"
            "s_waitcnt lgkmcnt(0)\n\t"
            "v_lshl_or_b32 v140, v185, 16, v184\n\t"
            "v_lshl_or_b32 v141, v187, 16, v186\n\t"
            "v_lshl_or_b32 v142, v189, 16, v188\n\t"
            "v_lshl_or_b32 v143, v191, 16, v190\n\t"
            "global_store_dwordx4 %[wo], v[128:131], %[stb] offset:0 sc1\n\t"
            "global_store_dwordx4 %[wo], v[132:135], %[stb] offset:256 sc1\n\t"
            "global_store_dwordx4 %[wo], v[136:139], %[stb] offset:512 sc1\n\t"
            "global_store_dwordx4 %[wo], v[140:143], %[stb] offset:768 sc1\n\t"
            "s_barrier\n\t"
            ".Lwp35:\n\t"
            "s_cbranch_execz .Lwp36\n\t"
            "s_lshl_b32 s76, s62, 2\n\t"
            ".Lzc5:\n\t"
            "v_cmp_eq_u32 vcc, v192, %[nvl]\n\t"
            "s_lshr_b64 s[74:75], vcc, s76\n\t"
            "s_and_b32 s74, s74, 15\n\t"
            "s_cmp_eq_u32 s74, 15\n\t"
            "s_cbranch_scc1 .Lzd5\n\t"
            "s_cmp_eq_u32 s73, 0\n\t"
            "s_cbranch_scc1 .Lzd5\n\t"
            "s_sub_u32 s73, s73, 1\n\t"
            "global_load_dword v192, %[po], %[ws] sc1\n\t"
            "s_waitcnt vmcnt(0)\n\t"
            "s_branch .Lzc5\n\t"
            ".Lzd5:\n\t"
            "s_add_u32 m0, %[ldsw], 0\n\t"
            "s_mul_i32 s76, s62, 458752\n\t"
            "s_add_u32 s70, s84, s76\n\t"
            "s_addc_u32 s71, s85, 0\n\t"
            "global_load_lds_dwordx4 %[wv], s[70:71] offset:0 sc1\n\t"
            "global_load_lds_dwordx4 %[wv], s[70:71] offset:1024 sc1\n\t"
            "global_load_lds_dwordx4 %[wv], s[70:71] offset:2048 sc1\n\t"
            "global_load_lds_dwordx4 %[wv], s[70:71] offset:3072 sc1\n\t"
            "s_add_u32 m0, %[ldsx], 0\n\t"
            "s_lshl_b32 s76, s62, 7\n\t"
            "s_add_u32 s68, %[xpl], s76\n\t"
            "s_addc_u32 s69, %[xph], 0\n\t"
            "global_load_lds_dwordx4 %[x0], s[68:69] offset:0\n\t"
            "global_load_lds_dwordx4 %[x1], s[68:69] offset:1024\n\t"
            "global_load_lds_dwordx4 %[x2], s[68:69] offset:2048\n\t"
            "global_load_lds_dwordx4 %[x3], s[68:69] offset:3072\n\t"
            "s_add_u32 m0, %[ldsx], 4096\n\t"
            "s_lshl_b32 s76, s62, 7\n\t"
            "s_add_u32 s76, s76, 65536\n\t"
            "s_add_u32 s68, %[xpl], s76\n\t"
            "s_addc_u32 s69, %[xph], 0\n\t"
            "global_load_lds_dwordx4 %[x0], s[68:69] offset:0\n\t"
            "global_load_lds_dwordx4 %[x1], s[68:69] offset:1024\n\t"
            "global_load_lds_dwordx4 %[x2], s[68:69] offset:2048\n\t"
            "global_load_lds_dwordx4 %[x3], s[68:69] offset:3072\n\t"
            "s_waitcnt vmcnt(12)\n\t"
            "s_mov_b64 exec, 15\n\t"
            "global_store_dword %[fob], %[nvl], %[ws] offset:0 sc1\n\t"
            "s_mov_b64 exec, -1\n\t"
            "s_barrier\n\t"
            "s_lshl_b32 s76, s63, 2\n\t"
            ".Lzc6:\n\t"
            "v_cmp_eq_u32 vcc, v192, %[nvl]\n\t"
            "s_lshr_b64 s[74:75], vcc, s76\n\t"
            "s_and_b32 s74, s74, 15\n\t"
            "s_cmp_eq_u32 s74, 15\n\t"
            "s_cbranch_scc1 .Lzd6\n\t"
            "s_cmp_eq_u32 s73, 0\n\t"
            "s_cbranch_scc1 .Lzd6\n\t"
            "s_sub_u32 s73, s73, 1\n\t"
            "global_load_dword v192, %[po], %[ws] sc1\n\t"
            "s_waitcnt vmcnt(0)\n\t"
            "s_branch .Lzc6\n\t"
            ".Lzd6:\n\t"
            "s_add_u32 m0, %[ldsw], 16384\n\t"
            "s_mul_i32 s76, s63, 458752\n\t"
            "s_add_u32 s70, s84, s76\n\t"
            "s_addc_u32 s71, s85, 0\n\t"
            "global_load_lds_dwordx4 %[wv], s[70:71] offset:0 sc1\n\t"
            "global_load_lds_dwordx4 %[wv], s[70:71] offset:1024 sc1\n\t"
            "global_load_lds_dwordx4 %[wv], s[70:71] offset:2048 sc1\n\t"
            "global_load_lds_dwordx4 %[wv], s[70:71] offset:3072 sc1\n\t"
            "s_add_u32 m0, %[ldsx], 32768\n\t"
            "s_lshl_b32 s76, s63, 7\n\t"
            "s_add_u32 s68, %[xpl], s76\n\t"
            "s_addc_u32 s69, %[xph], 0\n\t"
            "global_load_lds_dwordx4 %[x0], s[68:69] offset:0\n\t"
            "global_load_lds_dwordx4 %[x1], s[68:69] offset:1024\n\t"
            "global_load_lds_dwordx4 %[x2], s[68:69] offset:2048\n\t"
            "global_load_lds_dwordx4 %[x3], s[68:69] offset:3072\n\t"
            "s_add_u32 m0, %[ldsx], 36864\n\t"
            "s_lshl_b32 s76, s63, 7\n\t"
            "s_add_u32 s76, s76, 65536\n\t"
            "s_add_u32 s68, %[xpl], s76\n\t"
            "s_addc_u32 s69, %[xph], 0\n\t"
            "global_load_lds_dwordx4 %[x0], s[68:69] offset:0\n\t"
            "global_load_lds_dwordx4 %[x1], s[68:69] offset:1024\n\t"
            "global_load_lds_dwordx4 %[x2], s[68:69] offset:2048\n\t"
            "global_load_lds_dwordx4 %[x3], s[68:69] offset:3072\n\t"
            "s_waitcnt vmcnt(13)\n\t"
            "s_barrier\n\t"
            "global_load_dword v192, %[po], %[ws] sc1\n\t"
            "s_add_u32 m0, %[ldsx], 65536\n\t"
            "s_add_u32 s68, %[xpl], 1024\n\t"
            "s_addc_u32 s69, %[xph], 0\n\t"
            "global_load_lds_dwordx4 %[x0], s[68:69] offset:0\n\t"
            "global_load_lds_dwordx4 %[x1], s[68:69] offset:1024\n\t"
            "global_load_lds_dwordx4 %[x2], s[68:69] offset:2048\n\t"
            "global_load_lds_dwordx4 %[x3], s[68:69] offset:3072\n\t"
            "s_add_u32 m0, %[ldsx], 69632\n\t"
            "s_add_u32 s68, %[xpl], 66560\n\t"
            "s_addc_u32 s69, %[xph], 0\n\t"
            "global_load_lds_dwordx4 %[x0], s[68:69] offset:0\n\t"
            "global_load_lds_dwordx4 %[x1], s[68:69] offset:1024\n\t"
            "global_load_lds_dwordx4 %[x2], s[68:69] offset:2048\n\t"
            "global_load_lds_dwordx4 %[x3], s[68:69] offset:3072\n\t"
            "s_waitcnt vmcnt(8)\n\t"
            "s_mov_b32 s76, 32\n\t"
            ".Lzc7:\n\t"
            "v_cmp_eq_u32 vcc, v192, %[nvl]\n\t"
            "s_lshr_b64 s[74:75], vcc, s76\n\t"
            "s_and_b32 s74, s74, 15\n\t"
            "s_cmp_eq_u32 s74, 15\n\t"
            "s_cbranch_scc1 .Lzd7\n\t"
            "s_cmp_eq_u32 s73, 0\n\t"
            "s_cbranch_scc1 .Lzd7\n\t"
            "s_sub_u32 s73, s73, 1\n\t"
            "global_load_dword v192, %[po], %[ws] sc1\n\t"
            "s_waitcnt vmcnt(0)\n\t"
            "s_branch .Lzc7\n\t"
            ".Lzd7:\n\t"
            "s_add_u32 m0, %[ldsw], 32768\n\t"
            "s_add_u32 s70, s84, 3670016\n\t"
            "s_addc_u32 s71, s85, 0\n\t"
            "global_load_lds_dwordx4 %[wv], s[70:71] offset:0 sc1\n\t"
            "global_load_lds_dwordx4 %[wv], s[70:71] offset:1024 sc1\n\t"
            "global_load_lds_dwordx4 %[wv], s[70:71] offset:2048 sc1\n\t"
            "global_load_lds_dwordx4 %[wv], s[70:71] offset:3072 sc1\n\t"
            "s_barrier\n\t"
            ".Lwp36:\n\t"
            "s_cbranch_execz .Lwp37\n\t"
            "s_mov_b32 s76, 36\n\t"
            ".Lzc8:\n\t"
            "v_cmp_eq_u32 vcc, v192, %[nvl]\n\t"
            "s_lshr_b64 s[74:75], vcc, s76\n\t"
            "s_and_b32 s74, s74, 15\n\t"
            "s_cmp_eq_u32 s74, 15\n\t"
            "s_cbranch_scc1 .Lzd8\n\t"
            "s_cmp_eq_u32 s73, 0\n\t"
            "s_cbranch_scc1 .Lzd8\n\t"
            "s_sub_u32 s73, s73, 1\n\t"
            "global_load_dword v192, %[po], %[ws] sc1\n\t"
            "s_waitcnt vmcnt(0)\n\t"
            "s_branch .Lzc8\n\t"
            ".Lzd8:\n\t"
            "s_add_u32 m0, %[ldsw], 0\n\t"
            "s_add_u32 s70, s84, 4128768\n\t"
            "s_addc_u32 s71, s85, 0\n\t"
            "global_load_lds_dwordx4 %[wv], s[70:71] offset:0 sc1\n\t"
            "global_load_lds_dwordx4 %[wv], s[70:71] offset:1024 sc1\n\t"
            "global_load_lds_dwordx4 %[wv], s[70:71] offset:2048 sc1\n\t"
            "global_load_lds_dwordx4 %[wv], s[70:71] offset:3072 sc1\n\t"
            "s_add_u32 m0, %[ldsx], 0\n\t"
            "s_add_u32 s68, %[xpl], 1152\n\t"
            "s_addc_u32 s69, %[xph], 0\n\t"
            "global_load_lds_dwordx4 %[x0], s[68:69] offset:0\n\t"
            "global_load_lds_dwordx4 %[x1], s[68:69] offset:1024\n\t"
            "global_load_lds_dwordx4 %[x2], s[68:69] offset:2048\n\t"
            "global_load_lds_dwordx4 %[x3], s[68:69] offset:3072\n\t"
            "s_add_u32 m0, %[ldsx], 4096\n\t"
            "s_add_u32 s68, %[xpl], 66688\n\t"
            "s_addc_u32 s69, %[xph], 0\n\t"
            "global_load_lds_dwordx4 %[x0], s[68:69] offset:0\n\t"
            "global_load_lds_dwordx4 %[x1], s[68:69] offset:1024\n\t"
            "global_load_lds_dwordx4 %[x2], s[68:69] offset:2048\n\t"
            "global_load_lds_dwordx4 %[x3], s[68:69] offset:3072\n\t"
            "s_waitcnt vmcnt(12)\n\t"
            "s_barrier\n\t"
            "s_mov_b32 s76, 40\n\t"
            ".Lzc9:\n\t"
            "v_cmp_eq_u32 vcc, v192, %[nvl]\n\t"
            "s_lshr_b64 s[74:75], vcc, s76\n\t"
            "s_and_b32 s74, s74, 15\n\t"
            "s_cmp_eq_u32 s74, 15\n\t"
            "s_cbranch_scc1 .Lzd9\n\t"
            "s_cmp_eq_u32 s73, 0\n\t"
            "s_cbranch_scc1 .Lzd9\n\t"
            "s_sub_u32 s73, s73, 1\n\t"
            "global_load_dword v192, %[po], %[ws] sc1\n\t"
            "s_waitcnt vmcnt(0)\n\t"
            "s_branch .Lzc9\n\t"
            ".Lzd9:\n\t"
            "s_add_u32 m0, %[ldsw], 16384\n\t"
            "s_add_u32 s70, s84, 4587520\n\t"
            "s_addc_u32 s71, s85, 0\n\t"
            "global_load_lds_dwordx4 %[wv], s[70:71] offset:0 sc1\n\t"
            "global_load_lds_dwordx4 %[wv], s[70:71] offset:1024 sc1\n\t"
            "global_load_lds_dwordx4 %[wv], s[70:71] offset:2048 sc1\n\t"
            "global_load_lds_dwordx4 %[wv], s[70:71] offset:3072 sc1\n\t"
            "s_add_u32 m0, %[ldsx], 32768\n\t"
            "s_add_u32 s68, %[xpl], 1280\n\t"
            "s_addc_u32 s69, %[xph], 0\n\t"
            "global_load_lds_dwordx4 %[x0], s[68:69] offset:0\n\t"
            "global_load_lds_dwordx4 %[x1], s[68:69] offset:1024\n\t"
            "global_load_lds_dwordx4 %[x2], s[68:69] offset:2048\n\t"
            "global_load_lds_dwordx4 %[x3], s[68:69] offset:3072\n\t"
            "s_add_u32 m0, %[ldsx], 36864\n\t"
            "s_add_u32 s68, %[xpl], 66816\n\t"
            "s_addc_u32 s69, %[xph], 0\n\t"
            "global_load_lds_dwordx4 %[x0], s[68:69] offset:0\n\t"
            "global_load_lds_dwordx4 %[x1], s[68:69] offset:1024\n\t"
            "global_load_lds_dwordx4 %[x2], s[68:69] offset:2048\n\t"
            "global_load_lds_dwordx4 %[x3], s[68:69] offset:3072\n\t"
            "s_waitcnt vmcnt(12)\n\t"
            "s_barrier\n\t"
            "s_mov_b32 s76, 44\n\t"
            ".Lzc10:\n\t"
            "v_cmp_eq_u32 vcc, v192, %[nvl]\n\t"
            "s_lshr_b64 s[74:75], vcc, s76\n\t"
            "s_and_b32 s74, s74, 15\n\t"
            "s_cmp_eq_u32 s74, 15\n\t"
            "s_cbranch_scc1 .Lzd10\n\t"
            "s_cmp_eq_u32 s73, 0\n\t"
            "s_cbranch_scc1 .Lzd10\n\t"
            "s_sub_u32 s73, s73, 1\n\t"
            "global_load_dword v192, %[po], %[ws] sc1\n\t"
            "s_waitcnt vmcnt(0)\n\t"
            "s_branch .Lzc10\n\t"
            ".Lzd10:\n\t"
            "s_add_u32 m0, %[ldsw], 32768\n\t"
            "s_add_u32 s70, s84, 5046272\n\t"
            "s_addc_u32 s71, s85, 0\n\t"
            "global_load_lds_dwordx4 %[wv], s[70:71] offset:0 sc1\n\t"
            "global_load_lds_dwordx4 %[wv], s[70:71] offset:1024 sc1\n\t"
            "global_load_lds_dwordx4 %[wv], s[70:71] offset:2048 sc1\n\t"
            "global_load_lds_dwordx4 %[wv], s[70:71] offset:3072 sc1\n\t"
            "s_add_u32 m0, %[ldsx], 65536\n\t"
            "s_add_u32 s68, %[xpl], 1408\n\t"
            "s_addc_u32 s69, %[xph], 0\n\t"
            "global_load_lds_dwordx4 %[x0], s[68:69] offset:0\n\t"
            "global_load_lds_dwordx4 %[x1], s[68:69] offset:1024\n\t"
            "global_load_lds_dwordx4 %[x2], s[68:69] offset:2048\n\t"
            "global_load_lds_dwordx4 %[x3], s[68:69] offset:3072\n\t"
            "s_add_u32 m0, %[ldsx], 69632\n\t"
            "s_add_u32 s68, %[xpl], 66944\n\t"
            "s_addc_u32 s69, %[xph], 0\n\t"
            "global_load_lds_dwordx4 %[x0], s[68:69] offset:0\n\t"
            "global_load_lds_dwordx4 %[x1], s[68:69] offset:1024\n\t"
            "global_load_lds_dwordx4 %[x2], s[68:69] offset:2048\n\t"
            "global_load_lds_dwordx4 %[x3], s[68:69] offset:3072\n\t"
            "s_waitcnt vmcnt(12)\n\t"
            "s_barrier\n\t"
            ".Lwp37:\n\t"
            "s_cbranch_execz .Lwp97\n\t"
            "global_load_dword v192, %[po], %[ws] sc1\n\t"
            "s_add_u32 m0, %[ldsx], 0\n\t"
            "s_add_u32 s68, %[xpl], 1536\n\t"
            "s_addc_u32 s69, %[xph], 0\n\t"
            "global_load_lds_dwordx4 %[x0], s[68:69] offset:0\n\t"
            "global_load_lds_dwordx4 %[x1], s[68:69] offset:1024\n\t"
            "global_load_lds_dwordx4 %[x2], s[68:69] offset:2048\n\t"
            "global_load_lds_dwordx4 %[x3], s[68:69] offset:3072\n\t"
            "s_add_u32 m0, %[ldsx], 4096\n\t"
            "s_add_u32 s68, %[xpl], 67072\n\t"
            "s_addc_u32 s69, %[xph], 0\n\t"
            "global_load_lds_dwordx4 %[x0], s[68:69] offset:0\n\t"
            "global_load_lds_dwordx4 %[x1], s[68:69] offset:1024\n\t"
            "global_load_lds_dwordx4 %[x2], s[68:69] offset:2048\n\t"
            "global_load_lds_dwordx4 %[x3], s[68:69] offset:3072\n\t"
            "s_waitcnt vmcnt(8)\n\t"
            "s_mov_b32 s76, 48\n\t"
            ".Lzc11:\n\t"
            "v_cmp_eq_u32 vcc, v192, %[nvl]\n\t"
            "s_lshr_b64 s[74:75], vcc, s76\n\t"
            "s_and_b32 s74, s74, 15\n\t"
            "s_cmp_eq_u32 s74, 15\n\t"
            "s_cbranch_scc1 .Lzd11\n\t"
            "s_cmp_eq_u32 s73, 0\n\t"
            "s_cbranch_scc1 .Lzd11\n\t"
            "s_sub_u32 s73, s73, 1\n\t"
            "global_load_dword v192, %[po], %[ws] sc1\n\t"
            "s_waitcnt vmcnt(0)\n\t"
            "s_branch .Lzc11\n\t"
            ".Lzd11:\n\t"
            "s_add_u32 m0, %[ldsw], 0\n\t"
            "s_add_u32 s70, s84, 5505024\n\t"
            "s_addc_u32 s71, s85, 0\n\t"
            "global_load_lds_dwordx4 %[wv], s[70:71] offset:0 sc1\n\t"
            "global_load_lds_dwordx4 %[wv], s[70:71] offset:1024 sc1\n\t"
            "global_load_lds_dwordx4 %[wv], s[70:71] offset:2048 sc1\n\t"
            "global_load_lds_dwordx4 %[wv], s[70:71] offset:3072 sc1\n\t"
            "s_barrier\n\t"
            "s_mov_b32 s76, 52\n\t"
            ".Lzc12:\n\t"
            "v_cmp_eq_u32 vcc, v192, %[nvl]\n\t"
            "s_lshr_b64 s[74:75], vcc, s76\n\t"
            "s_and_b32 s74, s74, 4095\n\t"
            "s_cmp_eq_u32 s74, 4095\n\t"
            "s_cbranch_scc1 .Lzd12\n\t"
            "s_cmp_eq_u32 s73, 0\n\t"
            "s_cbranch_scc1 .Lzd12\n\t"
            "s_sub_u32 s73, s73, 1\n\t"
            "global_load_dword v192, %[po], %[ws] sc1\n\t"
            "s_waitcnt vmcnt(0)\n\t"
            "s_branch .Lzc12\n\t"
            ".Lzd12:\n\t"
            "s_add_u32 m0, %[ldsw], 16384\n\t"
            "s_add_u32 s70, s84, 5963776\n\t"
            "s_addc_u32 s71, s85, 0\n\t"
            "s_add_u32 s86, s84, 6421504\n\t"
            "s_addc_u32 s87, s85, 0\n\t"
            "global_load_lds_dwordx4 %[wv], s[70:71] offset:0 sc1\n\t"
            "global_load_lds_dwordx4 %[wv], s[86:87] offset:1024 sc1\n\t"
            "global_load_lds_dwordx4 %[wv], s[70:71] offset:2048 sc1\n\t"
            "global_load_lds_dwordx4 %[wv], s[86:87] offset:3072 sc1\n\t"
            "s_add_u32 m0, %[ldsx], 32768\n\t"
            "s_add_u32 s68, %[xpl], 1664\n\t"
            "s_addc_u32 s69, %[xph], 0\n\t"
            "global_load_lds_dwordx4 %[x0], s[68:69] offset:0\n\t"
            "global_load_lds_dwordx4 %[x1], s[68:69] offset:1024\n\t"
            "global_load_lds_dwordx4 %[x2], s[68:69] offset:2048\n\t"
            "global_load_lds_dwordx4 %[x3], s[68:69] offset:3072\n\t"
            "s_add_u32 m0, %[ldsx], 36864\n\t"
            "s_add_u32 s68, %[xpl], 67200\n\t"
            "s_addc_u32 s69, %[xph], 0\n\t"
            "global_load_lds_dwordx4 %[x0], s[68:69] offset:0\n\t"
            "global_load_lds_dwordx4 %[x1], s[68:69] offset:1024\n\t"
            "global_load_lds_dwordx4 %[x2], s[68:69] offset:2048\n\t"
            "global_load_lds_dwordx4 %[x3], s[68:69] offset:3072\n\t"
            "s_waitcnt vmcnt(12)\n\t"
            "s_barrier\n\t"
            ".Lwp38:\n\t"
            "s_cbranch_execz .Lwp39\n\t"
            "s_add_u32 m0, %[ldsx], 65536\n\t"
            "s_add_u32 s68, %[xpl], 1792\n\t"
            "s_addc_u32 s69, %[xph], 0\n\t"
            "global_load_lds_dwordx4 %[x0], s[68:69] offset:0\n\t"
            "global_load_lds_dwordx4 %[x1], s[68:69] offset:1024\n\t"
            "global_load_lds_dwordx4 %[x2], s[68:69] offset:2048\n\t"
            "global_load_lds_dwordx4 %[x3], s[68:69] offset:3072\n\t"
            "s_add_u32 m0, %[ldsx], 69632\n\t"
            "s_add_u32 s68, %[xpl], 67328\n\t"
            "s_addc_u32 s69, %[xph], 0\n\t"
            "global_load_lds_dwordx4 %[x0], s[68:69] offset:0\n\t"
            "global_load_lds_dwordx4 %[x1], s[68:69] offset:1024\n\t"
            "global_load_lds_dwordx4 %[x2], s[68:69] offset:2048\n\t"
            "global_load_lds_dwordx4 %[x3], s[68:69] offset:3072\n\t"
            "s_add_u32 m0, %[ldsw], 32768\n\t"
            "s_add_u32 s70, s84, 6881280\n\t"
            "s_addc_u32 s71, s85, 0\n\t"
            "s_add_u32 s86, s84, 5963776\n\t"
            "s_addc_u32 s87, s85, 0\n\t"
            "global_load_lds_dwordx4 %[wv], s[70:71] offset:0 sc1\n\t"
            "global_load_lds_dwordx4 %[wv], s[86:87] offset:1024 sc1\n\t"
            "global_load_lds_dwordx4 %[wv], s[70:71] offset:2048 sc1\n\t"
            "global_load_lds_dwordx4 %[wv], s[86:87] offset:3072 sc1\n\t"
            "s_waitcnt vmcnt(4)\n\t"
            "s_barrier\n\t"
            "s_add_u32 m0, %[ldsx], 0\n\t"
            "s_add_u32 s68, %[xpl], 1920\n\t"
            "s_addc_u32 s69, %[xph], 0\n\t"
            "global_load_lds_dwordx4 %[x0], s[68:69] offset:0\n\t"
            "global_load_lds_dwordx4 %[x1], s[68:69] offset:1024\n\t"
            "global_load_lds_dwordx4 %[x2], s[68:69] offset:2048\n\t"
            "global_load_lds_dwordx4 %[x3], s[68:69] offset:3072\n\t"
            "s_add_u32 m0, %[ldsx], 4096\n\t"
            "s_add_u32 s68, %[xpl], 67456\n\t"
            "s_addc_u32 s69, %[xph], 0\n\t"
            "global_load_lds_dwordx4 %[x0], s[68:69] offset:0\n\t"
            "global_load_lds_dwordx4 %[x1], s[68:69] offset:1024\n\t"
            "global_load_lds_dwordx4 %[x2], s[68:69] offset:2048\n\t"
            "global_load_lds_dwordx4 %[x3], s[68:69] offset:3072\n\t"
            "s_add_u32 m0, %[ldsw], 0\n\t"
            "s_add_u32 s70, s84, 6423552\n\t"
            "s_addc_u32 s71, s85, 0\n\t"
            "s_add_u32 s86, s84, 6881280\n\t"
            "s_addc_u32 s87, s85, 0\n\t"
            "global_load_lds_dwordx4 %[wv], s[70:71] offset:0 sc1\n\t"
            "global_load_lds_dwordx4 %[wv], s[86:87] offset:1024 sc1\n\t"
            "global_load_lds_dwordx4 %[wv], s[70:71] offset:2048 sc1\n\t"
            "global_load_lds_dwordx4 %[wv], s[86:87] offset:3072 sc1\n\t"
            "s_waitcnt vmcnt(4)\n\t"
            "s_barrier\n\t"
            "s_mov_b32 s76, 16\n\t"
            ".Lzc13:\n\t"
            "v_cmp_eq_u32 vcc, v192, %[nvl]\n\t"
            "s_lshr_b64 s[74:75], vcc, s76\n\t"
            "s_and_b32 s74, s74, 65535\n\t"
            "s_cmp_eq_u32 s74, 65535\n\t"
            "s_cbranch_scc1 .Lzd13\n\t"
            "s_cmp_eq_u32 s73, 0\n\t"
            "s_cbranch_scc1 .Lzd13\n\t"
            "s_sub_u32 s73, s73, 1\n\t"
            "global_load_dword v192, %[po], %[ws] sc1\n\t"
            "s_waitcnt vmcnt(0)\n\t"
            "s_branch .Lzc13\n\t"
            ".Lzd13:\n\t"
            "s_waitcnt vmcnt(0)\n\t"
            "s_barrier\n\t"
            "s_branch .Lzend\n\t"
            ".Lzcls1:\n\t"
            ".Lwp39:\n\t"
            "s_cbranch_execz .Lwp40\n\t"
            "s_lshl_b32 s76, s60, 2\n\t"
            ".Lzc14:\n\t"
            "v_cmp_eq_u32 vcc, v192, %[nvl]\n\t"
            "s_lshr_b64 s[74:75], vcc, s76\n\t"
            "s_and_b32 s74, s74, 15\n\t"
            "s_cmp_eq_u32 s74, 15\n\t"
            "s_cbranch_scc1 .Lzd14\n\t"
            "s_cmp_eq_u32 s73, 0\n\t"
            "s_cbranch_scc1 .Lzd14\n\t"
            "s_sub_u32 s73, s73, 1\n\t"
            "global_load_dword v192, %[po], %[ws] sc1\n\t"
            "s_waitcnt vmcnt(0)\n\t"
            "s_branch .Lzc14\n\t"
            ".Lzd14:\n\t"
            "s_add_u32 m0, %[ldsw], 16384\n\t"
            "s_mul_i32 s76, s60, 458752\n\t"
            "s_add_u32 s70, s84, s76\n\t"
            "s_addc_u32 s71, s85, 0\n\t"
            "global_load_lds_dwordx4 %[wv], s[70:71] offset:0 sc1\n\t"
            "global_load_lds_dwordx4 %[wv], s[70:71] offset:1024 sc1\n\t"
            "global_load_lds_dwordx4 %[wv], s[70:71] offset:2048 sc1\n\t"
            "global_load_lds_dwordx4 %[wv], s[70:71] offset:3072 sc1\n\t"
            "s_waitcnt vmcnt(8)\n\t"
            "s_mov_b64 exec, 3\n\t"
            "global_store_dword %[fo0], %[nvl], %[ws] offset:64 sc1\n\t"
            "s_mov_b64 exec, -1\n\t"
            "s_add_u32 m0, %[ldsx], 32768\n\t"
            "s_lshl_b32 s76, s60, 7\n\t"
            "s_add_u32 s68, %[xpl], s76\n\t"
            "s_addc_u32 s69, %[xph], 0\n\t"
            "global_load_lds_dwordx4 %[x0], s[68:69] offset:0\n\t"
            "global_load_lds_dwordx4 %[x1], s[68:69] offset:1024\n\t"
            "global_load_lds_dwordx4 %[x2], s[68:69] offset:2048\n\t"
            "global_load_lds_dwordx4 %[x3], s[68:69] offset:3072\n\t"
            "s_add_u32 m0, %[ldsx], 36864\n\t"
            "s_lshl_b32 s76, s60, 7\n\t"
            "s_add_u32 s76, s76, 65536\n\t"
            "s_add_u32 s68, %[xpl], s76\n\t"
            "s_addc_u32 s69, %[xph], 0\n\t"
            "global_load_lds_dwordx4 %[x0], s[68:69] offset:0\n\t"
            "global_load_lds_dwordx4 %[x1], s[68:69] offset:1024\n\t"
            "global_load_lds_dwordx4 %[x2], s[68:69] offset:2048\n\t"
            "global_load_lds_dwordx4 %[x3], s[68:69] offset:3072\n\t"
            "s_waitcnt vmcnt(13)\n\t"
            "s_barrier\n\t"
            "global_load_dword v192, %[po], %[ws] sc1\n\t"
            "s_add_u32 m0, %[ldsx], 65536\n\t"
            "s_lshl_b32 s76, s61, 7\n\t"
            "s_add_u32 s68, %[xpl], s76\n\t"
            "s_addc_u32 s69, %[xph], 0\n\t"
            "global_load_lds_dwordx4 %[x0], s[68:69] offset:0\n\t"
            "global_load_lds_dwordx4 %[x1], s[68:69] offset:1024\n\t"
            "global_load_lds_dwordx4 %[x2], s[68:69] offset:2048\n\t"
            "global_load_lds_dwordx4 %[x3], s[68:69] offset:3072\n\t"
            "s_add_u32 m0, %[ldsx], 69632\n\t"
            "s_lshl_b32 s76, s61, 7\n\t"
            "s_add_u32 s76, s76, 65536\n\t"
            "s_add_u32 s68, %[xpl], s76\n\t"
            "s_addc_u32 s69, %[xph], 0\n\t"
            "global_load_lds_dwordx4 %[x0], s[68:69] offset:0\n\t"
            "global_load_lds_dwordx4 %[x1], s[68:69] offset:1024\n\t"
            "global_load_lds_dwordx4 %[x2], s[68:69] offset:2048\n\t"
            "global_load_lds_dwordx4 %[x3], s[68:69] offset:3072\n\t"
            "s_waitcnt vmcnt(8)\n\t"
            "s_lshl_b32 s76, s61, 2\n\t"
            ".Lzc15:\n\t"
            "v_cmp_eq_u32 vcc, v192, %[nvl]\n\t"
            "s_lshr_b64 s[74:75], vcc, s76\n\t"
            "s_and_b32 s74, s74, 15\n\t"
            "s_cmp_eq_u32 s74, 15\n\t"
            "s_cbranch_scc1 .Lzd15\n\t"
            "s_cmp_eq_u32 s73, 0\n\t"
            "s_cbranch_scc1 .Lzd15\n\t"
            "s_sub_u32 s73, s73, 1\n\t"
            "global_load_dword v192, %[po], %[ws] sc1\n\t"
            "s_waitcnt vmcnt(0)\n\t"
            "s_branch .Lzc15\n\t"
            ".Lzd15:\n\t"
            "s_add_u32 m0, %[ldsw], 32768\n\t"
            "s_mul_i32 s76, s61, 458752\n\t"
            "s_add_u32 s70, s84, s76\n\t"
            "s_addc_u32 s71, s85, 0\n\t"
            "global_load_lds_dwordx4 %[wv], s[70:71] offset:0 sc1\n\t"
            "global_load_lds_dwordx4 %[wv], s[70:71] offset:1024 sc1\n\t"
            "global_load_lds_dwordx4 %[wv], s[70:71] offset:2048 sc1\n\t"
            "global_load_lds_dwordx4 %[wv], s[70:71] offset:3072 sc1\n\t"
            "s_barrier\n\t"
            ".Lwp40:\n\t"
            "s_cbranch_execz .Lwp41\n\t"
            "s_lshl_b32 s76, s62, 2\n\t"
            ".Lzc16:\n\t"
            "v_cmp_eq_u32 vcc, v192, %[nvl]\n\t"
            "s_lshr_b64 s[74:75], vcc, s76\n\t"
            "s_and_b32 s74, s74, 15\n\t"
            "s_cmp_eq_u32 s74, 15\n\t"
            "s_cbranch_scc1 .Lzd16\n\t"
            "s_cmp_eq_u32 s73, 0\n\t"
            "s_cbranch_scc1 .Lzd16\n\t"
            "s_sub_u32 s73, s73, 1\n\t"
            "global_load_dword v192, %[po], %[ws] sc1\n\t"
            "s_waitcnt vmcnt(0)\n\t"
            "s_branch .Lzc16\n\t"
            ".Lzd16:\n\t"
            "s_add_u32 m0, %[ldsw], 0\n\t"
            "s_mul_i32 s76, s62, 458752\n\t"
            "s_add_u32 s70, s84, s76\n\t"
            "s_addc_u32 s71, s85, 0\n\t"
            "global_load_lds_dwordx4 %[wv], s[70:71] offset:0 sc1\n\t"
            "global_load_lds_dwordx4 %[wv], s[70:71] offset:1024 sc1\n\t"
            "global_load_lds_dwordx4 %[wv], s[70:71] offset:2048 sc1\n\t"
            "global_load_lds_dwordx4 %[wv], s[70:71] offset:3072 sc1\n\t"
            "s_add_u32 m0, %[ldsx], 0\n\t"
            "s_lshl_b32 s76, s62, 7\n\t"
            "s_add_u32 s68, %[xpl], s76\n\t"
            "s_addc_u32 s69, %[xph], 0\n\t"
            "global_load_lds_dwordx4 %[x0], s[68:69] offset:0\n\t"
            "global_load_lds_dwordx4 %[x1], s[68:69] offset:1024\n\t"
            "global_load_lds_dwordx4 %[x2], s[68:69] offset:2048\n\t"
            "global_load_lds_dwordx4 %[x3], s[68:69] offset:3072\n\t"
            "s_add_u32 m0, %[ldsx], 4096\n\t"
            "s_lshl_b32 s76, s62, 7\n\t"
            "s_add_u32 s76, s76, 65536\n\t"
            "s_add_u32 s68, %[xpl], s76\n\t"
            "s_addc_u32 s69, %[xph], 0\n\t"
            "global_load_lds_dwordx4 %[x0], s[68:69] offset:0\n\t"
            "global_load_lds_dwordx4 %[x1], s[68:69] offset:1024\n\t"
            "global_load_lds_dwordx4 %[x2], s[68:69] offset:2048\n\t"
            "global_load_lds_dwordx4 %[x3], s[68:69] offset:3072\n\t"
            "s_waitcnt vmcnt(12)\n\t"
            "s_barrier\n\t"
            "s_lshl_b32 s76, s63, 2\n\t"
            ".Lzc17:\n\t"
            "v_cmp_eq_u32 vcc, v192, %[nvl]\n\t"
            "s_lshr_b64 s[74:75], vcc, s76\n\t"
            "s_and_b32 s74, s74, 15\n\t"
            "s_cmp_eq_u32 s74, 15\n\t"
            "s_cbranch_scc1 .Lzd17\n\t"
            "s_cmp_eq_u32 s73, 0\n\t"
            "s_cbranch_scc1 .Lzd17\n\t"
            "s_sub_u32 s73, s73, 1\n\t"
            "global_load_dword v192, %[po], %[ws] sc1\n\t"
            "s_waitcnt vmcnt(0)\n\t"
            "s_branch .Lzc17\n\t"
            ".Lzd17:\n\t"
            "s_add_u32 m0, %[ldsw], 16384\n\t"
            "s_mul_i32 s76, s63, 458752\n\t"
            "s_add_u32 s70, s84, s76\n\t"
            "s_addc_u32 s71, s85, 0\n\t"
            "global_load_lds_dwordx4 %[wv], s[70:71] offset:0 sc1\n\t"
            "global_load_lds_dwordx4 %[wv], s[70:71] offset:1024 sc1\n\t"
            "global_load_lds_dwordx4 %[wv], s[70:71] offset:2048 sc1\n\t"
            "global_load_lds_dwordx4 %[wv], s[70:71] offset:3072 sc1\n\t"
            "s_add_u32 m0, %[ldsx], 32768\n\t"
            "s_lshl_b32 s76, s63, 7\n\t"
            "s_add_u32 s68, %[xpl], s76\n\t"
            "s_addc_u32 s69, %[xph], 0\n\t"
            "global_load_lds_dwordx4 %[x0], s[68:69] offset:0\n\t"
            "global_load_lds_dwordx4 %[x1], s[68:69] offset:1024\n\t"
            "global_load_lds_dwordx4 %[x2], s[68:69] offset:2048\n\t"
            "global_load_lds_dwordx4 %[x3], s[68:69] offset:3072\n\t"
            "s_add_u32 m0, %[ldsx], 36864\n\t"
            "s_lshl_b32 s76, s63, 7\n\t"
            "s_add_u32 s76, s76, 65536\n\t"
            "s_add_u32 s68, %[xpl], s76\n\t"
            "s_addc_u32 s69, %[xph], 0\n\t"
            "global_load_lds_dwordx4 %[x0], s[68:69] offset:0\n\t"
            "global_load_lds_dwordx4 %[x1], s[68:69] offset:1024\n\t"
            "global_load_lds_dwordx4 %[x2], s[68:69] offset:2048\n\t"
            "global_load_lds_dwordx4 %[x3], s[68:69] offset:3072\n\t"
            "s_mov_b64 s[88:89], %[qbg]\n\t"
            "global_load_dwordx4 v[128:131], %[qo], s[88:89] \n\t"
            "s_add_u32 s88, s88, 14336\n\t"
            "s_addc_u32 s89, s89, 0\n\t"
            "global_load_dwordx4 v[132:135], %[qo], s[88:89] \n\t"
            "s_add_u32 s88, s88, 14336\n\t"
            "s_addc_u32 s89, s89, 0\n\t"
            "global_load_dwordx4 v[136:139], %[qo], s[88:89] \n\t"
            "s_add_u32 s88, s88, 14336\n\t"
            "s_addc_u32 s89, s89, 0\n\t"
            "global_load_dwordx4 v[140:143], %[qo], s[88:89] \n\t"
            "s_add_u32 s88, s88, 14336\n\t"
            "s_addc_u32 s89, s89, 0\n\t"
            "global_load_dwordx4 v[144:147], %[qo], s[88:89] \n\t"
            "s_add_u32 s88, s88, 14336\n\t"
            "s_addc_u32 s89, s89, 0\n\t"
            "global_load_dwordx4 v[148:151], %[qo], s[88:89] \n\t"
            "s_add_u32 s88, s88, 14336\n\t"
            "s_addc_u32 s89, s89, 0\n\t"
            "global_load_dwordx4 v[152:155], %[qo], s[88:89] \n\t"
            "s_add_u32 s88, s88, 14336\n\t"
            "s_addc_u32 s89, s89, 0\n\t"
            "global_load_dwordx4 v[156:159], %[qo], s[88:89] \n\t"
            "s_waitcnt vmcnt(20)\n\t"
            "s_barrier\n\t"
            ".Lwp41:\n\t"
            "s_cbranch_execz .Lwp97\n\t"
            "global_load_dword v192, %[po], %[ws] sc1\n\t"
            "s_add_u32 m0, %[ldsx], 65536\n\t"
            "s_add_u32 s68, %[xpl], 1024\n\t"
            "s_addc_u32 s69, %[xph], 0\n\t"
            "global_load_lds_dwordx4 %[x0], s[68:69] offset:0\n\t"
            "global_load_lds_dwordx4 %[x1], s[68:69] offset:1024\n\t"
            "global_load_lds_dwordx4 %[x2], s[68:69] offset:2048\n\t"
            "global_load_lds_dwordx4 %[x3], s[68:69] offset:3072\n\t"
            "s_add_u32 m0, %[ldsx], 69632\n\t"
            "s_add_u32 s68, %[xpl], 66560\n\t"
            "s_addc_u32 s69, %[xph], 0\n\t"
            "global_load_lds_dwordx4 %[x0], s[68:69] offset:0\n\t"
            "global_load_lds_dwordx4 %[x1], s[68:69] offset:1024\n\t"
            "global_load_lds_dwordx4 %[x2], s[68:69] offset:2048\n\t"
            "global_load_lds_dwordx4 %[x3], s[68:69] offset:3072\n\t"
            "s_waitcnt vmcnt(8)\n\t"
            "s_mov_b32 s76, 32\n\t"
            ".Lzc18:\n\t"
            "v_cmp_eq_u32 vcc, v192, %[nvl]\n\t"
            "s_lshr_b64 s[74:75], vcc, s76\n\t"
            "s_and_b32 s74, s74, 15\n\t"
            "s_cmp_eq_u32 s74, 15\n\t"
            "s_cbranch_scc1 .Lzd18\n\t"
            "s_cmp_eq_u32 s73, 0\n\t"
            "s_cbranch_scc1 .Lzd18\n\t"
            "s_sub_u32 s73, s73, 1\n\t"
            "global_load_dword v192, %[po], %[ws] sc1\n\t"
            "s_waitcnt vmcnt(0)\n\t"
            "s_branch .Lzc18\n\t"
            ".Lzd18:\n\t"
            "s_add_u32 m0, %[ldsw], 32768\n\t"
            "s_add_u32 s70, s84, 3670016\n\t"
            "s_addc_u32 s71, s85, 0\n\t"
            "global_load_lds_dwordx4 %[wv], s[70:71] offset:0 sc1\n\t"
            "global_load_lds_dwordx4 %[wv], s[70:71] offset:1024 sc1\n\t"
            "global_load_lds_dwordx4 %[wv], s[70:71] offset:2048 sc1\n\t"
            "global_load_lds_dwordx4 %[wv], s[70:71] offset:3072 sc1\n\t"
            "s_setprio 0\n\t"
            "v_lshl_add_u32 v160, v128, 8, %[lutl]\n\t"
            "ds_read_u16 v160, v160 offset:0\n\t"
            "v_lshl_add_u32 v161, v132, 8, %[lutl]\n\t"
            "ds_read_u16 v161, v161 offset:0\n\t"
            "v_lshl_add_u32 v162, v136, 8, %[lutl]\n\t"
            "ds_read_u16 v162, v162 offset:0\n\t"
            "v_lshl_add_u32 v163, v140, 8, %[lutl]\n\t"
            "ds_read_u16 v163, v163 offset:0\n\t"
            "v_lshl_add_u32 v164, v144, 8, %[lutl]\n\t"
            "ds_read_u16 v164, v164 offset:0\n\t"
            "v_lshl_add_u32 v165, v148, 8, %[lutl]\n\t"
            "ds_read_u16 v165, v165 offset:0\n\t"
            "v_lshl_add_u32 v166, v152, 8, %[lutl]\n\t"
            "ds_read_u16 v166, v166 offset:0\n\t"
            "v_lshl_add_u32 v167, v156, 8, %[lutl]\n\t"
            "ds_read_u16 v167, v167 offset:0\n\t"
            "v_lshl_add_u32 v168, v129, 8, %[lutl]\n\t"
            "ds_read_u16 v168, v168 offset:2\n\t"
            "v_lshl_add_u32 v169, v133, 8, %[lutl]\n\t"
            "ds_read_u16 v169, v169 offset:2\n\t"
            "v_lshl_add_u32 v170, v137, 8, %[lutl]\n\t"
            "ds_read_u16 v170, v170 offset:2\n\t"
            "v_lshl_add_u32 v171, v141, 8, %[lutl]\n\t"
            "ds_read_u16 v171, v171 offset:2\n\t"
            "v_lshl_add_u32 v172, v145, 8, %[lutl]\n\t"
            "ds_read_u16 v172, v172 offset:2\n\t"
            "v_lshl_add_u32 v173, v149, 8, %[lutl]\n\t"
            "ds_read_u16 v173, v173 offset:2\n\t"
            "v_lshl_add_u32 v174, v153, 8, %[lutl]\n\t"
            "ds_read_u16 v174, v174 offset:2\n\t"
            "v_lshl_add_u32 v175, v157, 8, %[lutl]\n\t"
            "ds_read_u16 v175, v175 offset:2\n\t"
            "s_setprio 0\n\t"
            "s_barrier\n\t"
            ".Lwp42:\n\t"
            "s_cbranch_execz .Lwp43\n\t"
            "s_mov_b32 s76, 36\n\t"
            ".Lzc19:\n\t"
            "v_cmp_eq_u32 vcc, v192, %[nvl]\n\t"
            "s_lshr_b64 s[74:75], vcc, s76\n\t"
            "s_and_b32 s74, s74, 15\n\t"
            "s_cmp_eq_u32 s74, 15\n\t"
            "s_cbranch_scc1 .Lzd19\n\t"
            "s_cmp_eq_u32 s73, 0\n\t"
            "s_cbranch_scc1 .Lzd19\n\t"
            "s_sub_u32 s73, s73, 1\n\t"
            "global_load_dword v192, %[po], %[ws] sc1\n\t"
            "s_waitcnt vmcnt(0)\n\t"
            "s_branch .Lzc19\n\t"
            ".Lzd19:\n\t"
            "s_add_u32 m0, %[ldsw], 0\n\t"
            "s_add_u32 s70, s84, 4128768\n\t"
            "s_addc_u32 s71, s85, 0\n\t"
            "global_load_lds_dwordx4 %[wv], s[70:71] offset:0 sc1\n\t"
            "global_load_lds_dwordx4 %[wv], s[70:71] offset:1024 sc1\n\t"
            "global_load_lds_dwordx4 %[wv], s[70:71] offset:2048 sc1\n\t"
            "global_load_lds_dwordx4 %[wv], s[70:71] offset:3072 sc1\n\t"
            "s_setprio 0\n\t"
            "s_add_u32 m0, %[ldsx], 0\n\t"
            "s_add_u32 s68, %[xpl], 1152\n\t"
            "s_addc_u32 s69, %[xph], 0\n\t"
            "global_load_lds_dwordx4 %[x0], s[68:69] offset:0\n\t"
            "v_lshl_add_u32 v176, v130, 8, %[lutl]\n\t"
            "ds_read_u16 v176, v176 offset:128\n\t"
            "v_lshl_add_u32 v177, v134, 8, %[lutl]\n\t"
            "ds_read_u16 v177, v177 offset:128\n\t"
            "global_load_lds_dwordx4 %[x1], s[68:69] offset:1024\n\t"
            "v_lshl_add_u32 v178, v138, 8, %[lutl]\n\t"
            "ds_read_u16 v178, v178 offset:128\n\t"
            "v_lshl_add_u32 v179, v142, 8, %[lutl]\n\t"
            "ds_read_u16 v179, v179 offset:128\n\t"
            "global_load_lds_dwordx4 %[x2], s[68:69] offset:2048\n\t"
            "v_lshl_add_u32 v180, v146, 8, %[lutl]\n\t"
            "ds_read_u16 v180, v180 offset:128\n\t"
            "v_lshl_add_u32 v181, v150, 8, %[lutl]\n\t"
            "ds_read_u16 v181, v181 offset:128\n\t"
            "global_load_lds_dwordx4 %[x3], s[68:69] offset:3072\n\t"
            "v_lshl_add_u32 v182, v154, 8, %[lutl]\n\t"
            "ds_read_u16 v182, v182 offset:128\n\t"
            "v_lshl_add_u32 v183, v158, 8, %[lutl]\n\t"
            "ds_read_u16 v183, v183 offset:128\n\t"
            "s_add_u32 m0, %[ldsx], 4096\n\t"
            "s_add_u32 s68, %[xpl], 66688\n\t"
            "s_addc_u32 s69, %[xph], 0\n\t"
            "global_load_lds_dwordx4 %[x0], s[68:69] offset:0\n\t"
            "v_lshl_add_u32 v184, v131, 8, %[lutl]\n\t"
            "ds_read_u16 v184, v184 offset:130\n\t"
            "v_lshl_add_u32 v185, v135, 8, %[lutl]\n\t"
            "ds_read_u16 v185, v185 offset:130\n\t"
            "global_load_lds_dwordx4 %[x1], s[68:69] offset:1024\n\t"
            "v_lshl_add_u32 v186, v139, 8, %[lutl]\n\t"
            "ds_read_u16 v186, v186 offset:130\n\t"
            "v_lshl_add_u32 v187, v143, 8, %[lutl]\n\t"
            "ds_read_u16 v187, v187 offset:130\n\t"
            "global_load_lds_dwordx4 %[x2], s[68:69] offset:2048\n\t"
            "v_lshl_add_u32 v188, v147, 8, %[lutl]\n\t"
            "ds_read_u16 v188, v188 offset:130\n\t"
            "v_lshl_add_u32 v189, v151, 8, %[lutl]\n\t"
            "ds_read_u16 v189, v189 offset:130\n\t"
            "global_load_lds_dwordx4 %[x3], s[68:69] offset:3072\n\t"
            "v_lshl_add_u32 v190, v155, 8, %[lutl]\n\t"
            "ds_read_u16 v190, v190 offset:130\n\t"
            "v_lshl_add_u32 v191, v159, 8, %[lutl]\n\t"
            "ds_read_u16 v191, v191 offset:130\n\t"
            "s_setprio 0\n\t"
            "s_waitcnt lgkmcnt(15)\n\t"
            "v_lshl_or_b32 v128, v161, 16, v160\n\t"
            "v_lshl_or_b32 v129, v163, 16, v162\n\t"
            "v_lshl_or_b32 v130, v165, 16, v164\n\t"
            "v_lshl_or_b32 v131, v167, 16, v166\n\t"
            "s_waitcnt lgkmcnt(15)\n\t"
            "v_lshl_or_b32 v132, v169, 16, v168\n\t"
            "v_lshl_or_b32 v133, v171, 16, v170\n\t"
            "v_lshl_or_b32 v134, v173, 16, v172\n\t"
            "v_lshl_or_b32 v135, v175, 16, v174\n\t"
            "s_waitcnt lgkmcnt(8)\n\t"
            "v_lshl_or_b32 v136, v177, 16, v176\n\t"
            "v_lshl_or_b32 v137, v179, 16, v178\n\t"
            "v_lshl_or_b32 v138, v181, 16, v180\n\t"
            "v_lshl_or_b32 v139, v183, 16, v182\n\t"
            "s_waitcnt lgkmcnt(0)\n\t"
            "v_lshl_or_b32 v140, v185, 16, v184\n\t"
            "v_lshl_or_b32 v141, v187, 16, v186\n\t"
            "v_lshl_or_b32 v142, v189, 16, v188\n\t"
            "v_lshl_or_b32 v143, v191, 16, v190\n\t"
            "global_store_dwordx4 %[wo], v[128:131], %[stb] offset:0 sc1\n\t"
            "global_store_dwordx4 %[wo], v[132:135], %[stb] offset:256 sc1\n\t"
            "global_store_dwordx4 %[wo], v[136:139], %[stb] offset:512 sc1\n\t"
            "global_store_dwordx4 %[wo], v[140:143], %[stb] offset:768 sc1\n\t"
            "s_waitcnt vmcnt(16)\n\t"
            "s_barrier\n\t"
            "s_mov_b32 s76, 40\n\t"
            ".Lzc20:\n\t"
            "v_cmp_eq_u32 vcc, v192, %[nvl]\n\t"
            "s_lshr_b64 s[74:75], vcc, s76\n\t"
            "s_and_b32 s74, s74, 15\n\t"
            "s_cmp_eq_u32 s74, 15\n\t"
            "s_cbranch_scc1 .Lzd20\n\t"
            "s_cmp_eq_u32 s73, 0\n\t"
            "s_cbranch_scc1 .Lzd20\n\t"
            "s_sub_u32 s73, s73, 1\n\t"
            "global_load_dword v192, %[po], %[ws] sc1\n\t"
            "s_waitcnt vmcnt(0)\n\t"
            "s_branch .Lzc20\n\t"
            ".Lzd20:\n\t"
            "s_add_u32 m0, %[ldsw], 16384\n\t"
            "s_add_u32 s70, s84, 4587520\n\t"
            "s_addc_u32 s71, s85, 0\n\t"
            "global_load_lds_dwordx4 %[wv], s[70:71] offset:0 sc1\n\t"
            "global_load_lds_dwordx4 %[wv], s[70:71] offset:1024 sc1\n\t"
            "global_load_lds_dwordx4 %[wv], s[70:71] offset:2048 sc1\n\t"
            "global_load_lds_dwordx4 %[wv], s[70:71] offset:3072 sc1\n\t"
            "s_add_u32 m0, %[ldsx], 32768\n\t"
            "s_add_u32 s68, %[xpl], 1280\n\t"
            "s_addc_u32 s69, %[xph], 0\n\t"
            "global_load_lds_dwordx4 %[x0], s[68:69] offset:0\n\t"
            "global_load_lds_dwordx4 %[x1], s[68:69] offset:1024\n\t"
            "global_load_lds_dwordx4 %[x2], s[68:69] offset:2048\n\t"
            "global_load_lds_dwordx4 %[x3], s[68:69] offset:3072\n\t"
            "s_add_u32 m0, %[ldsx], 36864\n\t"
            "s_add_u32 s68, %[xpl], 66816\n\t"
            "s_addc_u32 s69, %[xph], 0\n\t"
            "global_load_lds_dwordx4 %[x0], s[68:69] offset:0\n\t"
            "global_load_lds_dwordx4 %[x1], s[68:69] offset:1024\n\t"
            "global_load_lds_dwordx4 %[x2], s[68:69] offset:2048\n\t"
            "global_load_lds_dwordx4 %[x3], s[68:69] offset:3072\n\t"
            "s_waitcnt vmcnt(12)\n\t"
            "s_mov_b64 exec, 15\n\t"
            "global_store_dword %[fob], %[nvl], %[ws] offset:0 sc1\n\t"
            "s_mov_b64 exec, -1\n\t"
            "s_barrier\n\t"
            "s_mov_b32 s76, 44\n\t"
            ".Lzc21:\n\t"
            "v_cmp_eq_u32 vcc, v192, %[nvl]\n\t"
            "s_lshr_b64 s[74:75], vcc, s76\n\t"
            "s_and_b32 s74, s74, 15\n\t"
            "s_cmp_eq_u32 s74, 15\n\t"
            "s_cbranch_scc1 .Lzd21\n\t"
            "s_cmp_eq_u32 s73, 0\n\t"
            "s_cbranch_scc1 .Lzd21\n\t"
            "s_sub_u32 s73, s73, 1\n\t"
            "global_load_dword v192, %[po], %[ws] sc1\n\t"
            "s_waitcnt vmcnt(0)\n\t"
            "s_branch .Lzc21\n\t"
            ".Lzd21:\n\t"
            "s_add_u32 m0, %[ldsw], 32768\n\t"
            "s_add_u32 s70, s84, 5046272\n\t"
            "s_addc_u32 s71, s85, 0\n\t"
            "global_load_lds_dwordx4 %[wv], s[70:71] offset:0 sc1\n\t"
            "global_load_lds_dwordx4 %[wv], s[70:71] offset:1024 sc1\n\t"
            "global_load_lds_dwordx4 %[wv], s[70:71] offset:2048 sc1\n\t"
            "global_load_lds_dwordx4 %[wv], s[70:71] offset:3072 sc1\n\t"
            "s_add_u32 m0, %[ldsx], 65536\n\t"
            "s_add_u32 s68, %[xpl], 1408\n\t"
            "s_addc_u32 s69, %[xph], 0\n\t"
            "global_load_lds_dwordx4 %[x0], s[68:69] offset:0\n\t"
            "global_load_lds_dwordx4 %[x1], s[68:69] offset:1024\n\t"
            "global_load_lds_dwordx4 %[x2], s[68:69] offset:2048\n\t"
            "global_load_lds_dwordx4 %[x3], s[68:69] offset:3072\n\t"
            "s_add_u32 m0, %[ldsx], 69632\n\t"
            "s_add_u32 s68, %[xpl], 66944\n\t"
            "s_addc_u32 s69, %[xph], 0\n\t"
            "global_load_lds_dwordx4 %[x0], s[68:69] offset:0\n\t"
            "global_load_lds_dwordx4 %[x1], s[68:69] offset:1024\n\t"
            "global_load_lds_dwordx4 %[x2], s[68:69] offset:2048\n\t"
            "global_load_lds_dwordx4 %[x3], s[68:69] offset:3072\n\t"
            "s_waitcnt vmcnt(13)\n\t"
            "s_barrier\n\t"
            ".Lwp43:\n\t"
            "s_cbranch_execz .Lwp44\n\t"
            "global_load_dword v192, %[po], %[ws] sc1\n\t"
            "s_add_u32 m0, %[ldsx], 0\n\t"
            "s_add_u32 s68, %[xpl], 1536\n\t"
            "s_addc_u32 s69, %[xph], 0\n\t"
            "global_load_lds_dwordx4 %[x0], s[68:69] offset:0\n\t"
            "global_load_lds_dwordx4 %[x1], s[68:69] offset:1024\n\t"
            "global_load_lds_dwordx4 %[x2], s[68:69] offset:2048\n\t"
            "global_load_lds_dwordx4 %[x3], s[68:69] offset:3072\n\t"
            "s_add_u32 m0, %[ldsx], 4096\n\t"
            "s_add_u32 s68, %[xpl], 67072\n\t"
            "s_addc_u32 s69, %[xph], 0\n\t"
            "global_load_lds_dwordx4 %[x0], s[68:69] offset:0\n\t"
            "global_load_lds_dwordx4 %[x1], s[68:69] offset:1024\n\t"
            "global_load_lds_dwordx4 %[x2], s[68:69] offset:2048\n\t"
            "global_load_lds_dwordx4 %[x3], s[68:69] offset:3072\n\t"
            "s_waitcnt vmcnt(8)\n\t"
            "s_mov_b32 s76, 48\n\t"
            ".Lzc22:\n\t"
            "v_cmp_eq_u32 vcc, v192, %[nvl]\n\t"
            "s_lshr_b64 s[74:75], vcc, s76\n\t"
            "s_and_b32 s74, s74, 15\n\t"
            "s_cmp_eq_u32 s74, 15\n\t"
            "s_cbranch_scc1 .Lzd22\n\t"
            "s_cmp_eq_u32 s73, 0\n\t"
            "s_cbranch_scc1 .Lzd22\n\t"
            "s_sub_u32 s73, s73, 1\n\t"
            "global_load_dword v192, %[po], %[ws] sc1\n\t"
            "s_waitcnt vmcnt(0)\n\t"
            "s_branch .Lzc22\n\t"
            ".Lzd22:\n\t"
            "s_add_u32 m0, %[ldsw], 0\n\t"
            "s_add_u32 s70, s84, 5505024\n\t"
            "s_addc_u32 s71, s85, 0\n\t"
            "global_load_lds_dwordx4 %[wv], s[70:71] offset:0 sc1\n\t"
            "global_load_lds_dwordx4 %[wv], s[70:71] offset:1024 sc1\n\t"
            "global_load_lds_dwordx4 %[wv], s[70:71] offset:2048 sc1\n\t"
            "global_load_lds_dwordx4 %[wv], s[70:71] offset:3072 sc1\n\t"
            "s_barrier\n\t"
            "s_mov_b32 s76, 52\n\t"
            ".Lzc23:\n\t"
            "v_cmp_eq_u32 vcc, v192, %[nvl]\n\t"
            "s_lshr_b64 s[74:75], vcc, s76\n\t"
            "s_and_b32 s74, s74, 4095\n\t"
            "s_cmp_eq_u32 s74, 4095\n\t"
            "s_cbranch_scc1 .Lzd23\n\t"
            "s_cmp_eq_u32 s73, 0\n\t"
            "s_cbranch_scc1 .Lzd23\n\t"
            "s_sub_u32 s73, s73, 1\n\t"
            "global_load_dword v192, %[po], %[ws] sc1\n\t"
            "s_waitcnt vmcnt(0)\n\t"
            "s_branch .Lzc23\n\t"
            ".Lzd23:\n\t"
            "s_add_u32 m0, %[ldsw], 16384\n\t"
            "s_add_u32 s70, s84, 5963776\n\t"
            "s_addc_u32 s71, s85, 0\n\t"
            "s_add_u32 s86, s84, 6421504\n\t"
            "s_addc_u32 s87, s85, 0\n\t"
            "global_load_lds_dwordx4 %[wv], s[70:71] offset:0 sc1\n\t"
            "global_load_lds_dwordx4 %[wv], s[86:87] offset:1024 sc1\n\t"
            "global_load_lds_dwordx4 %[wv], s[70:71] offset:2048 sc1\n\t"
            "global_load_lds_dwordx4 %[wv], s[86:87] offset:3072 sc1\n\t"
            "s_add_u32 m0, %[ldsx], 32768\n\t"
            "s_add_u32 s68, %[xpl], 1664\n\t"
            "s_addc_u32 s69, %[xph], 0\n\t"
            "global_load_lds_dwordx4 %[x0], s[68:69] offset:0\n\t"
            "global_load_lds_dwordx4 %[x1], s[68:69] offset:1024\n\t"
            "global_load_lds_dwordx4 %[x2], s[68:69] offset:2048\n\t"
            "global_load_lds_dwordx4 %[x3], s[68:69] offset:3072\n\t"
            "s_add_u32 m0, %[ldsx], 36864\n\t"
            "s_add_u32 s68, %[xpl], 67200\n\t"
            "s_addc_u32 s69, %[xph], 0\n\t"
            "global_load_lds_dwordx4 %[x0], s[68:69] offset:0\n\t"
            "global_load_lds_dwordx4 %[x1], s[68:69] offset:1024\n\t"
            "global_load_lds_dwordx4 %[x2], s[68:69] offset:2048\n\t"
            "global_load_lds_dwordx4 %[x3], s[68:69] offset:3072\n\t"
            "s_waitcnt vmcnt(12)\n\t"
            "s_barrier\n\t"
            ".Lwp44:\n\t"
            "s_cbranch_execz .Lwp14\n\t"
            "s_add_u32 m0, %[ldsx], 65536\n\t"
            "s_add_u32 s68, %[xpl], 1792\n\t"
            "s_addc_u32 s69, %[xph], 0\n\t"
            "global_load_lds_dwordx4 %[x0], s[68:69] offset:0\n\t"
            "global_load_lds_dwordx4 %[x1], s[68:69] offset:1024\n\t"
            "global_load_lds_dwordx4 %[x2], s[68:69] offset:2048\n\t"
            "global_load_lds_dwordx4 %[x3], s[68:69] offset:3072\n\t"
            "s_add_u32 m0, %[ldsx], 69632\n\t"
            "s_add_u32 s68, %[xpl], 67328\n\t"
            "s_addc_u32 s69, %[xph], 0\n\t"
            "global_load_lds_dwordx4 %[x0], s[68:69] offset:0\n\t"
            "global_load_lds_dwordx4 %[x1], s[68:69] offset:1024\n\t"
            "global_load_lds_dwordx4 %[x2], s[68:69] offset:2048\n\t"
            "global_load_lds_dwordx4 %[x3], s[68:69] offset:3072\n\t"
            "s_add_u32 m0, %[ldsw], 32768\n\t"
            "s_add_u32 s70, s84, 6881280\n\t"
            "s_addc_u32 s71, s85, 0\n\t"
            "s_add_u32 s86, s84, 5963776\n\t"
            "s_addc_u32 s87, s85, 0\n\t"
            "global_load_lds_dwordx4 %[wv], s[70:71] offset:0 sc1\n\t"
            "global_load_lds_dwordx4 %[wv], s[86:87] offset:1024 sc1\n\t"
            "global_load_lds_dwordx4 %[wv], s[70:71] offset:2048 sc1\n\t"
            "global_load_lds_dwordx4 %[wv], s[86:87] offset:3072 sc1\n\t"
            "s_waitcnt vmcnt(4)\n\t"
            "s_barrier\n\t"
            "s_add_u32 m0, %[ldsx], 0\n\t"
            "s_add_u32 s68, %[xpl], 1920\n\t"
            "s_addc_u32 s69, %[xph], 0\n\t"
            "global_load_lds_dwordx4 %[x0], s[68:69] offset:0\n\t"
            "global_load_lds_dwordx4 %[x1], s[68:69] offset:1024\n\t"
            "global_load_lds_dwordx4 %[x2], s[68:69] offset:2048\n\t"
            "global_load_lds_dwordx4 %[x3], s[68:69] offset:3072\n\t"
            "s_add_u32 m0, %[ldsx], 4096\n\t"
            "s_add_u32 s68, %[xpl], 67456\n\t"
            "s_addc_u32 s69, %[xph], 0\n\t"
            "global_load_lds_dwordx4 %[x0], s[68:69] offset:0\n\t"
            "global_load_lds_dwordx4 %[x1], s[68:69] offset:1024\n\t"
            "global_load_lds_dwordx4 %[x2], s[68:69] offset:2048\n\t"
            "global_load_lds_dwordx4 %[x3], s[68:69] offset:3072\n\t"
            "s_add_u32 m0, %[ldsw], 0\n\t"
            "s_add_u32 s70, s84, 6423552\n\t"
            "s_addc_u32 s71, s85, 0\n\t"
            "s_add_u32 s86, s84, 6881280\n\t"
            "s_addc_u32 s87, s85, 0\n\t"
            "global_load_lds_dwordx4 %[wv], s[70:71] offset:0 sc1\n\t"
            "global_load_lds_dwordx4 %[wv], s[86:87] offset:1024 sc1\n\t"
            "global_load_lds_dwordx4 %[wv], s[70:71] offset:2048 sc1\n\t"
            "global_load_lds_dwordx4 %[wv], s[86:87] offset:3072 sc1\n\t"
            "s_waitcnt vmcnt(4)\n\t"
            "s_barrier\n\t"
            "s_mov_b32 s76, 16\n\t"
            ".Lzc24:\n\t"
            "v_cmp_eq_u32 vcc, v192, %[nvl]\n\t"
            "s_lshr_b64 s[74:75], vcc, s76\n\t"
            "s_and_b32 s74, s74, 65535\n\t"
            "s_cmp_eq_u32 s74, 65535\n\t"
            "s_cbranch_scc1 .Lzd24\n\t"
            "s_cmp_eq_u32 s73, 0\n\t"
            "s_cbranch_scc1 .Lzd24\n\t"
            "s_sub_u32 s73, s73, 1\n\t"
            "global_load_dword v192, %[po], %[ws] sc1\n\t"
            "s_waitcnt vmcnt(0)\n\t"
            "s_branch .Lzc24\n\t"
            ".Lzd24:\n\t"
            "s_waitcnt vmcnt(0)\n\t"
            "s_barrier\n\t"
            ".Lzend:\n\t"
            : [nvl] "+v"(nvl)
            : [x0] "v"(xoff[0]), [x1] "v"(xoff[1]), [x2] "v"(xoff[2]), [x3] "v"(xoff[3]), [wv] "v"(wv), [po] "v"(po), [qo] "v"(qoff[0]),
              [lutl] "v"(lutl), [wo] "v"(wo), [fo0] "v"(fo0), [fob] "v"(fob), [bd] "v"(bdst),
              [xpl] "s"((u32)KPTR(0)), [xph] "s"((u32)(KPTR(0) >> 32)), [ws] "s"(KPTR(4)),
              [qbg] "s"(qbg), [qa1] "s"(qa1), [tseq] "s"(tseq), [p] "s"(p), [h2] "s"(h2), [ldsx] "s"(ldsx), [ldsw] "s"(ldsw), [st1] "s"(st1), [st1l] "s"((u32)(unsigned long)st1), [st1h] "s"((u32)((unsigned long)st1 >> 32)), [stb] "s"(stb)
            : "vcc", "scc", "memory", "s56", "s57", "s58", "s59", "s60", "s61", "s62", "s63", "s64", "s65", "s66", "s67", "s68", "s69", "s70", "s71", "s72", "s73", "s74", "s75", "s76", "s77", "s78", "s79", "s80", "s81", "s82", "s83", "s84", "s85", "s86", "s87", "s88", "s89", "v128", "v129", "v130", "v131", "v132", "v133", "v134", "v135", "v136", "v137", "v138", "v139", "v140", "v141", "v142", "v143", "v144", "v145", "v146", "v147", "v148", "v149", "v150", "v151", "v152", "v153", "v154", "v155", "v156", "v157", "v158", "v159", "v160", "v161", "v162", "v163", "v164", "v165", "v166", "v167", "v168", "v169", "v170", "v171", "v172", "v173", "v174", "v175", "v176", "v177", "v178", "v179", "v180", "v181", "v182", "v183", "v184", "v185", "v186", "v187", "v188", "v189", "v190", "v191", "v192", "v193", "v194", "v195", "v196", "v197", "v198", "v199", "v200", "v201", "v202", "v203", "v204", "v205", "v206", "v207", "v208", "v209", "v210", "v211", "v212", "v213", "v214", "v215", "v216", "v217", "v218", "v219", "v220", "v221", "v222", "v223", "v224", "v225", "v226", "v227", "v228", "v229", "v230", "v231", "v232", "v233", "v234", "v235");
        if (mt == 0 && p == 0 && lane < 2) asm volatile("global_store_dword %0, %1, %2 sc1" :: "v"(no), "v"(nvl), "s"(KPTR(4)) : "memory");
        return;
    }

    const int c = lane & 15, g = lane >> 4;
    u32 oo[4];
    unsigned long ob[4];

    int xb0 = (w * 64 + c) * 128 + ((g ^ (c >> 1)) * 16);
    int xb1 = (w * 64 + c) * 128 + (((4 + g) ^ (c >> 1)) * 16);
    int ba = OFF_B + g * 2048 + c * 16;
#define LDX(a) (*(const bf16x8*)(smem + (a)))
#define MP(i, j, x0, w0, x1, w1) do { asm volatile("v_mfma_f32_16x16x32_bf16 %0, %1, %2, %0\n\tv_mfma_f32_16x16x32_bf16 %0, %3, %4, %0" \
                                      : "+v"(acc[i][j]) : "v"(x0), "v"(w0), "v"(x1), "v"(w1)); SB(); } while (0)
#define LD(d, a) do { d = LDX(a); SB(); } while (0)
#define CBAR() do { asm volatile("s_waitcnt lgkmcnt(0)" ::: "memory"); BAR(); asm volatile("" ::: "memory"); } while (0)

    u32 fo0, nvl;
    {
        const int p = w, l = lane & 31, h = lane >> 5;
        const int i2 = mt >> 1, h2 = mt & 1;
        u32 qoff[8];
#pragma unroll
        for (int r = 0; r < 8; ++r) qoff[r] = (u32)(((i2 * 64 + p * 16 + h * 8 + r) * N_ + col0 + l * 4) * 4);
        u32 lutl = (u32)(size_t)(smem) + OFF_L + l * 4;
        u32 bdst = (u32)(size_t)(smem) + OFF_B + p * 4096 + h * 2048 + ((l >> 4) * 64 + (l & 15)) * 16;
        u32 wo = (u32)(nt * 16384 + p * 4096 + h * 2048 + (l >> 4) * 1024 + (l & 15) * 16);
        u32 no = (u32)(nt * 256 + (lane & 1) * 4);
        u32 V[4][8];
        i32x4 Q0[8];
#define PK(b_, j, n) asm volatile("s_waitcnt lgkmcnt(" #n ")\n\t" \
                                    "v_lshl_or_b32 %0, %5, 16, %4\n\tv_lshl_or_b32 %1, %7, 16, %6\n\t" \
                                    "v_lshl_or_b32 %2, %9, 16, %8\n\tv_lshl_or_b32 %3, %11, 16, %10" \
                                    : "=&v"(b_[0]), "=&v"(b_[1]), "=&v"(b_[2]), "=&v"(b_[3]) \
                                    : "v"(V[j][0]), "v"(V[j][1]), "v"(V[j][2]), "v"(V[j][3]), "v"(V[j][4]), "v"(V[j][5]), "v"(V[j][6]), "v"(V[j][7]) : "memory")
#define WR(b_, j) asm volatile("ds_write_b128 %0, %1 offset:%2" :: "v"(bdst), "v"(b_), "i"((j) * 256) : "memory")
        {
            u32 lo = (u32)((p * 4 * N_ + col0 + (l * 2 + h) * 2) * 2);
            asm volatile("" : "+v"(lo), "+v"(no), "+v"(qoff[0]), "+v"(qoff[1]), "+v"(qoff[2]), "+v"(qoff[3]), "+v"(qoff[4]), "+v"(qoff[5]), "+v"(qoff[6]), "+v"(qoff[7]));
            KWAIT();
#pragma unroll
            for (int r = 0; r < 4; ++r) {
                const u16* base = (const u16*)KPTR(2) + r * N_;
                const u32 dst = (u32)(size_t)(smem) + OFF_L + (p * 4 + r) * 256;
                asm volatile("s_mov_b32 m0, %2\n\ts_nop 0\n\tglobal_load_lds_dword %0, %1" :: "v"(lo), "s"(base), "s"(dst) : "memory");
            }
        }
#pragma unroll
        for (int r = 0; r < 8; ++r) asm volatile("global_load_dwordx4 %0, %1, %2 " : "=v"(Q0[r]) : "v"(qoff[r]), "s"(KPTR(1)) : "memory");
        SB();
        BAR();
#pragma unroll
        for (int r = 0; r < 4; ++r) {
            oo[r] = (u32)(((row0 + w * 64 + g * 4 + r) * N_ + col0 + c * 4) * 2);
            ob[r] = KPTR(3) + (unsigned long)(r * 16 * N_ * 2);
            asm volatile("" : "+v"(oo[r]), "+s"(ob[r]));
        }
        asm volatile("" : "+v"(xb0), "+v"(xb1), "+v"(ba));
        const char* st0 = (const char*)KPTR(4) + WS_W + (long)i2 * TW + h2 * 512;
        fo0 = (u32)(WS_F + (nt * 4 + p) * 256 + lane * 4 + i2 * 16 + h2 * 8);
        asm volatile("" : "+v"(wo), "+v"(fo0), "+v"(bdst), "+v"(lutl));
        asm volatile("" : "+s"(st0));
        {
            unsigned long ex_;
            asm volatile("s_mov_b64 %0, exec\n\ts_mov_b64 exec, 0\n\t"
                         "s_cmp_eq_u32 %1, 0\n\ts_cbranch_scc1 .Lwp20\n\t"
                         "s_cmp_eq_u32 %1, 1\n\ts_cbranch_scc1 .Lwp11\n\t"
                         "s_cmp_eq_u32 %1, 2\n\ts_cbranch_scc1 .Lwp12\n\t"
                         "s_branch .Lwp13\n\t"
                         ".Lwp99:\n\ts_mov_b64 exec, %0" : "=&s"(ex_) : "s"(w) : "scc", "memory");
        }
        asm volatile("s_waitcnt vmcnt(8)" ::: "memory");
        BAR();
        PAD(20, 10);
        asm volatile("s_waitcnt vmcnt(7)" : "+v"(Q0[0]) :: "memory"); LUT1(Q0, 0, 0); LUT1(Q0, 1, 0); LUT1(Q0, 2, 0); LUT1(Q0, 3, 0); SB();
        asm volatile("s_waitcnt vmcnt(6)" : "+v"(Q0[1]) :: "memory"); LUT1(Q0, 0, 1); LUT1(Q0, 1, 1); LUT1(Q0, 2, 1); LUT1(Q0, 3, 1); SB();
        asm volatile("s_waitcnt vmcnt(5)" : "+v"(Q0[2]) :: "memory"); LUT1(Q0, 0, 2); LUT1(Q0, 1, 2); LUT1(Q0, 2, 2); LUT1(Q0, 3, 2); SB();
        asm volatile("s_waitcnt vmcnt(4)" : "+v"(Q0[3]) :: "memory"); LUT1(Q0, 0, 3); LUT1(Q0, 1, 3); LUT1(Q0, 2, 3); LUT1(Q0, 3, 3); SB();
        asm volatile("s_waitcnt vmcnt(3)" : "+v"(Q0[4]) :: "memory"); LUT1(Q0, 0, 4); LUT1(Q0, 1, 4); LUT1(Q0, 2, 4); LUT1(Q0, 3, 4); SB();
        asm volatile("s_waitcnt vmcnt(2)" : "+v"(Q0[5]) :: "memory"); LUT1(Q0, 0, 5); LUT1(Q0, 1, 5); LUT1(Q0, 2, 5); LUT1(Q0, 3, 5); SB();
        asm volatile("s_waitcnt vmcnt(1)" : "+v"(Q0[6]) :: "memory"); LUT1(Q0, 0, 6); LUT1(Q0, 1, 6); LUT1(Q0, 2, 6); LUT1(Q0, 3, 6); SB();
        asm volatile("s_waitcnt vmcnt(0)" : "+v"(Q0[7]) :: "memory"); LUT1(Q0, 0, 7); LUT1(Q0, 1, 7); LUT1(Q0, 2, 7); LUT1(Q0, 3, 7); SB();
        {   u32x4 b0, b1, b2, b3;


            PK(b0, 0, 3); WR(b0, 0); PK(b1, 1, 3); WR(b1, 1); PK(b2, 2, 3); WR(b2, 2); PK(b3, 3, 3); WR(b3, 3);

            SB();
        }
        asm volatile("s_waitcnt lgkmcnt(0)" ::: "memory");
        BAR();
    }
    asm volatile(
        "v_mov_b32 v236, %[xb0]\n\t"
        "v_mov_b32 v237, %[xb1]\n\t"
        "v_mov_b32 v238, %[ba]\n\t"
        ".Lwp10:\n\t"
        "s_cbranch_execz .Lwp99\n\t"
        "ds_read_b128 v[192:195], v238 offset:0\n\t"
        "ds_read_b128 v[128:131], v236 offset:0\n\t"
        "ds_read_b128 v[196:199], v238 offset:8192\n\t"
        "ds_read_b128 v[144:147], v237 offset:0\n\t"
        "ds_read_b128 v[204:207], v238 offset:8448\n\t"
        "ds_read_b128 v[200:203], v238 offset:256\n\t"
        "ds_read_b128 v[132:135], v236 offset:2048\n\t"
        "ds_read_b128 v[148:151], v237 offset:2048\n\t"
        "s_waitcnt lgkmcnt(6)\n\t"
        "v_mfma_f32_16x16x32_bf16 v[0:3], v[128:131], v[192:195], 0\n\t"
        "s_waitcnt lgkmcnt(4)\n\t"
        "v_mfma_f32_16x16x32_bf16 v[0:3], v[144:147], v[196:199], v[0:3]\n\t"
        "ds_read_b128 v[136:139], v236 offset:4096\n\t"
        "s_waitcnt lgkmcnt(4)\n\t"
        "v_mfma_f32_16x16x32_bf16 v[4:7], v[144:147], v[204:207], 0\n\t"
        "s_waitcnt lgkmcnt(3)\n\t"
        "v_mfma_f32_16x16x32_bf16 v[4:7], v[128:131], v[200:203], v[4:7]\n\t"
        "ds_read_b128 v[152:155], v237 offset:4096\n\t"
        "s_waitcnt lgkmcnt(3)\n\t"
        "v_mfma_f32_16x16x32_bf16 v[36:39], v[132:135], v[200:203], 0\n\t"
        "s_waitcnt lgkmcnt(2)\n\t"
        "v_mfma_f32_16x16x32_bf16 v[36:39], v[148:151], v[204:207], v[36:39]\n\t"
        "ds_read_b128 v[140:143], v236 offset:6144\n\t"
        "v_mfma_f32_16x16x32_bf16 v[32:35], v[148:151], v[196:199], 0\n\t"
        "v_mfma_f32_16x16x32_bf16 v[32:35], v[132:135], v[192:195], v[32:35]\n\t"
        "ds_read_b128 v[156:159], v237 offset:6144\n\t"
        "s_waitcnt lgkmcnt(3)\n\t"
        "v_mfma_f32_16x16x32_bf16 v[64:67], v[136:139], v[192:195], 0\n\t"
        "s_waitcnt lgkmcnt(2)\n\t"
        "v_mfma_f32_16x16x32_bf16 v[64:67], v[152:155], v[196:199], v[64:67]\n\t"
        "ds_read_b128 v[208:211], v238 offset:512\n\t"
        "v_mfma_f32_16x16x32_bf16 v[68:71], v[152:155], v[204:207], 0\n\t"
        "v_mfma_f32_16x16x32_bf16 v[68:71], v[136:139], v[200:203], v[68:71]\n\t"
        "ds_read_b128 v[212:215], v238 offset:8704\n\t"
        "s_waitcnt lgkmcnt(3)\n\t"
        "v_mfma_f32_16x16x32_bf16 v[100:103], v[140:143], v[200:203], 0\n\t"
        "s_waitcnt lgkmcnt(2)\n\t"
        "v_mfma_f32_16x16x32_bf16 v[100:103], v[156:159], v[204:207], v[100:103]\n\t"
        "ds_read_b128 v[220:223], v238 offset:8960\n\t"
        "v_mfma_f32_16x16x32_bf16 v[96:99], v[156:159], v[196:199], 0\n\t"
        "v_mfma_f32_16x16x32_bf16 v[96:99], v[140:143], v[192:195], v[96:99]\n\t"
        "ds_read_b128 v[216:219], v238 offset:768\n\t"
        "s_waitcnt lgkmcnt(3)\n\t"
        "v_mfma_f32_16x16x32_bf16 v[8:11], v[128:131], v[208:211], 0\n\t"
        "s_waitcnt lgkmcnt(2)\n\t"
        "v_mfma_f32_16x16x32_bf16 v[8:11], v[144:147], v[212:215], v[8:11]\n\t"
        "ds_read_b128 v[192:195], v238 offset:1024\n\t"
        "s_waitcnt lgkmcnt(2)\n\t"
        "v_mfma_f32_16x16x32_bf16 v[12:15], v[144:147], v[220:223], 0\n\t"
        "s_waitcnt lgkmcnt(1)\n\t"
        "v_mfma_f32_16x16x32_bf16 v[12:15], v[128:131], v[216:219], v[12:15]\n\t"
        "ds_read_b128 v[196:199], v238 offset:9216\n\t"
        "v_mfma_f32_16x16x32_bf16 v[44:47], v[132:135], v[216:219], 0\n\t"
        "v_mfma_f32_16x16x32_bf16 v[44:47], v[148:151], v[220:223], v[44:47]\n\t"
        "ds_read_b128 v[204:207], v238 offset:9472\n\t"
        "v_mfma_f32_16x16x32_bf16 v[40:43], v[148:151], v[212:215], 0\n\t"
        "v_mfma_f32_16x16x32_bf16 v[40:43], v[132:135], v[208:211], v[40:43]\n\t"
        "ds_read_b128 v[200:203], v238 offset:1280\n\t"
        "v_mfma_f32_16x16x32_bf16 v[72:75], v[136:139], v[208:211], 0\n\t"
        "v_mfma_f32_16x16x32_bf16 v[72:75], v[152:155], v[212:215], v[72:75]\n\t"
        "v_mfma_f32_16x16x32_bf16 v[76:79], v[152:155], v[220:223], 0\n\t"
        "v_mfma_f32_16x16x32_bf16 v[76:79], v[136:139], v[216:219], v[76:79]\n\t"
        "v_mfma_f32_16x16x32_bf16 v[108:111], v[140:143], v[216:219], 0\n\t"
        "v_mfma_f32_16x16x32_bf16 v[108:111], v[156:159], v[220:223], v[108:111]\n\t"
        "v_mfma_f32_16x16x32_bf16 v[104:107], v[156:159], v[212:215], 0\n\t"
        "v_mfma_f32_16x16x32_bf16 v[104:107], v[140:143], v[208:211], v[104:107]\n\t"
        "s_waitcnt lgkmcnt(0)\n\t"
        "v_mfma_f32_16x16x32_bf16 v[16:19], v[128:131], v[192:195], 0\n\t"
        "v_mfma_f32_16x16x32_bf16 v[16:19], v[144:147], v[196:199], v[16:19]\n\t"
        "ds_read_b128 v[208:211], v238 offset:1536\n\t"
        "v_mfma_f32_16x16x32_bf16 v[20:23], v[144:147], v[204:207], 0\n\t"
        "v_mfma_f32_16x16x32_bf16 v[20:23], v[128:131], v[200:203], v[20:23]\n\t"
        "ds_read_b128 v[212:215], v238 offset:9728\n\t"
        "v_mfma_f32_16x16x32_bf16 v[52:55], v[132:135], v[200:203], 0\n\t"
        "v_mfma_f32_16x16x32_bf16 v[52:55], v[148:151], v[204:207], v[52:55]\n\t"
        "ds_read_b128 v[220:223], v238 offset:9984\n\t"
        "v_mfma_f32_16x16x32_bf16 v[48:51], v[148:151], v[196:199], 0\n\t"
        "v_mfma_f32_16x16x32_bf16 v[48:51], v[132:135], v[192:195], v[48:51]\n\t"
        "ds_read_b128 v[216:219], v238 offset:1792\n\t"
        "v_mfma_f32_16x16x32_bf16 v[80:83], v[136:139], v[192:195], 0\n\t"
        "v_mfma_f32_16x16x32_bf16 v[80:83], v[152:155], v[196:199], v[80:83]\n\t"
        "v_mfma_f32_16x16x32_bf16 v[84:87], v[152:155], v[204:207], 0\n\t"
        "v_mfma_f32_16x16x32_bf16 v[84:87], v[136:139], v[200:203], v[84:87]\n\t"
        "s_mov_b32 s60, 32768\n\t"
        "s_mov_b32 s62, 5\n\t"
        "v_add_u32 v236, 32768, %[xb0]\n\t"
        "v_add_u32 v237, 32768, %[xb1]\n\t"
        "v_add_u32 v238, 16384, %[ba]\n\t"
        "v_mov_b64 v[24:25], 0\n\t"
        "v_mov_b64 v[26:27], 0\n\t"
        "v_mov_b64 v[28:29], 0\n\t"
        "v_mov_b64 v[30:31], 0\n\t"
        "v_mov_b64 v[56:57], 0\n\t"
        "v_mov_b64 v[58:59], 0\n\t"
        "v_mov_b64 v[60:61], 0\n\t"
        "v_mov_b64 v[62:63], 0\n\t"
        "v_mov_b64 v[88:89], 0\n\t"
        "v_mov_b64 v[90:91], 0\n\t"
        "v_mov_b64 v[92:93], 0\n\t"
        "v_mov_b64 v[94:95], 0\n\t"
        "v_mov_b64 v[120:121], 0\n\t"
        "v_mov_b64 v[122:123], 0\n\t"
        "v_mov_b64 v[124:125], 0\n\t"
        "v_mov_b64 v[126:127], 0\n\t"
        "v_mov_b64 v[116:117], 0\n\t"
        "v_mov_b64 v[118:119], 0\n\t"
        "v_mov_b64 v[112:113], 0\n\t"
        "v_mov_b64 v[114:115], 0\n\t"
        "s_nop 1\n\t"
        "s_mov_b64 exec, 0\n\t"
        "s_cmp_eq_u32 %[w], 0\n\t"
        "s_cbranch_scc1 .Lwp34\n\t"
        "s_cmp_eq_u32 %[w], 1\n\t"
        "s_cbranch_scc1 .Lwp38\n\t"
        "s_cmp_eq_u32 %[w], 2\n\t"
        "s_cbranch_scc1 .Lwp42\n\t"
        "s_branch .Lwp15\n\t"
        ".Lwp97:\n\t"
        "s_mov_b64 exec, -1\n\t"
        ".Lcloop:\n\t"
        "v_mfma_f32_16x16x32_bf16 v[116:119], v[140:143], v[200:203], v[116:119]\n\t"
        ".Lwp11:\n\t"
        "s_cbranch_execz .Lwp99\n\t"
        "s_sub_u32 s62, s62, 1\n\t"
        "v_mfma_f32_16x16x32_bf16 v[116:119], v[156:159], v[204:207], v[116:119]\n\t"
        "s_waitcnt lgkmcnt(0)\n\t"
        "v_mfma_f32_16x16x32_bf16 v[112:115], v[156:159], v[196:199], v[112:115]\n\t"
        "v_mfma_f32_16x16x32_bf16 v[112:115], v[140:143], v[192:195], v[112:115]\n\t"
        "s_barrier\n\t"
        "v_mfma_f32_16x16x32_bf16 v[24:27], v[128:131], v[208:211], v[24:27]\n\t"
        "v_mfma_f32_16x16x32_bf16 v[24:27], v[144:147], v[212:215], v[24:27]\n\t"
        "ds_read_b128 v[192:195], v238 offset:0\n\t"
        "ds_read_b128 v[160:163], v236 offset:0\n\t"
        "v_mfma_f32_16x16x32_bf16 v[28:31], v[144:147], v[220:223], v[28:31]\n\t"
        "v_mfma_f32_16x16x32_bf16 v[28:31], v[128:131], v[216:219], v[28:31]\n\t"
        "ds_read_b128 v[196:199], v238 offset:8192\n\t"
        "ds_read_b128 v[176:179], v237 offset:0\n\t"
        "v_mfma_f32_16x16x32_bf16 v[60:63], v[132:135], v[216:219], v[60:63]\n\t"
        "v_mfma_f32_16x16x32_bf16 v[60:63], v[148:151], v[220:223], v[60:63]\n\t"
        "ds_read_b128 v[204:207], v238 offset:8448\n\t"
        "v_mfma_f32_16x16x32_bf16 v[56:59], v[148:151], v[212:215], v[56:59]\n\t"
        "v_mfma_f32_16x16x32_bf16 v[56:59], v[132:135], v[208:211], v[56:59]\n\t"
        "ds_read_b128 v[200:203], v238 offset:256\n\t"
        "v_mfma_f32_16x16x32_bf16 v[88:91], v[136:139], v[208:211], v[88:91]\n\t"
        "v_mfma_f32_16x16x32_bf16 v[88:91], v[152:155], v[212:215], v[88:91]\n\t"
        "ds_read_b128 v[164:167], v236 offset:2048\n\t"
        "v_mfma_f32_16x16x32_bf16 v[92:95], v[152:155], v[220:223], v[92:95]\n\t"
        "v_mfma_f32_16x16x32_bf16 v[92:95], v[136:139], v[216:219], v[92:95]\n\t"
        "ds_read_b128 v[180:183], v237 offset:2048\n\t"
        "v_mfma_f32_16x16x32_bf16 v[124:127], v[140:143], v[216:219], v[124:127]\n\t"
        "v_mfma_f32_16x16x32_bf16 v[124:127], v[156:159], v[220:223], v[124:127]\n\t"
        "ds_read_b128 v[168:171], v236 offset:4096\n\t"
        "v_mfma_f32_16x16x32_bf16 v[120:123], v[156:159], v[212:215], v[120:123]\n\t"
        "v_mfma_f32_16x16x32_bf16 v[120:123], v[140:143], v[208:211], v[120:123]\n\t"
        "ds_read_b128 v[184:187], v237 offset:4096\n\t"
        "s_waitcnt lgkmcnt(4)\n\t"
        "v_mfma_f32_16x16x32_bf16 v[0:3], v[160:163], v[192:195], v[0:3]\n\t"
        "v_mfma_f32_16x16x32_bf16 v[0:3], v[176:179], v[196:199], v[0:3]\n\t"
        "ds_read_b128 v[172:175], v236 offset:6144\n\t"
        "ds_read_b128 v[208:211], v238 offset:512\n\t"
        "v_mfma_f32_16x16x32_bf16 v[4:7], v[176:179], v[204:207], v[4:7]\n\t"
        "v_mfma_f32_16x16x32_bf16 v[4:7], v[160:163], v[200:203], v[4:7]\n\t"
        "ds_read_b128 v[188:191], v237 offset:6144\n\t"
        "s_waitcnt lgkmcnt(5)\n\t"
        "v_mfma_f32_16x16x32_bf16 v[36:39], v[164:167], v[200:203], v[36:39]\n\t"
        "ds_read_b128 v[212:215], v238 offset:8704\n\t"
        "v_mfma_f32_16x16x32_bf16 v[36:39], v[180:183], v[204:207], v[36:39]\n\t"
        "ds_read_b128 v[220:223], v238 offset:8960\n\t"
        "v_mfma_f32_16x16x32_bf16 v[32:35], v[180:183], v[196:199], v[32:35]\n\t"
        "v_mfma_f32_16x16x32_bf16 v[32:35], v[164:167], v[192:195], v[32:35]\n\t"
        "ds_read_b128 v[216:219], v238 offset:768\n\t"
        "s_waitcnt lgkmcnt(6)\n\t"
        "v_mfma_f32_16x16x32_bf16 v[64:67], v[168:171], v[192:195], v[64:67]\n\t"
        "v_mfma_f32_16x16x32_bf16 v[64:67], v[184:187], v[196:199], v[64:67]\n\t"
        "v_mfma_f32_16x16x32_bf16 v[68:71], v[184:187], v[204:207], v[68:71]\n\t"
        "v_mfma_f32_16x16x32_bf16 v[68:71], v[168:171], v[200:203], v[68:71]\n\t"
        "s_waitcnt lgkmcnt(3)\n\t"
        "v_mfma_f32_16x16x32_bf16 v[100:103], v[172:175], v[200:203], v[100:103]\n\t"
        "v_mfma_f32_16x16x32_bf16 v[100:103], v[188:191], v[204:207], v[100:103]\n\t"
        "v_mfma_f32_16x16x32_bf16 v[96:99], v[188:191], v[196:199], v[96:99]\n\t"
        "v_mfma_f32_16x16x32_bf16 v[96:99], v[172:175], v[192:195], v[96:99]\n\t"
        ".Lwp12:\n\t"
        "s_cbranch_execz .Lwp99\n\t"
        "v_mfma_f32_16x16x32_bf16 v[8:11], v[160:163], v[208:211], v[8:11]\n\t"
        "s_waitcnt lgkmcnt(0)\n\t"
        "v_mfma_f32_16x16x32_bf16 v[8:11], v[176:179], v[212:215], v[8:11]\n\t"
        "ds_read_b128 v[192:195], v238 offset:1024\n\t"
        "v_mfma_f32_16x16x32_bf16 v[12:15], v[176:179], v[220:223], v[12:15]\n\t"
        "v_mfma_f32_16x16x32_bf16 v[12:15], v[160:163], v[216:219], v[12:15]\n\t"
        "ds_read_b128 v[196:199], v238 offset:9216\n\t"
        "v_mfma_f32_16x16x32_bf16 v[44:47], v[164:167], v[216:219], v[44:47]\n\t"
        "v_mfma_f32_16x16x32_bf16 v[44:47], v[180:183], v[220:223], v[44:47]\n\t"
        "ds_read_b128 v[204:207], v238 offset:9472\n\t"
        "v_mfma_f32_16x16x32_bf16 v[40:43], v[180:183], v[212:215], v[40:43]\n\t"
        "v_mfma_f32_16x16x32_bf16 v[40:43], v[164:167], v[208:211], v[40:43]\n\t"
        "ds_read_b128 v[200:203], v238 offset:1280\n\t"
        "v_mfma_f32_16x16x32_bf16 v[72:75], v[168:171], v[208:211], v[72:75]\n\t"
        "v_mfma_f32_16x16x32_bf16 v[72:75], v[184:187], v[212:215], v[72:75]\n\t"
        "v_mfma_f32_16x16x32_bf16 v[76:79], v[184:187], v[220:223], v[76:79]\n\t"
        "v_mfma_f32_16x16x32_bf16 v[76:79], v[168:171], v[216:219], v[76:79]\n\t"
        "v_mfma_f32_16x16x32_bf16 v[108:111], v[172:175], v[216:219], v[108:111]\n\t"
        "v_mfma_f32_16x16x32_bf16 v[108:111], v[188:191], v[220:223], v[108:111]\n\t"
        "v_mfma_f32_16x16x32_bf16 v[104:107], v[188:191], v[212:215], v[104:107]\n\t"
        "v_mfma_f32_16x16x32_bf16 v[104:107], v[172:175], v[208:211], v[104:107]\n\t"
        "s_waitcnt lgkmcnt(0)\n\t"
        "v_mfma_f32_16x16x32_bf16 v[16:19], v[160:163], v[192:195], v[16:19]\n\t"
        "v_mfma_f32_16x16x32_bf16 v[16:19], v[176:179], v[196:199], v[16:19]\n\t"
        "ds_read_b128 v[208:211], v238 offset:1536\n\t"
        "v_mfma_f32_16x16x32_bf16 v[20:23], v[176:179], v[204:207], v[20:23]\n\t"
        "v_mfma_f32_16x16x32_bf16 v[20:23], v[160:163], v[200:203], v[20:23]\n\t"
        "ds_read_b128 v[212:215], v238 offset:9728\n\t"
        "v_mfma_f32_16x16x32_bf16 v[52:55], v[164:167], v[200:203], v[52:55]\n\t"
        "v_mfma_f32_16x16x32_bf16 v[52:55], v[180:183], v[204:207], v[52:55]\n\t"
        "ds_read_b128 v[220:223], v238 offset:9984\n\t"
        "v_mfma_f32_16x16x32_bf16 v[48:51], v[180:183], v[196:199], v[48:51]\n\t"
        "v_mfma_f32_16x16x32_bf16 v[48:51], v[164:167], v[192:195], v[48:51]\n\t"
        "ds_read_b128 v[216:219], v238 offset:1792\n\t"
        "s_add_u32 s60, s60, 32768\n\t"
        "v_mfma_f32_16x16x32_bf16 v[80:83], v[168:171], v[192:195], v[80:83]\n\t"
        "s_cmp_eq_u32 s60, 98304\n\t"
        "s_cselect_b32 s60, 0, s60\n\t"
        "v_mfma_f32_16x16x32_bf16 v[80:83], v[184:187], v[196:199], v[80:83]\n\t"
        "v_add_u32 v236, s60, %[xb0]\n\t"
        "s_lshr_b32 s61, s60, 1\n\t"
        "v_mfma_f32_16x16x32_bf16 v[84:87], v[184:187], v[204:207], v[84:87]\n\t"
        "v_add_u32 v237, s60, %[xb1]\n\t"
        "v_add_u32 v238, s61, %[ba]\n\t"
        "v_mfma_f32_16x16x32_bf16 v[84:87], v[168:171], v[200:203], v[84:87]\n\t"
        "v_mfma_f32_16x16x32_bf16 v[116:119], v[172:175], v[200:203], v[116:119]\n\t"
        "v_mfma_f32_16x16x32_bf16 v[116:119], v[188:191], v[204:207], v[116:119]\n\t"
        "s_waitcnt lgkmcnt(0)\n\t"
        "v_mfma_f32_16x16x32_bf16 v[112:115], v[188:191], v[196:199], v[112:115]\n\t"
        "v_mfma_f32_16x16x32_bf16 v[112:115], v[172:175], v[192:195], v[112:115]\n\t"
        "s_barrier\n\t"
        "v_mfma_f32_16x16x32_bf16 v[24:27], v[160:163], v[208:211], v[24:27]\n\t"
        "v_mfma_f32_16x16x32_bf16 v[24:27], v[176:179], v[212:215], v[24:27]\n\t"
        "ds_read_b128 v[192:195], v238 offset:0\n\t"
        "ds_read_b128 v[128:131], v236 offset:0\n\t"
        "v_mfma_f32_16x16x32_bf16 v[28:31], v[176:179], v[220:223], v[28:31]\n\t"
        "v_mfma_f32_16x16x32_bf16 v[28:31], v[160:163], v[216:219], v[28:31]\n\t"
        "ds_read_b128 v[196:199], v238 offset:8192\n\t"
        "ds_read_b128 v[144:147], v237 offset:0\n\t"
        "v_mfma_f32_16x16x32_bf16 v[60:63], v[164:167], v[216:219], v[60:63]\n\t"
        "v_mfma_f32_16x16x32_bf16 v[60:63], v[180:183], v[220:223], v[60:63]\n\t"
        "ds_read_b128 v[204:207], v238 offset:8448\n\t"
        "v_mfma_f32_16x16x32_bf16 v[56:59], v[180:183], v[212:215], v[56:59]\n\t"
        "v_mfma_f32_16x16x32_bf16 v[56:59], v[164:167], v[208:211], v[56:59]\n\t"
        "ds_read_b128 v[200:203], v238 offset:256\n\t"
        "v_mfma_f32_16x16x32_bf16 v[88:91], v[168:171], v[208:211], v[88:91]\n\t"
        "v_mfma_f32_16x16x32_bf16 v[88:91], v[184:187], v[212:215], v[88:91]\n\t"
        "ds_read_b128 v[132:135], v236 offset:2048\n\t"
        "v_mfma_f32_16x16x32_bf16 v[92:95], v[184:187], v[220:223], v[92:95]\n\t"
        "v_mfma_f32_16x16x32_bf16 v[92:95], v[168:171], v[216:219], v[92:95]\n\t"
        "ds_read_b128 v[148:151], v237 offset:2048\n\t"
        "v_mfma_f32_16x16x32_bf16 v[124:127], v[172:175], v[216:219], v[124:127]\n\t"
        "v_mfma_f32_16x16x32_bf16 v[124:127], v[188:191], v[220:223], v[124:127]\n\t"
        "ds_read_b128 v[136:139], v236 offset:4096\n\t"
        "v_mfma_f32_16x16x32_bf16 v[120:123], v[188:191], v[212:215], v[120:123]\n\t"
        "v_mfma_f32_16x16x32_bf16 v[120:123], v[172:175], v[208:211], v[120:123]\n\t"
        "ds_read_b128 v[152:155], v237 offset:4096\n\t"
        "s_waitcnt lgkmcnt(4)\n\t"
        "v_mfma_f32_16x16x32_bf16 v[0:3], v[128:131], v[192:195], v[0:3]\n\t"
        "v_mfma_f32_16x16x32_bf16 v[0:3], v[144:147], v[196:199], v[0:3]\n\t"
        "ds_read_b128 v[140:143], v236 offset:6144\n\t"
        "ds_read_b128 v[208:211], v238 offset:512\n\t"
        "v_mfma_f32_16x16x32_bf16 v[4:7], v[144:147], v[204:207], v[4:7]\n\t"
        "v_mfma_f32_16x16x32_bf16 v[4:7], v[128:131], v[200:203], v[4:7]\n\t"
        "ds_read_b128 v[156:159], v237 offset:6144\n\t"
        "s_waitcnt lgkmcnt(5)\n\t"
        "v_mfma_f32_16x16x32_bf16 v[36:39], v[132:135], v[200:203], v[36:39]\n\t"
        "ds_read_b128 v[212:215], v238 offset:8704\n\t"
        "v_mfma_f32_16x16x32_bf16 v[36:39], v[148:151], v[204:207], v[36:39]\n\t"
        "ds_read_b128 v[220:223], v238 offset:8960\n\t"
        "v_mfma_f32_16x16x32_bf16 v[32:35], v[148:151], v[196:199], v[32:35]\n\t"
        "v_mfma_f32_16x16x32_bf16 v[32:35], v[132:135], v[192:195], v[32:35]\n\t"
        "ds_read_b128 v[216:219], v238 offset:768\n\t"
        "s_waitcnt lgkmcnt(6)\n\t"
        "v_mfma_f32_16x16x32_bf16 v[64:67], v[136:139], v[192:195], v[64:67]\n\t"
        "v_mfma_f32_16x16x32_bf16 v[64:67], v[152:155], v[196:199], v[64:67]\n\t"
        "v_mfma_f32_16x16x32_bf16 v[68:71], v[152:155], v[204:207], v[68:71]\n\t"
        "v_mfma_f32_16x16x32_bf16 v[68:71], v[136:139], v[200:203], v[68:71]\n\t"
        "s_waitcnt lgkmcnt(3)\n\t"
        "v_mfma_f32_16x16x32_bf16 v[100:103], v[140:143], v[200:203], v[100:103]\n\t"
        "v_mfma_f32_16x16x32_bf16 v[100:103], v[156:159], v[204:207], v[100:103]\n\t"
        "v_mfma_f32_16x16x32_bf16 v[96:99], v[156:159], v[196:199], v[96:99]\n\t"
        "v_mfma_f32_16x16x32_bf16 v[96:99], v[140:143], v[192:195], v[96:99]\n\t"
        ".Lwp13:\n\t"
        "s_cbranch_execz .Lwp99\n\t"
        "v_mfma_f32_16x16x32_bf16 v[8:11], v[128:131], v[208:211], v[8:11]\n\t"
        "s_waitcnt lgkmcnt(0)\n\t"
        "v_mfma_f32_16x16x32_bf16 v[8:11], v[144:147], v[212:215], v[8:11]\n\t"
        "ds_read_b128 v[192:195], v238 offset:1024\n\t"
        "v_mfma_f32_16x16x32_bf16 v[12:15], v[144:147], v[220:223], v[12:15]\n\t"
        "v_mfma_f32_16x16x32_bf16 v[12:15], v[128:131], v[216:219], v[12:15]\n\t"
        "ds_read_b128 v[196:199], v238 offset:9216\n\t"
        "v_mfma_f32_16x16x32_bf16 v[44:47], v[132:135], v[216:219], v[44:47]\n\t"
        "v_mfma_f32_16x16x32_bf16 v[44:47], v[148:151], v[220:223], v[44:47]\n\t"
        "ds_read_b128 v[204:207], v238 offset:9472\n\t"
        "v_mfma_f32_16x16x32_bf16 v[40:43], v[148:151], v[212:215], v[40:43]\n\t"
        "v_mfma_f32_16x16x32_bf16 v[40:43], v[132:135], v[208:211], v[40:43]\n\t"
        "ds_read_b128 v[200:203], v238 offset:1280\n\t"
        "v_mfma_f32_16x16x32_bf16 v[72:75], v[136:139], v[208:211], v[72:75]\n\t"
        "v_mfma_f32_16x16x32_bf16 v[72:75], v[152:155], v[212:215], v[72:75]\n\t"
        "v_mfma_f32_16x16x32_bf16 v[76:79], v[152:155], v[220:223], v[76:79]\n\t"
        "v_mfma_f32_16x16x32_bf16 v[76:79], v[136:139], v[216:219], v[76:79]\n\t"
        "v_mfma_f32_16x16x32_bf16 v[108:111], v[140:143], v[216:219], v[108:111]\n\t"
        "v_mfma_f32_16x16x32_bf16 v[108:111], v[156:159], v[220:223], v[108:111]\n\t"
        "v_mfma_f32_16x16x32_bf16 v[104:107], v[156:159], v[212:215], v[104:107]\n\t"
        "v_mfma_f32_16x16x32_bf16 v[104:107], v[140:143], v[208:211], v[104:107]\n\t"
        "s_waitcnt lgkmcnt(0)\n\t"
        "v_mfma_f32_16x16x32_bf16 v[16:19], v[128:131], v[192:195], v[16:19]\n\t"
        "v_mfma_f32_16x16x32_bf16 v[16:19], v[144:147], v[196:199], v[16:19]\n\t"
        "ds_read_b128 v[208:211], v238 offset:1536\n\t"
        "v_mfma_f32_16x16x32_bf16 v[20:23], v[144:147], v[204:207], v[20:23]\n\t"
        "v_mfma_f32_16x16x32_bf16 v[20:23], v[128:131], v[200:203], v[20:23]\n\t"
        "ds_read_b128 v[212:215], v238 offset:9728\n\t"
        "v_mfma_f32_16x16x32_bf16 v[52:55], v[132:135], v[200:203], v[52:55]\n\t"
        "v_mfma_f32_16x16x32_bf16 v[52:55], v[148:151], v[204:207], v[52:55]\n\t"
        "ds_read_b128 v[220:223], v238 offset:9984\n\t"
        "v_mfma_f32_16x16x32_bf16 v[48:51], v[148:151], v[196:199], v[48:51]\n\t"
        "v_mfma_f32_16x16x32_bf16 v[48:51], v[132:135], v[192:195], v[48:51]\n\t"
        "ds_read_b128 v[216:219], v238 offset:1792\n\t"
        "s_add_u32 s60, s60, 32768\n\t"
        "v_mfma_f32_16x16x32_bf16 v[80:83], v[136:139], v[192:195], v[80:83]\n\t"
        "s_cmp_eq_u32 s60, 98304\n\t"
        "s_cselect_b32 s60, 0, s60\n\t"
        "v_mfma_f32_16x16x32_bf16 v[80:83], v[152:155], v[196:199], v[80:83]\n\t"
        "v_add_u32 v236, s60, %[xb0]\n\t"
        "s_lshr_b32 s61, s60, 1\n\t"
        "v_mfma_f32_16x16x32_bf16 v[84:87], v[152:155], v[204:207], v[84:87]\n\t"
        "v_add_u32 v237, s60, %[xb1]\n\t"
        "v_add_u32 v238, s61, %[ba]\n\t"
        "v_mfma_f32_16x16x32_bf16 v[84:87], v[136:139], v[200:203], v[84:87]\n\t"
        "s_cmp_lg_u32 s62, -1\n\t"
        "s_cbranch_scc1 .Lcloop\n\t"
        "v_mfma_f32_16x16x32_bf16 v[116:119], v[140:143], v[200:203], v[116:119]\n\t"
        "v_mfma_f32_16x16x32_bf16 v[116:119], v[156:159], v[204:207], v[116:119]\n\t"
        "s_waitcnt lgkmcnt(0)\n\t"
        "v_mfma_f32_16x16x32_bf16 v[112:115], v[156:159], v[196:199], v[112:115]\n\t"
        "v_mfma_f32_16x16x32_bf16 v[112:115], v[140:143], v[192:195], v[112:115]\n\t"
        "s_barrier\n\t"
        "v_mfma_f32_16x16x32_bf16 v[24:27], v[128:131], v[208:211], v[24:27]\n\t"
        "s_setprio 2\n\t"
        ".Lwp14:\n\t"
        "s_cbranch_execz .Lwp97\n\t"
        "v_mfma_f32_16x16x32_bf16 v[24:27], v[144:147], v[212:215], v[24:27]\n\t"
        "ds_read_b128 v[160:163], %[ba] offset:16384\n\t"
        "ds_read_b128 v[164:167], v236 offset:0\n\t"
        "v_mfma_f32_16x16x32_bf16 v[28:31], v[144:147], v[220:223], v[28:31]\n\t"
        "v_mfma_f32_16x16x32_bf16 v[28:31], v[128:131], v[216:219], v[28:31]\n\t"
        "ds_read_b128 v[144:147], %[ba] offset:24576\n\t"
        "ds_read_b128 v[168:171], v237 offset:0\n\t"
        "v_mfma_f32_16x16x32_bf16 v[60:63], v[132:135], v[216:219], v[60:63]\n\t"
        "v_mfma_f32_16x16x32_bf16 v[60:63], v[148:151], v[220:223], v[60:63]\n\t"
        "ds_read_b128 v[128:131], %[ba] offset:24832\n\t"
        "ds_read_b128 v[172:175], %[ba] offset:16640\n\t"
        "v_mfma_f32_16x16x32_bf16 v[56:59], v[148:151], v[212:215], v[56:59]\n\t"
        "ds_read_b128 v[176:179], v236 offset:2048\n\t"
        "v_mfma_f32_16x16x32_bf16 v[56:59], v[132:135], v[208:211], v[56:59]\n\t"
        "ds_read_b128 v[148:151], v237 offset:2048\n\t"
        "v_mfma_f32_16x16x32_bf16 v[88:91], v[136:139], v[208:211], v[88:91]\n\t"
        "v_mfma_f32_16x16x32_bf16 v[88:91], v[152:155], v[212:215], v[88:91]\n\t"
        "ds_read_b128 v[132:135], v236 offset:4096\n\t"
        "v_mfma_f32_16x16x32_bf16 v[92:95], v[152:155], v[220:223], v[92:95]\n\t"
        "v_mfma_f32_16x16x32_bf16 v[92:95], v[136:139], v[216:219], v[92:95]\n\t"
        "ds_read_b128 v[152:155], v237 offset:4096\n\t"
        "v_mfma_f32_16x16x32_bf16 v[124:127], v[140:143], v[216:219], v[124:127]\n\t"
        "v_mfma_f32_16x16x32_bf16 v[124:127], v[156:159], v[220:223], v[124:127]\n\t"
        "ds_read_b128 v[136:139], v236 offset:6144\n\t"
        "v_mfma_f32_16x16x32_bf16 v[120:123], v[156:159], v[212:215], v[120:123]\n\t"
        "v_mfma_f32_16x16x32_bf16 v[120:123], v[140:143], v[208:211], v[120:123]\n\t"
        "ds_read_b128 v[156:159], v237 offset:6144\n\t"
        "s_waitcnt lgkmcnt(4)\n\t"
        "v_mfma_f32_16x16x32_bf16 v[0:3], v[164:167], v[160:163], v[0:3]\n\t"
        "v_mfma_f32_16x16x32_bf16 v[0:3], v[168:171], v[144:147], v[0:3]\n\t"
        "ds_read_b128 v[140:143], %[ba] offset:16896\n\t"
        "v_mfma_f32_16x16x32_bf16 v[4:7], v[168:171], v[128:131], v[4:7]\n\t"
        "v_mfma_f32_16x16x32_bf16 v[4:7], v[164:167], v[172:175], v[4:7]\n\t"
        "ds_read_b128 v[180:183], %[ba] offset:25088\n\t"
        "v_mfma_f32_16x16x32_bf16 v[36:39], v[176:179], v[172:175], v[36:39]\n\t"
        "v_mfma_f32_16x16x32_bf16 v[36:39], v[148:151], v[128:131], v[36:39]\n\t"
        "ds_read_b128 v[184:187], %[ba] offset:25344\n\t"
        "v_mfma_f32_16x16x32_bf16 v[32:35], v[148:151], v[144:147], v[32:35]\n\t"
        "v_mfma_f32_16x16x32_bf16 v[32:35], v[176:179], v[160:163], v[32:35]\n\t"
        "ds_read_b128 v[188:191], %[ba] offset:17152\n\t"
        "s_waitcnt lgkmcnt(4)\n\t"
        "v_mfma_f32_16x16x32_bf16 v[64:67], v[132:135], v[160:163], v[64:67]\n\t"
        "v_mfma_f32_16x16x32_bf16 v[64:67], v[152:155], v[144:147], v[64:67]\n\t"
        "ds_read_b128 v[192:195], %[ba] offset:17408\n\t"
        "v_mfma_f32_16x16x32_bf16 v[68:71], v[152:155], v[128:131], v[68:71]\n\t"
        "v_mfma_f32_16x16x32_bf16 v[68:71], v[132:135], v[172:175], v[68:71]\n\t"
        "ds_read_b128 v[196:199], v236 offset:32768\n\t"
        "v_mfma_f32_16x16x32_bf16 v[100:103], v[136:139], v[172:175], v[100:103]\n\t"
        "v_mfma_f32_16x16x32_bf16 v[100:103], v[156:159], v[128:131], v[100:103]\n\t"
        "ds_read_b128 v[172:175], %[ba] offset:25600\n\t"
        "v_mfma_f32_16x16x32_bf16 v[96:99], v[156:159], v[144:147], v[96:99]\n\t"
        "v_mfma_f32_16x16x32_bf16 v[96:99], v[136:139], v[160:163], v[96:99]\n\t"
        "ds_read_b128 v[128:131], v237 offset:32768\n\t"
        "s_waitcnt lgkmcnt(4)\n\t"
        "v_mfma_f32_16x16x32_bf16 v[8:11], v[164:167], v[140:143], v[8:11]\n\t"
        "v_mfma_f32_16x16x32_bf16 v[8:11], v[168:171], v[180:183], v[8:11]\n\t"
        "ds_read_b128 v[144:147], %[ba] offset:25856\n\t"
        "v_mfma_f32_16x16x32_bf16 v[12:15], v[168:171], v[184:187], v[12:15]\n\t"
        "v_mfma_f32_16x16x32_bf16 v[12:15], v[164:167], v[188:191], v[12:15]\n\t"
        "ds_read_b128 v[160:163], %[ba] offset:17664\n\t"
        "v_mfma_f32_16x16x32_bf16 v[44:47], v[176:179], v[188:191], v[44:47]\n\t"
        "v_mfma_f32_16x16x32_bf16 v[44:47], v[148:151], v[184:187], v[44:47]\n\t"
        "ds_read_b128 v[164:167], %[ba] offset:17920\n\t"
        "ds_read_b128 v[168:171], %[ba] offset:26112\n\t"
        "v_mfma_f32_16x16x32_bf16 v[40:43], v[148:151], v[180:183], v[40:43]\n\t"
        "v_mfma_f32_16x16x32_bf16 v[40:43], v[176:179], v[140:143], v[40:43]\n\t"
        "ds_read_b128 v[148:151], %[ba] offset:26368\n\t"
        "v_mfma_f32_16x16x32_bf16 v[72:75], v[132:135], v[140:143], v[72:75]\n\t"
        "v_mfma_f32_16x16x32_bf16 v[72:75], v[152:155], v[180:183], v[72:75]\n\t"
        "ds_read_b128 v[176:179], %[ba] offset:18176\n\t"
        "v_mfma_f32_16x16x32_bf16 v[76:79], v[152:155], v[184:187], v[76:79]\n\t"
        "v_mfma_f32_16x16x32_bf16 v[76:79], v[132:135], v[188:191], v[76:79]\n\t"
        "ds_read_b128 v[152:155], v236 offset:34816\n\t"
        "v_mfma_f32_16x16x32_bf16 v[108:111], v[136:139], v[188:191], v[108:111]\n\t"
        "v_mfma_f32_16x16x32_bf16 v[108:111], v[156:159], v[184:187], v[108:111]\n\t"
        "ds_read_b128 v[132:135], v237 offset:34816\n\t"
        "v_mfma_f32_16x16x32_bf16 v[104:107], v[156:159], v[180:183], v[104:107]\n\t"
        "v_mfma_f32_16x16x32_bf16 v[104:107], v[136:139], v[140:143], v[104:107]\n\t"
        "s_waitcnt lgkmcnt(3)\n\t"
        "v_mfma_f32_16x16x32_bf16 v[0:3], v[196:199], v[192:195], v[0:3]\n\t"
        "v_mfma_f32_16x16x32_bf16 v[0:3], v[128:131], v[172:175], v[0:3]\n\t"
        "v_mfma_f32_16x16x32_bf16 v[4:7], v[128:131], v[144:147], v[4:7]\n\t"
        "v_mfma_f32_16x16x32_bf16 v[4:7], v[196:199], v[160:163], v[4:7]\n\t"
        "ds_read_b128 v[136:139], v236 offset:36864\n\t"
        "v_mfma_f32_16x16x32_bf16 v[8:11], v[196:199], v[164:167], v[8:11]\n\t"
        "v_mfma_f32_16x16x32_bf16 v[8:11], v[128:131], v[168:171], v[8:11]\n\t"
        "ds_read_b128 v[140:143], v237 offset:36864\n\t"
        "v_mfma_f32_16x16x32_bf16 v[12:15], v[128:131], v[148:151], v[12:15]\n\t"
        "s_waitcnt lgkmcnt(2)\n\t"
        "v_mfma_f32_16x16x32_bf16 v[12:15], v[196:199], v[176:179], v[12:15]\n\t"
        "v_mfma_f32_16x16x32_bf16 v[32:35], v[152:155], v[192:195], v[32:35]\n\t"
        "v_mfma_f32_16x16x32_bf16 v[32:35], v[132:135], v[172:175], v[32:35]\n\t"
        "ds_read_b128 v[128:131], v236 offset:38912\n\t"
        "v_mfma_f32_16x16x32_bf16 v[36:39], v[132:135], v[144:147], v[36:39]\n\t"
        "v_mfma_f32_16x16x32_bf16 v[36:39], v[152:155], v[160:163], v[36:39]\n\t"
        "ds_read_b128 v[156:159], v237 offset:38912\n\t"
        "v_mfma_f32_16x16x32_bf16 v[40:43], v[152:155], v[164:167], v[40:43]\n\t"
        "v_mfma_f32_16x16x32_bf16 v[40:43], v[132:135], v[168:171], v[40:43]\n\t"
        "v_mfma_f32_16x16x32_bf16 v[44:47], v[132:135], v[148:151], v[44:47]\n\t"
        "v_mfma_f32_16x16x32_bf16 v[44:47], v[152:155], v[176:179], v[44:47]\n\t"
        "s_waitcnt lgkmcnt(0)\n\t"
        "s_barrier\n\t"
        "v_mfma_f32_16x16x32_bf16 v[64:67], v[136:139], v[192:195], v[64:67]\n\t"
        ".Lwp15:\n\t"
        "s_cbranch_execz .Lwp16\n\t"
        "v_mfma_f32_16x16x32_bf16 v[64:67], v[140:143], v[172:175], v[64:67]\n\t"
        "ds_read_b128 v[132:135], %[ba] offset:32768\n\t"
        "ds_read_b128 v[152:155], %[xb0] offset:0\n\t"
        "v_mfma_f32_16x16x32_bf16 v[68:71], v[140:143], v[144:147], v[68:71]\n\t"
        "v_mfma_f32_16x16x32_bf16 v[68:71], v[136:139], v[160:163], v[68:71]\n\t"
        "ds_read_b128 v[180:183], %[ba] offset:40960\n\t"
        "ds_read_b128 v[184:187], %[xb1] offset:0\n\t"
        "v_mfma_f32_16x16x32_bf16 v[72:75], v[136:139], v[164:167], v[72:75]\n\t"
        "v_mfma_f32_16x16x32_bf16 v[72:75], v[140:143], v[168:171], v[72:75]\n\t"
        "ds_read_b128 v[188:191], %[ba] offset:41216\n\t"
        "ds_read_b128 v[196:199], %[ba] offset:33024\n\t"
        "v_mfma_f32_16x16x32_bf16 v[76:79], v[140:143], v[148:151], v[76:79]\n\t"
        "v_mfma_f32_16x16x32_bf16 v[76:79], v[136:139], v[176:179], v[76:79]\n\t"
        "ds_read_b128 v[140:143], %[ba] offset:33280\n\t"
        "s_waitcnt lgkmcnt(5)\n\t"
        "v_mfma_f32_16x16x32_bf16 v[0:3], v[152:155], v[132:135], v[0:3]\n\t"
        "ds_read_b128 v[136:139], %[ba] offset:41472\n\t"
        "s_waitcnt lgkmcnt(4)\n\t"
        "v_mfma_f32_16x16x32_bf16 v[0:3], v[184:187], v[180:183], v[0:3]\n\t"
        "ds_read_b128 v[200:203], %[ba] offset:41728\n\t"
        "s_waitcnt lgkmcnt(4)\n\t"
        "v_mfma_f32_16x16x32_bf16 v[4:7], v[184:187], v[188:191], v[4:7]\n\t"
        "ds_read_b128 v[204:207], %[ba] offset:33536\n\t"
        "s_waitcnt lgkmcnt(4)\n\t"
        "v_mfma_f32_16x16x32_bf16 v[4:7], v[152:155], v[196:199], v[4:7]\n\t"
        "ds_read_b128 v[208:211], %[xb0] offset:2048\n\t"
        "s_waitcnt lgkmcnt(4)\n\t"
        "v_mfma_f32_16x16x32_bf16 v[8:11], v[152:155], v[140:143], v[8:11]\n\t"
        "s_waitcnt lgkmcnt(3)\n\t"
        "v_mfma_f32_16x16x32_bf16 v[8:11], v[184:187], v[136:139], v[8:11]\n\t"
        "ds_read_b128 v[212:215], %[xb1] offset:2048\n\t"
        "s_waitcnt lgkmcnt(3)\n\t"
        "v_mfma_f32_16x16x32_bf16 v[12:15], v[184:187], v[200:203], v[12:15]\n\t"
        "s_waitcnt lgkmcnt(2)\n\t"
        "v_mfma_f32_16x16x32_bf16 v[12:15], v[152:155], v[204:207], v[12:15]\n\t"
        "v_mfma_f32_16x16x32_bf16 v[96:99], v[128:131], v[192:195], v[96:99]\n\t"
        "v_mfma_f32_16x16x32_bf16 v[96:99], v[156:159], v[172:175], v[96:99]\n\t"
        "ds_read_b128 v[152:155], %[xb0] offset:4096\n\t"
        "v_cvt_pk_bf16_f32 v228, v0, v4\n\t"
        "v_mfma_f32_16x16x32_bf16 v[100:103], v[156:159], v[144:147], v[100:103]\n\t"
        "v_mfma_f32_16x16x32_bf16 v[100:103], v[128:131], v[160:163], v[100:103]\n\t"
        "ds_read_b128 v[144:147], %[xb1] offset:4096\n\t"
        "v_cvt_pk_bf16_f32 v229, v8, v12\n\t"
        "v_mfma_f32_16x16x32_bf16 v[104:107], v[128:131], v[164:167], v[104:107]\n\t"
        "global_store_dwordx2 %[oo0], v[228:229], %[ob0] offset:0 nt\n\t"
        "v_mfma_f32_16x16x32_bf16 v[104:107], v[156:159], v[168:171], v[104:107]\n\t"
        "ds_read_b128 v[160:163], %[xb0] offset:6144\n\t"
        "v_cvt_pk_bf16_f32 v231, v9, v13\n\t"
        "v_mfma_f32_16x16x32_bf16 v[108:111], v[156:159], v[148:151], v[108:111]\n\t"
        "v_cvt_pk_bf16_f32 v230, v1, v5\n\t"
        "v_mfma_f32_16x16x32_bf16 v[108:111], v[128:131], v[176:179], v[108:111]\n\t"
        "ds_read_b128 v[148:151], %[xb1] offset:6144\n\t"
        "s_waitcnt lgkmcnt(4)\n\t"
        "v_mfma_f32_16x16x32_bf16 v[32:35], v[208:211], v[132:135], v[32:35]\n\t"
        "global_store_dwordx2 %[oo1], v[230:231], %[ob0] offset:0 nt\n\t"
        "v_mfma_f32_16x16x32_bf16 v[32:35], v[212:215], v[180:183], v[32:35]\n\t"
        "ds_read_b128 v[128:131], v236 offset:0\n\t"
        "v_cvt_pk_bf16_f32 v233, v10, v14\n\t"
        "v_mfma_f32_16x16x32_bf16 v[36:39], v[212:215], v[188:191], v[36:39]\n\t"
        "v_cvt_pk_bf16_f32 v232, v2, v6\n\t"
        "v_mfma_f32_16x16x32_bf16 v[36:39], v[208:211], v[196:199], v[36:39]\n\t"
        "ds_read_b128 v[156:159], %[ba] offset:33792\n\t"
        "global_store_dwordx2 %[oo2], v[232:233], %[ob0] offset:0 nt\n\t"
        "v_mfma_f32_16x16x32_bf16 v[40:43], v[208:211], v[140:143], v[40:43]\n\t"
        "v_mfma_f32_16x16x32_bf16 v[40:43], v[212:215], v[136:139], v[40:43]\n\t"
        "ds_read_b128 v[164:167], v237 offset:0\n\t"
        "v_cvt_pk_bf16_f32 v235, v11, v15\n\t"
        "v_mfma_f32_16x16x32_bf16 v[44:47], v[212:215], v[200:203], v[44:47]\n\t"
        "v_cvt_pk_bf16_f32 v234, v3, v7\n\t"
        "v_mfma_f32_16x16x32_bf16 v[44:47], v[208:211], v[204:207], v[44:47]\n\t"
        "ds_read_b128 v[168:171], %[ba] offset:41984\n\t"
        "s_waitcnt lgkmcnt(4)\n\t"
        "v_mfma_f32_16x16x32_bf16 v[64:67], v[152:155], v[132:135], v[64:67]\n\t"
        "global_store_dwordx2 %[oo3], v[234:235], %[ob0] offset:0 nt\n\t"
        "v_mfma_f32_16x16x32_bf16 v[64:67], v[144:147], v[180:183], v[64:67]\n\t"
        "ds_read_b128 v[172:175], %[ba] offset:42240\n\t"
        "v_cvt_pk_bf16_f32 v228, v32, v36\n\t"
        "v_mfma_f32_16x16x32_bf16 v[68:71], v[144:147], v[188:191], v[68:71]\n\t"
        "v_mfma_f32_16x16x32_bf16 v[68:71], v[152:155], v[196:199], v[68:71]\n\t"
        "ds_read_b128 v[176:179], %[ba] offset:34048\n\t"
        "v_cvt_pk_bf16_f32 v229, v40, v44\n\t"
        "v_mfma_f32_16x16x32_bf16 v[72:75], v[152:155], v[140:143], v[72:75]\n\t"
        "global_store_dwordx2 %[oo0], v[228:229], %[ob1] offset:0 nt\n\t"
        "v_mfma_f32_16x16x32_bf16 v[72:75], v[144:147], v[136:139], v[72:75]\n\t"
        "ds_read_b128 v[184:187], %[ba] offset:34304\n\t"
        "ds_read_b128 v[192:195], %[ba] offset:42496\n\t"
        "v_mfma_f32_16x16x32_bf16 v[76:79], v[144:147], v[200:203], v[76:79]\n\t"
        "v_cvt_pk_bf16_f32 v231, v41, v45\n\t"
        "v_cvt_pk_bf16_f32 v230, v33, v37\n\t"
        "v_mfma_f32_16x16x32_bf16 v[76:79], v[152:155], v[204:207], v[76:79]\n\t"
        "ds_read_b128 v[144:147], %[ba] offset:42752\n\t"
        "global_store_dwordx2 %[oo1], v[230:231], %[ob1] offset:0 nt\n\t"
        "v_mfma_f32_16x16x32_bf16 v[96:99], v[160:163], v[132:135], v[96:99]\n\t"
        ".Lwp16:\n\t"
        "s_cbranch_execz .Lwp17\n\t"
        "v_mfma_f32_16x16x32_bf16 v[96:99], v[148:151], v[180:183], v[96:99]\n\t"
        "ds_read_b128 v[132:135], %[ba] offset:34560\n\t"
        "v_cvt_pk_bf16_f32 v233, v42, v46\n\t"
        "v_mfma_f32_16x16x32_bf16 v[100:103], v[148:151], v[188:191], v[100:103]\n\t"
        "v_cvt_pk_bf16_f32 v232, v34, v38\n\t"
        "v_mfma_f32_16x16x32_bf16 v[100:103], v[160:163], v[196:199], v[100:103]\n\t"
        "ds_read_b128 v[152:155], v236 offset:2048\n\t"
        "global_store_dwordx2 %[oo2], v[232:233], %[ob1] offset:0 nt\n\t"
        "v_mfma_f32_16x16x32_bf16 v[104:107], v[160:163], v[140:143], v[104:107]\n\t"
        "v_mfma_f32_16x16x32_bf16 v[104:107], v[148:151], v[136:139], v[104:107]\n\t"
        "ds_read_b128 v[140:143], v237 offset:2048\n\t"
        "v_cvt_pk_bf16_f32 v235, v43, v47\n\t"
        "v_mfma_f32_16x16x32_bf16 v[108:111], v[148:151], v[200:203], v[108:111]\n\t"
        "v_cvt_pk_bf16_f32 v234, v35, v39\n\t"
        "v_mfma_f32_16x16x32_bf16 v[108:111], v[160:163], v[204:207], v[108:111]\n\t"
        "global_store_dwordx2 %[oo3], v[234:235], %[ob1] offset:0 nt\n\t"
        "s_waitcnt lgkmcnt(3)\n\t"
        "v_mfma_f32_16x16x32_bf16 v[16:19], v[128:131], v[156:159], v[16:19]\n\t"
        "v_mfma_f32_16x16x32_bf16 v[16:19], v[164:167], v[168:171], v[16:19]\n\t"
        "ds_read_b128 v[136:139], v236 offset:4096\n\t"
        "v_cvt_pk_bf16_f32 v229, v72, v76\n\t"
        "v_mfma_f32_16x16x32_bf16 v[20:23], v[164:167], v[172:175], v[20:23]\n\t"
        "v_cvt_pk_bf16_f32 v228, v64, v68\n\t"
        "v_mfma_f32_16x16x32_bf16 v[20:23], v[128:131], v[176:179], v[20:23]\n\t"
        "ds_read_b128 v[148:151], v237 offset:4096\n\t"
        "global_store_dwordx2 %[oo0], v[228:229], %[ob2] offset:0 nt\n\t"
        "v_mfma_f32_16x16x32_bf16 v[24:27], v[128:131], v[184:187], v[24:27]\n\t"
        "v_mfma_f32_16x16x32_bf16 v[24:27], v[164:167], v[192:195], v[24:27]\n\t"
        "ds_read_b128 v[160:163], v236 offset:6144\n\t"
        "v_cvt_pk_bf16_f32 v231, v73, v77\n\t"
        "v_mfma_f32_16x16x32_bf16 v[28:31], v[164:167], v[144:147], v[28:31]\n\t"
        "v_cvt_pk_bf16_f32 v230, v65, v69\n\t"
        "s_waitcnt lgkmcnt(3)\n\t"
        "v_mfma_f32_16x16x32_bf16 v[28:31], v[128:131], v[132:135], v[28:31]\n\t"
        "ds_read_b128 v[164:167], v237 offset:6144\n\t"
        "global_store_dwordx2 %[oo1], v[230:231], %[ob2] offset:0 nt\n\t"
        "v_mfma_f32_16x16x32_bf16 v[48:51], v[152:155], v[156:159], v[48:51]\n\t"
        "v_mfma_f32_16x16x32_bf16 v[48:51], v[140:143], v[168:171], v[48:51]\n\t"
        "ds_read_b128 v[128:131], v236 offset:32768\n\t"
        "v_cvt_pk_bf16_f32 v233, v74, v78\n\t"
        "v_mfma_f32_16x16x32_bf16 v[52:55], v[140:143], v[172:175], v[52:55]\n\t"
        "v_cvt_pk_bf16_f32 v232, v66, v70\n\t"
        "v_mfma_f32_16x16x32_bf16 v[52:55], v[152:155], v[176:179], v[52:55]\n\t"
        "ds_read_b128 v[180:183], v237 offset:32768\n\t"
        "ds_read_b128 v[188:191], %[xb0] offset:0\n\t"
        "v_mfma_f32_16x16x32_bf16 v[56:59], v[152:155], v[184:187], v[56:59]\n\t"
        "global_store_dwordx2 %[oo2], v[232:233], %[ob2] offset:0 nt\n\t"
        "v_mfma_f32_16x16x32_bf16 v[56:59], v[140:143], v[192:195], v[56:59]\n\t"
        "v_cvt_pk_bf16_f32 v235, v75, v79\n\t"
        "v_cvt_pk_bf16_f32 v234, v67, v71\n\t"
        "v_mfma_f32_16x16x32_bf16 v[60:63], v[140:143], v[144:147], v[60:63]\n\t"
        "v_mfma_f32_16x16x32_bf16 v[60:63], v[152:155], v[132:135], v[60:63]\n\t"
        "global_store_dwordx2 %[oo3], v[234:235], %[ob2] offset:0 nt\n\t"
        "s_barrier\n\t"
        "s_waitcnt lgkmcnt(3)\n\t"
        "v_mfma_f32_16x16x32_bf16 v[80:83], v[136:139], v[156:159], v[80:83]\n\t"
        "v_mfma_f32_16x16x32_bf16 v[80:83], v[148:151], v[168:171], v[80:83]\n\t"
        "ds_read_b128 v[140:143], %[ba] offset:0\n\t"
        "ds_read_b128 v[152:155], %[xb1] offset:0\n\t"
        "v_mfma_f32_16x16x32_bf16 v[84:87], v[148:151], v[172:175], v[84:87]\n\t"
        "v_cvt_pk_bf16_f32 v229, v104, v108\n\t"
        "v_cvt_pk_bf16_f32 v228, v96, v100\n\t"
        "v_mfma_f32_16x16x32_bf16 v[84:87], v[136:139], v[176:179], v[84:87]\n\t"
        ".Lwp17:\n\t"
        "s_cbranch_execz .Lwp18\n\t"
        "ds_read_b128 v[196:199], %[ba] offset:8192\n\t"
        "v_mfma_f32_16x16x32_bf16 v[88:91], v[136:139], v[184:187], v[88:91]\n\t"
        "ds_read_b128 v[200:203], %[ba] offset:1024\n\t"
        "ds_read_b128 v[204:207], %[ba] offset:9216\n\t"
        "v_mfma_f32_16x16x32_bf16 v[88:91], v[148:151], v[192:195], v[88:91]\n\t"
        "global_store_dwordx2 %[oo0], v[228:229], %[ob3] offset:0 nt\n\t"
        "ds_read_b128 v[208:211], %[ba] offset:9472\n\t"
        "v_mfma_f32_16x16x32_bf16 v[92:95], v[148:151], v[144:147], v[92:95]\n\t"
        "ds_read_b128 v[212:215], %[ba] offset:1280\n\t"
        "ds_read_b128 v[216:219], %[ba] offset:8448\n\t"
        "v_mfma_f32_16x16x32_bf16 v[92:95], v[136:139], v[132:135], v[92:95]\n\t"
        "v_cvt_pk_bf16_f32 v231, v105, v109\n\t"
        "v_cvt_pk_bf16_f32 v230, v97, v101\n\t"
        "v_mfma_f32_16x16x32_bf16 v[112:115], v[160:163], v[156:159], v[112:115]\n\t"
        "ds_read_b128 v[136:139], %[ba] offset:256\n\t"
        "ds_read_b128 v[148:151], %[ba] offset:512\n\t"
        "v_mfma_f32_16x16x32_bf16 v[112:115], v[164:167], v[168:171], v[112:115]\n\t"
        "ds_read_b128 v[156:159], %[ba] offset:8704\n\t"
        "global_store_dwordx2 %[oo1], v[230:231], %[ob3] offset:0 nt\n\t"
        "v_mfma_f32_16x16x32_bf16 v[116:119], v[164:167], v[172:175], v[116:119]\n\t"
        "ds_read_b128 v[168:171], %[ba] offset:1536\n\t"
        "ds_read_b128 v[220:223], %[ba] offset:9728\n\t"
        "v_mfma_f32_16x16x32_bf16 v[116:119], v[160:163], v[176:179], v[116:119]\n\t"
        "v_cvt_pk_bf16_f32 v233, v106, v110\n\t"
        "v_cvt_pk_bf16_f32 v232, v98, v102\n\t"
        "v_mfma_f32_16x16x32_bf16 v[120:123], v[160:163], v[184:187], v[120:123]\n\t"
        "ds_read_b128 v[172:175], %[ba] offset:9984\n\t"
        "ds_read_b128 v[176:179], %[ba] offset:1792\n\t"
        "v_mfma_f32_16x16x32_bf16 v[120:123], v[164:167], v[192:195], v[120:123]\n\t"
        "global_store_dwordx2 %[oo2], v[232:233], %[ob3] offset:0 nt\n\t"
        "ds_read_b128 v[184:187], %[ba] offset:8960\n\t"
        "v_mfma_f32_16x16x32_bf16 v[124:127], v[164:167], v[144:147], v[124:127]\n\t"
        "v_cvt_pk_bf16_f32 v235, v107, v111\n\t"
        "v_cvt_pk_bf16_f32 v234, v99, v103\n\t"
        "v_mfma_f32_16x16x32_bf16 v[124:127], v[160:163], v[132:135], v[124:127]\n\t"
        "ds_read_b128 v[144:147], %[ba] offset:768\n\t"
        "s_waitcnt lgkmcnt(9)\n\t"
        "v_mfma_f32_16x16x32_bf16 v[16:19], v[128:131], v[140:143], v[16:19]\n\t"
        "global_store_dwordx2 %[oo3], v[234:235], %[ob3] offset:0 nt\n\t"
        "v_mfma_f32_16x16x32_bf16 v[16:19], v[180:183], v[196:199], v[16:19]\n\t"
        "ds_read_b128 v[132:135], v236 offset:34816\n\t"
        "v_mfma_f32_16x16x32_bf16 v[16:19], v[188:191], v[200:203], v[16:19]\n\t"
        "v_mfma_f32_16x16x32_bf16 v[16:19], v[152:155], v[204:207], v[16:19]\n\t"
        "ds_read_b128 v[160:163], v237 offset:34816\n\t"
        "v_mfma_f32_16x16x32_bf16 v[20:23], v[152:155], v[208:211], v[20:23]\n\t"
        "v_mfma_f32_16x16x32_bf16 v[20:23], v[188:191], v[212:215], v[20:23]\n\t"
        "ds_read_b128 v[164:167], %[xb0] offset:2048\n\t"
        "v_mfma_f32_16x16x32_bf16 v[20:23], v[180:183], v[216:219], v[20:23]\n\t"
        "s_waitcnt lgkmcnt(4)\n\t"
        "v_mfma_f32_16x16x32_bf16 v[20:23], v[128:131], v[136:139], v[20:23]\n\t"
        "ds_read_b128 v[192:195], %[xb1] offset:2048\n\t"
        "v_mfma_f32_16x16x32_bf16 v[24:27], v[128:131], v[148:151], v[24:27]\n\t"
        "v_mfma_f32_16x16x32_bf16 v[24:27], v[180:183], v[156:159], v[24:27]\n\t"
        "v_mfma_f32_16x16x32_bf16 v[24:27], v[188:191], v[168:171], v[24:27]\n\t"
        "v_mfma_f32_16x16x32_bf16 v[24:27], v[152:155], v[220:223], v[24:27]\n\t"
        "v_mfma_f32_16x16x32_bf16 v[28:31], v[152:155], v[172:175], v[28:31]\n\t"
        "v_mfma_f32_16x16x32_bf16 v[28:31], v[188:191], v[176:179], v[28:31]\n\t"
        "v_mfma_f32_16x16x32_bf16 v[28:31], v[180:183], v[184:187], v[28:31]\n\t"
        "s_waitcnt lgkmcnt(1)\n\t"
        "v_mfma_f32_16x16x32_bf16 v[28:31], v[128:131], v[144:147], v[28:31]\n\t"
        "v_mfma_f32_16x16x32_bf16 v[48:51], v[132:135], v[140:143], v[48:51]\n\t"
        "v_mfma_f32_16x16x32_bf16 v[48:51], v[160:163], v[196:199], v[48:51]\n\t"
        "ds_read_b128 v[128:131], v236 offset:36864\n\t"
        "v_cvt_pk_bf16_f32 v228, v16, v20\n\t"
        "v_mfma_f32_16x16x32_bf16 v[48:51], v[164:167], v[200:203], v[48:51]\n\t"
        "s_waitcnt lgkmcnt(1)\n\t"
        "v_mfma_f32_16x16x32_bf16 v[48:51], v[192:195], v[204:207], v[48:51]\n\t"
        "ds_read_b128 v[152:155], v237 offset:36864\n\t"
        "v_cvt_pk_bf16_f32 v229, v24, v28\n\t"
        "v_mfma_f32_16x16x32_bf16 v[52:55], v[192:195], v[208:211], v[52:55]\n\t"
        "global_store_dwordx2 %[oo0], v[228:229], %[ob0] offset:128 nt\n\t"
        "v_mfma_f32_16x16x32_bf16 v[52:55], v[164:167], v[212:215], v[52:55]\n\t"
        "ds_read_b128 v[180:183], %[xb0] offset:4096\n\t"
        "v_cvt_pk_bf16_f32 v231, v25, v29\n\t"
        "v_mfma_f32_16x16x32_bf16 v[52:55], v[160:163], v[216:219], v[52:55]\n\t"
        "v_cvt_pk_bf16_f32 v230, v17, v21\n\t"
        "v_mfma_f32_16x16x32_bf16 v[52:55], v[132:135], v[136:139], v[52:55]\n\t"
        "ds_read_b128 v[188:191], %[xb1] offset:4096\n\t"
        "global_store_dwordx2 %[oo1], v[230:231], %[ob0] offset:128 nt\n\t"
        "v_mfma_f32_16x16x32_bf16 v[56:59], v[132:135], v[148:151], v[56:59]\n\t"
        "v_mfma_f32_16x16x32_bf16 v[56:59], v[160:163], v[156:159], v[56:59]\n\t"
        "v_cvt_pk_bf16_f32 v233, v26, v30\n\t"
        "v_cvt_pk_bf16_f32 v232, v18, v22\n\t"
        "v_mfma_f32_16x16x32_bf16 v[56:59], v[164:167], v[168:171], v[56:59]\n\t"
        "v_mfma_f32_16x16x32_bf16 v[56:59], v[192:195], v[220:223], v[56:59]\n\t"
        "global_store_dwordx2 %[oo2], v[232:233], %[ob0] offset:128 nt\n\t"
        "v_mfma_f32_16x16x32_bf16 v[60:63], v[192:195], v[172:175], v[60:63]\n\t"
        "v_mfma_f32_16x16x32_bf16 v[60:63], v[164:167], v[176:179], v[60:63]\n\t"
        "v_cvt_pk_bf16_f32 v235, v27, v31\n\t"
        "v_cvt_pk_bf16_f32 v234, v19, v23\n\t"
        "v_mfma_f32_16x16x32_bf16 v[60:63], v[160:163], v[184:187], v[60:63]\n\t"
        ".Lwp18:\n\t"
        "s_cbranch_execz .Lwp97\n\t"
        "v_mfma_f32_16x16x32_bf16 v[60:63], v[132:135], v[144:147], v[60:63]\n\t"
        "global_store_dwordx2 %[oo3], v[234:235], %[ob0] offset:128 nt\n\t"
        "s_waitcnt lgkmcnt(0)\n\t"
        "v_mfma_f32_16x16x32_bf16 v[80:83], v[128:131], v[140:143], v[80:83]\n\t"
        "v_mfma_f32_16x16x32_bf16 v[80:83], v[152:155], v[196:199], v[80:83]\n\t"
        "ds_read_b128 v[132:135], v236 offset:38912\n\t"
        "v_cvt_pk_bf16_f32 v228, v48, v52\n\t"
        "v_mfma_f32_16x16x32_bf16 v[80:83], v[180:183], v[200:203], v[80:83]\n\t"
        "v_mfma_f32_16x16x32_bf16 v[80:83], v[188:191], v[204:207], v[80:83]\n\t"
        "ds_read_b128 v[160:163], v237 offset:38912\n\t"
        "v_cvt_pk_bf16_f32 v229, v56, v60\n\t"
        "v_mfma_f32_16x16x32_bf16 v[84:87], v[188:191], v[208:211], v[84:87]\n\t"
        "global_store_dwordx2 %[oo0], v[228:229], %[ob1] offset:128 nt\n\t"
        "v_mfma_f32_16x16x32_bf16 v[84:87], v[180:183], v[212:215], v[84:87]\n\t"
        "ds_read_b128 v[164:167], %[xb0] offset:6144\n\t"
        "v_cvt_pk_bf16_f32 v231, v57, v61\n\t"
        "v_mfma_f32_16x16x32_bf16 v[84:87], v[152:155], v[216:219], v[84:87]\n\t"
        "v_cvt_pk_bf16_f32 v230, v49, v53\n\t"
        "v_mfma_f32_16x16x32_bf16 v[84:87], v[128:131], v[136:139], v[84:87]\n\t"
        "ds_read_b128 v[192:195], %[xb1] offset:6144\n\t"
        "global_store_dwordx2 %[oo1], v[230:231], %[ob1] offset:128 nt\n\t"
        "v_mfma_f32_16x16x32_bf16 v[88:91], v[128:131], v[148:151], v[88:91]\n\t"
        "v_mfma_f32_16x16x32_bf16 v[88:91], v[152:155], v[156:159], v[88:91]\n\t"
        "v_cvt_pk_bf16_f32 v233, v58, v62\n\t"
        "v_cvt_pk_bf16_f32 v232, v50, v54\n\t"
        "v_mfma_f32_16x16x32_bf16 v[88:91], v[180:183], v[168:171], v[88:91]\n\t"
        "v_mfma_f32_16x16x32_bf16 v[88:91], v[188:191], v[220:223], v[88:91]\n\t"
        "global_store_dwordx2 %[oo2], v[232:233], %[ob1] offset:128 nt\n\t"
        "v_mfma_f32_16x16x32_bf16 v[92:95], v[188:191], v[172:175], v[92:95]\n\t"
        "v_mfma_f32_16x16x32_bf16 v[92:95], v[180:183], v[176:179], v[92:95]\n\t"
        "v_cvt_pk_bf16_f32 v235, v59, v63\n\t"
        "v_cvt_pk_bf16_f32 v234, v51, v55\n\t"
        "v_mfma_f32_16x16x32_bf16 v[92:95], v[152:155], v[184:187], v[92:95]\n\t"
        "v_mfma_f32_16x16x32_bf16 v[92:95], v[128:131], v[144:147], v[92:95]\n\t"
        "global_store_dwordx2 %[oo3], v[234:235], %[ob1] offset:128 nt\n\t"
        "s_waitcnt lgkmcnt(0)\n\t"
        "v_mfma_f32_16x16x32_bf16 v[112:115], v[132:135], v[140:143], v[112:115]\n\t"
        "v_mfma_f32_16x16x32_bf16 v[112:115], v[160:163], v[196:199], v[112:115]\n\t"
        "v_cvt_pk_bf16_f32 v228, v80, v84\n\t"
        "v_mfma_f32_16x16x32_bf16 v[112:115], v[164:167], v[200:203], v[112:115]\n\t"
        "v_mfma_f32_16x16x32_bf16 v[112:115], v[192:195], v[204:207], v[112:115]\n\t"
        "v_cvt_pk_bf16_f32 v229, v88, v92\n\t"
        "global_store_dwordx2 %[oo0], v[228:229], %[ob2] offset:128 nt\n\t"
        "v_mfma_f32_16x16x32_bf16 v[116:119], v[192:195], v[208:211], v[116:119]\n\t"
        "v_mfma_f32_16x16x32_bf16 v[116:119], v[164:167], v[212:215], v[116:119]\n\t"
        "v_cvt_pk_bf16_f32 v231, v89, v93\n\t"
        "v_cvt_pk_bf16_f32 v230, v81, v85\n\t"
        "v_mfma_f32_16x16x32_bf16 v[116:119], v[160:163], v[216:219], v[116:119]\n\t"
        "v_mfma_f32_16x16x32_bf16 v[116:119], v[132:135], v[136:139], v[116:119]\n\t"
        "global_store_dwordx2 %[oo1], v[230:231], %[ob2] offset:128 nt\n\t"
        "v_mfma_f32_16x16x32_bf16 v[120:123], v[132:135], v[148:151], v[120:123]\n\t"
        "v_mfma_f32_16x16x32_bf16 v[120:123], v[160:163], v[156:159], v[120:123]\n\t"
        "v_cvt_pk_bf16_f32 v233, v90, v94\n\t"
        "v_cvt_pk_bf16_f32 v232, v82, v86\n\t"
        "v_mfma_f32_16x16x32_bf16 v[120:123], v[164:167], v[168:171], v[120:123]\n\t"
        "v_mfma_f32_16x16x32_bf16 v[120:123], v[192:195], v[220:223], v[120:123]\n\t"
        "global_store_dwordx2 %[oo2], v[232:233], %[ob2] offset:128 nt\n\t"
        "v_mfma_f32_16x16x32_bf16 v[124:127], v[192:195], v[172:175], v[124:127]\n\t"
        "v_mfma_f32_16x16x32_bf16 v[124:127], v[164:167], v[176:179], v[124:127]\n\t"
        "v_cvt_pk_bf16_f32 v234, v113, v117\n\t"
        "v_cvt_pk_bf16_f32 v228, v112, v116\n\t"
        "v_mfma_f32_16x16x32_bf16 v[124:127], v[160:163], v[184:187], v[124:127]\n\t"
        "v_cvt_pk_bf16_f32 v231, v91, v95\n\t"
        "v_cvt_pk_bf16_f32 v230, v83, v87\n\t"
        "v_mfma_f32_16x16x32_bf16 v[124:127], v[132:135], v[144:147], v[124:127]\n\t"
        "v_cvt_pk_bf16_f32 v232, v115, v119\n\t"
        "v_cvt_pk_bf16_f32 v128, v114, v118\n\t"
        "global_store_dwordx2 %[oo3], v[230:231], %[ob2] offset:128 nt\n\t"
        "s_nop 7\n\t"
        "v_cvt_pk_bf16_f32 v229, v120, v124\n\t"
        "global_store_dwordx2 %[oo0], v[228:229], %[ob3] offset:128 nt\n\t"
        "v_cvt_pk_bf16_f32 v235, v121, v125\n\t"
        "global_store_dwordx2 %[oo1], v[234:235], %[ob3] offset:128 nt\n\t"
        "v_cvt_pk_bf16_f32 v129, v122, v126\n\t"
        "global_store_dwordx2 %[oo2], v[128:129], %[ob3] offset:128 nt\n\t"
        "v_cvt_pk_bf16_f32 v233, v123, v127\n\t"
        "global_store_dwordx2 %[oo3], v[232:233], %[ob3] offset:128 nt\n\t"
        :
        : [xb0] "v"(xb0), [xb1] "v"(xb1), [ba] "v"(ba), [oo0] "v"(oo[0]), [oo1] "v"(oo[1]), [oo2] "v"(oo[2]), [oo3] "v"(oo[3]),
          [ob0] "s"(ob[0]), [ob1] "s"(ob[1]), [ob2] "s"(ob[2]), [ob3] "s"(ob[3]), [w] "s"(w)
        : "scc", "memory", "s60", "s61", "s62", "s63", "s64", "s65", "s66", "s67", "v0", "v1", "v2", "v3", "v4", "v5", "v6", "v7", "v8", "v9", "v10", "v11", "v12", "v13", "v14", "v15", "v16", "v17", "v18", "v19", "v20", "v21", "v22", "v23", "v24", "v25", "v26", "v27", "v28", "v29", "v30", "v31", "v32", "v33", "v34", "v35", "v36", "v37", "v38", "v39", "v40", "v41", "v42", "v43", "v44", "v45", "v46", "v47", "v48", "v49", "v50", "v51", "v52", "v53", "v54", "v55", "v56", "v57", "v58", "v59", "v60", "v61", "v62", "v63", "v64", "v65", "v66", "v67", "v68", "v69", "v70", "v71", "v72", "v73", "v74", "v75", "v76", "v77", "v78", "v79", "v80", "v81", "v82", "v83", "v84", "v85", "v86", "v87", "v88", "v89", "v90", "v91", "v92", "v93", "v94", "v95", "v96", "v97", "v98", "v99", "v100", "v101", "v102", "v103", "v104", "v105", "v106", "v107", "v108", "v109", "v110", "v111", "v112", "v113", "v114", "v115", "v116", "v117", "v118", "v119", "v120", "v121", "v122", "v123", "v124", "v125", "v126", "v127", "v128", "v129", "v130", "v131", "v132", "v133", "v134", "v135", "v136", "v137", "v138", "v139", "v140", "v141", "v142", "v143", "v144", "v145", "v146", "v147", "v148", "v149", "v150", "v151", "v152", "v153", "v154", "v155", "v156", "v157", "v158", "v159", "v160", "v161", "v162", "v163", "v164", "v165", "v166", "v167", "v168", "v169", "v170", "v171", "v172", "v173", "v174", "v175", "v176", "v177", "v178", "v179", "v180", "v181", "v182", "v183", "v184", "v185", "v186", "v187", "v188", "v189", "v190", "v191", "v192", "v193", "v194", "v195", "v196", "v197", "v198", "v199", "v200", "v201", "v202", "v203", "v204", "v205", "v206", "v207", "v208", "v209", "v210", "v211", "v212", "v213", "v214", "v215", "v216", "v217", "v218", "v219", "v220", "v221", "v222", "v223", "v224", "v225", "v226", "v227", "v228", "v229", "v230", "v231", "v232", "v233", "v234", "v235", "v236", "v237", "v238", "v239");
}

extern "C" void kernel_launch(void* const* d_in, const int* in_sizes, int n_in,
                              void* d_out, int out_size, void* d_ws, size_t ws_size,
                              hipStream_t stream) {
    const u16* x = (const u16*)d_in[0];
    const int* qw = (const int*)d_in[1];
    const u16* lut = (const u16*)d_in[2];
    u16* out = (u16*)d_out;
    (void)hipFuncSetAttribute((const void*)fused_kernel, hipFuncAttributeMaxDynamicSharedMemorySize, LDS_BYTES);
    fused_kernel<<<(M_ / BM) * (N_ / BN) + NPF, 512, LDS_BYTES, stream>>>(x, qw, lut, out, (char*)d_ws);
}
```

```cpp
#include <hip/hip_runtime.h>
#include <hip/hip_bf16.h>

typedef unsigned short u16;
typedef unsigned int u32;
using bf16x8 = __attribute__((ext_vector_type(8))) short;
using f32x4  = __attribute__((ext_vector_type(4))) float;
using i32x4  = __attribute__((ext_vector_type(4))) int;
using u32x2  = __attribute__((ext_vector_type(2))) unsigned int;
using u32x4  = __attribute__((ext_vector_type(4))) unsigned int;

constexpr int M_ = 2048, K_ = 1024, N_ = 3584;
constexpr int BM = 256, BN = 128;
constexpr int NPF = 32;
#define XSLEEP 4
#define SP0 3
#define NQ1 4
#define CPF 116
#define CPFB 32
#define PACE 0
#define VORD 1
constexpr int XS = BM * 64 * 2;
constexpr int BS = BN * 32 * 2;
constexpr int OFF_B = 3 * XS;
constexpr int WSL = 2 * BS;
constexpr int OFF_L = OFF_B + 3 * WSL;
constexpr int OFF_D = OFF_L + 16 * BN * 2;
constexpr int WS_F = 28 * 256, WS_W = 65536;
constexpr unsigned TW = 28 * 16384;
constexpr int SPIN_MAX = 1 << 16;
constexpr int LDS_BYTES = OFF_D + 1024;

typedef __bf16 bf16x2_t __attribute__((ext_vector_type(2)));
typedef float f32x2_t __attribute__((ext_vector_type(2)));
__device__ __forceinline__ u32 pack_bf16(float lo, float hi) {
    f32x2_t f = {lo, hi};
    bf16x2_t b = __builtin_convertvector(f, bf16x2_t);
    return __builtin_bit_cast(u32, b);
}

#define ROT0 0
#define OWN(mt, nt) (((mt) >> 1) ^ (ROT0 ? (nt) & 3 : 0))
#define TLO(mt, nt) ((0 && ((mt) & 1) ? 0x75631240u : 0x76532140u) ^ ((u32)OWN(mt, nt) * 0x11111111u))
#define THI(mt, nt) (0xFEDCBA98u ^ ((u32)(0) * 0x11111111u))
#define BAR() __builtin_amdgcn_s_barrier()
#define SB() __builtin_amdgcn_sched_barrier(0)
#define WAITQ(n, Qa) asm volatile("s_waitcnt vmcnt(" #n ") lgkmcnt(0)" : "+v"(Qa[0]), "+v"(Qa[1]), "+v"(Qa[2]), "+v"(Qa[3]), \
                                  "+v"(Qa[4]), "+v"(Qa[5]), "+v"(Qa[6]), "+v"(Qa[7]) :: "memory")
#define PBAR() BAR()
#define PAD(id, nx) asm volatile(".Lwp%c0:\n\ts_cbranch_execz .Lwp%c1" :: "i"(id), "i"(nx))

__global__ __launch_bounds__(512, 2) void fused_kernel(const u16* __restrict__ x_, const int* __restrict__ qw_,
                                                       const u16* __restrict__ lut_, u16* __restrict__ out_, char* ws_) {
    extern __shared__ __attribute__((aligned(16))) char smem[];
    typedef unsigned long u64x4 __attribute__((ext_vector_type(4)));
    u64x4 ka;
    unsigned long kp[5], kw;
    asm volatile("s_load_dwordx8 %0, %2, 0x0\n\ts_load_dwordx2 %1, %2, 0x20" : "=&s"(ka), "=&s"(kw) : "s"(__builtin_amdgcn_kernarg_segment_ptr()));
    {
        unsigned long pc0, ex;
        u32 co;
        asm volatile("s_getpc_b64 %0\n\t"
                     "s_cmp_lt_u32 %4, %5\n\t"
                     "s_cselect_b32 m0, %6, 0\n\t"
                     "v_cmp_gt_u32 vcc, m0, %3\n\t"
                     "s_and_b32 m0, %4, 24\n\t"
                     "s_lshl_b32 m0, m0, 4\n\t"
                     "v_lshl_add_u32 %2, %3, 9, m0\n\t"
                     "s_mov_b32 m0, %7\n\t"
                     "s_and_saveexec_b64 %1, vcc\n\t"
                     "global_load_lds_dword %2, %0\n\t"
                     "s_mov_b64 exec, %1"
                     : "=&s"(pc0), "=&s"(ex), "=&v"(co) : "v"(threadIdx.x), "s"(blockIdx.x), "i"(CPFB), "i"(CPF / 4), "i"(OFF_D) : "vcc", "scc", "memory");
    }
#define KWAIT() do { SB(); asm volatile("s_waitcnt lgkmcnt(0)\n\ts_mov_b64 %0, %5\n\ts_mov_b64 %1, %6\n\ts_mov_b64 %2, %7\n\ts_mov_b64 %3, %8\n\ts_mov_b64 %4, %9" \
                     : "=&s"(kp[0]), "=&s"(kp[1]), "=&s"(kp[2]), "=&s"(kp[3]), "=&s"(kp[4]) : "s"(ka[0]), "s"(ka[1]), "s"(ka[2]), "s"(ka[3]), "s"(kw) : "memory"); SB(); } while (0)
#define KPTR(i) (kp[i])
    const int tid = threadIdx.x;
    const int w = __builtin_amdgcn_readfirstlane(tid >> 6);
    const int lane = tid & 63;
    const int bid = (int)blockIdx.x - NPF;
    const int xcd = blockIdx.x & 7, idx = bid >> 3;
    if (bid < 0) {
        const int pw = (blockIdx.x >> 3) * 8 + w;
        KWAIT();
        const char* qb = (const char*)KPTR(1) + (xcd >> 1) * 896 * 4 + (long)((xcd & 1) * 2) * (64 * N_ * 4);
        const char* xb = (const char*)KPTR(0) + (long)(xcd & 1) * 1024 * 2048;
        const u32 dst = (u32)(size_t)(smem) + w * 256;
        {
            const int m = (xcd & 1) * 4 + (blockIdx.x >> 3);
            const u32 off = (u32)((m * 256 + (w & 3) * 64 + lane) * 2048 + ((w >> 2) * 4 + (m >> 1)) * 128);
            if (0 && w >= 4) __builtin_amdgcn_s_sleep(0);
            asm volatile("s_mov_b32 m0, %2\n\ts_nop 0\n\tglobal_load_lds_dword %0, %1" :: "v"(off), "s"(KPTR(0)), "s"(dst) : "memory");
        }
#pragma unroll 1
        for (int G = pw; G < 168; G += 32) {
            int kind = 0, arg = 0, e = 0;
            if (G >= 0) { kind = 0; arg = 2; e = G - 0 + (xcd >> 1) * 4; }
            if (G >= 4) { kind = 0; arg = 3; e = G - 4 + (xcd >> 1) * 4; }
            if (G >= 8) { kind = 0; arg = 4; e = G - 8 + (xcd >> 1) * 4; }
            if (G >= 12) { kind = 1; arg = 8; e = G - 12; }
            if (G >= 32) { kind = 0; arg = 5; e = G - 32 + (xcd >> 1) * 4; }
            if (G >= 36) { kind = 1; arg = 8; e = G - 16; }
            if (G >= 64) { kind = 1; arg = 8; e = G - 16; }
            if (G >= 72) { kind = 0; arg = 6; e = G - 72 + (xcd >> 1) * 4; }
            if (G >= 76) { kind = 0; arg = 7; e = G - 76 + (xcd >> 1) * 4; }
            if (G >= 80) { kind = 1; arg = 12; e = G - 80; }
            if (G >= 96) { kind = 0; arg = 8; e = G - 96 + (xcd >> 1) * 4; }
            if (G >= 100) { kind = 0; arg = 9; e = G - 100 + (xcd >> 1) * 4; }
            if (G >= 104) { kind = 1; arg = 12; e = G - 88; }
            if (G >= 128) { kind = 0; arg = 10; e = G - 128 + (xcd >> 1) * 4; }
            if (G >= 132) { kind = 1; arg = 12; e = G - 92; }
            if (G >= 148) { kind = 0; arg = 11; e = G - 148 + (xcd >> 1) * 4; }
            if (G >= 152) { kind = 0; arg = 12; e = G - 152 + (xcd >> 1) * 4; }
            if (G >= 156) { kind = 0; arg = 13; e = G - 156 + (xcd >> 1) * 4; }
            if (G >= 160) { kind = 0; arg = 14; e = G - 160 + (xcd >> 1) * 4; }
            if (G >= 164) { kind = 0; arg = 15; e = G - 164 + (xcd >> 1) * 4; }
            asm volatile("s_waitcnt vmcnt(%0)" :: "i"(PACE) : "memory");
            if (kind) {
                const int L = e * 64 + lane;
                const int r = L / 28, c = L - r * 28;
                const u32 off = (u32)((arg * 64 + r) * (N_ * 4) + c * 128);
                asm volatile("s_mov_b32 m0, %2\n\ts_nop 0\n\tglobal_load_lds_dword %0, %1" :: "v"(off), "s"(qb), "s"(dst) : "memory");
            } else {
                const int t = ROT0 || arg >= 8 ? arg : (int)((TLO((xcd & 1) * 4 + (e >> 2), 0) >> (4 * arg)) & 15);
                const u32 off = (u32)((e * 64 + lane) * 2048 + t * 128);
                asm volatile("s_mov_b32 m0, %2\n\ts_nop 0\n\tglobal_load_lds_dword %0, %1" :: "v"(off), "s"(xb), "s"(dst) : "memory");
            }
        }
        asm volatile("s_waitcnt vmcnt(0)" ::: "memory");
        return;
    }
    const int mt = (xcd & 1) * 4 + (idx & 3);
    const int nt = (xcd >> 1) * 7 + (idx >> 2);
    const int row0 = mt * BM, col0 = nt * BN;

    if (__builtin_expect(w >= 4, 0)) {
        int lane;
        asm volatile("v_mbcnt_lo_u32_b32 %0, -1, 0\n\tv_mbcnt_hi_u32_b32 %0, -1, %0" : "=v"(lane));
        const int p = w - 4;
        const int l = lane & 31, h = lane >> 5;
        const int g = lane >> 4;
        u32 xoff[4];
#pragma unroll
        for (int i = 0; i < 4; ++i)
            xoff[i] = (u32)(((row0 + p * 64 + i * 8 + (lane >> 3)) * K_ + (((lane & 7) ^ ((i & 1) * 4 + g)) * 8)) * 2 - i * 1024);
        const u32 ldsx = (u32)(size_t)(smem) + p * 8192;
        auto issue_x1 = [&](int tile, int hh, int slotoff, int i) {
            const u16* base = (const u16*)KPTR(0) + (long)hh * 32 * K_ + tile * 64;
            const u32 dst = ldsx + slotoff + hh * 4096;
            if (i == 0) asm volatile("s_mov_b32 m0, %2\n\ts_nop 0\n\tglobal_load_lds_dwordx4 %0, %1" :: "v"(xoff[0]), "s"(base), "s"(dst) : "memory");
            else asm volatile("global_load_lds_dwordx4 %0, %1 offset:%2" :: "v"(xoff[i]), "s"(base), "i"(i * 1024) : "memory");
        };
        u32 qoff[8];
#pragma unroll
        for (int r = 0; r < 8; ++r) qoff[r] = (u32)(((p * 16 + h * 8 + r) * N_ + col0 + l * 4) * 4);
        auto issue_q1 = [&](int t, i32x4 (&Q)[8], int r) {
            const int* base = (const int*)KPTR(1) + (long)t * 64 * N_;
            asm volatile("global_load_dwordx4 %0, %1, %2 " : "=v"(Q[r]) : "v"(qoff[r]), "s"(base) : "memory");
        };
        u32 lutl = (u32)(size_t)(smem) + OFF_L + l * 4;
        u32 bdst = (u32)(size_t)(smem) + OFF_B + (p >> 1) * BS + ((2 * p + h) & 3) * 2048 + ((l >> 4) * 64 + (l & 15)) * 16;
        u32 V[4][8];
#define LUT1(Q, j, r) asm volatile("v_lshl_add_u32 %0, %1, 8, %2\n\tds_read_u16 %0, %0 offset:%3" : "=&v"(V[j][r]) \
                                   : "v"(Q[r][j]), "v"(lutl), "i"(((j) >> 1) * 128 + ((j) & 1) * 2) : "memory")
#define LUT4(Q, e) do { LUT1(Q, (e) >> 1, ((e) & 1) * 4); LUT1(Q, (e) >> 1, ((e) & 1) * 4 + 1); \
                        LUT1(Q, (e) >> 1, ((e) & 1) * 4 + 2); LUT1(Q, (e) >> 1, ((e) & 1) * 4 + 3); SB(); } while (0)
#define WRB(j, n) do { u32x4 b_; \
                       asm volatile("s_waitcnt lgkmcnt(" #n ")\n\t" \
                                    "v_lshl_or_b32 %0, %5, 16, %4\n\tv_lshl_or_b32 %1, %7, 16, %6\n\t" \
                                    "v_lshl_or_b32 %2, %9, 16, %8\n\tv_lshl_or_b32 %3, %11, 16, %10" \
                                    : "=&v"(b_[0]), "=&v"(b_[1]), "=&v"(b_[2]), "=&v"(b_[3]) \
                                    : "v"(V[j][0]), "v"(V[j][1]), "v"(V[j][2]), "v"(V[j][3]), "v"(V[j][4]), "v"(V[j][5]), "v"(V[j][6]), "v"(V[j][7]) : "memory"); \
                       asm volatile("ds_write_b128 %0, %1 offset:%2" :: "v"(bdst), "v"(b_), "i"((j) * 256) : "memory"); SB(); } while (0)
        const int i2 = OWN(mt, nt), h2 = mt & 1;
        const unsigned long tseq = ((unsigned long)THI(mt, nt) << 32) | TLO(mt, nt);
        const u32 ldsw = (u32)(size_t)(smem) + OFF_B + p * 4096;
        u32 wv = (u32)(nt * 16384 + p * 4096 + lane * 16);
        u32 wo = (u32)(nt * 16384 + p * 4096 + h * 2048 + (l >> 4) * 1024 + (l & 15) * 16);
        u32 po = (u32)(WS_F + (nt * 4 + p) * 256 + lane * 4);
        u32 fo0 = po + (u32)(i2 * 16 + h2 * 8);
        u32 no = (u32)(nt * 256 + (lane & 1) * 4);
        u32 nvl = no;
        KWAIT();
        BAR();
        const char* st1 = (const char*)KPTR(4) + WS_W + (long)(i2 + 4) * TW + h2 * 512;
        const char* qa1 = (const char*)KPTR(1);
        const int tb = 8 + (mt >> 1), kg = h2 * 4 + p;
        const char* qbg = (const char*)KPTR(1) + (long)(tb * 64 + kg * 8 - p * 16) * (N_ * 4);
        const char* stb = (const char*)KPTR(4) + WS_W + (long)tb * TW + (kg * 2048 - p * 4096);
        u32 fob = po + (u32)(((kg >> 1) - p) * 256 + tb * 16 + (kg & 1) * 8);
        asm volatile("" : "+v"(wo), "+v"(fo0), "+v"(fob), "+v"(wv), "+v"(po), "+v"(bdst), "+v"(lutl), "+v"(qoff[0]));
        asm volatile("" : "+v"(xoff[0]), "+v"(xoff[1]), "+v"(xoff[2]), "+v"(xoff[3]));
        asm volatile("" : "+s"(st1), "+s"(qa1), "+s"(qbg), "+s"(stb));
        {
            unsigned long ex_;
            asm volatile("s_mov_b64 %0, exec\n\ts_mov_b64 exec, 0\n\t"
                         "s_cmp_eq_u32 %1, 0\n\ts_cbranch_scc1 .Lwp30\n\t"
                         "s_cmp_eq_u32 %1, 1\n\ts_cbranch_scc1 .Lwp31\n\t"
                         "s_cmp_eq_u32 %1, 2\n\ts_cbranch_scc1 .Lwp32\n\t"
                         "s_branch .Lwp33\n\t"
                         ".Lwp98:\n\ts_mov_b64 exec, %0" : "=&s"(ex_) : "s"(p) : "scc", "memory");
        }
        asm volatile(
            "s_mov_b32 s73, 65536\n\t"
            "s_mov_b64 s[84:85], %[ws]\n\t"
            "s_add_u32 s84, s84, 65536\n\t"
            "s_addc_u32 s85, s85, 0\n\t"
            "s_mov_b64 s[56:57], %[tseq]\n\t"
            "s_bfe_u32 s40, s56, 262144\n\t"
            "s_bfe_u32 s41, s56, 262148\n\t"
            "s_bfe_u32 s42, s56, 262152\n\t"
            "s_bfe_u32 s43, s56, 262156\n\t"
            "s_bfe_u32 s44, s56, 262160\n\t"
            "s_bfe_u32 s45, s56, 262164\n\t"
            "s_bfe_u32 s46, s56, 262168\n\t"
            "s_bfe_u32 s47, s56, 262172\n\t"
            "s_bfe_u32 s48, s57, 262144\n\t"
            "s_bfe_u32 s49, s57, 262148\n\t"
            "s_bfe_u32 s50, s57, 262152\n\t"
            "s_bfe_u32 s51, s57, 262156\n\t"
            "s_bfe_u32 s52, s57, 262160\n\t"
            "s_bfe_u32 s53, s57, 262164\n\t"
            "s_bfe_u32 s54, s57, 262168\n\t"
            "s_bfe_u32 s55, s57, 262172\n\t"
            "s_barrier\n\t"
            ".Lwp30:\n\t"
            "s_cbranch_execz .Lwp98\n\t"
            "s_add_u32 m0, %[ldsx], 0\n\t"
            "s_lshl_b32 s76, s40, 7\n\t"
            "s_add_u32 s68, %[xpl], s76\n\t"
            "s_addc_u32 s69, %[xph], 0\n\t"
            "global_load_lds_dwordx4 %[x0], s[68:69] offset:0\n\t"
            "global_load_lds_dwordx4 %[x1], s[68:69] offset:1024\n\t"
            "global_load_lds_dwordx4 %[x2], s[68:69] offset:2048\n\t"
            "global_load_lds_dwordx4 %[x3], s[68:69] offset:3072\n\t"
            "s_add_u32 m0, %[ldsx], 4096\n\t"
            "s_lshl_b32 s76, s40, 7\n\t"
            "s_add_u32 s76, s76, 65536\n\t"
            "s_add_u32 s68, %[xpl], s76\n\t"
            "s_addc_u32 s69, %[xph], 0\n\t"
            "global_load_lds_dwordx4 %[x0], s[68:69] offset:0\n\t"
            "global_load_lds_dwordx4 %[x1], s[68:69] offset:1024\n\t"
            "global_load_lds_dwordx4 %[x2], s[68:69] offset:2048\n\t"
            "global_load_lds_dwordx4 %[x3], s[68:69] offset:3072\n\t"
            "global_load_dword %[nvl], %[nvl], %[ws] sc1\n\t"
            "s_mul_i32 s76, s41, 917504\n\t"
            "s_mov_b64 s[88:89], %[qa1]\n\t"
            "s_add_u32 s88, s88, s76\n\t"
            "s_addc_u32 s89, s89, 0\n\t"
            "global_load_dwordx4 v[128:131], %[qo], s[88:89] \n\t"
            "s_add_u32 s88, s88, 14336\n\t"
            "s_addc_u32 s89, s89, 0\n\t"
            "global_load_dwordx4 v[132:135], %[qo], s[88:89] \n\t"
            "s_add_u32 s88, s88, 14336\n\t"
            "s_addc_u32 s89, s89, 0\n\t"
            "global_load_dwordx4 v[136:139], %[qo], s[88:89] \n\t"
            "s_add_u32 s88, s88, 14336\n\t"
            "s_addc_u32 s89, s89, 0\n\t"
            "global_load_dwordx4 v[140:143], %[qo], s[88:89] \n\t"
            "s_add_u32 s88, s88, 14336\n\t"
            "s_addc_u32 s89, s89, 0\n\t"
            "global_load_dwordx4 v[144:147], %[qo], s[88:89] \n\t"
            "s_add_u32 s88, s88, 14336\n\t"
            "s_addc_u32 s89, s89, 0\n\t"
            "global_load_dwordx4 v[148:151], %[qo], s[88:89] \n\t"
            "s_add_u32 s88, s88, 14336\n\t"
            "s_addc_u32 s89, s89, 0\n\t"
            "global_load_dwordx4 v[152:155], %[qo], s[88:89] \n\t"
            "s_add_u32 s88, s88, 14336\n\t"
            "s_addc_u32 s89, s89, 0\n\t"
            "global_load_dwordx4 v[156:159], %[qo], s[88:89] \n\t"
            "s_waitcnt vmcnt(9)\n\t"
            "s_barrier\n\t"
            ".Lwp31:\n\t"
            "s_cbranch_execz .Lwp98\n\t"
            "s_lshl_b32 s76, %[h2], 9\n\t"
            "v_add_u32 v193, s76, %[bd]\n\t"
            "ds_read_b128 v[196:199], v193\n\t"
            "ds_read_b128 v[200:203], v193 offset:256\n\t"
            "s_sub_u32 s70, %[st1l], 1835008\n\t"
            "s_subb_u32 s71, %[st1h], 0\n\t"
            "s_waitcnt lgkmcnt(0)\n\t"
            "global_store_dwordx4 %[wo], v[196:199], s[70:71] sc1\n\t"
            "global_store_dwordx4 %[wo], v[200:203], s[70:71] offset:256 sc1\n\t"
            "s_waitcnt vmcnt(10)\n\t"
            "s_mov_b32 s76, 1664525\n\t"
            "v_mul_lo_u32 %[nvl], %[nvl], s76\n\t"
            "v_add_u32 %[nvl], 1013904223, %[nvl]\n\t"
            "s_setprio 1\n\t"
            "s_waitcnt vmcnt(9)\n\t"
            "v_lshl_add_u32 v160, v128, 8, %[lutl]\n\t"
            "ds_read_u16 v160, v160 offset:0\n\t"
            "v_lshl_add_u32 v168, v129, 8, %[lutl]\n\t"
            "ds_read_u16 v168, v168 offset:2\n\t"
            "v_lshl_add_u32 v176, v130, 8, %[lutl]\n\t"
            "ds_read_u16 v176, v176 offset:128\n\t"
            "v_lshl_add_u32 v184, v131, 8, %[lutl]\n\t"
            "ds_read_u16 v184, v184 offset:130\n\t"
            "s_add_u32 m0, %[ldsx], 32768\n\t"
            "s_lshl_b32 s76, s41, 7\n\t"
            "s_add_u32 s68, %[xpl], s76\n\t"
            "s_addc_u32 s69, %[xph], 0\n\t"
            "global_load_lds_dwordx4 %[x0], s[68:69] offset:0\n\t"
            "s_waitcnt vmcnt(9)\n\t"
            "v_lshl_add_u32 v161, v132, 8, %[lutl]\n\t"
            "ds_read_u16 v161, v161 offset:0\n\t"
            "v_lshl_add_u32 v169, v133, 8, %[lutl]\n\t"
            "ds_read_u16 v169, v169 offset:2\n\t"
            "v_lshl_add_u32 v177, v134, 8, %[lutl]\n\t"
            "ds_read_u16 v177, v177 offset:128\n\t"
            "v_lshl_add_u32 v185, v135, 8, %[lutl]\n\t"
            "ds_read_u16 v185, v185 offset:130\n\t"
            "global_load_lds_dwordx4 %[x1], s[68:69] offset:1024\n\t"
            "s_waitcnt vmcnt(9)\n\t"
            "v_lshl_add_u32 v162, v136, 8, %[lutl]\n\t"
            "ds_read_u16 v162, v162 offset:0\n\t"
            "v_lshl_add_u32 v170, v137, 8, %[lutl]\n\t"
            "ds_read_u16 v170, v170 offset:2\n\t"
            "v_lshl_add_u32 v178, v138, 8, %[lutl]\n\t"
            "ds_read_u16 v178, v178 offset:128\n\t"
            "v_lshl_add_u32 v186, v139, 8, %[lutl]\n\t"
            "ds_read_u16 v186, v186 offset:130\n\t"
            "global_load_lds_dwordx4 %[x2], s[68:69] offset:2048\n\t"
            "s_waitcnt vmcnt(9)\n\t"
            "v_lshl_add_u32 v163, v140, 8, %[lutl]\n\t"
            "ds_read_u16 v163, v163 offset:0\n\t"
            "v_lshl_add_u32 v171, v141, 8, %[lutl]\n\t"
            "ds_read_u16 v171, v171 offset:2\n\t"
            "v_lshl_add_u32 v179, v142, 8, %[lutl]\n\t"
            "ds_read_u16 v179, v179 offset:128\n\t"
            "v_lshl_add_u32 v187, v143, 8, %[lutl]\n\t"
            "ds_read_u16 v187, v187 offset:130\n\t"
            "global_load_lds_dwordx4 %[x3], s[68:69] offset:3072\n\t"
            "s_waitcnt vmcnt(9)\n\t"
            "v_lshl_add_u32 v164, v144, 8, %[lutl]\n\t"
            "ds_read_u16 v164, v164 offset:0\n\t"
            "v_lshl_add_u32 v172, v145, 8, %[lutl]\n\t"
            "ds_read_u16 v172, v172 offset:2\n\t"
            "v_lshl_add_u32 v180, v146, 8, %[lutl]\n\t"
            "ds_read_u16 v180, v180 offset:128\n\t"
            "v_lshl_add_u32 v188, v147, 8, %[lutl]\n\t"
            "ds_read_u16 v188, v188 offset:130\n\t"
            "s_add_u32 m0, %[ldsx], 36864\n\t"
            "s_lshl_b32 s76, s41, 7\n\t"
            "s_add_u32 s76, s76, 65536\n\t"
            "s_add_u32 s68, %[xpl], s76\n\t"
            "s_addc_u32 s69, %[xph], 0\n\t"
            "global_load_lds_dwordx4 %[x0], s[68:69] offset:0\n\t"
            "s_waitcnt vmcnt(9)\n\t"
            "v_lshl_add_u32 v165, v148, 8, %[lutl]\n\t"
            "ds_read_u16 v165, v165 offset:0\n\t"
            "v_lshl_add_u32 v173, v149, 8, %[lutl]\n\t"
            "ds_read_u16 v173, v173 offset:2\n\t"
            "v_lshl_add_u32 v181, v150, 8, %[lutl]\n\t"
            "ds_read_u16 v181, v181 offset:128\n\t"
            "v_lshl_add_u32 v189, v151, 8, %[lutl]\n\t"
            "ds_read_u16 v189, v189 offset:130\n\t"
            "global_load_lds_dwordx4 %[x1], s[68:69] offset:1024\n\t"
            "s_waitcnt vmcnt(9)\n\t"
            "v_lshl_add_u32 v166, v152, 8, %[lutl]\n\t"
            "ds_read_u16 v166, v166 offset:0\n\t"
            "v_lshl_add_u32 v174, v153, 8, %[lutl]\n\t"
            "ds_read_u16 v174, v174 offset:2\n\t"
            "v_lshl_add_u32 v182, v154, 8, %[lutl]\n\t"
            "ds_read_u16 v182, v182 offset:128\n\t"
            "v_lshl_add_u32 v190, v155, 8, %[lutl]\n\t"
            "ds_read_u16 v190, v190 offset:130\n\t"
            "global_load_lds_dwordx4 %[x2], s[68:69] offset:2048\n\t"
            "s_waitcnt vmcnt(9)\n\t"
            "v_lshl_add_u32 v167, v156, 8, %[lutl]\n\t"
            "ds_read_u16 v167, v167 offset:0\n\t"
            "v_lshl_add_u32 v175, v157, 8, %[lutl]\n\t"
            "ds_read_u16 v175, v175 offset:2\n\t"
            "v_lshl_add_u32 v183, v158, 8, %[lutl]\n\t"
            "ds_read_u16 v183, v183 offset:128\n\t"
            "v_lshl_add_u32 v191, v159, 8, %[lutl]\n\t"
            "ds_read_u16 v191, v191 offset:130\n\t"
            "global_load_lds_dwordx4 %[x3], s[68:69] offset:3072\n\t"
            "s_setprio 0\n\t"
            "s_waitcnt vmcnt(8)\n\t"
            "s_mov_b64 exec, 3\n\t"
            "global_store_dword %[fo0], %[nvl], %[ws] offset:0 sc1\n\t"
            "s_mov_b64 exec, -1\n\t"
            "s_waitcnt lgkmcnt(3)\n\t"
            "v_lshl_or_b32 v128, v161, 16, v160\n\t"
            "v_lshl_or_b32 v129, v163, 16, v162\n\t"
            "v_lshl_or_b32 v130, v165, 16, v164\n\t"
            "v_lshl_or_b32 v131, v167, 16, v166\n\t"
            "ds_write_b128 %[bd], v[128:131] offset:16384\n\t"
            "s_waitcnt lgkmcnt(3)\n\t"
            "v_lshl_or_b32 v132, v169, 16, v168\n\t"
            "v_lshl_or_b32 v133, v171, 16, v170\n\t"
            "v_lshl_or_b32 v134, v173, 16, v172\n\t"
            "v_lshl_or_b32 v135, v175, 16, v174\n\t"
            "ds_write_b128 %[bd], v[132:135] offset:16640\n\t"
            "s_waitcnt lgkmcnt(3)\n\t"
            "v_lshl_or_b32 v136, v177, 16, v176\n\t"
            "v_lshl_or_b32 v137, v179, 16, v178\n\t"
            "v_lshl_or_b32 v138, v181, 16, v180\n\t"
            "v_lshl_or_b32 v139, v183, 16, v182\n\t"
            "ds_write_b128 %[bd], v[136:139] offset:16896\n\t"
            "s_waitcnt lgkmcnt(3)\n\t"
            "v_lshl_or_b32 v140, v185, 16, v184\n\t"
            "v_lshl_or_b32 v141, v187, 16, v186\n\t"
            "v_lshl_or_b32 v142, v189, 16, v188\n\t"
            "v_lshl_or_b32 v143, v191, 16, v190\n\t"
            "ds_write_b128 %[bd], v[140:143] offset:17152\n\t"
            "s_add_u32 m0, %[ldsx], 65536\n\t"
            "s_lshl_b32 s76, s42, 7\n\t"
            "s_add_u32 s68, %[xpl], s76\n\t"
            "s_addc_u32 s69, %[xph], 0\n\t"
            "global_load_lds_dwordx4 %[x0], s[68:69] offset:0\n\t"
            "global_load_lds_dwordx4 %[x1], s[68:69] offset:1024\n\t"
            "global_load_lds_dwordx4 %[x2], s[68:69] offset:2048\n\t"
            "global_load_lds_dwordx4 %[x3], s[68:69] offset:3072\n\t"
            "s_add_u32 m0, %[ldsx], 69632\n\t"
            "s_lshl_b32 s76, s42, 7\n\t"
            "s_add_u32 s76, s76, 65536\n\t"
            "s_add_u32 s68, %[xpl], s76\n\t"
            "s_addc_u32 s69, %[xph], 0\n\t"
            "global_load_lds_dwordx4 %[x0], s[68:69] offset:0\n\t"
            "global_load_lds_dwordx4 %[x1], s[68:69] offset:1024\n\t"
            "global_load_lds_dwordx4 %[x2], s[68:69] offset:2048\n\t"
            "global_load_lds_dwordx4 %[x3], s[68:69] offset:3072\n\t"
            "s_waitcnt vmcnt(9) lgkmcnt(0)\n\t"
            "s_barrier\n\t"
            ".Lwp32:\n\t"
            "s_cbranch_execz .Lwp98\n\t"
            "global_load_dword v192, %[po], %[ws] sc1\n\t"
            "s_add_u32 m0, %[ldsx], 0\n\t"
            "s_lshl_b32 s76, s43, 7\n\t"
            "s_add_u32 s68, %[xpl], s76\n\t"
            "s_addc_u32 s69, %[xph], 0\n\t"
            "global_load_lds_dwordx4 %[x0], s[68:69] offset:0\n\t"
            "global_load_lds_dwordx4 %[x1], s[68:69] offset:1024\n\t"
            "global_load_lds_dwordx4 %[x2], s[68:69] offset:2048\n\t"
            "global_load_lds_dwordx4 %[x3], s[68:69] offset:3072\n\t"
            "s_add_u32 m0, %[ldsx], 4096\n\t"
            "s_lshl_b32 s76, s43, 7\n\t"
            "s_add_u32 s76, s76, 65536\n\t"
            "s_add_u32 s68, %[xpl], s76\n\t"
            "s_addc_u32 s69, %[xph], 0\n\t"
            "global_load_lds_dwordx4 %[x0], s[68:69] offset:0\n\t"
            "global_load_lds_dwordx4 %[x1], s[68:69] offset:1024\n\t"
            "global_load_lds_dwordx4 %[x2], s[68:69] offset:2048\n\t"
            "global_load_lds_dwordx4 %[x3], s[68:69] offset:3072\n\t"
            "s_waitcnt vmcnt(8)\n\t"
            "s_lshl_b32 s76, s42, 2\n\t"
            ".Lzc0:\n\t"
            "v_cmp_eq_u32 vcc, v192, %[nvl]\n\t"
            "s_lshr_b64 s[74:75], vcc, s76\n\t"
            "s_and_b32 s74, s74, 15\n\t"
            "s_cmp_eq_u32 s74, 15\n\t"
            "s_cbranch_scc1 .Lzd0\n\t"
            "s_cmp_eq_u32 s73, 0\n\t"
            "s_cbranch_scc1 .Lzd0\n\t"
            "s_sub_u32 s73, s73, 1\n\t"
            "global_load_dword v192, %[po], %[ws] sc1\n\t"
            "s_waitcnt vmcnt(0)\n\t"
            "s_branch .Lzc0\n\t"
            ".Lzd0:\n\t"
            "s_add_u32 m0, %[ldsw], 32768\n\t"
            "s_mul_i32 s76, s42, 458752\n\t"
            "s_add_u32 s70, s84, s76\n\t"
            "s_addc_u32 s71, s85, 0\n\t"
            "global_load_lds_dwordx4 %[wv], s[70:71] offset:0 sc1\n\t"
            "global_load_lds_dwordx4 %[wv], s[70:71] offset:1024 sc1\n\t"
            "global_load_lds_dwordx4 %[wv], s[70:71] offset:2048 sc1\n\t"
            "global_load_lds_dwordx4 %[wv], s[70:71] offset:3072 sc1\n\t"
            "s_cmp_lg_u32 %[h2], 0\n\t"
            "s_cbranch_scc1 .Lzs1\n\t"
            "global_store_dwordx4 %[wo], v[128:131], %[st1] sc1\n\t"
            "global_store_dwordx4 %[wo], v[132:135], %[st1] offset:256 sc1\n\t"
            "s_branch .Lzt1\n\t"
            ".Lzs1:\n\t"
            "global_store_dwordx4 %[wo], v[136:139], %[st1] sc1\n\t"
            "global_store_dwordx4 %[wo], v[140:143], %[st1] offset:256 sc1\n\t"
            ".Lzt1:\n\t"
            "s_lshl_b32 s76, s43, 2\n\t"
            ".Lzc2:\n\t"
            "v_cmp_eq_u32 vcc, v192, %[nvl]\n\t"
            "s_lshr_b64 s[74:75], vcc, s76\n\t"
            "s_and_b32 s74, s74, 15\n\t"
            "s_cmp_eq_u32 s74, 15\n\t"
            "s_cbranch_scc1 .Lzd2\n\t"
            "s_cmp_eq_u32 s73, 0\n\t"
            "s_cbranch_scc1 .Lzd2\n\t"
            "s_sub_u32 s73, s73, 1\n\t"
            "global_load_dword v192, %[po], %[ws] sc1\n\t"
            "s_waitcnt vmcnt(0)\n\t"
            "s_branch .Lzc2\n\t"
            ".Lzd2:\n\t"
            "s_add_u32 m0, %[ldsw], 0\n\t"
            "s_mul_i32 s76, s43, 458752\n\t"
            "s_add_u32 s70, s84, s76\n\t"
            "s_addc_u32 s71, s85, 0\n\t"
            "global_load_lds_dwordx4 %[wv], s[70:71] offset:0 sc1\n\t"
            "global_load_lds_dwordx4 %[wv], s[70:71] offset:1024 sc1\n\t"
            "global_load_lds_dwordx4 %[wv], s[70:71] offset:2048 sc1\n\t"
            "global_load_lds_dwordx4 %[wv], s[70:71] offset:3072 sc1\n\t"
            "s_waitcnt vmcnt(6)\n\t"
            "s_barrier\n\t"
            ".Lwp33:\n\t"
            "s_cbranch_execz .Lwp98\n\t"
            "s_lshl_b32 s76, s44, 2\n\t"
            ".Lzc3:\n\t"
            "v_cmp_eq_u32 vcc, v192, %[nvl]\n\t"
            "s_lshr_b64 s[74:75], vcc, s76\n\t"
            "s_and_b32 s74, s74, 15\n\t"
            "s_cmp_eq_u32 s74, 15\n\t"
            "s_cbranch_scc1 .Lzd3\n\t"
            "s_cmp_eq_u32 s73, 0\n\t"
            "s_cbranch_scc1 .Lzd3\n\t"
            "s_sub_u32 s73, s73, 1\n\t"
            "global_load_dword v192, %[po], %[ws] sc1\n\t"
            "s_waitcnt vmcnt(0)\n\t"
            "s_branch .Lzc3\n\t"
            ".Lzd3:\n\t"
            "s_add_u32 m0, %[ldsw], 16384\n\t"
            "s_mul_i32 s76, s44, 458752\n\t"
            "s_add_u32 s70, s84, s76\n\t"
            "s_addc_u32 s71, s85, 0\n\t"
            "global_load_lds_dwordx4 %[wv], s[70:71] offset:0 sc1\n\t"
            "global_load_lds_dwordx4 %[wv], s[70:71] offset:1024 sc1\n\t"
            "global_load_lds_dwordx4 %[wv], s[70:71] offset:2048 sc1\n\t"
            "global_load_lds_dwordx4 %[wv], s[70:71] offset:3072 sc1\n\t"
            "s_waitcnt vmcnt(8)\n\t"
            "s_mov_b64 exec, 3\n\t"
            "global_store_dword %[fo0], %[nvl], %[ws] offset:64 sc1\n\t"
            "s_mov_b64 exec, -1\n\t"
            "v_mbcnt_lo_u32_b32 v193, -1, 0\n\t"
            "v_mbcnt_hi_u32_b32 v193, -1, v193\n\t"
            "v_lshrrev_b32 v193, 5, v193\n\t"
            "v_mul_u32_u24 v194, 57344, v193\n\t"
            "v_sub_u32 %[qo], %[qo], v194\n\t"
            "v_mul_u32_u24 v194, 1792, v193\n\t"
            "v_sub_u32 %[wo], %[wo], v194\n\t"
            "s_mov_b64 s[88:89], %[qbg]\n\t"
            "global_load_dwordx4 v[204:207], %[qo], s[88:89] \n\t"
            "s_add_u32 s88, s88, 14336\n\t"
            "s_addc_u32 s89, s89, 0\n\t"
            "global_load_dwordx4 v[208:211], %[qo], s[88:89] \n\t"
            "s_add_u32 s88, s88, 14336\n\t"
            "s_addc_u32 s89, s89, 0\n\t"
            "global_load_dwordx4 v[212:215], %[qo], s[88:89] \n\t"
            "s_add_u32 s88, s88, 14336\n\t"
            "s_addc_u32 s89, s89, 0\n\t"
            "global_load_dwordx4 v[216:219], %[qo], s[88:89] \n\t"
            "s_add_u32 m0, %[ldsx], 32768\n\t"
            "s_lshl_b32 s76, s44, 7\n\t"
            "s_add_u32 s68, %[xpl], s76\n\t"
            "s_addc_u32 s69, %[xph], 0\n\t"
            "global_load_lds_dwordx4 %[x0], s[68:69] offset:0\n\t"
            "global_load_lds_dwordx4 %[x1], s[68:69] offset:1024\n\t"
            "global_load_lds_dwordx4 %[x2], s[68:69] offset:2048\n\t"
            "global_load_lds_dwordx4 %[x3], s[68:69] offset:3072\n\t"
            "s_add_u32 m0, %[ldsx], 36864\n\t"
            "s_lshl_b32 s76, s44, 7\n\t"
            "s_add_u32 s76, s76, 65536\n\t"
            "s_add_u32 s68, %[xpl], s76\n\t"
            "s_addc_u32 s69, %[xph], 0\n\t"
            "global_load_lds_dwordx4 %[x0], s[68:69] offset:0\n\t"
            "global_load_lds_dwordx4 %[x1], s[68:69] offset:1024\n\t"
            "global_load_lds_dwordx4 %[x2], s[68:69] offset:2048\n\t"
            "global_load_lds_dwordx4 %[x3], s[68:69] offset:3072\n\t"
            "s_waitcnt vmcnt(17)\n\t"
            "s_barrier\n\t"
            "global_load_dword v192, %[po], %[ws] sc1\n\t"
            "s_waitcnt vmcnt(9)\n\t"
            "s_setprio 0\n\t"
            "s_add_u32 m0, %[ldsx], 65536\n\t"
            "s_lshl_b32 s76, s45, 7\n\t"
            "s_add_u32 s68, %[xpl], s76\n\t"
            "s_addc_u32 s69, %[xph], 0\n\t"
            "global_load_lds_dwordx4 %[x0], s[68:69] offset:0\n\t"
            "v_lshl_add_u32 v160, v204, 8, %[lutl]\n\t"
            "ds_read_u16 v160, v160 offset:0\n\t"
            "v_lshl_add_u32 v161, v208, 8, %[lutl]\n\t"
            "ds_read_u16 v161, v161 offset:0\n\t"
            "global_load_lds_dwordx4 %[x1], s[68:69] offset:1024\n\t"
            "v_lshl_add_u32 v162, v212, 8, %[lutl]\n\t"
            "ds_read_u16 v162, v162 offset:0\n\t"
            "v_lshl_add_u32 v163, v216, 8, %[lutl]\n\t"
            "ds_read_u16 v163, v163 offset:0\n\t"
            "global_load_lds_dwordx4 %[x2], s[68:69] offset:2048\n\t"
            "v_lshl_add_u32 v168, v205, 8, %[lutl]\n\t"
            "ds_read_u16 v168, v168 offset:2\n\t"
            "v_lshl_add_u32 v169, v209, 8, %[lutl]\n\t"
            "ds_read_u16 v169, v169 offset:2\n\t"
            "global_load_lds_dwordx4 %[x3], s[68:69] offset:3072\n\t"
            "v_lshl_add_u32 v170, v213, 8, %[lutl]\n\t"
            "ds_read_u16 v170, v170 offset:2\n\t"
            "v_lshl_add_u32 v171, v217, 8, %[lutl]\n\t"
            "ds_read_u16 v171, v171 offset:2\n\t"
            "s_add_u32 m0, %[ldsx], 69632\n\t"
            "s_lshl_b32 s76, s45, 7\n\t"
            "s_add_u32 s76, s76, 65536\n\t"
            "s_add_u32 s68, %[xpl], s76\n\t"
            "s_addc_u32 s69, %[xph], 0\n\t"
            "global_load_lds_dwordx4 %[x0], s[68:69] offset:0\n\t"
            "v_lshl_add_u32 v176, v206, 8, %[lutl]\n\t"
            "ds_read_u16 v176, v176 offset:128\n\t"
            "v_lshl_add_u32 v177, v210, 8, %[lutl]\n\t"
            "ds_read_u16 v177, v177 offset:128\n\t"
            "global_load_lds_dwordx4 %[x1], s[68:69] offset:1024\n\t"
            "v_lshl_add_u32 v178, v214, 8, %[lutl]\n\t"
            "ds_read_u16 v178, v178 offset:128\n\t"
            "v_lshl_add_u32 v179, v218, 8, %[lutl]\n\t"
            "ds_read_u16 v179, v179 offset:128\n\t"
            "global_load_lds_dwordx4 %[x2], s[68:69] offset:2048\n\t"
            "v_lshl_add_u32 v184, v207, 8, %[lutl]\n\t"
            "ds_read_u16 v184, v184 offset:130\n\t"
            "v_lshl_add_u32 v185, v211, 8, %[lutl]\n\t"
            "ds_read_u16 v185, v185 offset:130\n\t"
            "global_load_lds_dwordx4 %[x3], s[68:69] offset:3072\n\t"
            "v_lshl_add_u32 v186, v215, 8, %[lutl]\n\t"
            "ds_read_u16 v186, v186 offset:130\n\t"
            "v_lshl_add_u32 v187, v219, 8, %[lutl]\n\t"
            "ds_read_u16 v187, v187 offset:130\n\t"
            "s_setprio 0\n\t"
            "s_waitcnt vmcnt(8)\n\t"
            "s_lshl_b32 s76, s45, 2\n\t"
            ".Lzc4:\n\t"
            "v_cmp_eq_u32 vcc, v192, %[nvl]\n\t"
            "s_lshr_b64 s[74:75], vcc, s76\n\t"
            "s_and_b32 s74, s74, 15\n\t"
            "s_cmp_eq_u32 s74, 15\n\t"
            "s_cbranch_scc1 .Lzd4\n\t"
            "s_cmp_eq_u32 s73, 0\n\t"
            "s_cbranch_scc1 .Lzd4\n\t"
            "s_sub_u32 s73, s73, 1\n\t"
            "global_load_dword v192, %[po], %[ws] sc1\n\t"
            "s_waitcnt vmcnt(0)\n\t"
            "s_branch .Lzc4\n\t"
            ".Lzd4:\n\t"
            "s_add_u32 m0, %[ldsw], 32768\n\t"
            "s_mul_i32 s76, s45, 458752\n\t"
            "s_add_u32 s70, s84, s76\n\t"
            "s_addc_u32 s71, s85, 0\n\t"
            "global_load_lds_dwordx4 %[wv], s[70:71] offset:0 sc1\n\t"
            "global_load_lds_dwordx4 %[wv], s[70:71] offset:1024 sc1\n\t"
            "global_load_lds_dwordx4 %[wv], s[70:71] offset:2048 sc1\n\t"
            "global_load_lds_dwordx4 %[wv], s[70:71] offset:3072 sc1\n\t"
            "s_waitcnt lgkmcnt(8)\n\t"
            "v_lshl_or_b32 v220, v161, 16, v160\n\t"
            "v_lshl_or_b32 v221, v163, 16, v162\n\t"
            "v_lshl_or_b32 v222, v169, 16, v168\n\t"
            "v_lshl_or_b32 v223, v171, 16, v170\n\t"
            "s_waitcnt lgkmcnt(0)\n\t"
            "v_lshl_or_b32 v224, v177, 16, v176\n\t"
            "v_lshl_or_b32 v225, v179, 16, v178\n\t"
            "v_lshl_or_b32 v226, v185, 16, v184\n\t"
            "v_lshl_or_b32 v227, v187, 16, v186\n\t"
            "s_nop 0\n\t"
            "v_permlane32_swap_b32 v220, v222\n\t"
            "v_permlane32_swap_b32 v221, v223\n\t"
            "v_permlane32_swap_b32 v224, v226\n\t"
            "v_permlane32_swap_b32 v225, v227\n\t"
            "s_mov_b64 s[88:89], %[stb]\n\t"
            "s_nop 0\n\t"
            "global_store_dwordx4 %[wo], v[220:223], s[88:89] offset:0 sc1\n\t"
            "global_store_dwordx4 %[wo], v[224:227], s[88:89] offset:512 sc1\n\t"
            "s_barrier\n\t"
            ".Lwp34:\n\t"
            "s_cbranch_execz .Lwp35\n\t"
            "s_lshl_b32 s76, s46, 2\n\t"
            ".Lzc5:\n\t"
            "v_cmp_eq_u32 vcc, v192, %[nvl]\n\t"
            "s_lshr_b64 s[74:75], vcc, s76\n\t"
            "s_and_b32 s74, s74, 15\n\t"
            "s_cmp_eq_u32 s74, 15\n\t"
            "s_cbranch_scc1 .Lzd5\n\t"
            "s_cmp_eq_u32 s73, 0\n\t"
            "s_cbranch_scc1 .Lzd5\n\t"
            "s_sub_u32 s73, s73, 1\n\t"
            "global_load_dword v192, %[po], %[ws] sc1\n\t"
            "s_waitcnt vmcnt(0)\n\t"
            "s_branch .Lzc5\n\t"
            ".Lzd5:\n\t"
            "s_add_u32 m0, %[ldsw], 0\n\t"
            "s_mul_i32 s76, s46, 458752\n\t"
            "s_add_u32 s70, s84, s76\n\t"
            "s_addc_u32 s71, s85, 0\n\t"
            "global_load_lds_dwordx4 %[wv], s[70:71] offset:0 sc1\n\t"
            "global_load_lds_dwordx4 %[wv], s[70:71] offset:1024 sc1\n\t"
            "global_load_lds_dwordx4 %[wv], s[70:71] offset:2048 sc1\n\t"
            "global_load_lds_dwordx4 %[wv], s[70:71] offset:3072 sc1\n\t"
            "s_add_u32 m0, %[ldsx], 0\n\t"
            "s_lshl_b32 s76, s46, 7\n\t"
            "s_add_u32 s68, %[xpl], s76\n\t"
            "s_addc_u32 s69, %[xph], 0\n\t"
            "global_load_lds_dwordx4 %[x0], s[68:69] offset:0\n\t"
            "global_load_lds_dwordx4 %[x1], s[68:69] offset:1024\n\t"
            "global_load_lds_dwordx4 %[x2], s[68:69] offset:2048\n\t"
            "global_load_lds_dwordx4 %[x3], s[68:69] offset:3072\n\t"
            "s_add_u32 m0, %[ldsx], 4096\n\t"
            "s_lshl_b32 s76, s46, 7\n\t"
            "s_add_u32 s76, s76, 65536\n\t"
            "s_add_u32 s68, %[xpl], s76\n\t"
            "s_addc_u32 s69, %[xph], 0\n\t"
            "global_load_lds_dwordx4 %[x0], s[68:69] offset:0\n\t"
            "global_load_lds_dwordx4 %[x1], s[68:69] offset:1024\n\t"
            "global_load_lds_dwordx4 %[x2], s[68:69] offset:2048\n\t"
            "global_load_lds_dwordx4 %[x3], s[68:69] offset:3072\n\t"
            "s_waitcnt vmcnt(12)\n\t"
            "s_mov_b64 exec, 3\n\t"
            "global_store_dword %[fob], %[nvl], %[ws] offset:0 sc1\n\t"
            "s_mov_b64 exec, -1\n\t"
            "s_barrier\n\t"
            "s_lshl_b32 s76, s47, 2\n\t"
            ".Lzc6:\n\t"
            "v_cmp_eq_u32 vcc, v192, %[nvl]\n\t"
            "s_lshr_b64 s[74:75], vcc, s76\n\t"
            "s_and_b32 s74, s74, 15\n\t"
            "s_cmp_eq_u32 s74, 15\n\t"
            "s_cbranch_scc1 .Lzd6\n\t"
            "s_cmp_eq_u32 s73, 0\n\t"
            "s_cbranch_scc1 .Lzd6\n\t"
            "s_sub_u32 s73, s73, 1\n\t"
            "global_load_dword v192, %[po], %[ws] sc1\n\t"
            "s_waitcnt vmcnt(0)\n\t"
            "s_branch .Lzc6\n\t"
            ".Lzd6:\n\t"
            "s_add_u32 m0, %[ldsw], 16384\n\t"
            "s_mul_i32 s76, s47, 458752\n\t"
            "s_add_u32 s70, s84, s76\n\t"
            "s_addc_u32 s71, s85, 0\n\t"
            "global_load_lds_dwordx4 %[wv], s[70:71] offset:0 sc1\n\t"
            "global_load_lds_dwordx4 %[wv], s[70:71] offset:1024 sc1\n\t"
            "global_load_lds_dwordx4 %[wv], s[70:71] offset:2048 sc1\n\t"
            "global_load_lds_dwordx4 %[wv], s[70:71] offset:3072 sc1\n\t"
            "s_add_u32 m0, %[ldsx], 32768\n\t"
            "s_lshl_b32 s76, s47, 7\n\t"
            "s_add_u32 s68, %[xpl], s76\n\t"
            "s_addc_u32 s69, %[xph], 0\n\t"
            "global_load_lds_dwordx4 %[x0], s[68:69] offset:0\n\t"
            "global_load_lds_dwordx4 %[x1], s[68:69] offset:1024\n\t"
            "global_load_lds_dwordx4 %[x2], s[68:69] offset:2048\n\t"
            "global_load_lds_dwordx4 %[x3], s[68:69] offset:3072\n\t"
            "s_add_u32 m0, %[ldsx], 36864\n\t"
            "s_lshl_b32 s76, s47, 7\n\t"
            "s_add_u32 s76, s76, 65536\n\t"
            "s_add_u32 s68, %[xpl], s76\n\t"
            "s_addc_u32 s69, %[xph], 0\n\t"
            "global_load_lds_dwordx4 %[x0], s[68:69] offset:0\n\t"
            "global_load_lds_dwordx4 %[x1], s[68:69] offset:1024\n\t"
            "global_load_lds_dwordx4 %[x2], s[68:69] offset:2048\n\t"
            "global_load_lds_dwordx4 %[x3], s[68:69] offset:3072\n\t"
            "s_mov_b64 s[88:89], %[qbg]\n\t"
            "s_add_u32 s88, s88, 3670016\n\t"
            "s_addc_u32 s89, s89, 0\n\t"
            "global_load_dwordx4 v[204:207], %[qo], s[88:89] \n\t"
            "s_add_u32 s88, s88, 14336\n\t"
            "s_addc_u32 s89, s89, 0\n\t"
            "global_load_dwordx4 v[208:211], %[qo], s[88:89] \n\t"
            "s_add_u32 s88, s88, 14336\n\t"
            "s_addc_u32 s89, s89, 0\n\t"
            "global_load_dwordx4 v[212:215], %[qo], s[88:89] \n\t"
            "s_add_u32 s88, s88, 14336\n\t"
            "s_addc_u32 s89, s89, 0\n\t"
            "global_load_dwordx4 v[216:219], %[qo], s[88:89] \n\t"
            "s_waitcnt vmcnt(17)\n\t"
            "s_barrier\n\t"
            ".Lwp35:\n\t"
            "s_cbranch_execz .Lwp36\n\t"
            "global_load_dword v192, %[po], %[ws] sc1\n\t"
            "s_add_u32 m0, %[ldsx], 65536\n\t"
            "s_lshl_b32 s76, s48, 7\n\t"
            "s_add_u32 s68, %[xpl], s76\n\t"
            "s_addc_u32 s69, %[xph], 0\n\t"
            "global_load_lds_dwordx4 %[x0], s[68:69] offset:0\n\t"
            "global_load_lds_dwordx4 %[x1], s[68:69] offset:1024\n\t"
            "global_load_lds_dwordx4 %[x2], s[68:69] offset:2048\n\t"
            "global_load_lds_dwordx4 %[x3], s[68:69] offset:3072\n\t"
            "s_add_u32 m0, %[ldsx], 69632\n\t"
            "s_lshl_b32 s76, s48, 7\n\t"
            "s_add_u32 s76, s76, 65536\n\t"
            "s_add_u32 s68, %[xpl], s76\n\t"
            "s_addc_u32 s69, %[xph], 0\n\t"
            "global_load_lds_dwordx4 %[x0], s[68:69] offset:0\n\t"
            "global_load_lds_dwordx4 %[x1], s[68:69] offset:1024\n\t"
            "global_load_lds_dwordx4 %[x2], s[68:69] offset:2048\n\t"
            "global_load_lds_dwordx4 %[x3], s[68:69] offset:3072\n\t"
            "s_waitcnt vmcnt(8)\n\t"
            "s_lshl_b32 s76, s48, 2\n\t"
            ".Lzc7:\n\t"
            "v_cmp_eq_u32 vcc, v192, %[nvl]\n\t"
            "s_lshr_b64 s[74:75], vcc, s76\n\t"
            "s_and_b32 s74, s74, 15\n\t"
            "s_cmp_eq_u32 s74, 15\n\t"
            "s_cbranch_scc1 .Lzd7\n\t"
            "s_cmp_eq_u32 s73, 0\n\t"
            "s_cbranch_scc1 .Lzd7\n\t"
            "s_sub_u32 s73, s73, 1\n\t"
            "global_load_dword v192, %[po], %[ws] sc1\n\t"
            "s_waitcnt vmcnt(0)\n\t"
            "s_branch .Lzc7\n\t"
            ".Lzd7:\n\t"
            "s_add_u32 m0, %[ldsw], 32768\n\t"
            "s_mul_i32 s76, s48, 458752\n\t"
            "s_add_u32 s70, s84, s76\n\t"
            "s_addc_u32 s71, s85, 0\n\t"
            "global_load_lds_dwordx4 %[wv], s[70:71] offset:0 sc1\n\t"
            "global_load_lds_dwordx4 %[wv], s[70:71] offset:1024 sc1\n\t"
            "global_load_lds_dwordx4 %[wv], s[70:71] offset:2048 sc1\n\t"
            "global_load_lds_dwordx4 %[wv], s[70:71] offset:3072 sc1\n\t"
            "s_barrier\n\t"
            "s_lshl_b32 s76, s49, 2\n\t"
            ".Lzc8:\n\t"
            "v_cmp_eq_u32 vcc, v192, %[nvl]\n\t"
            "s_lshr_b64 s[74:75], vcc, s76\n\t"
            "s_and_b32 s74, s74, 15\n\t"
            "s_cmp_eq_u32 s74, 15\n\t"
            "s_cbranch_scc1 .Lzd8\n\t"
            "s_cmp_eq_u32 s73, 0\n\t"
            "s_cbranch_scc1 .Lzd8\n\t"
            "s_sub_u32 s73, s73, 1\n\t"
            "global_load_dword v192, %[po], %[ws] sc1\n\t"
            "s_waitcnt vmcnt(0)\n\t"
            "s_branch .Lzc8\n\t"
            ".Lzd8:\n\t"
            "s_add_u32 m0, %[ldsw], 0\n\t"
            "s_mul_i32 s76, s49, 458752\n\t"
            "s_add_u32 s70, s84, s76\n\t"
            "s_addc_u32 s71, s85, 0\n\t"
            "global_load_lds_dwordx4 %[wv], s[70:71] offset:0 sc1\n\t"
            "global_load_lds_dwordx4 %[wv], s[70:71] offset:1024 sc1\n\t"
            "global_load_lds_dwordx4 %[wv], s[70:71] offset:2048 sc1\n\t"
            "global_load_lds_dwordx4 %[wv], s[70:71] offset:3072 sc1\n\t"
            "s_setprio 0\n\t"
            "s_add_u32 m0, %[ldsx], 0\n\t"
            "s_lshl_b32 s76, s49, 7\n\t"
            "s_add_u32 s68, %[xpl], s76\n\t"
            "s_addc_u32 s69, %[xph], 0\n\t"
            "global_load_lds_dwordx4 %[x0], s[68:69] offset:0\n\t"
            "v_lshl_add_u32 v160, v204, 8, %[lutl]\n\t"
            "ds_read_u16 v160, v160 offset:0\n\t"
            "v_lshl_add_u32 v161, v208, 8, %[lutl]\n\t"
            "ds_read_u16 v161, v161 offset:0\n\t"
            "global_load_lds_dwordx4 %[x1], s[68:69] offset:1024\n\t"
            "v_lshl_add_u32 v162, v212, 8, %[lutl]\n\t"
            "ds_read_u16 v162, v162 offset:0\n\t"
            "v_lshl_add_u32 v163, v216, 8, %[lutl]\n\t"
            "ds_read_u16 v163, v163 offset:0\n\t"
            "global_load_lds_dwordx4 %[x2], s[68:69] offset:2048\n\t"
            "v_lshl_add_u32 v168, v205, 8, %[lutl]\n\t"
            "ds_read_u16 v168, v168 offset:2\n\t"
            "v_lshl_add_u32 v169, v209, 8, %[lutl]\n\t"
            "ds_read_u16 v169, v169 offset:2\n\t"
            "global_load_lds_dwordx4 %[x3], s[68:69] offset:3072\n\t"
            "v_lshl_add_u32 v170, v213, 8, %[lutl]\n\t"
            "ds_read_u16 v170, v170 offset:2\n\t"
            "v_lshl_add_u32 v171, v217, 8, %[lutl]\n\t"
            "ds_read_u16 v171, v171 offset:2\n\t"
            "s_add_u32 m0, %[ldsx], 4096\n\t"
            "s_lshl_b32 s76, s49, 7\n\t"
            "s_add_u32 s76, s76, 65536\n\t"
            "s_add_u32 s68, %[xpl], s76\n\t"
            "s_addc_u32 s69, %[xph], 0\n\t"
            "global_load_lds_dwordx4 %[x0], s[68:69] offset:0\n\t"
            "v_lshl_add_u32 v176, v206, 8, %[lutl]\n\t"
            "ds_read_u16 v176, v176 offset:128\n\t"
            "v_lshl_add_u32 v177, v210, 8, %[lutl]\n\t"
            "ds_read_u16 v177, v177 offset:128\n\t"
            "global_load_lds_dwordx4 %[x1], s[68:69] offset:1024\n\t"
            "v_lshl_add_u32 v178, v214, 8, %[lutl]\n\t"
            "ds_read_u16 v178, v178 offset:128\n\t"
            "v_lshl_add_u32 v179, v218, 8, %[lutl]\n\t"
            "ds_read_u16 v179, v179 offset:128\n\t"
            "global_load_lds_dwordx4 %[x2], s[68:69] offset:2048\n\t"
            "v_lshl_add_u32 v184, v207, 8, %[lutl]\n\t"
            "ds_read_u16 v184, v184 offset:130\n\t"
            "v_lshl_add_u32 v185, v211, 8, %[lutl]\n\t"
            "ds_read_u16 v185, v185 offset:130\n\t"
            "global_load_lds_dwordx4 %[x3], s[68:69] offset:3072\n\t"
            "v_lshl_add_u32 v186, v215, 8, %[lutl]\n\t"
            "ds_read_u16 v186, v186 offset:130\n\t"
            "v_lshl_add_u32 v187, v219, 8, %[lutl]\n\t"
            "ds_read_u16 v187, v187 offset:130\n\t"
            "s_setprio 0\n\t"
            "s_waitcnt lgkmcnt(8)\n\t"
            "v_lshl_or_b32 v220, v161, 16, v160\n\t"
            "v_lshl_or_b32 v221, v163, 16, v162\n\t"
            "v_lshl_or_b32 v222, v169, 16, v168\n\t"
            "v_lshl_or_b32 v223, v171, 16, v170\n\t"
            "s_waitcnt lgkmcnt(0)\n\t"
            "v_lshl_or_b32 v224, v177, 16, v176\n\t"
            "v_lshl_or_b32 v225, v179, 16, v178\n\t"
            "v_lshl_or_b32 v226, v185, 16, v184\n\t"
            "v_lshl_or_b32 v227, v187, 16, v186\n\t"
            "s_nop 0\n\t"
            "v_permlane32_swap_b32 v220, v222\n\t"
            "v_permlane32_swap_b32 v221, v223\n\t"
            "v_permlane32_swap_b32 v224, v226\n\t"
            "v_permlane32_swap_b32 v225, v227\n\t"
            "s_mov_b64 s[88:89], %[stb]\n\t"
            "s_add_u32 s88, s88, 1835008\n\t"
            "s_addc_u32 s89, s89, 0\n\t"
            "global_store_dwordx4 %[wo], v[220:223], s[88:89] offset:0 sc1\n\t"
            "global_store_dwordx4 %[wo], v[224:227], s[88:89] offset:512 sc1\n\t"
            "s_waitcnt vmcnt(14)\n\t"
            "s_barrier\n\t"
            ".Lwp36:\n\t"
            "s_cbranch_execz .Lwp97\n\t"
            "s_lshl_b32 s76, s50, 2\n\t"
            ".Lzc9:\n\t"
            "v_cmp_eq_u32 vcc, v192, %[nvl]\n\t"
            "s_lshr_b64 s[74:75], vcc, s76\n\t"
            "s_and_b32 s74, s74, 15\n\t"
            "s_cmp_eq_u32 s74, 15\n\t"
            "s_cbranch_scc1 .Lzd9\n\t"
            "s_cmp_eq_u32 s73, 0\n\t"
            "s_cbranch_scc1 .Lzd9\n\t"
            "s_sub_u32 s73, s73, 1\n\t"
            "global_load_dword v192, %[po], %[ws] sc1\n\t"
            "s_waitcnt vmcnt(0)\n\t"
            "s_branch .Lzc9\n\t"
            ".Lzd9:\n\t"
            "s_add_u32 m0, %[ldsw], 16384\n\t"
            "s_mul_i32 s76, s50, 458752\n\t"
            "s_add_u32 s70, s84, s76\n\t"
            "s_addc_u32 s71, s85, 0\n\t"
            "global_load_lds_dwordx4 %[wv], s[70:71] offset:0 sc1\n\t"
            "global_load_lds_dwordx4 %[wv], s[70:71] offset:1024 sc1\n\t"
            "global_load_lds_dwordx4 %[wv], s[70:71] offset:2048 sc1\n\t"
            "global_load_lds_dwordx4 %[wv], s[70:71] offset:3072 sc1\n\t"
            "s_add_u32 m0, %[ldsx], 32768\n\t"
            "s_lshl_b32 s76, s50, 7\n\t"
            "s_add_u32 s68, %[xpl], s76\n\t"
            "s_addc_u32 s69, %[xph], 0\n\t"
            "global_load_lds_dwordx4 %[x0], s[68:69] offset:0\n\t"
            "global_load_lds_dwordx4 %[x1], s[68:69] offset:1024\n\t"
            "global_load_lds_dwordx4 %[x2], s[68:69] offset:2048\n\t"
            "global_load_lds_dwordx4 %[x3], s[68:69] offset:3072\n\t"
            "s_add_u32 m0, %[ldsx], 36864\n\t"
            "s_lshl_b32 s76, s50, 7\n\t"
            "s_add_u32 s76, s76, 65536\n\t"
            "s_add_u32 s68, %[xpl], s76\n\t"
            "s_addc_u32 s69, %[xph], 0\n\t"
            "global_load_lds_dwordx4 %[x0], s[68:69] offset:0\n\t"
            "global_load_lds_dwordx4 %[x1], s[68:69] offset:1024\n\t"
            "global_load_lds_dwordx4 %[x2], s[68:69] offset:2048\n\t"
            "global_load_lds_dwordx4 %[x3], s[68:69] offset:3072\n\t"
            "s_waitcnt vmcnt(12)\n\t"
            "s_mov_b64 exec, 3\n\t"
            "global_store_dword %[fob], %[nvl], %[ws] offset:64 sc1\n\t"
            "s_mov_b64 exec, -1\n\t"
            "s_barrier\n\t"
            "s_lshl_b32 s76, s51, 2\n\t"
            ".Lzc10:\n\t"
            "v_cmp_eq_u32 vcc, v192, %[nvl]\n\t"
            "s_lshr_b64 s[74:75], vcc, s76\n\t"
            "s_and_b32 s74, s74, 15\n\t"
            "s_cmp_eq_u32 s74, 15\n\t"
            "s_cbranch_scc1 .Lzd10\n\t"
            "s_cmp_eq_u32 s73, 0\n\t"
            "s_cbranch_scc1 .Lzd10\n\t"
            "s_sub_u32 s73, s73, 1\n\t"
            "global_load_dword v192, %[po], %[ws] sc1\n\t"
            "s_waitcnt vmcnt(0)\n\t"
            "s_branch .Lzc10\n\t"
            ".Lzd10:\n\t"
            "s_add_u32 m0, %[ldsw], 32768\n\t"
            "s_mul_i32 s76, s51, 458752\n\t"
            "s_add_u32 s70, s84, s76\n\t"
            "s_addc_u32 s71, s85, 0\n\t"
            "global_load_lds_dwordx4 %[wv], s[70:71] offset:0 sc1\n\t"
            "global_load_lds_dwordx4 %[wv], s[70:71] offset:1024 sc1\n\t"
            "global_load_lds_dwordx4 %[wv], s[70:71] offset:2048 sc1\n\t"
            "global_load_lds_dwordx4 %[wv], s[70:71] offset:3072 sc1\n\t"
            "s_add_u32 m0, %[ldsx], 65536\n\t"
            "s_lshl_b32 s76, s51, 7\n\t"
            "s_add_u32 s68, %[xpl], s76\n\t"
            "s_addc_u32 s69, %[xph], 0\n\t"
            "global_load_lds_dwordx4 %[x0], s[68:69] offset:0\n\t"
            "global_load_lds_dwordx4 %[x1], s[68:69] offset:1024\n\t"
            "global_load_lds_dwordx4 %[x2], s[68:69] offset:2048\n\t"
            "global_load_lds_dwordx4 %[x3], s[68:69] offset:3072\n\t"
            "s_add_u32 m0, %[ldsx], 69632\n\t"
            "s_lshl_b32 s76, s51, 7\n\t"
            "s_add_u32 s76, s76, 65536\n\t"
            "s_add_u32 s68, %[xpl], s76\n\t"
            "s_addc_u32 s69, %[xph], 0\n\t"
            "global_load_lds_dwordx4 %[x0], s[68:69] offset:0\n\t"
            "global_load_lds_dwordx4 %[x1], s[68:69] offset:1024\n\t"
            "global_load_lds_dwordx4 %[x2], s[68:69] offset:2048\n\t"
            "global_load_lds_dwordx4 %[x3], s[68:69] offset:3072\n\t"
            "s_waitcnt vmcnt(13)\n\t"
            "s_barrier\n\t"
            "global_load_dword v192, %[po], %[ws] sc1\n\t"
            "s_add_u32 m0, %[ldsx], 0\n\t"
            "s_lshl_b32 s76, s52, 7\n\t"
            "s_add_u32 s68, %[xpl], s76\n\t"
            "s_addc_u32 s69, %[xph], 0\n\t"
            "global_load_lds_dwordx4 %[x0], s[68:69] offset:0\n\t"
            "global_load_lds_dwordx4 %[x1], s[68:69] offset:1024\n\t"
            "global_load_lds_dwordx4 %[x2], s[68:69] offset:2048\n\t"
            "global_load_lds_dwordx4 %[x3], s[68:69] offset:3072\n\t"
            "s_add_u32 m0, %[ldsx], 4096\n\t"
            "s_lshl_b32 s76, s52, 7\n\t"
            "s_add_u32 s76, s76, 65536\n\t"
            "s_add_u32 s68, %[xpl], s76\n\t"
            "s_addc_u32 s69, %[xph], 0\n\t"
            "global_load_lds_dwordx4 %[x0], s[68:69] offset:0\n\t"
            "global_load_lds_dwordx4 %[x1], s[68:69] offset:1024\n\t"
            "global_load_lds_dwordx4 %[x2], s[68:69] offset:2048\n\t"
            "global_load_lds_dwordx4 %[x3], s[68:69] offset:3072\n\t"
            "s_waitcnt vmcnt(8)\n\t"
            "s_lshl_b32 s76, s52, 2\n\t"
            ".Lzc11:\n\t"
            "v_cmp_eq_u32 vcc, v192, %[nvl]\n\t"
            "s_lshr_b64 s[74:75], vcc, s76\n\t"
            "s_and_b32 s74, s74, 15\n\t"
            "s_cmp_eq_u32 s74, 15\n\t"
            "s_cbranch_scc1 .Lzd11\n\t"
            "s_cmp_eq_u32 s73, 0\n\t"
            "s_cbranch_scc1 .Lzd11\n\t"
            "s_sub_u32 s73, s73, 1\n\t"
            "global_load_dword v192, %[po], %[ws] sc1\n\t"
            "s_waitcnt vmcnt(0)\n\t"
            "s_branch .Lzc11\n\t"
            ".Lzd11:\n\t"
            "s_add_u32 m0, %[ldsw], 0\n\t"
            "s_mul_i32 s76, s52, 458752\n\t"
            "s_add_u32 s70, s84, s76\n\t"
            "s_addc_u32 s71, s85, 0\n\t"
            "global_load_lds_dwordx4 %[wv], s[70:71] offset:0 sc1\n\t"
            "global_load_lds_dwordx4 %[wv], s[70:71] offset:1024 sc1\n\t"
            "global_load_lds_dwordx4 %[wv], s[70:71] offset:2048 sc1\n\t"
            "global_load_lds_dwordx4 %[wv], s[70:71] offset:3072 sc1\n\t"
            "s_barrier\n\t"
            ".Lwp37:\n\t"
            "s_cbranch_execz .Lwp14\n\t"
            "s_mov_b32 s76, 48\n\t"
            ".Lzc12:\n\t"
            "v_cmp_eq_u32 vcc, v192, %[nvl]\n\t"
            "s_lshr_b64 s[74:75], vcc, s76\n\t"
            "s_and_b32 s74, s74, 65535\n\t"
            "s_cmp_eq_u32 s74, 65535\n\t"
            "s_cbranch_scc1 .Lzd12\n\t"
            "s_cmp_eq_u32 s73, 0\n\t"
            "s_cbranch_scc1 .Lzd12\n\t"
            "s_sub_u32 s73, s73, 1\n\t"
            "global_load_dword v192, %[po], %[ws] sc1\n\t"
            "s_waitcnt vmcnt(0)\n\t"
            "s_branch .Lzc12\n\t"
            ".Lzd12:\n\t"
            "s_add_u32 m0, %[ldsw], 16384\n\t"
            "s_mul_i32 s76, s53, 458752\n\t"
            "s_add_u32 s70, s84, s76\n\t"
            "s_addc_u32 s71, s85, 0\n\t"
            "s_mul_i32 s76, s54, 458752\n\t"
            "s_sub_u32 s76, s76, 1024\n\t"
            "s_add_u32 s86, s84, s76\n\t"
            "s_addc_u32 s87, s85, 0\n\t"
            "global_load_lds_dwordx4 %[wv], s[70:71] offset:0 sc1\n\t"
            "global_load_lds_dwordx4 %[wv], s[86:87] offset:1024 sc1\n\t"
            "global_load_lds_dwordx4 %[wv], s[70:71] offset:2048 sc1\n\t"
            "global_load_lds_dwordx4 %[wv], s[86:87] offset:3072 sc1\n\t"
            "s_add_u32 m0, %[ldsx], 32768\n\t"
            "s_lshl_b32 s76, s53, 7\n\t"
            "s_add_u32 s68, %[xpl], s76\n\t"
            "s_addc_u32 s69, %[xph], 0\n\t"
            "global_load_lds_dwordx4 %[x0], s[68:69] offset:0\n\t"
            "global_load_lds_dwordx4 %[x1], s[68:69] offset:1024\n\t"
            "global_load_lds_dwordx4 %[x2], s[68:69] offset:2048\n\t"
            "global_load_lds_dwordx4 %[x3], s[68:69] offset:3072\n\t"
            "s_add_u32 m0, %[ldsx], 36864\n\t"
            "s_lshl_b32 s76, s53, 7\n\t"
            "s_add_u32 s76, s76, 65536\n\t"
            "s_add_u32 s68, %[xpl], s76\n\t"
            "s_addc_u32 s69, %[xph], 0\n\t"
            "global_load_lds_dwordx4 %[x0], s[68:69] offset:0\n\t"
            "global_load_lds_dwordx4 %[x1], s[68:69] offset:1024\n\t"
            "global_load_lds_dwordx4 %[x2], s[68:69] offset:2048\n\t"
            "global_load_lds_dwordx4 %[x3], s[68:69] offset:3072\n\t"
            "s_waitcnt vmcnt(12)\n\t"
            "s_barrier\n\t"
            "s_add_u32 m0, %[ldsx], 65536\n\t"
            "s_lshl_b32 s76, s54, 7\n\t"
            "s_add_u32 s68, %[xpl], s76\n\t"
            "s_addc_u32 s69, %[xph], 0\n\t"
            "global_load_lds_dwordx4 %[x0], s[68:69] offset:0\n\t"
            "global_load_lds_dwordx4 %[x1], s[68:69] offset:1024\n\t"
            "global_load_lds_dwordx4 %[x2], s[68:69] offset:2048\n\t"
            "global_load_lds_dwordx4 %[x3], s[68:69] offset:3072\n\t"
            "s_add_u32 m0, %[ldsx], 69632\n\t"
            "s_lshl_b32 s76, s54, 7\n\t"
            "s_add_u32 s76, s76, 65536\n\t"
            "s_add_u32 s68, %[xpl], s76\n\t"
            "s_addc_u32 s69, %[xph], 0\n\t"
            "global_load_lds_dwordx4 %[x0], s[68:69] offset:0\n\t"
            "global_load_lds_dwordx4 %[x1], s[68:69] offset:1024\n\t"
            "global_load_lds_dwordx4 %[x2], s[68:69] offset:2048\n\t"
            "global_load_lds_dwordx4 %[x3], s[68:69] offset:3072\n\t"
            "s_add_u32 m0, %[ldsw], 32768\n\t"
            "s_mul_i32 s76, s55, 458752\n\t"
            "s_add_u32 s70, s84, s76\n\t"
            "s_addc_u32 s71, s85, 0\n\t"
            "s_mul_i32 s76, s53, 458752\n\t"
            "s_add_u32 s86, s84, s76\n\t"
            "s_addc_u32 s87, s85, 0\n\t"
            "global_load_lds_dwordx4 %[wv], s[70:71] offset:0 sc1\n\t"
            "global_load_lds_dwordx4 %[wv], s[86:87] offset:1024 sc1\n\t"
            "global_load_lds_dwordx4 %[wv], s[70:71] offset:2048 sc1\n\t"
            "global_load_lds_dwordx4 %[wv], s[86:87] offset:3072 sc1\n\t"
            "s_waitcnt vmcnt(4)\n\t"
            "s_barrier\n\t"
            "s_add_u32 m0, %[ldsx], 0\n\t"
            "s_lshl_b32 s76, s55, 7\n\t"
            "s_add_u32 s68, %[xpl], s76\n\t"
            "s_addc_u32 s69, %[xph], 0\n\t"
            "global_load_lds_dwordx4 %[x0], s[68:69] offset:0\n\t"
            "global_load_lds_dwordx4 %[x1], s[68:69] offset:1024\n\t"
            "global_load_lds_dwordx4 %[x2], s[68:69] offset:2048\n\t"
            "global_load_lds_dwordx4 %[x3], s[68:69] offset:3072\n\t"
            "s_add_u32 m0, %[ldsx], 4096\n\t"
            "s_lshl_b32 s76, s55, 7\n\t"
            "s_add_u32 s76, s76, 65536\n\t"
            "s_add_u32 s68, %[xpl], s76\n\t"
            "s_addc_u32 s69, %[xph], 0\n\t"
            "global_load_lds_dwordx4 %[x0], s[68:69] offset:0\n\t"
            "global_load_lds_dwordx4 %[x1], s[68:69] offset:1024\n\t"
            "global_load_lds_dwordx4 %[x2], s[68:69] offset:2048\n\t"
            "global_load_lds_dwordx4 %[x3], s[68:69] offset:3072\n\t"
            "s_add_u32 m0, %[ldsw], 0\n\t"
            "s_mul_i32 s76, s54, 458752\n\t"
            "s_add_u32 s76, s76, 1024\n\t"
            "s_add_u32 s70, s84, s76\n\t"
            "s_addc_u32 s71, s85, 0\n\t"
            "s_mul_i32 s76, s55, 458752\n\t"
            "s_add_u32 s86, s84, s76\n\t"
            "s_addc_u32 s87, s85, 0\n\t"
            "global_load_lds_dwordx4 %[wv], s[70:71] offset:0 sc1\n\t"
            "global_load_lds_dwordx4 %[wv], s[86:87] offset:1024 sc1\n\t"
            "global_load_lds_dwordx4 %[wv], s[70:71] offset:2048 sc1\n\t"
            "global_load_lds_dwordx4 %[wv], s[86:87] offset:3072 sc1\n\t"
            "s_waitcnt vmcnt(4)\n\t"
            "s_barrier\n\t"
            "s_mov_b32 s76, 16\n\t"
            ".Lzc13:\n\t"
            "v_cmp_eq_u32 vcc, v192, %[nvl]\n\t"
            "s_lshr_b64 s[74:75], vcc, s76\n\t"
            "s_and_b32 s74, s74, 65535\n\t"
            "s_cmp_eq_u32 s74, 65535\n\t"
            "s_cbranch_scc1 .Lzd13\n\t"
            "s_cmp_eq_u32 s73, 0\n\t"
            "s_cbranch_scc1 .Lzd13\n\t"
            "s_sub_u32 s73, s73, 1\n\t"
            "global_load_dword v192, %[po], %[ws] sc1\n\t"
            "s_waitcnt vmcnt(0)\n\t"
            "s_branch .Lzc13\n\t"
            ".Lzd13:\n\t"
            "s_waitcnt vmcnt(0)\n\t"
            "s_barrier\n\t"
            : [nvl] "+v"(nvl), [qo] "+v"(qoff[0]), [wo] "+v"(wo)
            : [x0] "v"(xoff[0]), [x1] "v"(xoff[1]), [x2] "v"(xoff[2]), [x3] "v"(xoff[3]), [wv] "v"(wv), [po] "v"(po),
              [lutl] "v"(lutl), [fo0] "v"(fo0), [fob] "v"(fob), [bd] "v"(bdst),
              [xpl] "s"((u32)KPTR(0)), [xph] "s"((u32)(KPTR(0) >> 32)), [ws] "s"(KPTR(4)),
              [qbg] "s"(qbg), [qa1] "s"(qa1), [tseq] "s"(tseq), [p] "s"(p), [h2] "s"(h2), [ldsx] "s"(ldsx), [ldsw] "s"(ldsw), [st1] "s"(st1), [st1l] "s"((u32)(unsigned long)st1), [st1h] "s"((u32)((unsigned long)st1 >> 32)), [stb] "s"(stb)
            : "vcc", "scc", "memory", "s40", "s41", "s42", "s43", "s44", "s45", "s46", "s47", "s48", "s49", "s50", "s51", "s52", "s53", "s54", "s55", "s56", "s57", "s58", "s59", "s60", "s61", "s62", "s63", "s64", "s65", "s66", "s67", "s68", "s69", "s70", "s71", "s72", "s73", "s74", "s75", "s76", "s77", "s78", "s79", "s80", "s81", "s82", "s83", "s84", "s85", "s86", "s87", "s88", "s89", "v128", "v129", "v130", "v131", "v132", "v133", "v134", "v135", "v136", "v137", "v138", "v139", "v140", "v141", "v142", "v143", "v144", "v145", "v146", "v147", "v148", "v149", "v150", "v151", "v152", "v153", "v154", "v155", "v156", "v157", "v158", "v159", "v160", "v161", "v162", "v163", "v164", "v165", "v166", "v167", "v168", "v169", "v170", "v171", "v172", "v173", "v174", "v175", "v176", "v177", "v178", "v179", "v180", "v181", "v182", "v183", "v184", "v185", "v186", "v187", "v188", "v189", "v190", "v191", "v192", "v193", "v194", "v195", "v196", "v197", "v198", "v199", "v200", "v201", "v202", "v203", "v204", "v205", "v206", "v207", "v208", "v209", "v210", "v211", "v212", "v213", "v214", "v215", "v216", "v217", "v218", "v219", "v220", "v221", "v222", "v223", "v224", "v225", "v226", "v227", "v228", "v229", "v230", "v231", "v232", "v233", "v234", "v235");
        if (mt == 0 && p == 0 && lane < 2) asm volatile("global_store_dword %0, %1, %2 sc1" :: "v"(no), "v"(nvl), "s"(KPTR(4)) : "memory");
        return;
    }

    const int c = lane & 15, g = lane >> 4;
    u32 oo[4];
    unsigned long ob[4];

    int xb0 = (w * 64 + c) * 128 + ((g ^ (c >> 1)) * 16);
    int xb1 = (w * 64 + c) * 128 + (((4 + g) ^ (c >> 1)) * 16);
    int ba = OFF_B + g * 2048 + c * 16;
#define LDX(a) (*(const bf16x8*)(smem + (a)))
#define MP(i, j, x0, w0, x1, w1) do { asm volatile("v_mfma_f32_16x16x32_bf16 %0, %1, %2, %0\n\tv_mfma_f32_16x16x32_bf16 %0, %3, %4, %0" \
                                      : "+v"(acc[i][j]) : "v"(x0), "v"(w0), "v"(x1), "v"(w1)); SB(); } while (0)
#define LD(d, a) do { d = LDX(a); SB(); } while (0)
#define CBAR() do { asm volatile("s_waitcnt lgkmcnt(0)" ::: "memory"); BAR(); asm volatile("" ::: "memory"); } while (0)

    u32 fo0, nvl;
    {
        const int p = w, l = lane & 31, h = lane >> 5;
        const int i2 = OWN(mt, nt), h2 = mt & 1;
        u32 qoff[8];
#pragma unroll
        for (int r = 0; r < 8; ++r) qoff[r] = (u32)(((i2 * 64 + p * 16 + h * 8 + r) * N_ + col0 + l * 4) * 4);
        u32 lutl = (u32)(size_t)(smem) + OFF_L + l * 4;
        u32 bdst = (u32)(size_t)(smem) + OFF_B + p * 4096 + h * 2048 + ((l >> 4) * 64 + (l & 15)) * 16;
        u32 wo = (u32)(nt * 16384 + p * 4096 + h * 2048 + (l >> 4) * 1024 + (l & 15) * 16);
        u32 no = (u32)(nt * 256 + (lane & 1) * 4);
        u32 V[4][8];
        i32x4 Q0[8];
#define PK(b_, j, n) asm volatile("s_waitcnt lgkmcnt(" #n ")\n\t" \
                                    "v_lshl_or_b32 %0, %5, 16, %4\n\tv_lshl_or_b32 %1, %7, 16, %6\n\t" \
                                    "v_lshl_or_b32 %2, %9, 16, %8\n\tv_lshl_or_b32 %3, %11, 16, %10" \
                                    : "=&v"(b_[0]), "=&v"(b_[1]), "=&v"(b_[2]), "=&v"(b_[3]) \
                                    : "v"(V[j][0]), "v"(V[j][1]), "v"(V[j][2]), "v"(V[j][3]), "v"(V[j][4]), "v"(V[j][5]), "v"(V[j][6]), "v"(V[j][7]) : "memory")
#define WR(b_, j) asm volatile("ds_write_b128 %0, %1 offset:%2" :: "v"(bdst), "v"(b_), "i"((j) * 256) : "memory")
        {
            u32 lo = (u32)((p * 4 * N_ + col0 + (l * 2 + h) * 2) * 2);
            asm volatile("" : "+v"(lo), "+v"(no), "+v"(qoff[0]), "+v"(qoff[1]), "+v"(qoff[2]), "+v"(qoff[3]), "+v"(qoff[4]), "+v"(qoff[5]), "+v"(qoff[6]), "+v"(qoff[7]));
            KWAIT();
#pragma unroll
            for (int r = 0; r < 4; ++r) {
                const u16* base = (const u16*)KPTR(2) + r * N_;
                const u32 dst = (u32)(size_t)(smem) + OFF_L + (p * 4 + r) * 256;
                asm volatile("s_mov_b32 m0, %2\n\ts_nop 0\n\tglobal_load_lds_dword %0, %1" :: "v"(lo), "s"(base), "s"(dst) : "memory");
            }
        }
#pragma unroll
        for (int r = 0; r < 8; ++r) asm volatile("global_load_dwordx4 %0, %1, %2 " : "=v"(Q0[r]) : "v"(qoff[r]), "s"(KPTR(1)) : "memory");
        SB();
        BAR();
#pragma unroll
        for (int r = 0; r < 4; ++r) {
            oo[r] = (u32)(((row0 + w * 64 + g * 4 + r) * N_ + col0 + c * 4) * 2);
            ob[r] = KPTR(3) + (unsigned long)(r * 16 * N_ * 2);
            asm volatile("" : "+v"(oo[r]), "+s"(ob[r]));
        }
        asm volatile("" : "+v"(xb0), "+v"(xb1), "+v"(ba));
        const char* st0 = (const char*)KPTR(4) + WS_W + (long)i2 * TW + h2 * 512;
        fo0 = (u32)(WS_F + (nt * 4 + p) * 256 + lane * 4 + i2 * 16 + h2 * 8);
        asm volatile("" : "+v"(wo), "+v"(fo0), "+v"(bdst), "+v"(lutl));
        asm volatile("" : "+s"(st0));
        {
            unsigned long ex_;
            asm volatile("s_mov_b64 %0, exec\n\ts_mov_b64 exec, 0\n\t"
                         "s_cmp_eq_u32 %1, 0\n\ts_cbranch_scc1 .Lwp20\n\t"
                         "s_cmp_eq_u32 %1, 1\n\ts_cbranch_scc1 .Lwp11\n\t"
                         "s_cmp_eq_u32 %1, 2\n\ts_cbranch_scc1 .Lwp12\n\t"
                         "s_branch .Lwp13\n\t"
                         ".Lwp99:\n\ts_mov_b64 exec, %0" : "=&s"(ex_) : "s"(w) : "scc", "memory");
        }
        asm volatile("s_waitcnt vmcnt(8)" ::: "memory");
        BAR();
        PAD(20, 10);
        asm volatile("s_waitcnt vmcnt(7)" : "+v"(Q0[0]) :: "memory"); LUT1(Q0, 0, 0); LUT1(Q0, 1, 0); LUT1(Q0, 2, 0); LUT1(Q0, 3, 0); SB();
        asm volatile("s_waitcnt vmcnt(6)" : "+v"(Q0[1]) :: "memory"); LUT1(Q0, 0, 1); LUT1(Q0, 1, 1); LUT1(Q0, 2, 1); LUT1(Q0, 3, 1); SB();
        asm volatile("s_waitcnt vmcnt(5)" : "+v"(Q0[2]) :: "memory"); LUT1(Q0, 0, 2); LUT1(Q0, 1, 2); LUT1(Q0, 2, 2); LUT1(Q0, 3, 2); SB();
        asm volatile("s_waitcnt vmcnt(4)" : "+v"(Q0[3]) :: "memory"); LUT1(Q0, 0, 3); LUT1(Q0, 1, 3); LUT1(Q0, 2, 3); LUT1(Q0, 3, 3); SB();
        asm volatile("s_waitcnt vmcnt(3)" : "+v"(Q0[4]) :: "memory"); LUT1(Q0, 0, 4); LUT1(Q0, 1, 4); LUT1(Q0, 2, 4); LUT1(Q0, 3, 4); SB();
        asm volatile("s_waitcnt vmcnt(2)" : "+v"(Q0[5]) :: "memory"); LUT1(Q0, 0, 5); LUT1(Q0, 1, 5); LUT1(Q0, 2, 5); LUT1(Q0, 3, 5); SB();
        asm volatile("s_waitcnt vmcnt(1)" : "+v"(Q0[6]) :: "memory"); LUT1(Q0, 0, 6); LUT1(Q0, 1, 6); LUT1(Q0, 2, 6); LUT1(Q0, 3, 6); SB();
        asm volatile("s_waitcnt vmcnt(0)" : "+v"(Q0[7]) :: "memory"); LUT1(Q0, 0, 7); LUT1(Q0, 1, 7); LUT1(Q0, 2, 7); LUT1(Q0, 3, 7); SB();
        {   u32x4 b0, b1, b2, b3;

            PK(b0, 0, 3); WR(b0, 0); PK(b1, 1, 3); WR(b1, 1); PK(b2, 2, 3); WR(b2, 2); PK(b3, 3, 3); WR(b3, 3);

            SB();
        }
        asm volatile("s_waitcnt lgkmcnt(0)" ::: "memory");
        BAR();
    }
    asm volatile(
        "v_mov_b32 v232, %[xb0]\n\t"
        "v_mov_b32 v233, %[xb1]\n\t"
        "v_mov_b32 v234, %[ba]\n\t"
        ".Lwp10:\n\t"
        "s_cbranch_execz .Lwp99\n\t"
        "ds_read_b128 v[192:195], v234 offset:0\n\t"
        "ds_read_b128 v[128:131], v232 offset:0\n\t"
        "ds_read_b128 v[196:199], v234 offset:8192\n\t"
        "ds_read_b128 v[144:147], v233 offset:0\n\t"
        "ds_read_b128 v[204:207], v234 offset:8448\n\t"
        "ds_read_b128 v[200:203], v234 offset:256\n\t"
        "ds_read_b128 v[132:135], v232 offset:2048\n\t"
        "ds_read_b128 v[148:151], v233 offset:2048\n\t"
        "s_waitcnt lgkmcnt(6)\n\t"
        "v_mfma_f32_16x16x32_bf16 v[0:3], v[128:131], v[192:195], 0\n\t"
        "s_waitcnt lgkmcnt(4)\n\t"
        "v_mfma_f32_16x16x32_bf16 v[0:3], v[144:147], v[196:199], v[0:3]\n\t"
        "ds_read_b128 v[136:139], v232 offset:4096\n\t"
        "s_waitcnt lgkmcnt(4)\n\t"
        "v_mfma_f32_16x16x32_bf16 v[4:7], v[144:147], v[204:207], 0\n\t"
        "s_waitcnt lgkmcnt(3)\n\t"
        "v_mfma_f32_16x16x32_bf16 v[4:7], v[128:131], v[200:203], v[4:7]\n\t"
        "ds_read_b128 v[152:155], v233 offset:4096\n\t"
        "s_waitcnt lgkmcnt(3)\n\t"
        "v_mfma_f32_16x16x32_bf16 v[36:39], v[132:135], v[200:203], 0\n\t"
        "s_waitcnt lgkmcnt(2)\n\t"
        "v_mfma_f32_16x16x32_bf16 v[36:39], v[148:151], v[204:207], v[36:39]\n\t"
        "ds_read_b128 v[140:143], v232 offset:6144\n\t"
        "v_mfma_f32_16x16x32_bf16 v[32:35], v[148:151], v[196:199], 0\n\t"
        "v_mfma_f32_16x16x32_bf16 v[32:35], v[132:135], v[192:195], v[32:35]\n\t"
        "ds_read_b128 v[156:159], v233 offset:6144\n\t"
        "s_waitcnt lgkmcnt(3)\n\t"
        "v_mfma_f32_16x16x32_bf16 v[64:67], v[136:139], v[192:195], 0\n\t"
        "s_waitcnt lgkmcnt(2)\n\t"
        "v_mfma_f32_16x16x32_bf16 v[64:67], v[152:155], v[196:199], v[64:67]\n\t"
        "ds_read_b128 v[208:211], v234 offset:512\n\t"
        "v_mfma_f32_16x16x32_bf16 v[68:71], v[152:155], v[204:207], 0\n\t"
        "v_mfma_f32_16x16x32_bf16 v[68:71], v[136:139], v[200:203], v[68:71]\n\t"
        "ds_read_b128 v[212:215], v234 offset:8704\n\t"
        "s_waitcnt lgkmcnt(3)\n\t"
        "v_mfma_f32_16x16x32_bf16 v[100:103], v[140:143], v[200:203], 0\n\t"
        "s_waitcnt lgkmcnt(2)\n\t"
        "v_mfma_f32_16x16x32_bf16 v[100:103], v[156:159], v[204:207], v[100:103]\n\t"
        "ds_read_b128 v[220:223], v234 offset:8960\n\t"
        "v_mfma_f32_16x16x32_bf16 v[96:99], v[156:159], v[196:199], 0\n\t"
        "v_mfma_f32_16x16x32_bf16 v[96:99], v[140:143], v[192:195], v[96:99]\n\t"
        "ds_read_b128 v[216:219], v234 offset:768\n\t"
        "s_waitcnt lgkmcnt(3)\n\t"
        "v_mfma_f32_16x16x32_bf16 v[8:11], v[128:131], v[208:211], 0\n\t"
        "s_waitcnt lgkmcnt(2)\n\t"
        "v_mfma_f32_16x16x32_bf16 v[8:11], v[144:147], v[212:215], v[8:11]\n\t"
        "ds_read_b128 v[192:195], v234 offset:1024\n\t"
        "s_waitcnt lgkmcnt(2)\n\t"
        "v_mfma_f32_16x16x32_bf16 v[12:15], v[144:147], v[220:223], 0\n\t"
        "s_waitcnt lgkmcnt(1)\n\t"
        "v_mfma_f32_16x16x32_bf16 v[12:15], v[128:131], v[216:219], v[12:15]\n\t"
        "ds_read_b128 v[196:199], v234 offset:9216\n\t"
        "v_mfma_f32_16x16x32_bf16 v[44:47], v[132:135], v[216:219], 0\n\t"
        "v_mfma_f32_16x16x32_bf16 v[44:47], v[148:151], v[220:223], v[44:47]\n\t"
        "ds_read_b128 v[204:207], v234 offset:9472\n\t"
        "v_mfma_f32_16x16x32_bf16 v[40:43], v[148:151], v[212:215], 0\n\t"
        "v_mfma_f32_16x16x32_bf16 v[40:43], v[132:135], v[208:211], v[40:43]\n\t"
        "ds_read_b128 v[200:203], v234 offset:1280\n\t"
        "v_mfma_f32_16x16x32_bf16 v[72:75], v[136:139], v[208:211], 0\n\t"
        "v_mfma_f32_16x16x32_bf16 v[72:75], v[152:155], v[212:215], v[72:75]\n\t"
        "v_mfma_f32_16x16x32_bf16 v[76:79], v[152:155], v[220:223], 0\n\t"
        "v_mfma_f32_16x16x32_bf16 v[76:79], v[136:139], v[216:219], v[76:79]\n\t"
        "v_mfma_f32_16x16x32_bf16 v[108:111], v[140:143], v[216:219], 0\n\t"
        "v_mfma_f32_16x16x32_bf16 v[108:111], v[156:159], v[220:223], v[108:111]\n\t"
        "v_mfma_f32_16x16x32_bf16 v[104:107], v[156:159], v[212:215], 0\n\t"
        "v_mfma_f32_16x16x32_bf16 v[104:107], v[140:143], v[208:211], v[104:107]\n\t"
        "s_waitcnt lgkmcnt(0)\n\t"
        "v_mfma_f32_16x16x32_bf16 v[16:19], v[128:131], v[192:195], 0\n\t"
        "v_mfma_f32_16x16x32_bf16 v[16:19], v[144:147], v[196:199], v[16:19]\n\t"
        "ds_read_b128 v[208:211], v234 offset:1536\n\t"
        "v_mfma_f32_16x16x32_bf16 v[20:23], v[144:147], v[204:207], 0\n\t"
        "v_mfma_f32_16x16x32_bf16 v[20:23], v[128:131], v[200:203], v[20:23]\n\t"
        "ds_read_b128 v[212:215], v234 offset:9728\n\t"
        "v_mfma_f32_16x16x32_bf16 v[52:55], v[132:135], v[200:203], 0\n\t"
        "v_mfma_f32_16x16x32_bf16 v[52:55], v[148:151], v[204:207], v[52:55]\n\t"
        "ds_read_b128 v[220:223], v234 offset:9984\n\t"
        "v_mfma_f32_16x16x32_bf16 v[48:51], v[148:151], v[196:199], 0\n\t"
        "v_mfma_f32_16x16x32_bf16 v[48:51], v[132:135], v[192:195], v[48:51]\n\t"
        "ds_read_b128 v[216:219], v234 offset:1792\n\t"
        "v_mfma_f32_16x16x32_bf16 v[80:83], v[136:139], v[192:195], 0\n\t"
        "v_mfma_f32_16x16x32_bf16 v[80:83], v[152:155], v[196:199], v[80:83]\n\t"
        "v_mfma_f32_16x16x32_bf16 v[84:87], v[152:155], v[204:207], 0\n\t"
        "v_mfma_f32_16x16x32_bf16 v[84:87], v[136:139], v[200:203], v[84:87]\n\t"
        "s_mov_b32 s60, 32768\n\t"
        "s_mov_b32 s62, 5\n\t"
        "v_add_u32 v232, 32768, %[xb0]\n\t"
        "v_add_u32 v233, 32768, %[xb1]\n\t"
        "v_add_u32 v234, 16384, %[ba]\n\t"
        "v_mov_b64 v[24:25], 0\n\t"
        "v_mov_b64 v[26:27], 0\n\t"
        "v_mov_b64 v[28:29], 0\n\t"
        "v_mov_b64 v[30:31], 0\n\t"
        "v_mov_b64 v[56:57], 0\n\t"
        "v_mov_b64 v[58:59], 0\n\t"
        "v_mov_b64 v[60:61], 0\n\t"
        "v_mov_b64 v[62:63], 0\n\t"
        "v_mov_b64 v[88:89], 0\n\t"
        "v_mov_b64 v[90:91], 0\n\t"
        "v_mov_b64 v[92:93], 0\n\t"
        "v_mov_b64 v[94:95], 0\n\t"
        "v_mov_b64 v[120:121], 0\n\t"
        "v_mov_b64 v[122:123], 0\n\t"
        "v_mov_b64 v[124:125], 0\n\t"
        "v_mov_b64 v[126:127], 0\n\t"
        "v_mov_b64 v[116:117], 0\n\t"
        "v_mov_b64 v[118:119], 0\n\t"
        "v_mov_b64 v[112:113], 0\n\t"
        "v_mov_b64 v[114:115], 0\n\t"
        "s_nop 1\n\t"
        "s_mov_b64 exec, 0\n\t"
        "s_cmp_eq_u32 %[w], 0\n\t"
        "s_cbranch_scc1 .Lwp34\n\t"
        "s_cmp_eq_u32 %[w], 1\n\t"
        "s_cbranch_scc1 .Lwp37\n\t"
        "s_cmp_eq_u32 %[w], 2\n\t"
        "s_cbranch_scc1 .Lwp15\n\t"
        "s_branch .Lwp17\n\t"
        ".Lwp97:\n\t"
        "s_mov_b64 exec, -1\n\t"
        ".Lcloop:\n\t"
        "v_mfma_f32_16x16x32_bf16 v[116:119], v[140:143], v[200:203], v[116:119]\n\t"
        ".Lwp11:\n\t"
        "s_cbranch_execz .Lwp99\n\t"
        "s_sub_u32 s62, s62, 1\n\t"
        "v_mfma_f32_16x16x32_bf16 v[116:119], v[156:159], v[204:207], v[116:119]\n\t"
        "s_waitcnt lgkmcnt(0)\n\t"
        "v_mfma_f32_16x16x32_bf16 v[112:115], v[156:159], v[196:199], v[112:115]\n\t"
        "v_mfma_f32_16x16x32_bf16 v[112:115], v[140:143], v[192:195], v[112:115]\n\t"
        "s_barrier\n\t"
        "v_mfma_f32_16x16x32_bf16 v[24:27], v[128:131], v[208:211], v[24:27]\n\t"
        "v_mfma_f32_16x16x32_bf16 v[24:27], v[144:147], v[212:215], v[24:27]\n\t"
        "ds_read_b128 v[192:195], v234 offset:0\n\t"
        "ds_read_b128 v[160:163], v232 offset:0\n\t"
        "v_mfma_f32_16x16x32_bf16 v[28:31], v[144:147], v[220:223], v[28:31]\n\t"
        "v_mfma_f32_16x16x32_bf16 v[28:31], v[128:131], v[216:219], v[28:31]\n\t"
        "ds_read_b128 v[196:199], v234 offset:8192\n\t"
        "ds_read_b128 v[176:179], v233 offset:0\n\t"
        "v_mfma_f32_16x16x32_bf16 v[60:63], v[132:135], v[216:219], v[60:63]\n\t"
        "v_mfma_f32_16x16x32_bf16 v[60:63], v[148:151], v[220:223], v[60:63]\n\t"
        "ds_read_b128 v[204:207], v234 offset:8448\n\t"
        "v_mfma_f32_16x16x32_bf16 v[56:59], v[148:151], v[212:215], v[56:59]\n\t"
        "v_mfma_f32_16x16x32_bf16 v[56:59], v[132:135], v[208:211], v[56:59]\n\t"
        "ds_read_b128 v[200:203], v234 offset:256\n\t"
        "v_mfma_f32_16x16x32_bf16 v[88:91], v[136:139], v[208:211], v[88:91]\n\t"
        "v_mfma_f32_16x16x32_bf16 v[88:91], v[152:155], v[212:215], v[88:91]\n\t"
        "ds_read_b128 v[164:167], v232 offset:2048\n\t"
        "v_mfma_f32_16x16x32_bf16 v[92:95], v[152:155], v[220:223], v[92:95]\n\t"
        "v_mfma_f32_16x16x32_bf16 v[92:95], v[136:139], v[216:219], v[92:95]\n\t"
        "ds_read_b128 v[180:183], v233 offset:2048\n\t"
        "v_mfma_f32_16x16x32_bf16 v[124:127], v[140:143], v[216:219], v[124:127]\n\t"
        "v_mfma_f32_16x16x32_bf16 v[124:127], v[156:159], v[220:223], v[124:127]\n\t"
        "ds_read_b128 v[168:171], v232 offset:4096\n\t"
        "v_mfma_f32_16x16x32_bf16 v[120:123], v[156:159], v[212:215], v[120:123]\n\t"
        "v_mfma_f32_16x16x32_bf16 v[120:123], v[140:143], v[208:211], v[120:123]\n\t"
        "ds_read_b128 v[184:187], v233 offset:4096\n\t"
        "s_waitcnt lgkmcnt(4)\n\t"
        "v_mfma_f32_16x16x32_bf16 v[0:3], v[160:163], v[192:195], v[0:3]\n\t"
        "v_mfma_f32_16x16x32_bf16 v[0:3], v[176:179], v[196:199], v[0:3]\n\t"
        "ds_read_b128 v[172:175], v232 offset:6144\n\t"
        "ds_read_b128 v[208:211], v234 offset:512\n\t"
        "v_mfma_f32_16x16x32_bf16 v[4:7], v[176:179], v[204:207], v[4:7]\n\t"
        "v_mfma_f32_16x16x32_bf16 v[4:7], v[160:163], v[200:203], v[4:7]\n\t"
        "ds_read_b128 v[188:191], v233 offset:6144\n\t"
        "s_waitcnt lgkmcnt(5)\n\t"
        "v_mfma_f32_16x16x32_bf16 v[36:39], v[164:167], v[200:203], v[36:39]\n\t"
        "ds_read_b128 v[212:215], v234 offset:8704\n\t"
        "v_mfma_f32_16x16x32_bf16 v[36:39], v[180:183], v[204:207], v[36:39]\n\t"
        "ds_read_b128 v[220:223], v234 offset:8960\n\t"
        "v_mfma_f32_16x16x32_bf16 v[32:35], v[180:183], v[196:199], v[32:35]\n\t"
        "v_mfma_f32_16x16x32_bf16 v[32:35], v[164:167], v[192:195], v[32:35]\n\t"
        "ds_read_b128 v[216:219], v234 offset:768\n\t"
        "s_waitcnt lgkmcnt(6)\n\t"
        "v_mfma_f32_16x16x32_bf16 v[64:67], v[168:171], v[192:195], v[64:67]\n\t"
        "v_mfma_f32_16x16x32_bf16 v[64:67], v[184:187], v[196:199], v[64:67]\n\t"
        "v_mfma_f32_16x16x32_bf16 v[68:71], v[184:187], v[204:207], v[68:71]\n\t"
        "v_mfma_f32_16x16x32_bf16 v[68:71], v[168:171], v[200:203], v[68:71]\n\t"
        "s_waitcnt lgkmcnt(3)\n\t"
        "v_mfma_f32_16x16x32_bf16 v[100:103], v[172:175], v[200:203], v[100:103]\n\t"
        "v_mfma_f32_16x16x32_bf16 v[100:103], v[188:191], v[204:207], v[100:103]\n\t"
        "v_mfma_f32_16x16x32_bf16 v[96:99], v[188:191], v[196:199], v[96:99]\n\t"
        "v_mfma_f32_16x16x32_bf16 v[96:99], v[172:175], v[192:195], v[96:99]\n\t"
        ".Lwp12:\n\t"
        "s_cbranch_execz .Lwp99\n\t"
        "v_mfma_f32_16x16x32_bf16 v[8:11], v[160:163], v[208:211], v[8:11]\n\t"
        "s_waitcnt lgkmcnt(0)\n\t"
        "v_mfma_f32_16x16x32_bf16 v[8:11], v[176:179], v[212:215], v[8:11]\n\t"
        "ds_read_b128 v[192:195], v234 offset:1024\n\t"
        "v_mfma_f32_16x16x32_bf16 v[12:15], v[176:179], v[220:223], v[12:15]\n\t"
        "v_mfma_f32_16x16x32_bf16 v[12:15], v[160:163], v[216:219], v[12:15]\n\t"
        "ds_read_b128 v[196:199], v234 offset:9216\n\t"
        "v_mfma_f32_16x16x32_bf16 v[44:47], v[164:167], v[216:219], v[44:47]\n\t"
        "v_mfma_f32_16x16x32_bf16 v[44:47], v[180:183], v[220:223], v[44:47]\n\t"
        "ds_read_b128 v[204:207], v234 offset:9472\n\t"
        "v_mfma_f32_16x16x32_bf16 v[40:43], v[180:183], v[212:215], v[40:43]\n\t"
        "v_mfma_f32_16x16x32_bf16 v[40:43], v[164:167], v[208:211], v[40:43]\n\t"
        "ds_read_b128 v[200:203], v234 offset:1280\n\t"
        "v_mfma_f32_16x16x32_bf16 v[72:75], v[168:171], v[208:211], v[72:75]\n\t"
        "v_mfma_f32_16x16x32_bf16 v[72:75], v[184:187], v[212:215], v[72:75]\n\t"
        "v_mfma_f32_16x16x32_bf16 v[76:79], v[184:187], v[220:223], v[76:79]\n\t"
        "v_mfma_f32_16x16x32_bf16 v[76:79], v[168:171], v[216:219], v[76:79]\n\t"
        "v_mfma_f32_16x16x32_bf16 v[108:111], v[172:175], v[216:219], v[108:111]\n\t"
        "v_mfma_f32_16x16x32_bf16 v[108:111], v[188:191], v[220:223], v[108:111]\n\t"
        "v_mfma_f32_16x16x32_bf16 v[104:107], v[188:191], v[212:215], v[104:107]\n\t"
        "v_mfma_f32_16x16x32_bf16 v[104:107], v[172:175], v[208:211], v[104:107]\n\t"
        "s_waitcnt lgkmcnt(0)\n\t"
        "v_mfma_f32_16x16x32_bf16 v[16:19], v[160:163], v[192:195], v[16:19]\n\t"
        "v_mfma_f32_16x16x32_bf16 v[16:19], v[176:179], v[196:199], v[16:19]\n\t"
        "ds_read_b128 v[208:211], v234 offset:1536\n\t"
        "v_mfma_f32_16x16x32_bf16 v[20:23], v[176:179], v[204:207], v[20:23]\n\t"
        "v_mfma_f32_16x16x32_bf16 v[20:23], v[160:163], v[200:203], v[20:23]\n\t"
        "ds_read_b128 v[212:215], v234 offset:9728\n\t"
        "v_mfma_f32_16x16x32_bf16 v[52:55], v[164:167], v[200:203], v[52:55]\n\t"
        "v_mfma_f32_16x16x32_bf16 v[52:55], v[180:183], v[204:207], v[52:55]\n\t"
        "ds_read_b128 v[220:223], v234 offset:9984\n\t"
        "v_mfma_f32_16x16x32_bf16 v[48:51], v[180:183], v[196:199], v[48:51]\n\t"
        "v_mfma_f32_16x16x32_bf16 v[48:51], v[164:167], v[192:195], v[48:51]\n\t"
        "ds_read_b128 v[216:219], v234 offset:1792\n\t"
        "s_add_u32 s60, s60, 32768\n\t"
        "v_mfma_f32_16x16x32_bf16 v[80:83], v[168:171], v[192:195], v[80:83]\n\t"
        "s_cmp_eq_u32 s60, 98304\n\t"
        "s_cselect_b32 s60, 0, s60\n\t"
        "v_mfma_f32_16x16x32_bf16 v[80:83], v[184:187], v[196:199], v[80:83]\n\t"
        "v_add_u32 v232, s60, %[xb0]\n\t"
        "s_lshr_b32 s61, s60, 1\n\t"
        "v_mfma_f32_16x16x32_bf16 v[84:87], v[184:187], v[204:207], v[84:87]\n\t"
        "v_add_u32 v233, s60, %[xb1]\n\t"
        "v_add_u32 v234, s61, %[ba]\n\t"
        "v_mfma_f32_16x16x32_bf16 v[84:87], v[168:171], v[200:203], v[84:87]\n\t"
        "v_mfma_f32_16x16x32_bf16 v[116:119], v[172:175], v[200:203], v[116:119]\n\t"
        "v_mfma_f32_16x16x32_bf16 v[116:119], v[188:191], v[204:207], v[116:119]\n\t"
        "s_waitcnt lgkmcnt(0)\n\t"
        "v_mfma_f32_16x16x32_bf16 v[112:115], v[188:191], v[196:199], v[112:115]\n\t"
        "v_mfma_f32_16x16x32_bf16 v[112:115], v[172:175], v[192:195], v[112:115]\n\t"
        "s_barrier\n\t"
        "v_mfma_f32_16x16x32_bf16 v[24:27], v[160:163], v[208:211], v[24:27]\n\t"
        "v_mfma_f32_16x16x32_bf16 v[24:27], v[176:179], v[212:215], v[24:27]\n\t"
        "ds_read_b128 v[192:195], v234 offset:0\n\t"
        "ds_read_b128 v[128:131], v232 offset:0\n\t"
        "v_mfma_f32_16x16x32_bf16 v[28:31], v[176:179], v[220:223], v[28:31]\n\t"
        "v_mfma_f32_16x16x32_bf16 v[28:31], v[160:163], v[216:219], v[28:31]\n\t"
        "ds_read_b128 v[196:199], v234 offset:8192\n\t"
        "ds_read_b128 v[144:147], v233 offset:0\n\t"
        "v_mfma_f32_16x16x32_bf16 v[60:63], v[164:167], v[216:219], v[60:63]\n\t"
        "v_mfma_f32_16x16x32_bf16 v[60:63], v[180:183], v[220:223], v[60:63]\n\t"
        "ds_read_b128 v[204:207], v234 offset:8448\n\t"
        "v_mfma_f32_16x16x32_bf16 v[56:59], v[180:183], v[212:215], v[56:59]\n\t"
        "v_mfma_f32_16x16x32_bf16 v[56:59], v[164:167], v[208:211], v[56:59]\n\t"
        "ds_read_b128 v[200:203], v234 offset:256\n\t"
        "v_mfma_f32_16x16x32_bf16 v[88:91], v[168:171], v[208:211], v[88:91]\n\t"
        "v_mfma_f32_16x16x32_bf16 v[88:91], v[184:187], v[212:215], v[88:91]\n\t"
        "ds_read_b128 v[132:135], v232 offset:2048\n\t"
        "v_mfma_f32_16x16x32_bf16 v[92:95], v[184:187], v[220:223], v[92:95]\n\t"
        "v_mfma_f32_16x16x32_bf16 v[92:95], v[168:171], v[216:219], v[92:95]\n\t"
        "ds_read_b128 v[148:151], v233 offset:2048\n\t"
        "v_mfma_f32_16x16x32_bf16 v[124:127], v[172:175], v[216:219], v[124:127]\n\t"
        "v_mfma_f32_16x16x32_bf16 v[124:127], v[188:191], v[220:223], v[124:127]\n\t"
        "ds_read_b128 v[136:139], v232 offset:4096\n\t"
        "v_mfma_f32_16x16x32_bf16 v[120:123], v[188:191], v[212:215], v[120:123]\n\t"
        "v_mfma_f32_16x16x32_bf16 v[120:123], v[172:175], v[208:211], v[120:123]\n\t"
        "ds_read_b128 v[152:155], v233 offset:4096\n\t"
        "s_waitcnt lgkmcnt(4)\n\t"
        "v_mfma_f32_16x16x32_bf16 v[0:3], v[128:131], v[192:195], v[0:3]\n\t"
        "v_mfma_f32_16x16x32_bf16 v[0:3], v[144:147], v[196:199], v[0:3]\n\t"
        "ds_read_b128 v[140:143], v232 offset:6144\n\t"
        "ds_read_b128 v[208:211], v234 offset:512\n\t"
        "v_mfma_f32_16x16x32_bf16 v[4:7], v[144:147], v[204:207], v[4:7]\n\t"
        "v_mfma_f32_16x16x32_bf16 v[4:7], v[128:131], v[200:203], v[4:7]\n\t"
        "ds_read_b128 v[156:159], v233 offset:6144\n\t"
        "s_waitcnt lgkmcnt(5)\n\t"
        "v_mfma_f32_16x16x32_bf16 v[36:39], v[132:135], v[200:203], v[36:39]\n\t"
        "ds_read_b128 v[212:215], v234 offset:8704\n\t"
        "v_mfma_f32_16x16x32_bf16 v[36:39], v[148:151], v[204:207], v[36:39]\n\t"
        "ds_read_b128 v[220:223], v234 offset:8960\n\t"
        "v_mfma_f32_16x16x32_bf16 v[32:35], v[148:151], v[196:199], v[32:35]\n\t"
        "v_mfma_f32_16x16x32_bf16 v[32:35], v[132:135], v[192:195], v[32:35]\n\t"
        "ds_read_b128 v[216:219], v234 offset:768\n\t"
        "s_waitcnt lgkmcnt(6)\n\t"
        "v_mfma_f32_16x16x32_bf16 v[64:67], v[136:139], v[192:195], v[64:67]\n\t"
        "v_mfma_f32_16x16x32_bf16 v[64:67], v[152:155], v[196:199], v[64:67]\n\t"
        "v_mfma_f32_16x16x32_bf16 v[68:71], v[152:155], v[204:207], v[68:71]\n\t"
        "v_mfma_f32_16x16x32_bf16 v[68:71], v[136:139], v[200:203], v[68:71]\n\t"
        "s_waitcnt lgkmcnt(3)\n\t"
        "v_mfma_f32_16x16x32_bf16 v[100:103], v[140:143], v[200:203], v[100:103]\n\t"
        "v_mfma_f32_16x16x32_bf16 v[100:103], v[156:159], v[204:207], v[100:103]\n\t"
        "v_mfma_f32_16x16x32_bf16 v[96:99], v[156:159], v[196:199], v[96:99]\n\t"
        "v_mfma_f32_16x16x32_bf16 v[96:99], v[140:143], v[192:195], v[96:99]\n\t"
        ".Lwp13:\n\t"
        "s_cbranch_execz .Lwp99\n\t"
        "v_mfma_f32_16x16x32_bf16 v[8:11], v[128:131], v[208:211], v[8:11]\n\t"
        "s_waitcnt lgkmcnt(0)\n\t"
        "v_mfma_f32_16x16x32_bf16 v[8:11], v[144:147], v[212:215], v[8:11]\n\t"
        "ds_read_b128 v[192:195], v234 offset:1024\n\t"
        "v_mfma_f32_16x16x32_bf16 v[12:15], v[144:147], v[220:223], v[12:15]\n\t"
        "v_mfma_f32_16x16x32_bf16 v[12:15], v[128:131], v[216:219], v[12:15]\n\t"
        "ds_read_b128 v[196:199], v234 offset:9216\n\t"
        "v_mfma_f32_16x16x32_bf16 v[44:47], v[132:135], v[216:219], v[44:47]\n\t"
        "v_mfma_f32_16x16x32_bf16 v[44:47], v[148:151], v[220:223], v[44:47]\n\t"
        "ds_read_b128 v[204:207], v234 offset:9472\n\t"
        "v_mfma_f32_16x16x32_bf16 v[40:43], v[148:151], v[212:215], v[40:43]\n\t"
        "v_mfma_f32_16x16x32_bf16 v[40:43], v[132:135], v[208:211], v[40:43]\n\t"
        "ds_read_b128 v[200:203], v234 offset:1280\n\t"
        "v_mfma_f32_16x16x32_bf16 v[72:75], v[136:139], v[208:211], v[72:75]\n\t"
        "v_mfma_f32_16x16x32_bf16 v[72:75], v[152:155], v[212:215], v[72:75]\n\t"
        "v_mfma_f32_16x16x32_bf16 v[76:79], v[152:155], v[220:223], v[76:79]\n\t"
        "v_mfma_f32_16x16x32_bf16 v[76:79], v[136:139], v[216:219], v[76:79]\n\t"
        "v_mfma_f32_16x16x32_bf16 v[108:111], v[140:143], v[216:219], v[108:111]\n\t"
        "v_mfma_f32_16x16x32_bf16 v[108:111], v[156:159], v[220:223], v[108:111]\n\t"
        "v_mfma_f32_16x16x32_bf16 v[104:107], v[156:159], v[212:215], v[104:107]\n\t"
        "v_mfma_f32_16x16x32_bf16 v[104:107], v[140:143], v[208:211], v[104:107]\n\t"
        "s_waitcnt lgkmcnt(0)\n\t"
        "v_mfma_f32_16x16x32_bf16 v[16:19], v[128:131], v[192:195], v[16:19]\n\t"
        "v_mfma_f32_16x16x32_bf16 v[16:19], v[144:147], v[196:199], v[16:19]\n\t"
        "ds_read_b128 v[208:211], v234 offset:1536\n\t"
        "v_mfma_f32_16x16x32_bf16 v[20:23], v[144:147], v[204:207], v[20:23]\n\t"
        "v_mfma_f32_16x16x32_bf16 v[20:23], v[128:131], v[200:203], v[20:23]\n\t"
        "ds_read_b128 v[212:215], v234 offset:9728\n\t"
        "v_mfma_f32_16x16x32_bf16 v[52:55], v[132:135], v[200:203], v[52:55]\n\t"
        "v_mfma_f32_16x16x32_bf16 v[52:55], v[148:151], v[204:207], v[52:55]\n\t"
        "ds_read_b128 v[220:223], v234 offset:9984\n\t"
        "v_mfma_f32_16x16x32_bf16 v[48:51], v[148:151], v[196:199], v[48:51]\n\t"
        "v_mfma_f32_16x16x32_bf16 v[48:51], v[132:135], v[192:195], v[48:51]\n\t"
        "ds_read_b128 v[216:219], v234 offset:1792\n\t"
        "s_add_u32 s60, s60, 32768\n\t"
        "v_mfma_f32_16x16x32_bf16 v[80:83], v[136:139], v[192:195], v[80:83]\n\t"
        "s_cmp_eq_u32 s60, 98304\n\t"
        "s_cselect_b32 s60, 0, s60\n\t"
        "v_mfma_f32_16x16x32_bf16 v[80:83], v[152:155], v[196:199], v[80:83]\n\t"
        "v_add_u32 v232, s60, %[xb0]\n\t"
        "s_lshr_b32 s61, s60, 1\n\t"
        "v_mfma_f32_16x16x32_bf16 v[84:87], v[152:155], v[204:207], v[84:87]\n\t"
        "v_add_u32 v233, s60, %[xb1]\n\t"
        "v_add_u32 v234, s61, %[ba]\n\t"
        "v_mfma_f32_16x16x32_bf16 v[84:87], v[136:139], v[200:203], v[84:87]\n\t"
        "s_cmp_lg_u32 s62, -1\n\t"
        "s_cbranch_scc1 .Lcloop\n\t"
        "v_mfma_f32_16x16x32_bf16 v[116:119], v[140:143], v[200:203], v[116:119]\n\t"
        "v_mfma_f32_16x16x32_bf16 v[116:119], v[156:159], v[204:207], v[116:119]\n\t"
        "s_waitcnt lgkmcnt(0)\n\t"
        "v_mfma_f32_16x16x32_bf16 v[112:115], v[156:159], v[196:199], v[112:115]\n\t"
        "v_mfma_f32_16x16x32_bf16 v[112:115], v[140:143], v[192:195], v[112:115]\n\t"
        "s_barrier\n\t"
        "v_mfma_f32_16x16x32_bf16 v[24:27], v[128:131], v[208:211], v[24:27]\n\t"
        "s_setprio 2\n\t"
        ".Lwp14:\n\t"
        "s_cbranch_execz .Lwp97\n\t"
        "v_mfma_f32_16x16x32_bf16 v[24:27], v[144:147], v[212:215], v[24:27]\n\t"
        "ds_read_b128 v[160:163], %[ba] offset:16384\n\t"
        "ds_read_b128 v[164:167], v232 offset:0\n\t"
        "v_mfma_f32_16x16x32_bf16 v[28:31], v[144:147], v[220:223], v[28:31]\n\t"
        "v_mfma_f32_16x16x32_bf16 v[28:31], v[128:131], v[216:219], v[28:31]\n\t"
        "ds_read_b128 v[144:147], %[ba] offset:24576\n\t"
        "ds_read_b128 v[168:171], v233 offset:0\n\t"
        "v_mfma_f32_16x16x32_bf16 v[60:63], v[132:135], v[216:219], v[60:63]\n\t"
        "v_mfma_f32_16x16x32_bf16 v[60:63], v[148:151], v[220:223], v[60:63]\n\t"
        "ds_read_b128 v[128:131], %[ba] offset:24832\n\t"
        "ds_read_b128 v[172:175], %[ba] offset:16640\n\t"
        "v_mfma_f32_16x16x32_bf16 v[56:59], v[148:151], v[212:215], v[56:59]\n\t"
        "ds_read_b128 v[176:179], v232 offset:2048\n\t"
        "v_mfma_f32_16x16x32_bf16 v[56:59], v[132:135], v[208:211], v[56:59]\n\t"
        "ds_read_b128 v[148:151], v233 offset:2048\n\t"
        "v_mfma_f32_16x16x32_bf16 v[88:91], v[136:139], v[208:211], v[88:91]\n\t"
        "v_mfma_f32_16x16x32_bf16 v[88:91], v[152:155], v[212:215], v[88:91]\n\t"
        "ds_read_b128 v[132:135], v232 offset:4096\n\t"
        "v_mfma_f32_16x16x32_bf16 v[92:95], v[152:155], v[220:223], v[92:95]\n\t"
        "v_mfma_f32_16x16x32_bf16 v[92:95], v[136:139], v[216:219], v[92:95]\n\t"
        "ds_read_b128 v[152:155], v233 offset:4096\n\t"
        "v_mfma_f32_16x16x32_bf16 v[124:127], v[140:143], v[216:219], v[124:127]\n\t"
        "v_mfma_f32_16x16x32_bf16 v[124:127], v[156:159], v[220:223], v[124:127]\n\t"
        "ds_read_b128 v[136:139], v232 offset:6144\n\t"
        "v_mfma_f32_16x16x32_bf16 v[120:123], v[156:159], v[212:215], v[120:123]\n\t"
        "v_mfma_f32_16x16x32_bf16 v[120:123], v[140:143], v[208:211], v[120:123]\n\t"
        "ds_read_b128 v[156:159], v233 offset:6144\n\t"
        "s_waitcnt lgkmcnt(4)\n\t"
        "v_mfma_f32_16x16x32_bf16 v[0:3], v[164:167], v[160:163], v[0:3]\n\t"
        "v_mfma_f32_16x16x32_bf16 v[0:3], v[168:171], v[144:147], v[0:3]\n\t"
        "ds_read_b128 v[140:143], %[ba] offset:16896\n\t"
        "v_mfma_f32_16x16x32_bf16 v[4:7], v[168:171], v[128:131], v[4:7]\n\t"
        "v_mfma_f32_16x16x32_bf16 v[4:7], v[164:167], v[172:175], v[4:7]\n\t"
        "ds_read_b128 v[180:183], %[ba] offset:25088\n\t"
        "v_mfma_f32_16x16x32_bf16 v[36:39], v[176:179], v[172:175], v[36:39]\n\t"
        "v_mfma_f32_16x16x32_bf16 v[36:39], v[148:151], v[128:131], v[36:39]\n\t"
        "ds_read_b128 v[184:187], %[ba] offset:25344\n\t"
        "v_mfma_f32_16x16x32_bf16 v[32:35], v[148:151], v[144:147], v[32:35]\n\t"
        "v_mfma_f32_16x16x32_bf16 v[32:35], v[176:179], v[160:163], v[32:35]\n\t"
        "ds_read_b128 v[188:191], %[ba] offset:17152\n\t"
        "s_waitcnt lgkmcnt(4)\n\t"
        "v_mfma_f32_16x16x32_bf16 v[64:67], v[132:135], v[160:163], v[64:67]\n\t"
        "v_mfma_f32_16x16x32_bf16 v[64:67], v[152:155], v[144:147], v[64:67]\n\t"
        "ds_read_b128 v[192:195], %[ba] offset:17408\n\t"
        "v_mfma_f32_16x16x32_bf16 v[68:71], v[152:155], v[128:131], v[68:71]\n\t"
        "v_mfma_f32_16x16x32_bf16 v[68:71], v[132:135], v[172:175], v[68:71]\n\t"
        "ds_read_b128 v[196:199], v232 offset:32768\n\t"
        "v_mfma_f32_16x16x32_bf16 v[100:103], v[136:139], v[172:175], v[100:103]\n\t"
        "v_mfma_f32_16x16x32_bf16 v[100:103], v[156:159], v[128:131], v[100:103]\n\t"
        "ds_read_b128 v[172:175], %[ba] offset:25600\n\t"
        "v_mfma_f32_16x16x32_bf16 v[96:99], v[156:159], v[144:147], v[96:99]\n\t"
        "v_mfma_f32_16x16x32_bf16 v[96:99], v[136:139], v[160:163], v[96:99]\n\t"
        "ds_read_b128 v[128:131], v233 offset:32768\n\t"
        "s_waitcnt lgkmcnt(4)\n\t"
        "v_mfma_f32_16x16x32_bf16 v[8:11], v[164:167], v[140:143], v[8:11]\n\t"
        "v_mfma_f32_16x16x32_bf16 v[8:11], v[168:171], v[180:183], v[8:11]\n\t"
        "ds_read_b128 v[144:147], %[ba] offset:25856\n\t"
        "v_mfma_f32_16x16x32_bf16 v[12:15], v[168:171], v[184:187], v[12:15]\n\t"
        "v_mfma_f32_16x16x32_bf16 v[12:15], v[164:167], v[188:191], v[12:15]\n\t"
        "ds_read_b128 v[160:163], %[ba] offset:17664\n\t"
        "v_mfma_f32_16x16x32_bf16 v[44:47], v[176:179], v[188:191], v[44:47]\n\t"
        "v_mfma_f32_16x16x32_bf16 v[44:47], v[148:151], v[184:187], v[44:47]\n\t"
        "ds_read_b128 v[164:167], %[ba] offset:17920\n\t"
        "ds_read_b128 v[168:171], %[ba] offset:26112\n\t"
        "v_mfma_f32_16x16x32_bf16 v[40:43], v[148:151], v[180:183], v[40:43]\n\t"
        "v_mfma_f32_16x16x32_bf16 v[40:43], v[176:179], v[140:143], v[40:43]\n\t"
        "ds_read_b128 v[148:151], %[ba] offset:26368\n\t"
        "v_mfma_f32_16x16x32_bf16 v[72:75], v[132:135], v[140:143], v[72:75]\n\t"
        "v_mfma_f32_16x16x32_bf16 v[72:75], v[152:155], v[180:183], v[72:75]\n\t"
        "ds_read_b128 v[176:179], %[ba] offset:18176\n\t"
        "v_mfma_f32_16x16x32_bf16 v[76:79], v[152:155], v[184:187], v[76:79]\n\t"
        "v_mfma_f32_16x16x32_bf16 v[76:79], v[132:135], v[188:191], v[76:79]\n\t"
        "ds_read_b128 v[152:155], v232 offset:34816\n\t"
        "v_mfma_f32_16x16x32_bf16 v[108:111], v[136:139], v[188:191], v[108:111]\n\t"
        "v_mfma_f32_16x16x32_bf16 v[108:111], v[156:159], v[184:187], v[108:111]\n\t"
        "ds_read_b128 v[132:135], v233 offset:34816\n\t"
        "v_mfma_f32_16x16x32_bf16 v[104:107], v[156:159], v[180:183], v[104:107]\n\t"
        "v_mfma_f32_16x16x32_bf16 v[104:107], v[136:139], v[140:143], v[104:107]\n\t"
        "s_waitcnt lgkmcnt(3)\n\t"
        "v_mfma_f32_16x16x32_bf16 v[0:3], v[196:199], v[192:195], v[0:3]\n\t"
        "v_mfma_f32_16x16x32_bf16 v[0:3], v[128:131], v[172:175], v[0:3]\n\t"
        "v_mfma_f32_16x16x32_bf16 v[4:7], v[128:131], v[144:147], v[4:7]\n\t"
        "v_mfma_f32_16x16x32_bf16 v[4:7], v[196:199], v[160:163], v[4:7]\n\t"
        "ds_read_b128 v[136:139], v232 offset:36864\n\t"
        "v_mfma_f32_16x16x32_bf16 v[8:11], v[196:199], v[164:167], v[8:11]\n\t"
        "v_mfma_f32_16x16x32_bf16 v[8:11], v[128:131], v[168:171], v[8:11]\n\t"
        "ds_read_b128 v[140:143], v233 offset:36864\n\t"
        "v_mfma_f32_16x16x32_bf16 v[12:15], v[128:131], v[148:151], v[12:15]\n\t"
        "s_waitcnt lgkmcnt(2)\n\t"
        "v_mfma_f32_16x16x32_bf16 v[12:15], v[196:199], v[176:179], v[12:15]\n\t"
        "v_mfma_f32_16x16x32_bf16 v[32:35], v[152:155], v[192:195], v[32:35]\n\t"
        "v_mfma_f32_16x16x32_bf16 v[32:35], v[132:135], v[172:175], v[32:35]\n\t"
        "ds_read_b128 v[128:131], v232 offset:38912\n\t"
        "v_mfma_f32_16x16x32_bf16 v[36:39], v[132:135], v[144:147], v[36:39]\n\t"
        "v_mfma_f32_16x16x32_bf16 v[36:39], v[152:155], v[160:163], v[36:39]\n\t"
        "ds_read_b128 v[156:159], v233 offset:38912\n\t"
        "v_mfma_f32_16x16x32_bf16 v[40:43], v[152:155], v[164:167], v[40:43]\n\t"
        "v_mfma_f32_16x16x32_bf16 v[40:43], v[132:135], v[168:171], v[40:43]\n\t"
        "v_mfma_f32_16x16x32_bf16 v[44:47], v[132:135], v[148:151], v[44:47]\n\t"
        "v_mfma_f32_16x16x32_bf16 v[44:47], v[152:155], v[176:179], v[44:47]\n\t"
        "s_waitcnt lgkmcnt(0)\n\t"
        "s_barrier\n\t"
        "v_mfma_f32_16x16x32_bf16 v[64:67], v[136:139], v[192:195], v[64:67]\n\t"
        ".Lwp15:\n\t"
        "s_cbranch_execz .Lwp16\n\t"
        "v_mfma_f32_16x16x32_bf16 v[64:67], v[140:143], v[172:175], v[64:67]\n\t"
        "ds_read_b128 v[132:135], %[ba] offset:32768\n\t"
        "ds_read_b128 v[152:155], %[xb0] offset:0\n\t"
        "v_mfma_f32_16x16x32_bf16 v[68:71], v[140:143], v[144:147], v[68:71]\n\t"
        "v_mfma_f32_16x16x32_bf16 v[68:71], v[136:139], v[160:163], v[68:71]\n\t"
        "ds_read_b128 v[180:183], %[ba] offset:40960\n\t"
        "ds_read_b128 v[184:187], %[xb1] offset:0\n\t"
        "v_mfma_f32_16x16x32_bf16 v[72:75], v[136:139], v[164:167], v[72:75]\n\t"
        "v_mfma_f32_16x16x32_bf16 v[72:75], v[140:143], v[168:171], v[72:75]\n\t"
        "ds_read_b128 v[188:191], %[ba] offset:41216\n\t"
        "ds_read_b128 v[196:199], %[ba] offset:33024\n\t"
        "v_mfma_f32_16x16x32_bf16 v[76:79], v[140:143], v[148:151], v[76:79]\n\t"
        "v_mfma_f32_16x16x32_bf16 v[76:79], v[136:139], v[176:179], v[76:79]\n\t"
        "ds_read_b128 v[140:143], %[ba] offset:33280\n\t"
        "s_waitcnt lgkmcnt(5)\n\t"
        "v_mfma_f32_16x16x32_bf16 v[0:3], v[152:155], v[132:135], v[0:3]\n\t"
        "ds_read_b128 v[136:139], %[ba] offset:41472\n\t"
        "s_waitcnt lgkmcnt(4)\n\t"
        "v_mfma_f32_16x16x32_bf16 v[0:3], v[184:187], v[180:183], v[0:3]\n\t"
        "ds_read_b128 v[200:203], %[ba] offset:41728\n\t"
        "s_waitcnt lgkmcnt(4)\n\t"
        "v_mfma_f32_16x16x32_bf16 v[4:7], v[184:187], v[188:191], v[4:7]\n\t"
        "ds_read_b128 v[204:207], %[ba] offset:33536\n\t"
        "s_waitcnt lgkmcnt(4)\n\t"
        "v_mfma_f32_16x16x32_bf16 v[4:7], v[152:155], v[196:199], v[4:7]\n\t"
        "ds_read_b128 v[208:211], %[xb0] offset:2048\n\t"
        "s_waitcnt lgkmcnt(4)\n\t"
        "v_mfma_f32_16x16x32_bf16 v[8:11], v[152:155], v[140:143], v[8:11]\n\t"
        "s_waitcnt lgkmcnt(3)\n\t"
        "v_mfma_f32_16x16x32_bf16 v[8:11], v[184:187], v[136:139], v[8:11]\n\t"
        "ds_read_b128 v[212:215], %[xb1] offset:2048\n\t"
        "s_waitcnt lgkmcnt(3)\n\t"
        "v_mfma_f32_16x16x32_bf16 v[12:15], v[184:187], v[200:203], v[12:15]\n\t"
        "s_waitcnt lgkmcnt(2)\n\t"
        "v_mfma_f32_16x16x32_bf16 v[12:15], v[152:155], v[204:207], v[12:15]\n\t"
        "v_mfma_f32_16x16x32_bf16 v[96:99], v[128:131], v[192:195], v[96:99]\n\t"
        "v_mfma_f32_16x16x32_bf16 v[96:99], v[156:159], v[172:175], v[96:99]\n\t"
        "ds_read_b128 v[152:155], %[xb0] offset:4096\n\t"
        "v_cvt_pk_bf16_f32 v224, v0, v4\n\t"
        "v_mfma_f32_16x16x32_bf16 v[100:103], v[156:159], v[144:147], v[100:103]\n\t"
        "v_mfma_f32_16x16x32_bf16 v[100:103], v[128:131], v[160:163], v[100:103]\n\t"
        "ds_read_b128 v[144:147], %[xb1] offset:4096\n\t"
        "v_cvt_pk_bf16_f32 v225, v8, v12\n\t"
        "v_mfma_f32_16x16x32_bf16 v[104:107], v[128:131], v[164:167], v[104:107]\n\t"
        "global_store_dwordx2 %[oo0], v[224:225], %[ob0] offset:0 nt\n\t"
        "v_mfma_f32_16x16x32_bf16 v[104:107], v[156:159], v[168:171], v[104:107]\n\t"
        "ds_read_b128 v[160:163], %[xb0] offset:6144\n\t"
        "v_cvt_pk_bf16_f32 v227, v9, v13\n\t"
        "v_mfma_f32_16x16x32_bf16 v[108:111], v[156:159], v[148:151], v[108:111]\n\t"
        "v_cvt_pk_bf16_f32 v226, v1, v5\n\t"
        "v_mfma_f32_16x16x32_bf16 v[108:111], v[128:131], v[176:179], v[108:111]\n\t"
        "ds_read_b128 v[148:151], %[xb1] offset:6144\n\t"
        "s_waitcnt lgkmcnt(4)\n\t"
        "v_mfma_f32_16x16x32_bf16 v[32:35], v[208:211], v[132:135], v[32:35]\n\t"
        "global_store_dwordx2 %[oo1], v[226:227], %[ob0] offset:0 nt\n\t"
        "v_mfma_f32_16x16x32_bf16 v[32:35], v[212:215], v[180:183], v[32:35]\n\t"
        "ds_read_b128 v[128:131], v232 offset:0\n\t"
        "v_cvt_pk_bf16_f32 v229, v10, v14\n\t"
        "v_mfma_f32_16x16x32_bf16 v[36:39], v[212:215], v[188:191], v[36:39]\n\t"
        "v_cvt_pk_bf16_f32 v228, v2, v6\n\t"
        "v_mfma_f32_16x16x32_bf16 v[36:39], v[208:211], v[196:199], v[36:39]\n\t"
        "ds_read_b128 v[156:159], %[ba] offset:33792\n\t"
        "global_store_dwordx2 %[oo2], v[228:229], %[ob0] offset:0 nt\n\t"
        "v_mfma_f32_16x16x32_bf16 v[40:43], v[208:211], v[140:143], v[40:43]\n\t"
        "v_mfma_f32_16x16x32_bf16 v[40:43], v[212:215], v[136:139], v[40:43]\n\t"
        "ds_read_b128 v[164:167], v233 offset:0\n\t"
        "v_cvt_pk_bf16_f32 v231, v11, v15\n\t"
        "v_mfma_f32_16x16x32_bf16 v[44:47], v[212:215], v[200:203], v[44:47]\n\t"
        "v_cvt_pk_bf16_f32 v230, v3, v7\n\t"
        "v_mfma_f32_16x16x32_bf16 v[44:47], v[208:211], v[204:207], v[44:47]\n\t"
        "ds_read_b128 v[168:171], %[ba] offset:41984\n\t"
        "s_waitcnt lgkmcnt(4)\n\t"
        "v_mfma_f32_16x16x32_bf16 v[64:67], v[152:155], v[132:135], v[64:67]\n\t"
        "global_store_dwordx2 %[oo3], v[230:231], %[ob0] offset:0 nt\n\t"
        "v_mfma_f32_16x16x32_bf16 v[64:67], v[144:147], v[180:183], v[64:67]\n\t"
        "ds_read_b128 v[172:175], %[ba] offset:42240\n\t"
        "v_cvt_pk_bf16_f32 v224, v32, v36\n\t"
        "v_mfma_f32_16x16x32_bf16 v[68:71], v[144:147], v[188:191], v[68:71]\n\t"
        "v_mfma_f32_16x16x32_bf16 v[68:71], v[152:155], v[196:199], v[68:71]\n\t"
        "ds_read_b128 v[176:179], %[ba] offset:34048\n\t"
        "v_cvt_pk_bf16_f32 v225, v40, v44\n\t"
        "v_mfma_f32_16x16x32_bf16 v[72:75], v[152:155], v[140:143], v[72:75]\n\t"
        "global_store_dwordx2 %[oo0], v[224:225], %[ob1] offset:0 nt\n\t"
        "v_mfma_f32_16x16x32_bf16 v[72:75], v[144:147], v[136:139], v[72:75]\n\t"
        "ds_read_b128 v[184:187], %[ba] offset:34304\n\t"
        "ds_read_b128 v[192:195], %[ba] offset:42496\n\t"
        "v_mfma_f32_16x16x32_bf16 v[76:79], v[144:147], v[200:203], v[76:79]\n\t"
        "v_cvt_pk_bf16_f32 v227, v41, v45\n\t"
        "v_cvt_pk_bf16_f32 v226, v33, v37\n\t"
        "v_mfma_f32_16x16x32_bf16 v[76:79], v[152:155], v[204:207], v[76:79]\n\t"
        "ds_read_b128 v[144:147], %[ba] offset:42752\n\t"
        "global_store_dwordx2 %[oo1], v[226:227], %[ob1] offset:0 nt\n\t"
        "v_mfma_f32_16x16x32_bf16 v[96:99], v[160:163], v[132:135], v[96:99]\n\t"
        ".Lwp16:\n\t"
        "s_cbranch_execz .Lwp97\n\t"
        "v_mfma_f32_16x16x32_bf16 v[96:99], v[148:151], v[180:183], v[96:99]\n\t"
        "ds_read_b128 v[132:135], %[ba] offset:34560\n\t"
        "v_cvt_pk_bf16_f32 v229, v42, v46\n\t"
        "v_mfma_f32_16x16x32_bf16 v[100:103], v[148:151], v[188:191], v[100:103]\n\t"
        "v_cvt_pk_bf16_f32 v228, v34, v38\n\t"
        "v_mfma_f32_16x16x32_bf16 v[100:103], v[160:163], v[196:199], v[100:103]\n\t"
        "ds_read_b128 v[152:155], v232 offset:2048\n\t"
        "global_store_dwordx2 %[oo2], v[228:229], %[ob1] offset:0 nt\n\t"
        "v_mfma_f32_16x16x32_bf16 v[104:107], v[160:163], v[140:143], v[104:107]\n\t"
        "v_mfma_f32_16x16x32_bf16 v[104:107], v[148:151], v[136:139], v[104:107]\n\t"
        "ds_read_b128 v[140:143], v233 offset:2048\n\t"
        "v_cvt_pk_bf16_f32 v231, v43, v47\n\t"
        "v_mfma_f32_16x16x32_bf16 v[108:111], v[148:151], v[200:203], v[108:111]\n\t"
        "v_cvt_pk_bf16_f32 v230, v35, v39\n\t"
        "v_mfma_f32_16x16x32_bf16 v[108:111], v[160:163], v[204:207], v[108:111]\n\t"
        "global_store_dwordx2 %[oo3], v[230:231], %[ob1] offset:0 nt\n\t"
        "s_waitcnt lgkmcnt(3)\n\t"
        "v_mfma_f32_16x16x32_bf16 v[16:19], v[128:131], v[156:159], v[16:19]\n\t"
        "v_mfma_f32_16x16x32_bf16 v[16:19], v[164:167], v[168:171], v[16:19]\n\t"
        "ds_read_b128 v[136:139], v232 offset:4096\n\t"
        "v_cvt_pk_bf16_f32 v225, v72, v76\n\t"
        "v_mfma_f32_16x16x32_bf16 v[20:23], v[164:167], v[172:175], v[20:23]\n\t"
        "v_cvt_pk_bf16_f32 v224, v64, v68\n\t"
        "v_mfma_f32_16x16x32_bf16 v[20:23], v[128:131], v[176:179], v[20:23]\n\t"
        "ds_read_b128 v[148:151], v233 offset:4096\n\t"
        "global_store_dwordx2 %[oo0], v[224:225], %[ob2] offset:0 nt\n\t"
        "v_mfma_f32_16x16x32_bf16 v[24:27], v[128:131], v[184:187], v[24:27]\n\t"
        "v_mfma_f32_16x16x32_bf16 v[24:27], v[164:167], v[192:195], v[24:27]\n\t"
        "ds_read_b128 v[160:163], v232 offset:6144\n\t"
        "v_cvt_pk_bf16_f32 v227, v73, v77\n\t"
        "v_mfma_f32_16x16x32_bf16 v[28:31], v[164:167], v[144:147], v[28:31]\n\t"
        "v_cvt_pk_bf16_f32 v226, v65, v69\n\t"
        "s_waitcnt lgkmcnt(3)\n\t"
        "v_mfma_f32_16x16x32_bf16 v[28:31], v[128:131], v[132:135], v[28:31]\n\t"
        "ds_read_b128 v[164:167], v233 offset:6144\n\t"
        "global_store_dwordx2 %[oo1], v[226:227], %[ob2] offset:0 nt\n\t"
        "v_mfma_f32_16x16x32_bf16 v[48:51], v[152:155], v[156:159], v[48:51]\n\t"
        "v_mfma_f32_16x16x32_bf16 v[48:51], v[140:143], v[168:171], v[48:51]\n\t"
        "ds_read_b128 v[128:131], v232 offset:32768\n\t"
        "v_cvt_pk_bf16_f32 v229, v74, v78\n\t"
        "v_mfma_f32_16x16x32_bf16 v[52:55], v[140:143], v[172:175], v[52:55]\n\t"
        "v_cvt_pk_bf16_f32 v228, v66, v70\n\t"
        "v_mfma_f32_16x16x32_bf16 v[52:55], v[152:155], v[176:179], v[52:55]\n\t"
        "ds_read_b128 v[180:183], v233 offset:32768\n\t"
        "ds_read_b128 v[188:191], %[xb0] offset:0\n\t"
        "v_mfma_f32_16x16x32_bf16 v[56:59], v[152:155], v[184:187], v[56:59]\n\t"
        "global_store_dwordx2 %[oo2], v[228:229], %[ob2] offset:0 nt\n\t"
        "v_mfma_f32_16x16x32_bf16 v[56:59], v[140:143], v[192:195], v[56:59]\n\t"
        "v_cvt_pk_bf16_f32 v231, v75, v79\n\t"
        "v_cvt_pk_bf16_f32 v230, v67, v71\n\t"
        "v_mfma_f32_16x16x32_bf16 v[60:63], v[140:143], v[144:147], v[60:63]\n\t"
        "v_mfma_f32_16x16x32_bf16 v[60:63], v[152:155], v[132:135], v[60:63]\n\t"
        "global_store_dwordx2 %[oo3], v[230:231], %[ob2] offset:0 nt\n\t"
        "s_barrier\n\t"
        "s_waitcnt lgkmcnt(3)\n\t"
        "v_mfma_f32_16x16x32_bf16 v[80:83], v[136:139], v[156:159], v[80:83]\n\t"
        "v_mfma_f32_16x16x32_bf16 v[80:83], v[148:151], v[168:171], v[80:83]\n\t"
        "ds_read_b128 v[140:143], %[ba] offset:0\n\t"
        "ds_read_b128 v[152:155], %[xb1] offset:0\n\t"
        "v_mfma_f32_16x16x32_bf16 v[84:87], v[148:151], v[172:175], v[84:87]\n\t"
        "v_cvt_pk_bf16_f32 v225, v104, v108\n\t"
        "v_cvt_pk_bf16_f32 v224, v96, v100\n\t"
        "v_mfma_f32_16x16x32_bf16 v[84:87], v[136:139], v[176:179], v[84:87]\n\t"
        ".Lwp17:\n\t"
        "s_cbranch_execz .Lwp18\n\t"
        "ds_read_b128 v[196:199], %[ba] offset:8192\n\t"
        "v_mfma_f32_16x16x32_bf16 v[88:91], v[136:139], v[184:187], v[88:91]\n\t"
        "ds_read_b128 v[200:203], %[ba] offset:1024\n\t"
        "ds_read_b128 v[204:207], %[ba] offset:9216\n\t"
        "v_mfma_f32_16x16x32_bf16 v[88:91], v[148:151], v[192:195], v[88:91]\n\t"
        "global_store_dwordx2 %[oo0], v[224:225], %[ob3] offset:0 nt\n\t"
        "ds_read_b128 v[208:211], %[ba] offset:9472\n\t"
        "v_mfma_f32_16x16x32_bf16 v[92:95], v[148:151], v[144:147], v[92:95]\n\t"
        "ds_read_b128 v[212:215], %[ba] offset:1280\n\t"
        "ds_read_b128 v[216:219], %[ba] offset:8448\n\t"
        "v_mfma_f32_16x16x32_bf16 v[92:95], v[136:139], v[132:135], v[92:95]\n\t"
        "v_cvt_pk_bf16_f32 v227, v105, v109\n\t"
        "v_cvt_pk_bf16_f32 v226, v97, v101\n\t"
        "v_mfma_f32_16x16x32_bf16 v[112:115], v[160:163], v[156:159], v[112:115]\n\t"
        "ds_read_b128 v[136:139], %[ba] offset:256\n\t"
        "ds_read_b128 v[148:151], %[ba] offset:512\n\t"
        "v_mfma_f32_16x16x32_bf16 v[112:115], v[164:167], v[168:171], v[112:115]\n\t"
        "ds_read_b128 v[156:159], %[ba] offset:8704\n\t"
        "global_store_dwordx2 %[oo1], v[226:227], %[ob3] offset:0 nt\n\t"
        "v_mfma_f32_16x16x32_bf16 v[116:119], v[164:167], v[172:175], v[116:119]\n\t"
        "ds_read_b128 v[168:171], %[ba] offset:1536\n\t"
        "ds_read_b128 v[220:223], %[ba] offset:9728\n\t"
        "v_mfma_f32_16x16x32_bf16 v[116:119], v[160:163], v[176:179], v[116:119]\n\t"
        "v_cvt_pk_bf16_f32 v229, v106, v110\n\t"
        "v_cvt_pk_bf16_f32 v228, v98, v102\n\t"
        "v_mfma_f32_16x16x32_bf16 v[120:123], v[160:163], v[184:187], v[120:123]\n\t"
        "ds_read_b128 v[172:175], %[ba] offset:9984\n\t"
        "ds_read_b128 v[176:179], %[ba] offset:1792\n\t"
        "v_mfma_f32_16x16x32_bf16 v[120:123], v[164:167], v[192:195], v[120:123]\n\t"
        "global_store_dwordx2 %[oo2], v[228:229], %[ob3] offset:0 nt\n\t"
        "ds_read_b128 v[184:187], %[ba] offset:8960\n\t"
        "v_mfma_f32_16x16x32_bf16 v[124:127], v[164:167], v[144:147], v[124:127]\n\t"
        "v_cvt_pk_bf16_f32 v231, v107, v111\n\t"
        "v_cvt_pk_bf16_f32 v230, v99, v103\n\t"
        "v_mfma_f32_16x16x32_bf16 v[124:127], v[160:163], v[132:135], v[124:127]\n\t"
        "ds_read_b128 v[144:147], %[ba] offset:768\n\t"
        "s_waitcnt lgkmcnt(9)\n\t"
        "v_mfma_f32_16x16x32_bf16 v[16:19], v[128:131], v[140:143], v[16:19]\n\t"
        "global_store_dwordx2 %[oo3], v[230:231], %[ob3] offset:0 nt\n\t"
        "v_mfma_f32_16x16x32_bf16 v[16:19], v[180:183], v[196:199], v[16:19]\n\t"
        "ds_read_b128 v[132:135], v232 offset:34816\n\t"
        "v_mfma_f32_16x16x32_bf16 v[16:19], v[188:191], v[200:203], v[16:19]\n\t"
        "v_mfma_f32_16x16x32_bf16 v[16:19], v[152:155], v[204:207], v[16:19]\n\t"
        "ds_read_b128 v[160:163], v233 offset:34816\n\t"
        "v_mfma_f32_16x16x32_bf16 v[20:23], v[152:155], v[208:211], v[20:23]\n\t"
        "v_mfma_f32_16x16x32_bf16 v[20:23], v[188:191], v[212:215], v[20:23]\n\t"
        "ds_read_b128 v[164:167], %[xb0] offset:2048\n\t"
        "v_mfma_f32_16x16x32_bf16 v[20:23], v[180:183], v[216:219], v[20:23]\n\t"
        "s_waitcnt lgkmcnt(4)\n\t"
        "v_mfma_f32_16x16x32_bf16 v[20:23], v[128:131], v[136:139], v[20:23]\n\t"
        "ds_read_b128 v[192:195], %[xb1] offset:2048\n\t"
        "v_mfma_f32_16x16x32_bf16 v[24:27], v[128:131], v[148:151], v[24:27]\n\t"
        "v_mfma_f32_16x16x32_bf16 v[24:27], v[180:183], v[156:159], v[24:27]\n\t"
        "v_mfma_f32_16x16x32_bf16 v[24:27], v[188:191], v[168:171], v[24:27]\n\t"
        "v_mfma_f32_16x16x32_bf16 v[24:27], v[152:155], v[220:223], v[24:27]\n\t"
        "v_mfma_f32_16x16x32_bf16 v[28:31], v[152:155], v[172:175], v[28:31]\n\t"
        "v_mfma_f32_16x16x32_bf16 v[28:31], v[188:191], v[176:179], v[28:31]\n\t"
        "v_mfma_f32_16x16x32_bf16 v[28:31], v[180:183], v[184:187], v[28:31]\n\t"
        "s_waitcnt lgkmcnt(1)\n\t"
        "v_mfma_f32_16x16x32_bf16 v[28:31], v[128:131], v[144:147], v[28:31]\n\t"
        "v_mfma_f32_16x16x32_bf16 v[48:51], v[132:135], v[140:143], v[48:51]\n\t"
        "v_mfma_f32_16x16x32_bf16 v[48:51], v[160:163], v[196:199], v[48:51]\n\t"
        "ds_read_b128 v[128:131], v232 offset:36864\n\t"
        "v_cvt_pk_bf16_f32 v224, v16, v20\n\t"
        "v_mfma_f32_16x16x32_bf16 v[48:51], v[164:167], v[200:203], v[48:51]\n\t"
        "s_waitcnt lgkmcnt(1)\n\t"
        "v_mfma_f32_16x16x32_bf16 v[48:51], v[192:195], v[204:207], v[48:51]\n\t"
        "ds_read_b128 v[152:155], v233 offset:36864\n\t"
        "v_cvt_pk_bf16_f32 v225, v24, v28\n\t"
        "v_mfma_f32_16x16x32_bf16 v[52:55], v[192:195], v[208:211], v[52:55]\n\t"
        "global_store_dwordx2 %[oo0], v[224:225], %[ob0] offset:128 nt\n\t"
        "v_mfma_f32_16x16x32_bf16 v[52:55], v[164:167], v[212:215], v[52:55]\n\t"
        "ds_read_b128 v[180:183], %[xb0] offset:4096\n\t"
        "v_cvt_pk_bf16_f32 v227, v25, v29\n\t"
        "v_mfma_f32_16x16x32_bf16 v[52:55], v[160:163], v[216:219], v[52:55]\n\t"
        "v_cvt_pk_bf16_f32 v226, v17, v21\n\t"
        "v_mfma_f32_16x16x32_bf16 v[52:55], v[132:135], v[136:139], v[52:55]\n\t"
        "ds_read_b128 v[188:191], %[xb1] offset:4096\n\t"
        "global_store_dwordx2 %[oo1], v[226:227], %[ob0] offset:128 nt\n\t"
        "v_mfma_f32_16x16x32_bf16 v[56:59], v[132:135], v[148:151], v[56:59]\n\t"
        "v_mfma_f32_16x16x32_bf16 v[56:59], v[160:163], v[156:159], v[56:59]\n\t"
        "v_cvt_pk_bf16_f32 v229, v26, v30\n\t"
        "v_cvt_pk_bf16_f32 v228, v18, v22\n\t"
        "v_mfma_f32_16x16x32_bf16 v[56:59], v[164:167], v[168:171], v[56:59]\n\t"
        "v_mfma_f32_16x16x32_bf16 v[56:59], v[192:195], v[220:223], v[56:59]\n\t"
        "global_store_dwordx2 %[oo2], v[228:229], %[ob0] offset:128 nt\n\t"
        "v_mfma_f32_16x16x32_bf16 v[60:63], v[192:195], v[172:175], v[60:63]\n\t"
        "v_mfma_f32_16x16x32_bf16 v[60:63], v[164:167], v[176:179], v[60:63]\n\t"
        "v_cvt_pk_bf16_f32 v231, v27, v31\n\t"
        "v_cvt_pk_bf16_f32 v230, v19, v23\n\t"
        "v_mfma_f32_16x16x32_bf16 v[60:63], v[160:163], v[184:187], v[60:63]\n\t"
        ".Lwp18:\n\t"
        "s_cbranch_execz .Lwp97\n\t"
        "v_mfma_f32_16x16x32_bf16 v[60:63], v[132:135], v[144:147], v[60:63]\n\t"
        "global_store_dwordx2 %[oo3], v[230:231], %[ob0] offset:128 nt\n\t"
        "s_waitcnt lgkmcnt(0)\n\t"
        "v_mfma_f32_16x16x32_bf16 v[80:83], v[128:131], v[140:143], v[80:83]\n\t"
        "v_mfma_f32_16x16x32_bf16 v[80:83], v[152:155], v[196:199], v[80:83]\n\t"
        "ds_read_b128 v[132:135], v232 offset:38912\n\t"
        "v_cvt_pk_bf16_f32 v224, v48, v52\n\t"
        "v_mfma_f32_16x16x32_bf16 v[80:83], v[180:183], v[200:203], v[80:83]\n\t"
        "v_mfma_f32_16x16x32_bf16 v[80:83], v[188:191], v[204:207], v[80:83]\n\t"
        "ds_read_b128 v[160:163], v233 offset:38912\n\t"
        "v_cvt_pk_bf16_f32 v225, v56, v60\n\t"
        "v_mfma_f32_16x16x32_bf16 v[84:87], v[188:191], v[208:211], v[84:87]\n\t"
        "global_store_dwordx2 %[oo0], v[224:225], %[ob1] offset:128 nt\n\t"
        "v_mfma_f32_16x16x32_bf16 v[84:87], v[180:183], v[212:215], v[84:87]\n\t"
        "ds_read_b128 v[164:167], %[xb0] offset:6144\n\t"
        "v_cvt_pk_bf16_f32 v227, v57, v61\n\t"
        "v_mfma_f32_16x16x32_bf16 v[84:87], v[152:155], v[216:219], v[84:87]\n\t"
        "v_cvt_pk_bf16_f32 v226, v49, v53\n\t"
        "v_mfma_f32_16x16x32_bf16 v[84:87], v[128:131], v[136:139], v[84:87]\n\t"
        "ds_read_b128 v[192:195], %[xb1] offset:6144\n\t"
        "global_store_dwordx2 %[oo1], v[226:227], %[ob1] offset:128 nt\n\t"
        "v_mfma_f32_16x16x32_bf16 v[88:91], v[128:131], v[148:151], v[88:91]\n\t"
        "v_mfma_f32_16x16x32_bf16 v[88:91], v[152:155], v[156:159], v[88:91]\n\t"
        "v_cvt_pk_bf16_f32 v229, v58, v62\n\t"
        "v_cvt_pk_bf16_f32 v228, v50, v54\n\t"
        "v_mfma_f32_16x16x32_bf16 v[88:91], v[180:183], v[168:171], v[88:91]\n\t"
        "v_mfma_f32_16x16x32_bf16 v[88:91], v[188:191], v[220:223], v[88:91]\n\t"
        "global_store_dwordx2 %[oo2], v[228:229], %[ob1] offset:128 nt\n\t"
        "v_mfma_f32_16x16x32_bf16 v[92:95], v[188:191], v[172:175], v[92:95]\n\t"
        "v_mfma_f32_16x16x32_bf16 v[92:95], v[180:183], v[176:179], v[92:95]\n\t"
        "v_cvt_pk_bf16_f32 v231, v59, v63\n\t"
        "v_cvt_pk_bf16_f32 v230, v51, v55\n\t"
        "v_mfma_f32_16x16x32_bf16 v[92:95], v[152:155], v[184:187], v[92:95]\n\t"
        "v_mfma_f32_16x16x32_bf16 v[92:95], v[128:131], v[144:147], v[92:95]\n\t"
        "global_store_dwordx2 %[oo3], v[230:231], %[ob1] offset:128 nt\n\t"
        "s_waitcnt lgkmcnt(0)\n\t"
        "v_mfma_f32_16x16x32_bf16 v[112:115], v[132:135], v[140:143], v[112:115]\n\t"
        "v_mfma_f32_16x16x32_bf16 v[112:115], v[160:163], v[196:199], v[112:115]\n\t"
        "v_cvt_pk_bf16_f32 v224, v80, v84\n\t"
        "v_mfma_f32_16x16x32_bf16 v[112:115], v[164:167], v[200:203], v[112:115]\n\t"
        "v_mfma_f32_16x16x32_bf16 v[112:115], v[192:195], v[204:207], v[112:115]\n\t"
        "v_cvt_pk_bf16_f32 v225, v88, v92\n\t"
        "global_store_dwordx2 %[oo0], v[224:225], %[ob2] offset:128 nt\n\t"
        "v_mfma_f32_16x16x32_bf16 v[116:119], v[192:195], v[208:211], v[116:119]\n\t"
        "v_mfma_f32_16x16x32_bf16 v[116:119], v[164:167], v[212:215], v[116:119]\n\t"
        "v_cvt_pk_bf16_f32 v227, v89, v93\n\t"
        "v_cvt_pk_bf16_f32 v226, v81, v85\n\t"
        "v_mfma_f32_16x16x32_bf16 v[116:119], v[160:163], v[216:219], v[116:119]\n\t"
        "v_mfma_f32_16x16x32_bf16 v[116:119], v[132:135], v[136:139], v[116:119]\n\t"
        "global_store_dwordx2 %[oo1], v[226:227], %[ob2] offset:128 nt\n\t"
        "v_mfma_f32_16x16x32_bf16 v[120:123], v[132:135], v[148:151], v[120:123]\n\t"
        "v_mfma_f32_16x16x32_bf16 v[120:123], v[160:163], v[156:159], v[120:123]\n\t"
        "v_cvt_pk_bf16_f32 v229, v90, v94\n\t"
        "v_cvt_pk_bf16_f32 v228, v82, v86\n\t"
        "v_mfma_f32_16x16x32_bf16 v[120:123], v[164:167], v[168:171], v[120:123]\n\t"
        "v_mfma_f32_16x16x32_bf16 v[120:123], v[192:195], v[220:223], v[120:123]\n\t"
        "global_store_dwordx2 %[oo2], v[228:229], %[ob2] offset:128 nt\n\t"
        "v_mfma_f32_16x16x32_bf16 v[124:127], v[192:195], v[172:175], v[124:127]\n\t"
        "v_mfma_f32_16x16x32_bf16 v[124:127], v[164:167], v[176:179], v[124:127]\n\t"
        "v_cvt_pk_bf16_f32 v230, v113, v117\n\t"
        "v_cvt_pk_bf16_f32 v224, v112, v116\n\t"
        "v_mfma_f32_16x16x32_bf16 v[124:127], v[160:163], v[184:187], v[124:127]\n\t"
        "v_cvt_pk_bf16_f32 v227, v91, v95\n\t"
        "v_cvt_pk_bf16_f32 v226, v83, v87\n\t"
        "v_mfma_f32_16x16x32_bf16 v[124:127], v[132:135], v[144:147], v[124:127]\n\t"
        "v_cvt_pk_bf16_f32 v228, v115, v119\n\t"
        "v_cvt_pk_bf16_f32 v128, v114, v118\n\t"
        "global_store_dwordx2 %[oo3], v[226:227], %[ob2] offset:128 nt\n\t"
        "s_nop 7\n\t"
        "v_cvt_pk_bf16_f32 v225, v120, v124\n\t"
        "global_store_dwordx2 %[oo0], v[224:225], %[ob3] offset:128 nt\n\t"
        "v_cvt_pk_bf16_f32 v231, v121, v125\n\t"
        "global_store_dwordx2 %[oo1], v[230:231], %[ob3] offset:128 nt\n\t"
        "v_cvt_pk_bf16_f32 v129, v122, v126\n\t"
        "global_store_dwordx2 %[oo2], v[128:129], %[ob3] offset:128 nt\n\t"
        "v_cvt_pk_bf16_f32 v229, v123, v127\n\t"
        "global_store_dwordx2 %[oo3], v[228:229], %[ob3] offset:128 nt\n\t"
        :
        : [xb0] "v"(xb0), [xb1] "v"(xb1), [ba] "v"(ba), [oo0] "v"(oo[0]), [oo1] "v"(oo[1]), [oo2] "v"(oo[2]), [oo3] "v"(oo[3]),
          [ob0] "s"(ob[0]), [ob1] "s"(ob[1]), [ob2] "s"(ob[2]), [ob3] "s"(ob[3]), [w] "s"(w)
        : "scc", "memory", "s60", "s61", "s62", "s63", "s64", "s65", "s66", "s67", "v0", "v1", "v2", "v3", "v4", "v5", "v6", "v7", "v8", "v9", "v10", "v11", "v12", "v13", "v14", "v15", "v16", "v17", "v18", "v19", "v20", "v21", "v22", "v23", "v24", "v25", "v26", "v27", "v28", "v29", "v30", "v31", "v32", "v33", "v34", "v35", "v36", "v37", "v38", "v39", "v40", "v41", "v42", "v43", "v44", "v45", "v46", "v47", "v48", "v49", "v50", "v51", "v52", "v53", "v54", "v55", "v56", "v57", "v58", "v59", "v60", "v61", "v62", "v63", "v64", "v65", "v66", "v67", "v68", "v69", "v70", "v71", "v72", "v73", "v74", "v75", "v76", "v77", "v78", "v79", "v80", "v81", "v82", "v83", "v84", "v85", "v86", "v87", "v88", "v89", "v90", "v91", "v92", "v93", "v94", "v95", "v96", "v97", "v98", "v99", "v100", "v101", "v102", "v103", "v104", "v105", "v106", "v107", "v108", "v109", "v110", "v111", "v112", "v113", "v114", "v115", "v116", "v117", "v118", "v119", "v120", "v121", "v122", "v123", "v124", "v125", "v126", "v127", "v128", "v129", "v130", "v131", "v132", "v133", "v134", "v135", "v136", "v137", "v138", "v139", "v140", "v141", "v142", "v143", "v144", "v145", "v146", "v147", "v148", "v149", "v150", "v151", "v152", "v153", "v154", "v155", "v156", "v157", "v158", "v159", "v160", "v161", "v162", "v163", "v164", "v165", "v166", "v167", "v168", "v169", "v170", "v171", "v172", "v173", "v174", "v175", "v176", "v177", "v178", "v179", "v180", "v181", "v182", "v183", "v184", "v185", "v186", "v187", "v188", "v189", "v190", "v191", "v192", "v193", "v194", "v195", "v196", "v197", "v198", "v199", "v200", "v201", "v202", "v203", "v204", "v205", "v206", "v207", "v208", "v209", "v210", "v211", "v212", "v213", "v214", "v215", "v216", "v217", "v218", "v219", "v220", "v221", "v222", "v223", "v224", "v225", "v226", "v227", "v228", "v229", "v230", "v231", "v232", "v233", "v234", "v235");
}

extern "C" void kernel_launch(void* const* d_in, const int* in_sizes, int n_in,
                              void* d_out, int out_size, void* d_ws, size_t ws_size,
                              hipStream_t stream) {
    const u16* x = (const u16*)d_in[0];
    const int* qw = (const int*)d_in[1];
    const u16* lut = (const u16*)d_in[2];
    u16* out = (u16*)d_out;
    (void)hipFuncSetAttribute((const void*)fused_kernel, hipFuncAttributeMaxDynamicSharedMemorySize, LDS_BYTES);
    fused_kernel<<<(M_ / BM) * (N_ / BN) + NPF, 512, LDS_BYTES, stream>>>(x, qw, lut, out, (char*)d_ws);
}
```

```cpp
#include <hip/hip_runtime.h>
#include <hip/hip_bf16.h>

typedef unsigned short u16;
typedef unsigned int u32;
using bf16x8 = __attribute__((ext_vector_type(8))) short;
using f32x4  = __attribute__((ext_vector_type(4))) float;
using i32x4  = __attribute__((ext_vector_type(4))) int;
using u32x2  = __attribute__((ext_vector_type(2))) unsigned int;
using u32x4  = __attribute__((ext_vector_type(4))) unsigned int;

constexpr int M_ = 2048, K_ = 1024, N_ = 3584;
constexpr int BM = 256, BN = 128;
constexpr int NPF = 32;
#define XSLEEP 4
#define SP0 3
#define NQ1 4
#define CPF 116
#define CPFB 32
#define PACE 0
#define VORD 1
constexpr int XS = BM * 64 * 2;
constexpr int BS = BN * 32 * 2;
constexpr int OFF_B = 3 * XS;
constexpr int WSL = 2 * BS;
constexpr int OFF_L = OFF_B + 3 * WSL;
constexpr int OFF_D = OFF_L + 16 * BN * 2;
constexpr int WS_F = 28 * 256, WS_W = 65536;
constexpr unsigned TW = 28 * 16384;
constexpr int SPIN_MAX = 1 << 16;
constexpr int LDS_BYTES = OFF_D + 1024;

typedef __bf16 bf16x2_t __attribute__((ext_vector_type(2)));
typedef float f32x2_t __attribute__((ext_vector_type(2)));
__device__ __forceinline__ u32 pack_bf16(float lo, float hi) {
    f32x2_t f = {lo, hi};
    bf16x2_t b = __builtin_convertvector(f, bf16x2_t);
    return __builtin_bit_cast(u32, b);
}

#define ROT0 0
#define OWN(mt, nt) (((mt) >> 1) ^ (ROT0 ? (nt) & 3 : 0))
#define TLO(mt, nt) ((0 && ((mt) & 1) ? 0x75631240u : 0x76532140u) ^ ((u32)OWN(mt, nt) * 0x11111111u))
#define THI(mt, nt) (0xFEDCBA98u ^ ((u32)(0) * 0x11111111u))
#define BAR() __builtin_amdgcn_s_barrier()
#define SB() __builtin_amdgcn_sched_barrier(0)
#define WAITQ(n, Qa) asm volatile("s_waitcnt vmcnt(" #n ") lgkmcnt(0)" : "+v"(Qa[0]), "+v"(Qa[1]), "+v"(Qa[2]), "+v"(Qa[3]), \
                                  "+v"(Qa[4]), "+v"(Qa[5]), "+v"(Qa[6]), "+v"(Qa[7]) :: "memory")
#define PBAR() BAR()
#define PAD(id, nx) asm volatile(".Lwp%c0:\n\ts_cbranch_execz .Lwp%c1" :: "i"(id), "i"(nx))

__global__ __launch_bounds__(512, 2) void fused_kernel(const u16* __restrict__ x_, const int* __restrict__ qw_,
                                                       const u16* __restrict__ lut_, u16* __restrict__ out_, char* ws_) {
    extern __shared__ __attribute__((aligned(16))) char smem[];
    typedef unsigned long u64x4 __attribute__((ext_vector_type(4)));
    u64x4 ka;
    unsigned long kp[5], kw;
    asm volatile("s_load_dwordx8 %0, %2, 0x0\n\ts_load_dwordx2 %1, %2, 0x20" : "=&s"(ka), "=&s"(kw) : "s"(__builtin_amdgcn_kernarg_segment_ptr()));
    {
        unsigned long pc0, ex;
        u32 co;
        asm volatile("s_getpc_b64 %0\n\t"
                     "s_cmp_lt_u32 %4, %5\n\t"
                     "s_cselect_b32 m0, %6, 0\n\t"
                     "v_cmp_gt_u32 vcc, m0, %3\n\t"
                     "s_and_b32 m0, %4, 24\n\t"
                     "s_lshl_b32 m0, m0, 4\n\t"
                     "v_lshl_add_u32 %2, %3, 9, m0\n\t"
                     "s_mov_b32 m0, %7\n\t"
                     "s_and_saveexec_b64 %1, vcc\n\t"
                     "global_load_lds_dword %2, %0\n\t"
                     "s_mov_b64 exec, %1"
                     : "=&s"(pc0), "=&s"(ex), "=&v"(co) : "v"(threadIdx.x), "s"(blockIdx.x), "i"(CPFB), "i"(CPF / 4), "i"(OFF_D) : "vcc", "scc", "memory");
    }
#define KWAIT() do { SB(); asm volatile("s_waitcnt lgkmcnt(0)\n\ts_mov_b64 %0, %5\n\ts_mov_b64 %1, %6\n\ts_mov_b64 %2, %7\n\ts_mov_b64 %3, %8\n\ts_mov_b64 %4, %9" \
                     : "=&s"(kp[0]), "=&s"(kp[1]), "=&s"(kp[2]), "=&s"(kp[3]), "=&s"(kp[4]) : "s"(ka[0]), "s"(ka[1]), "s"(ka[2]), "s"(ka[3]), "s"(kw) : "memory"); SB(); } while (0)
#define KPTR(i) (kp[i])
    const int tid = threadIdx.x;
    const int w = __builtin_amdgcn_readfirstlane(tid >> 6);
    const int lane = tid & 63;
    const int bid = (int)blockIdx.x - NPF;
    const int xcd = blockIdx.x & 7, idx = bid >> 3;
    if (bid < 0) {
        const int pw = (blockIdx.x >> 3) * 8 + w;
        KWAIT();
        const char* qb = (const char*)KPTR(1) + (xcd >> 1) * 896 * 4 + (long)((xcd & 1) * 2) * (64 * N_ * 4);
        const char* xb = (const char*)KPTR(0) + (long)(xcd & 1) * 1024 * 2048;
        const u32 dst = (u32)(size_t)(smem) + w * 256;
        {
            const int m = (xcd & 1) * 4 + (blockIdx.x >> 3);
            const u32 off = (u32)((m * 256 + (w & 3) * 64 + lane) * 2048 + ((TLO(m, 0) >> (w & 4)) & 15) * 128);
            if (0 && w >= 4) __builtin_amdgcn_s_sleep(0);
            asm volatile("s_mov_b32 m0, %2\n\ts_nop 0\n\tglobal_load_lds_dword %0, %1" :: "v"(off), "s"(KPTR(0)), "s"(dst) : "memory");
        }
#pragma unroll 1
        for (int G = pw; G < 168; G += 32) {
            int kind = 0, arg = 0, e = 0;
            if (G >= 0) { kind = 0; arg = 2; e = G - 0 + (xcd >> 1) * 4; }
            if (G >= 4) { kind = 0; arg = 3; e = G - 4 + (xcd >> 1) * 4; }
            if (G >= 8) { kind = 0; arg = 4; e = G - 8 + (xcd >> 1) * 4; }
            if (G >= 12) { kind = 1; arg = 8; e = G - 12; }
            if (G >= 32) { kind = 0; arg = 5; e = G - 32 + (xcd >> 1) * 4; }
            if (G >= 36) { kind = 1; arg = 8; e = G - 16; }
            if (G >= 64) { kind = 1; arg = 8; e = G - 16; }
            if (G >= 72) { kind = 0; arg = 6; e = G - 72 + (xcd >> 1) * 4; }
            if (G >= 76) { kind = 0; arg = 7; e = G - 76 + (xcd >> 1) * 4; }
            if (G >= 80) { kind = 1; arg = 12; e = G - 80; }
            if (G >= 96) { kind = 0; arg = 8; e = G - 96 + (xcd >> 1) * 4; }
            if (G >= 100) { kind = 0; arg = 9; e = G - 100 + (xcd >> 1) * 4; }
            if (G >= 104) { kind = 1; arg = 12; e = G - 88; }
            if (G >= 128) { kind = 0; arg = 10; e = G - 128 + (xcd >> 1) * 4; }
            if (G >= 132) { kind = 1; arg = 12; e = G - 92; }
            if (G >= 148) { kind = 0; arg = 11; e = G - 148 + (xcd >> 1) * 4; }
            if (G >= 152) { kind = 0; arg = 12; e = G - 152 + (xcd >> 1) * 4; }
            if (G >= 156) { kind = 0; arg = 13; e = G - 156 + (xcd >> 1) * 4; }
            if (G >= 160) { kind = 0; arg = 14; e = G - 160 + (xcd >> 1) * 4; }
            if (G >= 164) { kind = 0; arg = 15; e = G - 164 + (xcd >> 1) * 4; }
            asm volatile("s_waitcnt vmcnt(%0)" :: "i"(PACE) : "memory");
            if (kind) {
                const int L = e * 64 + lane;
                const int r = L / 28, c = L - r * 28;
                const u32 off = (u32)((arg * 64 + r) * (N_ * 4) + c * 128);
                asm volatile("s_mov_b32 m0, %2\n\ts_nop 0\n\tglobal_load_lds_dword %0, %1" :: "v"(off), "s"(qb), "s"(dst) : "memory");
            } else {
                const int t = ROT0 || arg >= 8 ? arg : (int)((TLO((xcd & 1) * 4 + (e >> 2), 0) >> (4 * arg)) & 15);
                const u32 off = (u32)((e * 64 + lane) * 2048 + t * 128);
                asm volatile("s_mov_b32 m0, %2\n\ts_nop 0\n\tglobal_load_lds_dword %0, %1" :: "v"(off), "s"(xb), "s"(dst) : "memory");
            }
        }
        asm volatile("s_waitcnt vmcnt(0)" ::: "memory");
        return;
    }
    const int mt = (xcd & 1) * 4 + (idx & 3);
    const int nt = (xcd >> 1) * 7 + (idx >> 2);
    const int row0 = mt * BM, col0 = nt * BN;

    if (__builtin_expect(w >= 4, 0)) {
        int lane;
        asm volatile("v_mbcnt_lo_u32_b32 %0, -1, 0\n\tv_mbcnt_hi_u32_b32 %0, -1, %0" : "=v"(lane));
        const int p = w - 4;
        const int l = lane & 31, h = lane >> 5;
        const int g = lane >> 4;
        u32 xoff[4];
#pragma unroll
        for (int i = 0; i < 4; ++i)
            xoff[i] = (u32)(((row0 + p * 64 + i * 8 + (lane >> 3)) * K_ + (((lane & 7) ^ ((i & 1) * 4 + g)) * 8)) * 2 - i * 1024);
        const u32 ldsx = (u32)(size_t)(smem) + p * 8192;
        auto issue_x1 = [&](int tile, int hh, int slotoff, int i) {
            const u16* base = (const u16*)KPTR(0) + (long)hh * 32 * K_ + tile * 64;
            const u32 dst = ldsx + slotoff + hh * 4096;
            if (i == 0) asm volatile("s_mov_b32 m0, %2\n\ts_nop 0\n\tglobal_load_lds_dwordx4 %0, %1" :: "v"(xoff[0]), "s"(base), "s"(dst) : "memory");
            else asm volatile("global_load_lds_dwordx4 %0, %1 offset:%2" :: "v"(xoff[i]), "s"(base), "i"(i * 1024) : "memory");
        };
        u32 qoff[8];
#pragma unroll
        for (int r = 0; r < 8; ++r) qoff[r] = (u32)(((p * 16 + h * 8 + r) * N_ + col0 + l * 4) * 4);
        auto issue_q1 = [&](int t, i32x4 (&Q)[8], int r) {
            const int* base = (const int*)KPTR(1) + (long)t * 64 * N_;
            asm volatile("global_load_dwordx4 %0, %1, %2 " : "=v"(Q[r]) : "v"(qoff[r]), "s"(base) : "memory");
        };
        u32 lutl = (u32)(size_t)(smem) + OFF_L + l * 4;
        u32 bdst = (u32)(size_t)(smem) + OFF_B + (p >> 1) * BS + ((2 * p + h) & 3) * 2048 + ((l >> 4) * 64 + (l & 15)) * 16;
        u32 V[4][8];
#define LUT1(Q, j, r) asm volatile("v_lshl_add_u32 %0, %1, 8, %2\n\tds_read_u16 %0, %0 offset:%3" : "=&v"(V[j][r]) \
                                   : "v"(Q[r][j]), "v"(lutl), "i"(((j) >> 1) * 128 + ((j) & 1) * 2) : "memory")
#define LUT4(Q, e) do { LUT1(Q, (e) >> 1, ((e) & 1) * 4); LUT1(Q, (e) >> 1, ((e) & 1) * 4 + 1); \
                        LUT1(Q, (e) >> 1, ((e) & 1) * 4 + 2); LUT1(Q, (e) >> 1, ((e) & 1) * 4 + 3); SB(); } while (0)
#define WRB(j, n) do { u32x4 b_; \
                       asm volatile("s_waitcnt lgkmcnt(" #n ")\n\t" \
                                    "v_lshl_or_b32 %0, %5, 16, %4\n\tv_lshl_or_b32 %1, %7, 16, %6\n\t" \
                                    "v_lshl_or_b32 %2, %9, 16, %8\n\tv_lshl_or_b32 %3, %11, 16, %10" \
                                    : "=&v"(b_[0]), "=&v"(b_[1]), "=&v"(b_[2]), "=&v"(b_[3]) \
                                    : "v"(V[j][0]), "v"(V[j][1]), "v"(V[j][2]), "v"(V[j][3]), "v"(V[j][4]), "v"(V[j][5]), "v"(V[j][6]), "v"(V[j][7]) : "memory"); \
                       asm volatile("ds_write_b128 %0, %1 offset:%2" :: "v"(bdst), "v"(b_), "i"((j) * 256) : "memory"); SB(); } while (0)
        const int i2 = OWN(mt, nt), h2 = mt & 1;
        const unsigned long tseq = ((unsigned long)THI(mt, nt) << 32) | TLO(mt, nt);
        const u32 ldsw = (u32)(size_t)(smem) + OFF_B + p * 4096;
        u32 wv = (u32)(nt * 16384 + p * 4096 + lane * 16);
        u32 wo = (u32)(nt * 16384 + p * 4096 + h * 2048 + (l >> 4) * 1024 + (l & 15) * 16);
        u32 po = (u32)(WS_F + (nt * 4 + p) * 256 + lane * 4);
        u32 fo0 = po + (u32)(i2 * 16 + h2 * 8);
        u32 no = (u32)(nt * 256 + (lane & 1) * 4);
        u32 nvl = no;
        KWAIT();
        const char* st1 = (const char*)KPTR(4) + WS_W + (long)(i2 + 4) * TW + h2 * 512;
        const char* qa1 = (const char*)KPTR(1);
        const int tb = 8 + (mt >> 1), kg = h2 * 4 + p;
        const char* qbg = (const char*)KPTR(1) + (long)(tb * 64 + kg * 8 - p * 16) * (N_ * 4);
        const char* stb = (const char*)KPTR(4) + WS_W + (long)tb * TW + (kg * 2048 - p * 4096);
        u32 fob = po + (u32)(((kg >> 1) - p) * 256 + tb * 16 + (kg & 1) * 8);
        asm volatile("" : "+v"(wo), "+v"(fo0), "+v"(fob), "+v"(wv), "+v"(po), "+v"(bdst), "+v"(lutl), "+v"(qoff[0]));
        asm volatile("" : "+v"(xoff[0]), "+v"(xoff[1]), "+v"(xoff[2]), "+v"(xoff[3]));
        asm volatile("" : "+s"(st1), "+s"(qa1), "+s"(qbg), "+s"(stb));
        {
            unsigned long ex_;
            asm volatile("s_mov_b64 %0, exec\n\ts_mov_b64 exec, 0\n\t"
                         "s_cmp_eq_u32 %1, 0\n\ts_cbranch_scc1 .Lwp30\n\t"
                         "s_cmp_eq_u32 %1, 1\n\ts_cbranch_scc1 .Lwp31\n\t"
                         "s_cmp_eq_u32 %1, 2\n\ts_cbranch_scc1 .Lwp32\n\t"
                         "s_branch .Lwp33\n\t"
                         ".Lwp98:\n\ts_mov_b64 exec, %0" : "=&s"(ex_) : "s"(p) : "scc", "memory");
        }
        asm volatile(
            "s_mov_b32 s73, 65536\n\t"
            "s_mov_b64 s[84:85], %[ws]\n\t"
            "s_add_u32 s84, s84, 65536\n\t"
            "s_addc_u32 s85, s85, 0\n\t"
            "s_mov_b64 s[56:57], %[tseq]\n\t"
            "s_bfe_u32 s40, s56, 262144\n\t"
            "s_bfe_u32 s41, s56, 262148\n\t"
            "s_bfe_u32 s42, s56, 262152\n\t"
            "s_bfe_u32 s43, s56, 262156\n\t"
            "s_bfe_u32 s44, s56, 262160\n\t"
            "s_bfe_u32 s45, s56, 262164\n\t"
            "s_bfe_u32 s46, s56, 262168\n\t"
            "s_bfe_u32 s47, s56, 262172\n\t"
            "s_bfe_u32 s48, s57, 262144\n\t"
            "s_bfe_u32 s49, s57, 262148\n\t"
            "s_bfe_u32 s50, s57, 262152\n\t"
            "s_bfe_u32 s51, s57, 262156\n\t"
            "s_bfe_u32 s52, s57, 262160\n\t"
            "s_bfe_u32 s53, s57, 262164\n\t"
            "s_bfe_u32 s54, s57, 262168\n\t"
            "s_bfe_u32 s55, s57, 262172\n\t"
            "s_barrier\n\t"
            ".Lwp30:\n\t"
            "s_cbranch_execz .Lwp98\n\t"
            "s_add_u32 m0, %[ldsx], 0\n\t"
            "s_lshl_b32 s76, s40, 7\n\t"
            "s_add_u32 s68, %[xpl], s76\n\t"
            "s_addc_u32 s69, %[xph], 0\n\t"
            "global_load_lds_dwordx4 %[x0], s[68:69] offset:0\n\t"
            "global_load_lds_dwordx4 %[x1], s[68:69] offset:1024\n\t"
            "global_load_lds_dwordx4 %[x2], s[68:69] offset:2048\n\t"
            "global_load_lds_dwordx4 %[x3], s[68:69] offset:3072\n\t"
            "s_add_u32 m0, %[ldsx], 4096\n\t"
            "s_lshl_b32 s76, s40, 7\n\t"
            "s_add_u32 s76, s76, 65536\n\t"
            "s_add_u32 s68, %[xpl], s76\n\t"
            "s_addc_u32 s69, %[xph], 0\n\t"
            "global_load_lds_dwordx4 %[x0], s[68:69] offset:0\n\t"
            "global_load_lds_dwordx4 %[x1], s[68:69] offset:1024\n\t"
            "global_load_lds_dwordx4 %[x2], s[68:69] offset:2048\n\t"
            "global_load_lds_dwordx4 %[x3], s[68:69] offset:3072\n\t"
            "global_load_dword %[nvl], %[nvl], %[ws] sc1\n\t"
            "s_mul_i32 s76, s41, 917504\n\t"
            "s_mov_b64 s[88:89], %[qa1]\n\t"
            "s_add_u32 s88, s88, s76\n\t"
            "s_addc_u32 s89, s89, 0\n\t"
            "global_load_dwordx4 v[128:131], %[qo], s[88:89] \n\t"
            "s_add_u32 s88, s88, 14336\n\t"
            "s_addc_u32 s89, s89, 0\n\t"
            "global_load_dwordx4 v[132:135], %[qo], s[88:89] \n\t"
            "s_add_u32 s88, s88, 14336\n\t"
            "s_addc_u32 s89, s89, 0\n\t"
            "global_load_dwordx4 v[136:139], %[qo], s[88:89] \n\t"
            "s_add_u32 s88, s88, 14336\n\t"
            "s_addc_u32 s89, s89, 0\n\t"
            "global_load_dwordx4 v[140:143], %[qo], s[88:89] \n\t"
            "s_add_u32 s88, s88, 14336\n\t"
            "s_addc_u32 s89, s89, 0\n\t"
            "global_load_dwordx4 v[144:147], %[qo], s[88:89] \n\t"
            "s_add_u32 s88, s88, 14336\n\t"
            "s_addc_u32 s89, s89, 0\n\t"
            "global_load_dwordx4 v[148:151], %[qo], s[88:89] \n\t"
            "s_add_u32 s88, s88, 14336\n\t"
            "s_addc_u32 s89, s89, 0\n\t"
            "global_load_dwordx4 v[152:155], %[qo], s[88:89] \n\t"
            "s_add_u32 s88, s88, 14336\n\t"
            "s_addc_u32 s89, s89, 0\n\t"
            "global_load_dwordx4 v[156:159], %[qo], s[88:89] \n\t"
            "s_waitcnt vmcnt(9)\n\t"
            "s_barrier\n\t"
            ".Lwp31:\n\t"
            "s_cbranch_execz .Lwp98\n\t"
            "s_lshl_b32 s76, %[h2], 9\n\t"
            "v_add_u32 v193, s76, %[bd]\n\t"
            "ds_read_b128 v[196:199], v193\n\t"
            "ds_read_b128 v[200:203], v193 offset:256\n\t"
            "s_sub_u32 s70, %[st1l], 1835008\n\t"
            "s_subb_u32 s71, %[st1h], 0\n\t"
            "s_waitcnt lgkmcnt(0)\n\t"
            "global_store_dwordx4 %[wo], v[196:199], s[70:71] sc1\n\t"
            "global_store_dwordx4 %[wo], v[200:203], s[70:71] offset:256 sc1\n\t"
            "s_waitcnt vmcnt(10)\n\t"
            "s_mov_b32 s76, 1664525\n\t"
            "v_mul_lo_u32 %[nvl], %[nvl], s76\n\t"
            "v_add_u32 %[nvl], 1013904223, %[nvl]\n\t"
            "s_setprio 1\n\t"
            "s_waitcnt vmcnt(9)\n\t"
            "v_lshl_add_u32 v160, v128, 8, %[lutl]\n\t"
            "ds_read_u16 v160, v160 offset:0\n\t"
            "v_lshl_add_u32 v168, v129, 8, %[lutl]\n\t"
            "ds_read_u16 v168, v168 offset:2\n\t"
            "v_lshl_add_u32 v176, v130, 8, %[lutl]\n\t"
            "ds_read_u16 v176, v176 offset:128\n\t"
            "v_lshl_add_u32 v184, v131, 8, %[lutl]\n\t"
            "ds_read_u16 v184, v184 offset:130\n\t"
            "s_add_u32 m0, %[ldsx], 32768\n\t"
            "s_lshl_b32 s76, s41, 7\n\t"
            "s_add_u32 s68, %[xpl], s76\n\t"
            "s_addc_u32 s69, %[xph], 0\n\t"
            "global_load_lds_dwordx4 %[x0], s[68:69] offset:0\n\t"
            "s_waitcnt vmcnt(9)\n\t"
            "v_lshl_add_u32 v161, v132, 8, %[lutl]\n\t"
            "ds_read_u16 v161, v161 offset:0\n\t"
            "v_lshl_add_u32 v169, v133, 8, %[lutl]\n\t"
            "ds_read_u16 v169, v169 offset:2\n\t"
            "v_lshl_add_u32 v177, v134, 8, %[lutl]\n\t"
            "ds_read_u16 v177, v177 offset:128\n\t"
            "v_lshl_add_u32 v185, v135, 8, %[lutl]\n\t"
            "ds_read_u16 v185, v185 offset:130\n\t"
            "global_load_lds_dwordx4 %[x1], s[68:69] offset:1024\n\t"
            "s_waitcnt vmcnt(9)\n\t"
            "v_lshl_add_u32 v162, v136, 8, %[lutl]\n\t"
            "ds_read_u16 v162, v162 offset:0\n\t"
            "v_lshl_add_u32 v170, v137, 8, %[lutl]\n\t"
            "ds_read_u16 v170, v170 offset:2\n\t"
            "v_lshl_add_u32 v178, v138, 8, %[lutl]\n\t"
            "ds_read_u16 v178, v178 offset:128\n\t"
            "v_lshl_add_u32 v186, v139, 8, %[lutl]\n\t"
            "ds_read_u16 v186, v186 offset:130\n\t"
            "global_load_lds_dwordx4 %[x2], s[68:69] offset:2048\n\t"
            "s_waitcnt vmcnt(9)\n\t"
            "v_lshl_add_u32 v163, v140, 8, %[lutl]\n\t"
            "ds_read_u16 v163, v163 offset:0\n\t"
            "v_lshl_add_u32 v171, v141, 8, %[lutl]\n\t"
            "ds_read_u16 v171, v171 offset:2\n\t"
            "v_lshl_add_u32 v179, v142, 8, %[lutl]\n\t"
            "ds_read_u16 v179, v179 offset:128\n\t"
            "v_lshl_add_u32 v187, v143, 8, %[lutl]\n\t"
            "ds_read_u16 v187, v187 offset:130\n\t"
            "global_load_lds_dwordx4 %[x3], s[68:69] offset:3072\n\t"
            "s_waitcnt vmcnt(9)\n\t"
            "v_lshl_add_u32 v164, v144, 8, %[lutl]\n\t"
            "ds_read_u16 v164, v164 offset:0\n\t"
            "v_lshl_add_u32 v172, v145, 8, %[lutl]\n\t"
            "ds_read_u16 v172, v172 offset:2\n\t"
            "v_lshl_add_u32 v180, v146, 8, %[lutl]\n\t"
            "ds_read_u16 v180, v180 offset:128\n\t"
            "v_lshl_add_u32 v188, v147, 8, %[lutl]\n\t"
            "ds_read_u16 v188, v188 offset:130\n\t"
            "s_add_u32 m0, %[ldsx], 36864\n\t"
            "s_lshl_b32 s76, s41, 7\n\t"
            "s_add_u32 s76, s76, 65536\n\t"
            "s_add_u32 s68, %[xpl], s76\n\t"
            "s_addc_u32 s69, %[xph], 0\n\t"
            "global_load_lds_dwordx4 %[x0], s[68:69] offset:0\n\t"
            "s_waitcnt vmcnt(9)\n\t"
            "v_lshl_add_u32 v165, v148, 8, %[lutl]\n\t"
            "ds_read_u16 v165, v165 offset:0\n\t"
            "v_lshl_add_u32 v173, v149, 8, %[lutl]\n\t"
            "ds_read_u16 v173, v173 offset:2\n\t"
            "v_lshl_add_u32 v181, v150, 8, %[lutl]\n\t"
            "ds_read_u16 v181, v181 offset:128\n\t"
            "v_lshl_add_u32 v189, v151, 8, %[lutl]\n\t"
            "ds_read_u16 v189, v189 offset:130\n\t"
            "global_load_lds_dwordx4 %[x1], s[68:69] offset:1024\n\t"
            "s_waitcnt vmcnt(9)\n\t"
            "v_lshl_add_u32 v166, v152, 8, %[lutl]\n\t"
            "ds_read_u16 v166, v166 offset:0\n\t"
            "v_lshl_add_u32 v174, v153, 8, %[lutl]\n\t"
            "ds_read_u16 v174, v174 offset:2\n\t"
            "v_lshl_add_u32 v182, v154, 8, %[lutl]\n\t"
            "ds_read_u16 v182, v182 offset:128\n\t"
            "v_lshl_add_u32 v190, v155, 8, %[lutl]\n\t"
            "ds_read_u16 v190, v190 offset:130\n\t"
            "global_load_lds_dwordx4 %[x2], s[68:69] offset:2048\n\t"
            "s_waitcnt vmcnt(9)\n\t"
            "v_lshl_add_u32 v167, v156, 8, %[lutl]\n\t"
            "ds_read_u16 v167, v167 offset:0\n\t"
            "v_lshl_add_u32 v175, v157, 8, %[lutl]\n\t"
            "ds_read_u16 v175, v175 offset:2\n\t"
            "v_lshl_add_u32 v183, v158, 8, %[lutl]\n\t"
            "ds_read_u16 v183, v183 offset:128\n\t"
            "v_lshl_add_u32 v191, v159, 8, %[lutl]\n\t"
            "ds_read_u16 v191, v191 offset:130\n\t"
            "global_load_lds_dwordx4 %[x3], s[68:69] offset:3072\n\t"
            "s_setprio 0\n\t"
            "s_waitcnt vmcnt(8)\n\t"
            "s_mov_b64 exec, 3\n\t"
            "global_store_dword %[fo0], %[nvl], %[ws] offset:0 sc1\n\t"
            "s_mov_b64 exec, -1\n\t"
            "s_waitcnt lgkmcnt(3)\n\t"
            "v_lshl_or_b32 v128, v161, 16, v160\n\t"
            "v_lshl_or_b32 v129, v163, 16, v162\n\t"
            "v_lshl_or_b32 v130, v165, 16, v164\n\t"
            "v_lshl_or_b32 v131, v167, 16, v166\n\t"
            "ds_write_b128 %[bd], v[128:131] offset:16384\n\t"
            "s_waitcnt lgkmcnt(3)\n\t"
            "v_lshl_or_b32 v132, v169, 16, v168\n\t"
            "v_lshl_or_b32 v133, v171, 16, v170\n\t"
            "v_lshl_or_b32 v134, v173, 16, v172\n\t"
            "v_lshl_or_b32 v135, v175, 16, v174\n\t"
            "ds_write_b128 %[bd], v[132:135] offset:16640\n\t"
            "s_waitcnt lgkmcnt(3)\n\t"
            "v_lshl_or_b32 v136, v177, 16, v176\n\t"
            "v_lshl_or_b32 v137, v179, 16, v178\n\t"
            "v_lshl_or_b32 v138, v181, 16, v180\n\t"
            "v_lshl_or_b32 v139, v183, 16, v182\n\t"
            "ds_write_b128 %[bd], v[136:139] offset:16896\n\t"
            "s_waitcnt lgkmcnt(3)\n\t"
            "v_lshl_or_b32 v140, v185, 16, v184\n\t"
            "v_lshl_or_b32 v141, v187, 16, v186\n\t"
            "v_lshl_or_b32 v142, v189, 16, v188\n\t"
            "v_lshl_or_b32 v143, v191, 16, v190\n\t"
            "ds_write_b128 %[bd], v[140:143] offset:17152\n\t"
            "s_add_u32 m0, %[ldsx], 65536\n\t"
            "s_lshl_b32 s76, s42, 7\n\t"
            "s_add_u32 s68, %[xpl], s76\n\t"
            "s_addc_u32 s69, %[xph], 0\n\t"
            "global_load_lds_dwordx4 %[x0], s[68:69] offset:0\n\t"
            "global_load_lds_dwordx4 %[x1], s[68:69] offset:1024\n\t"
            "global_load_lds_dwordx4 %[x2], s[68:69] offset:2048\n\t"
            "global_load_lds_dwordx4 %[x3], s[68:69] offset:3072\n\t"
            "s_add_u32 m0, %[ldsx], 69632\n\t"
            "s_lshl_b32 s76, s42, 7\n\t"
            "s_add_u32 s76, s76, 65536\n\t"
            "s_add_u32 s68, %[xpl], s76\n\t"
            "s_addc_u32 s69, %[xph], 0\n\t"
            "global_load_lds_dwordx4 %[x0], s[68:69] offset:0\n\t"
            "global_load_lds_dwordx4 %[x1], s[68:69] offset:1024\n\t"
            "global_load_lds_dwordx4 %[x2], s[68:69] offset:2048\n\t"
            "global_load_lds_dwordx4 %[x3], s[68:69] offset:3072\n\t"
            "s_waitcnt vmcnt(9) lgkmcnt(0)\n\t"
            "s_barrier\n\t"
            ".Lwp32:\n\t"
            "s_cbranch_execz .Lwp98\n\t"
            "global_load_dword v192, %[po], %[ws] sc1\n\t"
            "s_add_u32 m0, %[ldsx], 0\n\t"
            "s_lshl_b32 s76, s43, 7\n\t"
            "s_add_u32 s68, %[xpl], s76\n\t"
            "s_addc_u32 s69, %[xph], 0\n\t"
            "global_load_lds_dwordx4 %[x0], s[68:69] offset:0\n\t"
            "global_load_lds_dwordx4 %[x1], s[68:69] offset:1024\n\t"
            "global_load_lds_dwordx4 %[x2], s[68:69] offset:2048\n\t"
            "global_load_lds_dwordx4 %[x3], s[68:69] offset:3072\n\t"
            "s_add_u32 m0, %[ldsx], 4096\n\t"
            "s_lshl_b32 s76, s43, 7\n\t"
            "s_add_u32 s76, s76, 65536\n\t"
            "s_add_u32 s68, %[xpl], s76\n\t"
            "s_addc_u32 s69, %[xph], 0\n\t"
            "global_load_lds_dwordx4 %[x0], s[68:69] offset:0\n\t"
            "global_load_lds_dwordx4 %[x1], s[68:69] offset:1024\n\t"
            "global_load_lds_dwordx4 %[x2], s[68:69] offset:2048\n\t"
            "global_load_lds_dwordx4 %[x3], s[68:69] offset:3072\n\t"
            "s_waitcnt vmcnt(8)\n\t"
            "s_lshl_b32 s76, s42, 2\n\t"
            ".Lzc0:\n\t"
            "v_cmp_eq_u32 vcc, v192, %[nvl]\n\t"
            "s_lshr_b64 s[74:75], vcc, s76\n\t"
            "s_and_b32 s74, s74, 15\n\t"
            "s_cmp_eq_u32 s74, 15\n\t"
            "s_cbranch_scc1 .Lzd0\n\t"
            "s_cmp_eq_u32 s73, 0\n\t"
            "s_cbranch_scc1 .Lzd0\n\t"
            "s_sub_u32 s73, s73, 1\n\t"
            "global_load_dword v192, %[po], %[ws] sc1\n\t"
            "s_waitcnt vmcnt(0)\n\t"
            "s_branch .Lzc0\n\t"
            ".Lzd0:\n\t"
            "s_add_u32 m0, %[ldsw], 32768\n\t"
            "s_mul_i32 s76, s42, 458752\n\t"
            "s_add_u32 s70, s84, s76\n\t"
            "s_addc_u32 s71, s85, 0\n\t"
            "global_load_lds_dwordx4 %[wv], s[70:71] offset:0 sc1\n\t"
            "global_load_lds_dwordx4 %[wv], s[70:71] offset:1024 sc1\n\t"
            "global_load_lds_dwordx4 %[wv], s[70:71] offset:2048 sc1\n\t"
            "global_load_lds_dwordx4 %[wv], s[70:71] offset:3072 sc1\n\t"
            "s_cmp_lg_u32 %[h2], 0\n\t"
            "s_cbranch_scc1 .Lzs1\n\t"
            "global_store_dwordx4 %[wo], v[128:131], %[st1] sc1\n\t"
            "global_store_dwordx4 %[wo], v[132:135], %[st1] offset:256 sc1\n\t"
            "s_branch .Lzt1\n\t"
            ".Lzs1:\n\t"
            "global_store_dwordx4 %[wo], v[136:139], %[st1] sc1\n\t"
            "global_store_dwordx4 %[wo], v[140:143], %[st1] offset:256 sc1\n\t"
            ".Lzt1:\n\t"
            "s_lshl_b32 s76, s43, 2\n\t"
            ".Lzc2:\n\t"
            "v_cmp_eq_u32 vcc, v192, %[nvl]\n\t"
            "s_lshr_b64 s[74:75], vcc, s76\n\t"
            "s_and_b32 s74, s74, 15\n\t"
            "s_cmp_eq_u32 s74, 15\n\t"
            "s_cbranch_scc1 .Lzd2\n\t"
            "s_cmp_eq_u32 s73, 0\n\t"
            "s_cbranch_scc1 .Lzd2\n\t"
            "s_sub_u32 s73, s73, 1\n\t"
            "global_load_dword v192, %[po], %[ws] sc1\n\t"
            "s_waitcnt vmcnt(0)\n\t"
            "s_branch .Lzc2\n\t"
            ".Lzd2:\n\t"
            "s_add_u32 m0, %[ldsw], 0\n\t"
            "s_mul_i32 s76, s43, 458752\n\t"
            "s_add_u32 s70, s84, s76\n\t"
            "s_addc_u32 s71, s85, 0\n\t"
            "global_load_lds_dwordx4 %[wv], s[70:71] offset:0 sc1\n\t"
            "global_load_lds_dwordx4 %[wv], s[70:71] offset:1024 sc1\n\t"
            "global_load_lds_dwordx4 %[wv], s[70:71] offset:2048 sc1\n\t"
            "global_load_lds_dwordx4 %[wv], s[70:71] offset:3072 sc1\n\t"
            "s_waitcnt vmcnt(6)\n\t"
            "s_barrier\n\t"
            ".Lwp33:\n\t"
            "s_cbranch_execz .Lwp98\n\t"
            "s_lshl_b32 s76, s44, 2\n\t"
            ".Lzc3:\n\t"
            "v_cmp_eq_u32 vcc, v192, %[nvl]\n\t"
            "s_lshr_b64 s[74:75], vcc, s76\n\t"
            "s_and_b32 s74, s74, 15\n\t"
            "s_cmp_eq_u32 s74, 15\n\t"
            "s_cbranch_scc1 .Lzd3\n\t"
            "s_cmp_eq_u32 s73, 0\n\t"
            "s_cbranch_scc1 .Lzd3\n\t"
            "s_sub_u32 s73, s73, 1\n\t"
            "global_load_dword v192, %[po], %[ws] sc1\n\t"
            "s_waitcnt vmcnt(0)\n\t"
            "s_branch .Lzc3\n\t"
            ".Lzd3:\n\t"
            "s_add_u32 m0, %[ldsw], 16384\n\t"
            "s_mul_i32 s76, s44, 458752\n\t"
            "s_add_u32 s70, s84, s76\n\t"
            "s_addc_u32 s71, s85, 0\n\t"
            "global_load_lds_dwordx4 %[wv], s[70:71] offset:0 sc1\n\t"
            "global_load_lds_dwordx4 %[wv], s[70:71] offset:1024 sc1\n\t"
            "global_load_lds_dwordx4 %[wv], s[70:71] offset:2048 sc1\n\t"
            "global_load_lds_dwordx4 %[wv], s[70:71] offset:3072 sc1\n\t"
            "s_waitcnt vmcnt(8)\n\t"
            "s_mov_b64 exec, 3\n\t"
            "global_store_dword %[fo0], %[nvl], %[ws] offset:64 sc1\n\t"
            "s_mov_b64 exec, -1\n\t"
            "v_mbcnt_lo_u32_b32 v193, -1, 0\n\t"
            "v_mbcnt_hi_u32_b32 v193, -1, v193\n\t"
            "v_lshrrev_b32 v193, 5, v193\n\t"
            "v_mul_u32_u24 v194, 57344, v193\n\t"
            "v_sub_u32 %[qo], %[qo], v194\n\t"
            "v_mul_u32_u24 v194, 1792, v193\n\t"
            "v_sub_u32 %[wo], %[wo], v194\n\t"
            "s_mov_b64 s[88:89], %[qbg]\n\t"
            "global_load_dwordx4 v[204:207], %[qo], s[88:89] \n\t"
            "s_add_u32 s88, s88, 14336\n\t"
            "s_addc_u32 s89, s89, 0\n\t"
            "global_load_dwordx4 v[208:211], %[qo], s[88:89] \n\t"
            "s_add_u32 s88, s88, 14336\n\t"
            "s_addc_u32 s89, s89, 0\n\t"
            "global_load_dwordx4 v[212:215], %[qo], s[88:89] \n\t"
            "s_add_u32 s88, s88, 14336\n\t"
            "s_addc_u32 s89, s89, 0\n\t"
            "global_load_dwordx4 v[216:219], %[qo], s[88:89] \n\t"
            "s_add_u32 m0, %[ldsx], 32768\n\t"
            "s_lshl_b32 s76, s44, 7\n\t"
            "s_add_u32 s68, %[xpl], s76\n\t"
            "s_addc_u32 s69, %[xph], 0\n\t"
            "global_load_lds_dwordx4 %[x0], s[68:69] offset:0\n\t"
            "global_load_lds_dwordx4 %[x1], s[68:69] offset:1024\n\t"
            "global_load_lds_dwordx4 %[x2], s[68:69] offset:2048\n\t"
            "global_load_lds_dwordx4 %[x3], s[68:69] offset:3072\n\t"
            "s_add_u32 m0, %[ldsx], 36864\n\t"
            "s_lshl_b32 s76, s44, 7\n\t"
            "s_add_u32 s76, s76, 65536\n\t"
            "s_add_u32 s68, %[xpl], s76\n\t"
            "s_addc_u32 s69, %[xph], 0\n\t"
            "global_load_lds_dwordx4 %[x0], s[68:69] offset:0\n\t"
            "global_load_lds_dwordx4 %[x1], s[68:69] offset:1024\n\t"
            "global_load_lds_dwordx4 %[x2], s[68:69] offset:2048\n\t"
            "global_load_lds_dwordx4 %[x3], s[68:69] offset:3072\n\t"
            "s_waitcnt vmcnt(17)\n\t"
            "s_barrier\n\t"
            ".Lwp34:\n\t"
            "s_cbranch_execz .Lwp35\n\t"
            "global_load_dword v192, %[po], %[ws] sc1\n\t"
            "s_waitcnt vmcnt(9)\n\t"
            "s_setprio 0\n\t"
            "s_add_u32 m0, %[ldsx], 65536\n\t"
            "s_lshl_b32 s76, s45, 7\n\t"
            "s_add_u32 s68, %[xpl], s76\n\t"
            "s_addc_u32 s69, %[xph], 0\n\t"
            "global_load_lds_dwordx4 %[x0], s[68:69] offset:0\n\t"
            "v_lshl_add_u32 v160, v204, 8, %[lutl]\n\t"
            "ds_read_u16 v160, v160 offset:0\n\t"
            "v_lshl_add_u32 v161, v208, 8, %[lutl]\n\t"
            "ds_read_u16 v161, v161 offset:0\n\t"
            "global_load_lds_dwordx4 %[x1], s[68:69] offset:1024\n\t"
            "v_lshl_add_u32 v162, v212, 8, %[lutl]\n\t"
            "ds_read_u16 v162, v162 offset:0\n\t"
            "v_lshl_add_u32 v163, v216, 8, %[lutl]\n\t"
            "ds_read_u16 v163, v163 offset:0\n\t"
            "global_load_lds_dwordx4 %[x2], s[68:69] offset:2048\n\t"
            "v_lshl_add_u32 v168, v205, 8, %[lutl]\n\t"
            "ds_read_u16 v168, v168 offset:2\n\t"
            "v_lshl_add_u32 v169, v209, 8, %[lutl]\n\t"
            "ds_read_u16 v169, v169 offset:2\n\t"
            "global_load_lds_dwordx4 %[x3], s[68:69] offset:3072\n\t"
            "v_lshl_add_u32 v170, v213, 8, %[lutl]\n\t"
            "ds_read_u16 v170, v170 offset:2\n\t"
            "v_lshl_add_u32 v171, v217, 8, %[lutl]\n\t"
            "ds_read_u16 v171, v171 offset:2\n\t"
            "s_add_u32 m0, %[ldsx], 69632\n\t"
            "s_lshl_b32 s76, s45, 7\n\t"
            "s_add_u32 s76, s76, 65536\n\t"
            "s_add_u32 s68, %[xpl], s76\n\t"
            "s_addc_u32 s69, %[xph], 0\n\t"
            "global_load_lds_dwordx4 %[x0], s[68:69] offset:0\n\t"
            "v_lshl_add_u32 v176, v206, 8, %[lutl]\n\t"
            "ds_read_u16 v176, v176 offset:128\n\t"
            "v_lshl_add_u32 v177, v210, 8, %[lutl]\n\t"
            "ds_read_u16 v177, v177 offset:128\n\t"
            "global_load_lds_dwordx4 %[x1], s[68:69] offset:1024\n\t"
            "v_lshl_add_u32 v178, v214, 8, %[lutl]\n\t"
            "ds_read_u16 v178, v178 offset:128\n\t"
            "v_lshl_add_u32 v179, v218, 8, %[lutl]\n\t"
            "ds_read_u16 v179, v179 offset:128\n\t"
            "global_load_lds_dwordx4 %[x2], s[68:69] offset:2048\n\t"
            "v_lshl_add_u32 v184, v207, 8, %[lutl]\n\t"
            "ds_read_u16 v184, v184 offset:130\n\t"
            "v_lshl_add_u32 v185, v211, 8, %[lutl]\n\t"
            "ds_read_u16 v185, v185 offset:130\n\t"
            "global_load_lds_dwordx4 %[x3], s[68:69] offset:3072\n\t"
            "v_lshl_add_u32 v186, v215, 8, %[lutl]\n\t"
            "ds_read_u16 v186, v186 offset:130\n\t"
            "v_lshl_add_u32 v187, v219, 8, %[lutl]\n\t"
            "ds_read_u16 v187, v187 offset:130\n\t"
            "s_setprio 0\n\t"
            "s_waitcnt vmcnt(8)\n\t"
            "s_lshl_b32 s76, s45, 2\n\t"
            ".Lzc4:\n\t"
            "v_cmp_eq_u32 vcc, v192, %[nvl]\n\t"
            "s_lshr_b64 s[74:75], vcc, s76\n\t"
            "s_and_b32 s74, s74, 15\n\t"
            "s_cmp_eq_u32 s74, 15\n\t"
            "s_cbranch_scc1 .Lzd4\n\t"
            "s_cmp_eq_u32 s73, 0\n\t"
            "s_cbranch_scc1 .Lzd4\n\t"
            "s_sub_u32 s73, s73, 1\n\t"
            "global_load_dword v192, %[po], %[ws] sc1\n\t"
            "s_waitcnt vmcnt(0)\n\t"
            "s_branch .Lzc4\n\t"
            ".Lzd4:\n\t"
            "s_add_u32 m0, %[ldsw], 32768\n\t"
            "s_mul_i32 s76, s45, 458752\n\t"
            "s_add_u32 s70, s84, s76\n\t"
            "s_addc_u32 s71, s85, 0\n\t"
            "global_load_lds_dwordx4 %[wv], s[70:71] offset:0 sc1\n\t"
            "global_load_lds_dwordx4 %[wv], s[70:71] offset:1024 sc1\n\t"
            "global_load_lds_dwordx4 %[wv], s[70:71] offset:2048 sc1\n\t"
            "global_load_lds_dwordx4 %[wv], s[70:71] offset:3072 sc1\n\t"
            "s_waitcnt lgkmcnt(8)\n\t"
            "v_lshl_or_b32 v220, v161, 16, v160\n\t"
            "v_lshl_or_b32 v221, v163, 16, v162\n\t"
            "v_lshl_or_b32 v222, v169, 16, v168\n\t"
            "v_lshl_or_b32 v223, v171, 16, v170\n\t"
            "s_waitcnt lgkmcnt(0)\n\t"
            "v_lshl_or_b32 v224, v177, 16, v176\n\t"
            "v_lshl_or_b32 v225, v179, 16, v178\n\t"
            "v_lshl_or_b32 v226, v185, 16, v184\n\t"
            "v_lshl_or_b32 v227, v187, 16, v186\n\t"
            "s_nop 0\n\t"
            "v_permlane32_swap_b32 v220, v222\n\t"
            "v_permlane32_swap_b32 v221, v223\n\t"
            "v_permlane32_swap_b32 v224, v226\n\t"
            "v_permlane32_swap_b32 v225, v227\n\t"
            "s_mov_b64 s[88:89], %[stb]\n\t"
            "s_nop 0\n\t"
            "global_store_dwordx4 %[wo], v[220:223], s[88:89] offset:0 sc1\n\t"
            "global_store_dwordx4 %[wo], v[224:227], s[88:89] offset:512 sc1\n\t"
            "s_barrier\n\t"
            ".Lwp35:\n\t"
            "s_cbranch_execz .Lwp36\n\t"
            "s_lshl_b32 s76, s46, 2\n\t"
            ".Lzc5:\n\t"
            "v_cmp_eq_u32 vcc, v192, %[nvl]\n\t"
            "s_lshr_b64 s[74:75], vcc, s76\n\t"
            "s_and_b32 s74, s74, 15\n\t"
            "s_cmp_eq_u32 s74, 15\n\t"
            "s_cbranch_scc1 .Lzd5\n\t"
            "s_cmp_eq_u32 s73, 0\n\t"
            "s_cbranch_scc1 .Lzd5\n\t"
            "s_sub_u32 s73, s73, 1\n\t"
            "global_load_dword v192, %[po], %[ws] sc1\n\t"
            "s_waitcnt vmcnt(0)\n\t"
            "s_branch .Lzc5\n\t"
            ".Lzd5:\n\t"
            "s_add_u32 m0, %[ldsw], 0\n\t"
            "s_mul_i32 s76, s46, 458752\n\t"
            "s_add_u32 s70, s84, s76\n\t"
            "s_addc_u32 s71, s85, 0\n\t"
            "global_load_lds_dwordx4 %[wv], s[70:71] offset:0 sc1\n\t"
            "global_load_lds_dwordx4 %[wv], s[70:71] offset:1024 sc1\n\t"
            "global_load_lds_dwordx4 %[wv], s[70:71] offset:2048 sc1\n\t"
            "global_load_lds_dwordx4 %[wv], s[70:71] offset:3072 sc1\n\t"
            "s_add_u32 m0, %[ldsx], 0\n\t"
            "s_lshl_b32 s76, s46, 7\n\t"
            "s_add_u32 s68, %[xpl], s76\n\t"
            "s_addc_u32 s69, %[xph], 0\n\t"
            "global_load_lds_dwordx4 %[x0], s[68:69] offset:0\n\t"
            "global_load_lds_dwordx4 %[x1], s[68:69] offset:1024\n\t"
            "global_load_lds_dwordx4 %[x2], s[68:69] offset:2048\n\t"
            "global_load_lds_dwordx4 %[x3], s[68:69] offset:3072\n\t"
            "s_add_u32 m0, %[ldsx], 4096\n\t"
            "s_lshl_b32 s76, s46, 7\n\t"
            "s_add_u32 s76, s76, 65536\n\t"
            "s_add_u32 s68, %[xpl], s76\n\t"
            "s_addc_u32 s69, %[xph], 0\n\t"
            "global_load_lds_dwordx4 %[x0], s[68:69] offset:0\n\t"
            "global_load_lds_dwordx4 %[x1], s[68:69] offset:1024\n\t"
            "global_load_lds_dwordx4 %[x2], s[68:69] offset:2048\n\t"
            "global_load_lds_dwordx4 %[x3], s[68:69] offset:3072\n\t"
            "s_waitcnt vmcnt(12)\n\t"
            "s_mov_b64 exec, 3\n\t"
            "global_store_dword %[fob], %[nvl], %[ws] offset:0 sc1\n\t"
            "s_mov_b64 exec, -1\n\t"
            "s_barrier\n\t"
            "s_lshl_b32 s76, s47, 2\n\t"
            ".Lzc6:\n\t"
            "v_cmp_eq_u32 vcc, v192, %[nvl]\n\t"
            "s_lshr_b64 s[74:75], vcc, s76\n\t"
            "s_and_b32 s74, s74, 15\n\t"
            "s_cmp_eq_u32 s74, 15\n\t"
            "s_cbranch_scc1 .Lzd6\n\t"
            "s_cmp_eq_u32 s73, 0\n\t"
            "s_cbranch_scc1 .Lzd6\n\t"
            "s_sub_u32 s73, s73, 1\n\t"
            "global_load_dword v192, %[po], %[ws] sc1\n\t"
            "s_waitcnt vmcnt(0)\n\t"
            "s_branch .Lzc6\n\t"
            ".Lzd6:\n\t"
            "s_add_u32 m0, %[ldsw], 16384\n\t"
            "s_mul_i32 s76, s47, 458752\n\t"
            "s_add_u32 s70, s84, s76\n\t"
            "s_addc_u32 s71, s85, 0\n\t"
            "global_load_lds_dwordx4 %[wv], s[70:71] offset:0 sc1\n\t"
            "global_load_lds_dwordx4 %[wv], s[70:71] offset:1024 sc1\n\t"
            "global_load_lds_dwordx4 %[wv], s[70:71] offset:2048 sc1\n\t"
            "global_load_lds_dwordx4 %[wv], s[70:71] offset:3072 sc1\n\t"
            "s_add_u32 m0, %[ldsx], 32768\n\t"
            "s_lshl_b32 s76, s47, 7\n\t"
            "s_add_u32 s68, %[xpl], s76\n\t"
            "s_addc_u32 s69, %[xph], 0\n\t"
            "global_load_lds_dwordx4 %[x0], s[68:69] offset:0\n\t"
            "global_load_lds_dwordx4 %[x1], s[68:69] offset:1024\n\t"
            "global_load_lds_dwordx4 %[x2], s[68:69] offset:2048\n\t"
            "global_load_lds_dwordx4 %[x3], s[68:69] offset:3072\n\t"
            "s_add_u32 m0, %[ldsx], 36864\n\t"
            "s_lshl_b32 s76, s47, 7\n\t"
            "s_add_u32 s76, s76, 65536\n\t"
            "s_add_u32 s68, %[xpl], s76\n\t"
            "s_addc_u32 s69, %[xph], 0\n\t"
            "global_load_lds_dwordx4 %[x0], s[68:69] offset:0\n\t"
            "global_load_lds_dwordx4 %[x1], s[68:69] offset:1024\n\t"
            "global_load_lds_dwordx4 %[x2], s[68:69] offset:2048\n\t"
            "global_load_lds_dwordx4 %[x3], s[68:69] offset:3072\n\t"
            "s_mov_b64 s[88:89], %[qbg]\n\t"
            "s_add_u32 s88, s88, 3670016\n\t"
            "s_addc_u32 s89, s89, 0\n\t"
            "global_load_dwordx4 v[204:207], %[qo], s[88:89] \n\t"
            "s_add_u32 s88, s88, 14336\n\t"
            "s_addc_u32 s89, s89, 0\n\t"
            "global_load_dwordx4 v[208:211], %[qo], s[88:89] \n\t"
            "s_add_u32 s88, s88, 14336\n\t"
            "s_addc_u32 s89, s89, 0\n\t"
            "global_load_dwordx4 v[212:215], %[qo], s[88:89] \n\t"
            "s_add_u32 s88, s88, 14336\n\t"
            "s_addc_u32 s89, s89, 0\n\t"
            "global_load_dwordx4 v[216:219], %[qo], s[88:89] \n\t"
            "s_waitcnt vmcnt(17)\n\t"
            "s_barrier\n\t"
            ".Lwp36:\n\t"
            "s_cbranch_execz .Lwp37\n\t"
            "global_load_dword v192, %[po], %[ws] sc1\n\t"
            "s_add_u32 m0, %[ldsx], 65536\n\t"
            "s_lshl_b32 s76, s48, 7\n\t"
            "s_add_u32 s68, %[xpl], s76\n\t"
            "s_addc_u32 s69, %[xph], 0\n\t"
            "global_load_lds_dwordx4 %[x0], s[68:69] offset:0\n\t"
            "global_load_lds_dwordx4 %[x1], s[68:69] offset:1024\n\t"
            "global_load_lds_dwordx4 %[x2], s[68:69] offset:2048\n\t"
            "global_load_lds_dwordx4 %[x3], s[68:69] offset:3072\n\t"
            "s_add_u32 m0, %[ldsx], 69632\n\t"
            "s_lshl_b32 s76, s48, 7\n\t"
            "s_add_u32 s76, s76, 65536\n\t"
            "s_add_u32 s68, %[xpl], s76\n\t"
            "s_addc_u32 s69, %[xph], 0\n\t"
            "global_load_lds_dwordx4 %[x0], s[68:69] offset:0\n\t"
            "global_load_lds_dwordx4 %[x1], s[68:69] offset:1024\n\t"
            "global_load_lds_dwordx4 %[x2], s[68:69] offset:2048\n\t"
            "global_load_lds_dwordx4 %[x3], s[68:69] offset:3072\n\t"
            "s_waitcnt vmcnt(8)\n\t"
            "s_lshl_b32 s76, s48, 2\n\t"
            ".Lzc7:\n\t"
            "v_cmp_eq_u32 vcc, v192, %[nvl]\n\t"
            "s_lshr_b64 s[74:75], vcc, s76\n\t"
            "s_and_b32 s74, s74, 15\n\t"
            "s_cmp_eq_u32 s74, 15\n\t"
            "s_cbranch_scc1 .Lzd7\n\t"
            "s_cmp_eq_u32 s73, 0\n\t"
            "s_cbranch_scc1 .Lzd7\n\t"
            "s_sub_u32 s73, s73, 1\n\t"
            "global_load_dword v192, %[po], %[ws] sc1\n\t"
            "s_waitcnt vmcnt(0)\n\t"
            "s_branch .Lzc7\n\t"
            ".Lzd7:\n\t"
            "s_add_u32 m0, %[ldsw], 32768\n\t"
            "s_mul_i32 s76, s48, 458752\n\t"
            "s_add_u32 s70, s84, s76\n\t"
            "s_addc_u32 s71, s85, 0\n\t"
            "global_load_lds_dwordx4 %[wv], s[70:71] offset:0 sc1\n\t"
            "global_load_lds_dwordx4 %[wv], s[70:71] offset:1024 sc1\n\t"
            "global_load_lds_dwordx4 %[wv], s[70:71] offset:2048 sc1\n\t"
            "global_load_lds_dwordx4 %[wv], s[70:71] offset:3072 sc1\n\t"
            "s_barrier\n\t"
            ".Lwp37:\n\t"
            "s_cbranch_execz .Lwp97\n\t"
            "s_lshl_b32 s76, s49, 2\n\t"
            ".Lzc8:\n\t"
            "v_cmp_eq_u32 vcc, v192, %[nvl]\n\t"
            "s_lshr_b64 s[74:75], vcc, s76\n\t"
            "s_and_b32 s74, s74, 15\n\t"
            "s_cmp_eq_u32 s74, 15\n\t"
            "s_cbranch_scc1 .Lzd8\n\t"
            "s_cmp_eq_u32 s73, 0\n\t"
            "s_cbranch_scc1 .Lzd8\n\t"
            "s_sub_u32 s73, s73, 1\n\t"
            "global_load_dword v192, %[po], %[ws] sc1\n\t"
            "s_waitcnt vmcnt(0)\n\t"
            "s_branch .Lzc8\n\t"
            ".Lzd8:\n\t"
            "s_add_u32 m0, %[ldsw], 0\n\t"
            "s_mul_i32 s76, s49, 458752\n\t"
            "s_add_u32 s70, s84, s76\n\t"
            "s_addc_u32 s71, s85, 0\n\t"
            "global_load_lds_dwordx4 %[wv], s[70:71] offset:0 sc1\n\t"
            "global_load_lds_dwordx4 %[wv], s[70:71] offset:1024 sc1\n\t"
            "global_load_lds_dwordx4 %[wv], s[70:71] offset:2048 sc1\n\t"
            "global_load_lds_dwordx4 %[wv], s[70:71] offset:3072 sc1\n\t"
            "s_setprio 0\n\t"
            "s_add_u32 m0, %[ldsx], 0\n\t"
            "s_lshl_b32 s76, s49, 7\n\t"
            "s_add_u32 s68, %[xpl], s76\n\t"
            "s_addc_u32 s69, %[xph], 0\n\t"
            "global_load_lds_dwordx4 %[x0], s[68:69] offset:0\n\t"
            "v_lshl_add_u32 v160, v204, 8, %[lutl]\n\t"
            "ds_read_u16 v160, v160 offset:0\n\t"
            "v_lshl_add_u32 v161, v208, 8, %[lutl]\n\t"
            "ds_read_u16 v161, v161 offset:0\n\t"
            "global_load_lds_dwordx4 %[x1], s[68:69] offset:1024\n\t"
            "v_lshl_add_u32 v162, v212, 8, %[lutl]\n\t"
            "ds_read_u16 v162, v162 offset:0\n\t"
            "v_lshl_add_u32 v163, v216, 8, %[lutl]\n\t"
            "ds_read_u16 v163, v163 offset:0\n\t"
            "global_load_lds_dwordx4 %[x2], s[68:69] offset:2048\n\t"
            "v_lshl_add_u32 v168, v205, 8, %[lutl]\n\t"
            "ds_read_u16 v168, v168 offset:2\n\t"
            "v_lshl_add_u32 v169, v209, 8, %[lutl]\n\t"
            "ds_read_u16 v169, v169 offset:2\n\t"
            "global_load_lds_dwordx4 %[x3], s[68:69] offset:3072\n\t"
            "v_lshl_add_u32 v170, v213, 8, %[lutl]\n\t"
            "ds_read_u16 v170, v170 offset:2\n\t"
            "v_lshl_add_u32 v171, v217, 8, %[lutl]\n\t"
            "ds_read_u16 v171, v171 offset:2\n\t"
            "s_add_u32 m0, %[ldsx], 4096\n\t"
            "s_lshl_b32 s76, s49, 7\n\t"
            "s_add_u32 s76, s76, 65536\n\t"
            "s_add_u32 s68, %[xpl], s76\n\t"
            "s_addc_u32 s69, %[xph], 0\n\t"
            "global_load_lds_dwordx4 %[x0], s[68:69] offset:0\n\t"
            "v_lshl_add_u32 v176, v206, 8, %[lutl]\n\t"
            "ds_read_u16 v176, v176 offset:128\n\t"
            "v_lshl_add_u32 v177, v210, 8, %[lutl]\n\t"
            "ds_read_u16 v177, v177 offset:128\n\t"
            "global_load_lds_dwordx4 %[x1], s[68:69] offset:1024\n\t"
            "v_lshl_add_u32 v178, v214, 8, %[lutl]\n\t"
            "ds_read_u16 v178, v178 offset:128\n\t"
            "v_lshl_add_u32 v179, v218, 8, %[lutl]\n\t"
            "ds_read_u16 v179, v179 offset:128\n\t"
            "global_load_lds_dwordx4 %[x2], s[68:69] offset:2048\n\t"
            "v_lshl_add_u32 v184, v207, 8, %[lutl]\n\t"
            "ds_read_u16 v184, v184 offset:130\n\t"
            "v_lshl_add_u32 v185, v211, 8, %[lutl]\n\t"
            "ds_read_u16 v185, v185 offset:130\n\t"
            "global_load_lds_dwordx4 %[x3], s[68:69] offset:3072\n\t"
            "v_lshl_add_u32 v186, v215, 8, %[lutl]\n\t"
            "ds_read_u16 v186, v186 offset:130\n\t"
            "v_lshl_add_u32 v187, v219, 8, %[lutl]\n\t"
            "ds_read_u16 v187, v187 offset:130\n\t"
            "s_setprio 0\n\t"
            "s_waitcnt lgkmcnt(8)\n\t"
            "v_lshl_or_b32 v220, v161, 16, v160\n\t"
            "v_lshl_or_b32 v221, v163, 16, v162\n\t"
            "v_lshl_or_b32 v222, v169, 16, v168\n\t"
            "v_lshl_or_b32 v223, v171, 16, v170\n\t"
            "s_waitcnt lgkmcnt(0)\n\t"
            "v_lshl_or_b32 v224, v177, 16, v176\n\t"
            "v_lshl_or_b32 v225, v179, 16, v178\n\t"
            "v_lshl_or_b32 v226, v185, 16, v184\n\t"
            "v_lshl_or_b32 v227, v187, 16, v186\n\t"
            "s_nop 0\n\t"
            "v_permlane32_swap_b32 v220, v222\n\t"
            "v_permlane32_swap_b32 v221, v223\n\t"
            "v_permlane32_swap_b32 v224, v226\n\t"
            "v_permlane32_swap_b32 v225, v227\n\t"
            "s_mov_b64 s[88:89], %[stb]\n\t"
            "s_add_u32 s88, s88, 1835008\n\t"
            "s_addc_u32 s89, s89, 0\n\t"
            "global_store_dwordx4 %[wo], v[220:223], s[88:89] offset:0 sc1\n\t"
            "global_store_dwordx4 %[wo], v[224:227], s[88:89] offset:512 sc1\n\t"
            "s_waitcnt vmcnt(14)\n\t"
            "s_barrier\n\t"
            ".Lwp38:\n\t"
            "s_cbranch_execz .Lwp39\n\t"
            "s_lshl_b32 s76, s50, 2\n\t"
            ".Lzc9:\n\t"
            "v_cmp_eq_u32 vcc, v192, %[nvl]\n\t"
            "s_lshr_b64 s[74:75], vcc, s76\n\t"
            "s_and_b32 s74, s74, 15\n\t"
            "s_cmp_eq_u32 s74, 15\n\t"
            "s_cbranch_scc1 .Lzd9\n\t"
            "s_cmp_eq_u32 s73, 0\n\t"
            "s_cbranch_scc1 .Lzd9\n\t"
            "s_sub_u32 s73, s73, 1\n\t"
            "global_load_dword v192, %[po], %[ws] sc1\n\t"
            "s_waitcnt vmcnt(0)\n\t"
            "s_branch .Lzc9\n\t"
            ".Lzd9:\n\t"
            "s_add_u32 m0, %[ldsw], 16384\n\t"
            "s_mul_i32 s76, s50, 458752\n\t"
            "s_add_u32 s70, s84, s76\n\t"
            "s_addc_u32 s71, s85, 0\n\t"
            "global_load_lds_dwordx4 %[wv], s[70:71] offset:0 sc1\n\t"
            "global_load_lds_dwordx4 %[wv], s[70:71] offset:1024 sc1\n\t"
            "global_load_lds_dwordx4 %[wv], s[70:71] offset:2048 sc1\n\t"
            "global_load_lds_dwordx4 %[wv], s[70:71] offset:3072 sc1\n\t"
            "s_add_u32 m0, %[ldsx], 32768\n\t"
            "s_lshl_b32 s76, s50, 7\n\t"
            "s_add_u32 s68, %[xpl], s76\n\t"
            "s_addc_u32 s69, %[xph], 0\n\t"
            "global_load_lds_dwordx4 %[x0], s[68:69] offset:0\n\t"
            "global_load_lds_dwordx4 %[x1], s[68:69] offset:1024\n\t"
            "global_load_lds_dwordx4 %[x2], s[68:69] offset:2048\n\t"
            "global_load_lds_dwordx4 %[x3], s[68:69] offset:3072\n\t"
            "s_add_u32 m0, %[ldsx], 36864\n\t"
            "s_lshl_b32 s76, s50, 7\n\t"
            "s_add_u32 s76, s76, 65536\n\t"
            "s_add_u32 s68, %[xpl], s76\n\t"
            "s_addc_u32 s69, %[xph], 0\n\t"
            "global_load_lds_dwordx4 %[x0], s[68:69] offset:0\n\t"
            "global_load_lds_dwordx4 %[x1], s[68:69] offset:1024\n\t"
            "global_load_lds_dwordx4 %[x2], s[68:69] offset:2048\n\t"
            "global_load_lds_dwordx4 %[x3], s[68:69] offset:3072\n\t"
            "s_waitcnt vmcnt(12)\n\t"
            "s_mov_b64 exec, 3\n\t"
            "global_store_dword %[fob], %[nvl], %[ws] offset:64 sc1\n\t"
            "s_mov_b64 exec, -1\n\t"
            "s_barrier\n\t"
            "s_lshl_b32 s76, s51, 2\n\t"
            ".Lzc10:\n\t"
            "v_cmp_eq_u32 vcc, v192, %[nvl]\n\t"
            "s_lshr_b64 s[74:75], vcc, s76\n\t"
            "s_and_b32 s74, s74, 15\n\t"
            "s_cmp_eq_u32 s74, 15\n\t"
            "s_cbranch_scc1 .Lzd10\n\t"
            "s_cmp_eq_u32 s73, 0\n\t"
            "s_cbranch_scc1 .Lzd10\n\t"
            "s_sub_u32 s73, s73, 1\n\t"
            "global_load_dword v192, %[po], %[ws] sc1\n\t"
            "s_waitcnt vmcnt(0)\n\t"
            "s_branch .Lzc10\n\t"
            ".Lzd10:\n\t"
            "s_add_u32 m0, %[ldsw], 32768\n\t"
            "s_mul_i32 s76, s51, 458752\n\t"
            "s_add_u32 s70, s84, s76\n\t"
            "s_addc_u32 s71, s85, 0\n\t"
            "global_load_lds_dwordx4 %[wv], s[70:71] offset:0 sc1\n\t"
            "global_load_lds_dwordx4 %[wv], s[70:71] offset:1024 sc1\n\t"
            "global_load_lds_dwordx4 %[wv], s[70:71] offset:2048 sc1\n\t"
            "global_load_lds_dwordx4 %[wv], s[70:71] offset:3072 sc1\n\t"
            "s_add_u32 m0, %[ldsx], 65536\n\t"
            "s_lshl_b32 s76, s51, 7\n\t"
            "s_add_u32 s68, %[xpl], s76\n\t"
            "s_addc_u32 s69, %[xph], 0\n\t"
            "global_load_lds_dwordx4 %[x0], s[68:69] offset:0\n\t"
            "global_load_lds_dwordx4 %[x1], s[68:69] offset:1024\n\t"
            "global_load_lds_dwordx4 %[x2], s[68:69] offset:2048\n\t"
            "global_load_lds_dwordx4 %[x3], s[68:69] offset:3072\n\t"
            "s_add_u32 m0, %[ldsx], 69632\n\t"
            "s_lshl_b32 s76, s51, 7\n\t"
            "s_add_u32 s76, s76, 65536\n\t"
            "s_add_u32 s68, %[xpl], s76\n\t"
            "s_addc_u32 s69, %[xph], 0\n\t"
            "global_load_lds_dwordx4 %[x0], s[68:69] offset:0\n\t"
            "global_load_lds_dwordx4 %[x1], s[68:69] offset:1024\n\t"
            "global_load_lds_dwordx4 %[x2], s[68:69] offset:2048\n\t"
            "global_load_lds_dwordx4 %[x3], s[68:69] offset:3072\n\t"
            "s_waitcnt vmcnt(13)\n\t"
            "s_barrier\n\t"
            ".Lwp39:\n\t"
            "s_cbranch_execz .Lwp40\n\t"
            "global_load_dword v192, %[po], %[ws] sc1\n\t"
            "s_add_u32 m0, %[ldsx], 0\n\t"
            "s_lshl_b32 s76, s52, 7\n\t"
            "s_add_u32 s68, %[xpl], s76\n\t"
            "s_addc_u32 s69, %[xph], 0\n\t"
            "global_load_lds_dwordx4 %[x0], s[68:69] offset:0\n\t"
            "global_load_lds_dwordx4 %[x1], s[68:69] offset:1024\n\t"
            "global_load_lds_dwordx4 %[x2], s[68:69] offset:2048\n\t"
            "global_load_lds_dwordx4 %[x3], s[68:69] offset:3072\n\t"
            "s_add_u32 m0, %[ldsx], 4096\n\t"
            "s_lshl_b32 s76, s52, 7\n\t"
            "s_add_u32 s76, s76, 65536\n\t"
            "s_add_u32 s68, %[xpl], s76\n\t"
            "s_addc_u32 s69, %[xph], 0\n\t"
            "global_load_lds_dwordx4 %[x0], s[68:69] offset:0\n\t"
            "global_load_lds_dwordx4 %[x1], s[68:69] offset:1024\n\t"
            "global_load_lds_dwordx4 %[x2], s[68:69] offset:2048\n\t"
            "global_load_lds_dwordx4 %[x3], s[68:69] offset:3072\n\t"
            "s_waitcnt vmcnt(8)\n\t"
            "s_lshl_b32 s76, s52, 2\n\t"
            ".Lzc11:\n\t"
            "v_cmp_eq_u32 vcc, v192, %[nvl]\n\t"
            "s_lshr_b64 s[74:75], vcc, s76\n\t"
            "s_and_b32 s74, s74, 15\n\t"
            "s_cmp_eq_u32 s74, 15\n\t"
            "s_cbranch_scc1 .Lzd11\n\t"
            "s_cmp_eq_u32 s73, 0\n\t"
            "s_cbranch_scc1 .Lzd11\n\t"
            "s_sub_u32 s73, s73, 1\n\t"
            "global_load_dword v192, %[po], %[ws] sc1\n\t"
            "s_waitcnt vmcnt(0)\n\t"
            "s_branch .Lzc11\n\t"
            ".Lzd11:\n\t"
            "s_add_u32 m0, %[ldsw], 0\n\t"
            "s_mul_i32 s76, s52, 458752\n\t"
            "s_add_u32 s70, s84, s76\n\t"
            "s_addc_u32 s71, s85, 0\n\t"
            "global_load_lds_dwordx4 %[wv], s[70:71] offset:0 sc1\n\t"
            "global_load_lds_dwordx4 %[wv], s[70:71] offset:1024 sc1\n\t"
            "global_load_lds_dwordx4 %[wv], s[70:71] offset:2048 sc1\n\t"
            "global_load_lds_dwordx4 %[wv], s[70:71] offset:3072 sc1\n\t"
            "s_barrier\n\t"
            "s_mov_b32 s76, 48\n\t"
            ".Lzc12:\n\t"
            "v_cmp_eq_u32 vcc, v192, %[nvl]\n\t"
            "s_lshr_b64 s[74:75], vcc, s76\n\t"
            "s_and_b32 s74, s74, 65535\n\t"
            "s_cmp_eq_u32 s74, 65535\n\t"
            "s_cbranch_scc1 .Lzd12\n\t"
            "s_cmp_eq_u32 s73, 0\n\t"
            "s_cbranch_scc1 .Lzd12\n\t"
            "s_sub_u32 s73, s73, 1\n\t"
            "global_load_dword v192, %[po], %[ws] sc1\n\t"
            "s_waitcnt vmcnt(0)\n\t"
            "s_branch .Lzc12\n\t"
            ".Lzd12:\n\t"
            "s_add_u32 m0, %[ldsw], 16384\n\t"
            "s_mul_i32 s76, s53, 458752\n\t"
            "s_add_u32 s70, s84, s76\n\t"
            "s_addc_u32 s71, s85, 0\n\t"
            "s_mul_i32 s76, s54, 458752\n\t"
            "s_sub_u32 s76, s76, 1024\n\t"
            "s_add_u32 s86, s84, s76\n\t"
            "s_addc_u32 s87, s85, 0\n\t"
            "global_load_lds_dwordx4 %[wv], s[70:71] offset:0 sc1\n\t"
            "global_load_lds_dwordx4 %[wv], s[86:87] offset:1024 sc1\n\t"
            "global_load_lds_dwordx4 %[wv], s[70:71] offset:2048 sc1\n\t"
            "global_load_lds_dwordx4 %[wv], s[86:87] offset:3072 sc1\n\t"
            "s_add_u32 m0, %[ldsx], 32768\n\t"
            "s_lshl_b32 s76, s53, 7\n\t"
            "s_add_u32 s68, %[xpl], s76\n\t"
            "s_addc_u32 s69, %[xph], 0\n\t"
            "global_load_lds_dwordx4 %[x0], s[68:69] offset:0\n\t"
            "global_load_lds_dwordx4 %[x1], s[68:69] offset:1024\n\t"
            "global_load_lds_dwordx4 %[x2], s[68:69] offset:2048\n\t"
            "global_load_lds_dwordx4 %[x3], s[68:69] offset:3072\n\t"
            "s_add_u32 m0, %[ldsx], 36864\n\t"
            "s_lshl_b32 s76, s53, 7\n\t"
            "s_add_u32 s76, s76, 65536\n\t"
            "s_add_u32 s68, %[xpl], s76\n\t"
            "s_addc_u32 s69, %[xph], 0\n\t"
            "global_load_lds_dwordx4 %[x0], s[68:69] offset:0\n\t"
            "global_load_lds_dwordx4 %[x1], s[68:69] offset:1024\n\t"
            "global_load_lds_dwordx4 %[x2], s[68:69] offset:2048\n\t"
            "global_load_lds_dwordx4 %[x3], s[68:69] offset:3072\n\t"
            "s_waitcnt vmcnt(12)\n\t"
            "s_barrier\n\t"
            ".Lwp40:\n\t"
            "s_cbranch_execz .Lwp97\n\t"
            "s_add_u32 m0, %[ldsx], 65536\n\t"
            "s_lshl_b32 s76, s54, 7\n\t"
            "s_add_u32 s68, %[xpl], s76\n\t"
            "s_addc_u32 s69, %[xph], 0\n\t"
            "global_load_lds_dwordx4 %[x0], s[68:69] offset:0\n\t"
            "global_load_lds_dwordx4 %[x1], s[68:69] offset:1024\n\t"
            "global_load_lds_dwordx4 %[x2], s[68:69] offset:2048\n\t"
            "global_load_lds_dwordx4 %[x3], s[68:69] offset:3072\n\t"
            "s_add_u32 m0, %[ldsx], 69632\n\t"
            "s_lshl_b32 s76, s54, 7\n\t"
            "s_add_u32 s76, s76, 65536\n\t"
            "s_add_u32 s68, %[xpl], s76\n\t"
            "s_addc_u32 s69, %[xph], 0\n\t"
            "global_load_lds_dwordx4 %[x0], s[68:69] offset:0\n\t"
            "global_load_lds_dwordx4 %[x1], s[68:69] offset:1024\n\t"
            "global_load_lds_dwordx4 %[x2], s[68:69] offset:2048\n\t"
            "global_load_lds_dwordx4 %[x3], s[68:69] offset:3072\n\t"
            "s_add_u32 m0, %[ldsw], 32768\n\t"
            "s_mul_i32 s76, s55, 458752\n\t"
            "s_add_u32 s70, s84, s76\n\t"
            "s_addc_u32 s71, s85, 0\n\t"
            "s_mul_i32 s76, s53, 458752\n\t"
            "s_add_u32 s86, s84, s76\n\t"
            "s_addc_u32 s87, s85, 0\n\t"
            "global_load_lds_dwordx4 %[wv], s[70:71] offset:0 sc1\n\t"
            "global_load_lds_dwordx4 %[wv], s[86:87] offset:1024 sc1\n\t"
            "global_load_lds_dwordx4 %[wv], s[70:71] offset:2048 sc1\n\t"
            "global_load_lds_dwordx4 %[wv], s[86:87] offset:3072 sc1\n\t"
            "s_waitcnt vmcnt(4)\n\t"
            "s_barrier\n\t"
            "s_add_u32 m0, %[ldsx], 0\n\t"
            "s_lshl_b32 s76, s55, 7\n\t"
            "s_add_u32 s68, %[xpl], s76\n\t"
            "s_addc_u32 s69, %[xph], 0\n\t"
            "global_load_lds_dwordx4 %[x0], s[68:69] offset:0\n\t"
            "global_load_lds_dwordx4 %[x1], s[68:69] offset:1024\n\t"
            "global_load_lds_dwordx4 %[x2], s[68:69] offset:2048\n\t"
            "global_load_lds_dwordx4 %[x3], s[68:69] offset:3072\n\t"
            "s_add_u32 m0, %[ldsx], 4096\n\t"
            "s_lshl_b32 s76, s55, 7\n\t"
            "s_add_u32 s76, s76, 65536\n\t"
            "s_add_u32 s68, %[xpl], s76\n\t"
            "s_addc_u32 s69, %[xph], 0\n\t"
            "global_load_lds_dwordx4 %[x0], s[68:69] offset:0\n\t"
            "global_load_lds_dwordx4 %[x1], s[68:69] offset:1024\n\t"
            "global_load_lds_dwordx4 %[x2], s[68:69] offset:2048\n\t"
            "global_load_lds_dwordx4 %[x3], s[68:69] offset:3072\n\t"
            "s_add_u32 m0, %[ldsw], 0\n\t"
            "s_mul_i32 s76, s54, 458752\n\t"
            "s_add_u32 s76, s76, 1024\n\t"
            "s_add_u32 s70, s84, s76\n\t"
            "s_addc_u32 s71, s85, 0\n\t"
            "s_mul_i32 s76, s55, 458752\n\t"
            "s_add_u32 s86, s84, s76\n\t"
            "s_addc_u32 s87, s85, 0\n\t"
            "global_load_lds_dwordx4 %[wv], s[70:71] offset:0 sc1\n\t"
            "global_load_lds_dwordx4 %[wv], s[86:87] offset:1024 sc1\n\t"
            "global_load_lds_dwordx4 %[wv], s[70:71] offset:2048 sc1\n\t"
            "global_load_lds_dwordx4 %[wv], s[86:87] offset:3072 sc1\n\t"
            "s_waitcnt vmcnt(4)\n\t"
            "s_barrier\n\t"
            ".Lwp41:\n\t"
            "s_cbranch_execz .Lwp14\n\t"
            "s_mov_b32 s76, 16\n\t"
            ".Lzc13:\n\t"
            "v_cmp_eq_u32 vcc, v192, %[nvl]\n\t"
            "s_lshr_b64 s[74:75], vcc, s76\n\t"
            "s_and_b32 s74, s74, 65535\n\t"
            "s_cmp_eq_u32 s74, 65535\n\t"
            "s_cbranch_scc1 .Lzd13\n\t"
            "s_cmp_eq_u32 s73, 0\n\t"
            "s_cbranch_scc1 .Lzd13\n\t"
            "s_sub_u32 s73, s73, 1\n\t"
            "global_load_dword v192, %[po], %[ws] sc1\n\t"
            "s_waitcnt vmcnt(0)\n\t"
            "s_branch .Lzc13\n\t"
            ".Lzd13:\n\t"
            "s_waitcnt vmcnt(0)\n\t"
            "s_barrier\n\t"
            : [nvl] "+v"(nvl), [qo] "+v"(qoff[0]), [wo] "+v"(wo)
            : [x0] "v"(xoff[0]), [x1] "v"(xoff[1]), [x2] "v"(xoff[2]), [x3] "v"(xoff[3]), [wv] "v"(wv), [po] "v"(po),
              [lutl] "v"(lutl), [fo0] "v"(fo0), [fob] "v"(fob), [bd] "v"(bdst),
              [xpl] "s"((u32)KPTR(0)), [xph] "s"((u32)(KPTR(0) >> 32)), [ws] "s"(KPTR(4)),
              [qbg] "s"(qbg), [qa1] "s"(qa1), [tseq] "s"(tseq), [p] "s"(p), [h2] "s"(h2), [ldsx] "s"(ldsx), [ldsw] "s"(ldsw), [st1] "s"(st1), [st1l] "s"((u32)(unsigned long)st1), [st1h] "s"((u32)((unsigned long)st1 >> 32)), [stb] "s"(stb)
            : "vcc", "scc", "memory", "s40", "s41", "s42", "s43", "s44", "s45", "s46", "s47", "s48", "s49", "s50", "s51", "s52", "s53", "s54", "s55", "s56", "s57", "s58", "s59", "s60", "s61", "s62", "s63", "s64", "s65", "s66", "s67", "s68", "s69", "s70", "s71", "s72", "s73", "s74", "s75", "s76", "s77", "s78", "s79", "s80", "s81", "s82", "s83", "s84", "s85", "s86", "s87", "s88", "s89", "v128", "v129", "v130", "v131", "v132", "v133", "v134", "v135", "v136", "v137", "v138", "v139", "v140", "v141", "v142", "v143", "v144", "v145", "v146", "v147", "v148", "v149", "v150", "v151", "v152", "v153", "v154", "v155", "v156", "v157", "v158", "v159", "v160", "v161", "v162", "v163", "v164", "v165", "v166", "v167", "v168", "v169", "v170", "v171", "v172", "v173", "v174", "v175", "v176", "v177", "v178", "v179", "v180", "v181", "v182", "v183", "v184", "v185", "v186", "v187", "v188", "v189", "v190", "v191", "v192", "v193", "v194", "v195", "v196", "v197", "v198", "v199", "v200", "v201", "v202", "v203", "v204", "v205", "v206", "v207", "v208", "v209", "v210", "v211", "v212", "v213", "v214", "v215", "v216", "v217", "v218", "v219", "v220", "v221", "v222", "v223", "v224", "v225", "v226", "v227", "v228", "v229", "v230", "v231", "v232", "v233", "v234", "v235");
        if (mt == 0 && p == 0 && lane < 2) asm volatile("global_store_dword %0, %1, %2 sc1" :: "v"(no), "v"(nvl), "s"(KPTR(4)) : "memory");
        return;
    }

    u32 oo[4];
    unsigned long ob[4];

    int xb0 = 0;
    int xb1 = 0;
    int ba;
#define LDX(a) (*(const bf16x8*)(smem + (a)))
#define MP(i, j, x0, w0, x1, w1) do { asm volatile("v_mfma_f32_16x16x32_bf16 %0, %1, %2, %0\n\tv_mfma_f32_16x16x32_bf16 %0, %3, %4, %0" \
                                      : "+v"(acc[i][j]) : "v"(x0), "v"(w0), "v"(x1), "v"(w1)); SB(); } while (0)
#define LD(d, a) do { d = LDX(a); SB(); } while (0)
#define CBAR() do { asm volatile("s_waitcnt lgkmcnt(0)" ::: "memory"); BAR(); asm volatile("" ::: "memory"); } while (0)

    {
        u32 V[4][8];
        i32x4 Q0[8];
#define PK(b_, j, n) asm volatile("s_waitcnt lgkmcnt(" #n ")\n\t" \
                                    "v_lshl_or_b32 %0, %5, 16, %4\n\tv_lshl_or_b32 %1, %7, 16, %6\n\t" \
                                    "v_lshl_or_b32 %2, %9, 16, %8\n\tv_lshl_or_b32 %3, %11, 16, %10" \
                                    : "=&v"(b_[0]), "=&v"(b_[1]), "=&v"(b_[2]), "=&v"(b_[3]) \
                                    : "v"(V[j][0]), "v"(V[j][1]), "v"(V[j][2]), "v"(V[j][3]), "v"(V[j][4]), "v"(V[j][5]), "v"(V[j][6]), "v"(V[j][7]) : "memory")
#define WR(b_, j) asm volatile("ds_write_b128 %0, %1 offset:%2" :: "v"(bdst), "v"(b_), "i"((j) * 256) : "memory")
        {
            const u32 l = tid & 31, h = (tid >> 5) & 1;
            const u32 lo = l * 8 + h * 4, qv = h * (8 * N_ * 4) + l * 16;
            const u32 ls = (u32)((w * 4 * N_ + col0) * 2), qs = (u32)(((OWN(mt, nt) * 64 + w * 16) * N_ + col0) * 4);
            const u32 ld = (u32)(size_t)(smem) + OFF_L + w * 1024;
            asm volatile("s_mov_b32 m0, %[ld]\n\t"
                         "s_waitcnt lgkmcnt(0)\n\t"
                         "s_mov_b64 s[60:61], %[lp]\n\ts_add_u32 s60, s60, %[ls]\n\ts_addc_u32 s61, s61, 0\n\t"
                         "s_mov_b64 s[62:63], %[qp]\n\ts_add_u32 s62, s62, %[qs]\n\ts_addc_u32 s63, s63, 0\n\t"
                         "global_load_lds_dword %[lo], s[60:61] offset:0\n\t"
                         "s_add_u32 s60, s60, 6912\n\ts_addc_u32 s61, s61, 0\n\t"
                         "global_load_lds_dword %[lo], s[60:61] offset:256\n\t"
                         "s_add_u32 s60, s60, 6912\n\ts_addc_u32 s61, s61, 0\n\t"
                         "global_load_lds_dword %[lo], s[60:61] offset:512\n\t"
                         "s_add_u32 s60, s60, 6912\n\ts_addc_u32 s61, s61, 0\n\t"
                         "global_load_lds_dword %[lo], s[60:61] offset:768\n\t"
                         "global_load_dwordx4 %[q0], %[qv], s[62:63]\n\t"
                         "s_add_u32 s62, s62, 14336\n\ts_addc_u32 s63, s63, 0\n\t"
                         "global_load_dwordx4 %[q1], %[qv], s[62:63]\n\t"
                         "s_add_u32 s62, s62, 14336\n\ts_addc_u32 s63, s63, 0\n\t"
                         "global_load_dwordx4 %[q2], %[qv], s[62:63]\n\t"
                         "s_add_u32 s62, s62, 14336\n\ts_addc_u32 s63, s63, 0\n\t"
                         "global_load_dwordx4 %[q3], %[qv], s[62:63]\n\t"
                         "s_add_u32 s62, s62, 14336\n\ts_addc_u32 s63, s63, 0\n\t"
                         "global_load_dwordx4 %[q4], %[qv], s[62:63]\n\t"
                         "s_add_u32 s62, s62, 14336\n\ts_addc_u32 s63, s63, 0\n\t"
                         "global_load_dwordx4 %[q5], %[qv], s[62:63]\n\t"
                         "s_add_u32 s62, s62, 14336\n\ts_addc_u32 s63, s63, 0\n\t"
                         "global_load_dwordx4 %[q6], %[qv], s[62:63]\n\t"
                         "s_add_u32 s62, s62, 14336\n\ts_addc_u32 s63, s63, 0\n\t"
                         "global_load_dwordx4 %[q7], %[qv], s[62:63]\n\t"
                         : [q0] "=&v"(Q0[0]), [q1] "=&v"(Q0[1]), [q2] "=&v"(Q0[2]), [q3] "=&v"(Q0[3]),
                           [q4] "=&v"(Q0[4]), [q5] "=&v"(Q0[5]), [q6] "=&v"(Q0[6]), [q7] "=&v"(Q0[7])
                         : [lo] "v"(lo), [qv] "v"(qv), [ls] "s"(ls), [qs] "s"(qs), [ld] "s"(ld), [lp] "s"(ka[2]), [qp] "s"(ka[1])
                         : "s60", "s61", "s62", "s63", "scc", "memory");
            SB();
        }
        KWAIT();
        int ln = lane;
        asm volatile("" : "+v"(ln));
        const int c = ln & 15, g = ln >> 4, l = ln & 31, h = ln >> 5;
        xb0 = (w * 64 + c) * 128 + ((g ^ (c >> 1)) * 16);
        xb1 = xb0 ^ 64;
        ba = OFF_B + g * 2048 + c * 16;
        u32 lutl = (u32)(size_t)(smem) + OFF_L + l * 4;
        u32 bdst = (u32)(size_t)(smem) + OFF_B + w * 4096 + h * 2048 + ((l >> 4) * 64 + (l & 15)) * 16;
#pragma unroll
        for (int r = 0; r < 4; ++r) {
            oo[r] = (u32)(((row0 + w * 64 + g * 4 + r) * N_ + col0 + c * 4) * 2);
            ob[r] = KPTR(3) + (unsigned long)(r * 16 * N_ * 2);
            asm volatile("" : "+v"(oo[r]), "+s"(ob[r]));
        }
        asm volatile("" : "+v"(xb0), "+v"(xb1), "+v"(ba));
        asm volatile("" : "+v"(bdst), "+v"(lutl));
        {
            unsigned long ex_;
            asm volatile("s_mov_b64 %0, exec\n\ts_mov_b64 exec, 0\n\t"
                         "s_cmp_eq_u32 %1, 0\n\ts_cbranch_scc1 .Lwp20\n\t"
                         "s_cmp_eq_u32 %1, 1\n\ts_cbranch_scc1 .Lwp11\n\t"
                         "s_cmp_eq_u32 %1, 2\n\ts_cbranch_scc1 .Lwp12\n\t"
                         "s_branch .Lwp13\n\t"
                         ".Lwp99:\n\ts_mov_b64 exec, %0" : "=&s"(ex_) : "s"(w) : "scc", "memory");
        }
        asm volatile("s_waitcnt vmcnt(8)" ::: "memory");
        BAR();
        PAD(20, 10);
        asm volatile("s_waitcnt vmcnt(7)" : "+v"(Q0[0]) :: "memory"); LUT1(Q0, 0, 0); LUT1(Q0, 1, 0); LUT1(Q0, 2, 0); LUT1(Q0, 3, 0); SB();
        asm volatile("s_waitcnt vmcnt(6)" : "+v"(Q0[1]) :: "memory"); LUT1(Q0, 0, 1); LUT1(Q0, 1, 1); LUT1(Q0, 2, 1); LUT1(Q0, 3, 1); SB();
        asm volatile("s_waitcnt vmcnt(5)" : "+v"(Q0[2]) :: "memory"); LUT1(Q0, 0, 2); LUT1(Q0, 1, 2); LUT1(Q0, 2, 2); LUT1(Q0, 3, 2); SB();
        asm volatile("s_waitcnt vmcnt(4)" : "+v"(Q0[3]) :: "memory"); LUT1(Q0, 0, 3); LUT1(Q0, 1, 3); LUT1(Q0, 2, 3); LUT1(Q0, 3, 3); SB();
        asm volatile("s_waitcnt vmcnt(3)" : "+v"(Q0[4]) :: "memory"); LUT1(Q0, 0, 4); LUT1(Q0, 1, 4); LUT1(Q0, 2, 4); LUT1(Q0, 3, 4); SB();
        asm volatile("s_waitcnt vmcnt(2)" : "+v"(Q0[5]) :: "memory"); LUT1(Q0, 0, 5); LUT1(Q0, 1, 5); LUT1(Q0, 2, 5); LUT1(Q0, 3, 5); SB();
        asm volatile("s_waitcnt vmcnt(1)" : "+v"(Q0[6]) :: "memory"); LUT1(Q0, 0, 6); LUT1(Q0, 1, 6); LUT1(Q0, 2, 6); LUT1(Q0, 3, 6); SB();
        asm volatile("s_waitcnt vmcnt(0)" : "+v"(Q0[7]) :: "memory"); LUT1(Q0, 0, 7); LUT1(Q0, 1, 7); LUT1(Q0, 2, 7); LUT1(Q0, 3, 7); SB();
        {   u32x4 b0, b1, b2, b3;
            PK(b0, 0, 3); WR(b0, 0); PK(b1, 1, 3); WR(b1, 1); PK(b2, 2, 3); WR(b2, 2); PK(b3, 3, 3); WR(b3, 3);
            SB();
        }
        asm volatile("s_waitcnt lgkmcnt(0)" ::: "memory");
        BAR();
    }
    asm volatile(
        "v_mov_b32 v232, %[xb0]\n\t"
        "v_mov_b32 v233, %[xb1]\n\t"
        "v_mov_b32 v234, %[ba]\n\t"
        ".Lwp10:\n\t"
        "s_cbranch_execz .Lwp99\n\t"
        "ds_read_b128 v[192:195], v234 offset:0\n\t"
        "ds_read_b128 v[128:131], v232 offset:0\n\t"
        "ds_read_b128 v[196:199], v234 offset:8192\n\t"
        "ds_read_b128 v[144:147], v233 offset:0\n\t"
        "ds_read_b128 v[204:207], v234 offset:8448\n\t"
        "ds_read_b128 v[200:203], v234 offset:256\n\t"
        "ds_read_b128 v[132:135], v232 offset:2048\n\t"
        "ds_read_b128 v[148:151], v233 offset:2048\n\t"
        "s_waitcnt lgkmcnt(6)\n\t"
        "v_mfma_f32_16x16x32_bf16 v[0:3], v[128:131], v[192:195], 0\n\t"
        "s_waitcnt lgkmcnt(4)\n\t"
        "v_mfma_f32_16x16x32_bf16 v[0:3], v[144:147], v[196:199], v[0:3]\n\t"
        "ds_read_b128 v[136:139], v232 offset:4096\n\t"
        "s_waitcnt lgkmcnt(4)\n\t"
        "v_mfma_f32_16x16x32_bf16 v[4:7], v[144:147], v[204:207], 0\n\t"
        "s_waitcnt lgkmcnt(3)\n\t"
        "v_mfma_f32_16x16x32_bf16 v[4:7], v[128:131], v[200:203], v[4:7]\n\t"
        "ds_read_b128 v[152:155], v233 offset:4096\n\t"
        "s_waitcnt lgkmcnt(3)\n\t"
        "v_mfma_f32_16x16x32_bf16 v[36:39], v[132:135], v[200:203], 0\n\t"
        "s_waitcnt lgkmcnt(2)\n\t"
        "v_mfma_f32_16x16x32_bf16 v[36:39], v[148:151], v[204:207], v[36:39]\n\t"
        "ds_read_b128 v[140:143], v232 offset:6144\n\t"
        "v_mfma_f32_16x16x32_bf16 v[32:35], v[148:151], v[196:199], 0\n\t"
        "v_mfma_f32_16x16x32_bf16 v[32:35], v[132:135], v[192:195], v[32:35]\n\t"
        "ds_read_b128 v[156:159], v233 offset:6144\n\t"
        "s_waitcnt lgkmcnt(3)\n\t"
        "v_mfma_f32_16x16x32_bf16 v[64:67], v[136:139], v[192:195], 0\n\t"
        "s_waitcnt lgkmcnt(2)\n\t"
        "v_mfma_f32_16x16x32_bf16 v[64:67], v[152:155], v[196:199], v[64:67]\n\t"
        "ds_read_b128 v[208:211], v234 offset:512\n\t"
        "v_mfma_f32_16x16x32_bf16 v[68:71], v[152:155], v[204:207], 0\n\t"
        "v_mfma_f32_16x16x32_bf16 v[68:71], v[136:139], v[200:203], v[68:71]\n\t"
        "ds_read_b128 v[212:215], v234 offset:8704\n\t"
        "s_waitcnt lgkmcnt(3)\n\t"
        "v_mfma_f32_16x16x32_bf16 v[100:103], v[140:143], v[200:203], 0\n\t"
        "s_waitcnt lgkmcnt(2)\n\t"
        "v_mfma_f32_16x16x32_bf16 v[100:103], v[156:159], v[204:207], v[100:103]\n\t"
        "ds_read_b128 v[220:223], v234 offset:8960\n\t"
        "v_mfma_f32_16x16x32_bf16 v[96:99], v[156:159], v[196:199], 0\n\t"
        "v_mfma_f32_16x16x32_bf16 v[96:99], v[140:143], v[192:195], v[96:99]\n\t"
        "ds_read_b128 v[216:219], v234 offset:768\n\t"
        "s_waitcnt lgkmcnt(3)\n\t"
        "v_mfma_f32_16x16x32_bf16 v[8:11], v[128:131], v[208:211], 0\n\t"
        "s_waitcnt lgkmcnt(2)\n\t"
        "v_mfma_f32_16x16x32_bf16 v[8:11], v[144:147], v[212:215], v[8:11]\n\t"
        "ds_read_b128 v[192:195], v234 offset:1024\n\t"
        "s_waitcnt lgkmcnt(2)\n\t"
        "v_mfma_f32_16x16x32_bf16 v[12:15], v[144:147], v[220:223], 0\n\t"
        "s_waitcnt lgkmcnt(1)\n\t"
        "v_mfma_f32_16x16x32_bf16 v[12:15], v[128:131], v[216:219], v[12:15]\n\t"
        "ds_read_b128 v[196:199], v234 offset:9216\n\t"
        "v_mfma_f32_16x16x32_bf16 v[44:47], v[132:135], v[216:219], 0\n\t"
        "v_mfma_f32_16x16x32_bf16 v[44:47], v[148:151], v[220:223], v[44:47]\n\t"
        "ds_read_b128 v[204:207], v234 offset:9472\n\t"
        "v_mfma_f32_16x16x32_bf16 v[40:43], v[148:151], v[212:215], 0\n\t"
        "v_mfma_f32_16x16x32_bf16 v[40:43], v[132:135], v[208:211], v[40:43]\n\t"
        "ds_read_b128 v[200:203], v234 offset:1280\n\t"
        "v_mfma_f32_16x16x32_bf16 v[72:75], v[136:139], v[208:211], 0\n\t"
        "v_mfma_f32_16x16x32_bf16 v[72:75], v[152:155], v[212:215], v[72:75]\n\t"
        "v_mfma_f32_16x16x32_bf16 v[76:79], v[152:155], v[220:223], 0\n\t"
        "v_mfma_f32_16x16x32_bf16 v[76:79], v[136:139], v[216:219], v[76:79]\n\t"
        "v_mfma_f32_16x16x32_bf16 v[108:111], v[140:143], v[216:219], 0\n\t"
        "v_mfma_f32_16x16x32_bf16 v[108:111], v[156:159], v[220:223], v[108:111]\n\t"
        "v_mfma_f32_16x16x32_bf16 v[104:107], v[156:159], v[212:215], 0\n\t"
        "v_mfma_f32_16x16x32_bf16 v[104:107], v[140:143], v[208:211], v[104:107]\n\t"
        "s_waitcnt lgkmcnt(0)\n\t"
        "v_mfma_f32_16x16x32_bf16 v[16:19], v[128:131], v[192:195], 0\n\t"
        "v_mfma_f32_16x16x32_bf16 v[16:19], v[144:147], v[196:199], v[16:19]\n\t"
        "ds_read_b128 v[208:211], v234 offset:1536\n\t"
        "v_mfma_f32_16x16x32_bf16 v[20:23], v[144:147], v[204:207], 0\n\t"
        "v_mfma_f32_16x16x32_bf16 v[20:23], v[128:131], v[200:203], v[20:23]\n\t"
        "ds_read_b128 v[212:215], v234 offset:9728\n\t"
        "v_mfma_f32_16x16x32_bf16 v[52:55], v[132:135], v[200:203], 0\n\t"
        "v_mfma_f32_16x16x32_bf16 v[52:55], v[148:151], v[204:207], v[52:55]\n\t"
        "ds_read_b128 v[220:223], v234 offset:9984\n\t"
        "v_mfma_f32_16x16x32_bf16 v[48:51], v[148:151], v[196:199], 0\n\t"
        "v_mfma_f32_16x16x32_bf16 v[48:51], v[132:135], v[192:195], v[48:51]\n\t"
        "ds_read_b128 v[216:219], v234 offset:1792\n\t"
        "v_mfma_f32_16x16x32_bf16 v[80:83], v[136:139], v[192:195], 0\n\t"
        "v_mfma_f32_16x16x32_bf16 v[80:83], v[152:155], v[196:199], v[80:83]\n\t"
        "v_mfma_f32_16x16x32_bf16 v[84:87], v[152:155], v[204:207], 0\n\t"
        "v_mfma_f32_16x16x32_bf16 v[84:87], v[136:139], v[200:203], v[84:87]\n\t"
        "s_mov_b32 s60, 32768\n\t"
        "s_mov_b32 s62, 5\n\t"
        "v_add_u32 v232, 32768, %[xb0]\n\t"
        "v_add_u32 v233, 32768, %[xb1]\n\t"
        "v_add_u32 v234, 16384, %[ba]\n\t"
        "v_mov_b64 v[24:25], 0\n\t"
        "v_mov_b64 v[26:27], 0\n\t"
        "v_mov_b64 v[28:29], 0\n\t"
        "v_mov_b64 v[30:31], 0\n\t"
        "v_mov_b64 v[56:57], 0\n\t"
        "v_mov_b64 v[58:59], 0\n\t"
        "v_mov_b64 v[60:61], 0\n\t"
        "v_mov_b64 v[62:63], 0\n\t"
        "v_mov_b64 v[88:89], 0\n\t"
        "v_mov_b64 v[90:91], 0\n\t"
        "v_mov_b64 v[92:93], 0\n\t"
        "v_mov_b64 v[94:95], 0\n\t"
        "v_mov_b64 v[120:121], 0\n\t"
        "v_mov_b64 v[122:123], 0\n\t"
        "v_mov_b64 v[124:125], 0\n\t"
        "v_mov_b64 v[126:127], 0\n\t"
        "v_mov_b64 v[116:117], 0\n\t"
        "v_mov_b64 v[118:119], 0\n\t"
        "v_mov_b64 v[112:113], 0\n\t"
        "v_mov_b64 v[114:115], 0\n\t"
        "s_nop 1\n\t"
        "s_mov_b64 exec, 0\n\t"
        "s_cmp_eq_u32 %[w], 0\n\t"
        "s_cbranch_scc1 .Lwp34\n\t"
        "s_cmp_eq_u32 %[w], 1\n\t"
        "s_cbranch_scc1 .Lwp38\n\t"
        "s_cmp_eq_u32 %[w], 2\n\t"
        "s_cbranch_scc1 .Lwp41\n\t"
        "s_branch .Lwp16\n\t"
        ".Lwp97:\n\t"
        "s_mov_b64 exec, -1\n\t"
        ".Lcloop:\n\t"
        "v_mfma_f32_16x16x32_bf16 v[116:119], v[140:143], v[200:203], v[116:119]\n\t"
        ".Lwp11:\n\t"
        "s_cbranch_execz .Lwp99\n\t"
        "s_sub_u32 s62, s62, 1\n\t"
        "v_mfma_f32_16x16x32_bf16 v[116:119], v[156:159], v[204:207], v[116:119]\n\t"
        "s_waitcnt lgkmcnt(0)\n\t"
        "v_mfma_f32_16x16x32_bf16 v[112:115], v[156:159], v[196:199], v[112:115]\n\t"
        "v_mfma_f32_16x16x32_bf16 v[112:115], v[140:143], v[192:195], v[112:115]\n\t"
        "s_barrier\n\t"
        "v_mfma_f32_16x16x32_bf16 v[24:27], v[128:131], v[208:211], v[24:27]\n\t"
        "v_mfma_f32_16x16x32_bf16 v[24:27], v[144:147], v[212:215], v[24:27]\n\t"
        "ds_read_b128 v[192:195], v234 offset:0\n\t"
        "ds_read_b128 v[160:163], v232 offset:0\n\t"
        "v_mfma_f32_16x16x32_bf16 v[28:31], v[144:147], v[220:223], v[28:31]\n\t"
        "v_mfma_f32_16x16x32_bf16 v[28:31], v[128:131], v[216:219], v[28:31]\n\t"
        "ds_read_b128 v[196:199], v234 offset:8192\n\t"
        "ds_read_b128 v[176:179], v233 offset:0\n\t"
        "v_mfma_f32_16x16x32_bf16 v[60:63], v[132:135], v[216:219], v[60:63]\n\t"
        "v_mfma_f32_16x16x32_bf16 v[60:63], v[148:151], v[220:223], v[60:63]\n\t"
        "ds_read_b128 v[204:207], v234 offset:8448\n\t"
        "v_mfma_f32_16x16x32_bf16 v[56:59], v[148:151], v[212:215], v[56:59]\n\t"
        "v_mfma_f32_16x16x32_bf16 v[56:59], v[132:135], v[208:211], v[56:59]\n\t"
        "ds_read_b128 v[200:203], v234 offset:256\n\t"
        "v_mfma_f32_16x16x32_bf16 v[88:91], v[136:139], v[208:211], v[88:91]\n\t"
        "v_mfma_f32_16x16x32_bf16 v[88:91], v[152:155], v[212:215], v[88:91]\n\t"
        "ds_read_b128 v[164:167], v232 offset:2048\n\t"
        "v_mfma_f32_16x16x32_bf16 v[92:95], v[152:155], v[220:223], v[92:95]\n\t"
        "v_mfma_f32_16x16x32_bf16 v[92:95], v[136:139], v[216:219], v[92:95]\n\t"
        "ds_read_b128 v[180:183], v233 offset:2048\n\t"
        "v_mfma_f32_16x16x32_bf16 v[124:127], v[140:143], v[216:219], v[124:127]\n\t"
        "v_mfma_f32_16x16x32_bf16 v[124:127], v[156:159], v[220:223], v[124:127]\n\t"
        "ds_read_b128 v[168:171], v232 offset:4096\n\t"
        "v_mfma_f32_16x16x32_bf16 v[120:123], v[156:159], v[212:215], v[120:123]\n\t"
        "v_mfma_f32_16x16x32_bf16 v[120:123], v[140:143], v[208:211], v[120:123]\n\t"
        "ds_read_b128 v[184:187], v233 offset:4096\n\t"
        "s_waitcnt lgkmcnt(4)\n\t"
        "v_mfma_f32_16x16x32_bf16 v[0:3], v[160:163], v[192:195], v[0:3]\n\t"
        "v_mfma_f32_16x16x32_bf16 v[0:3], v[176:179], v[196:199], v[0:3]\n\t"
        "ds_read_b128 v[172:175], v232 offset:6144\n\t"
        "ds_read_b128 v[208:211], v234 offset:512\n\t"
        "v_mfma_f32_16x16x32_bf16 v[4:7], v[176:179], v[204:207], v[4:7]\n\t"
        "v_mfma_f32_16x16x32_bf16 v[4:7], v[160:163], v[200:203], v[4:7]\n\t"
        "ds_read_b128 v[188:191], v233 offset:6144\n\t"
        "s_waitcnt lgkmcnt(5)\n\t"
        "v_mfma_f32_16x16x32_bf16 v[36:39], v[164:167], v[200:203], v[36:39]\n\t"
        "ds_read_b128 v[212:215], v234 offset:8704\n\t"
        "v_mfma_f32_16x16x32_bf16 v[36:39], v[180:183], v[204:207], v[36:39]\n\t"
        "ds_read_b128 v[220:223], v234 offset:8960\n\t"
        "v_mfma_f32_16x16x32_bf16 v[32:35], v[180:183], v[196:199], v[32:35]\n\t"
        "v_mfma_f32_16x16x32_bf16 v[32:35], v[164:167], v[192:195], v[32:35]\n\t"
        "ds_read_b128 v[216:219], v234 offset:768\n\t"
        "s_waitcnt lgkmcnt(6)\n\t"
        "v_mfma_f32_16x16x32_bf16 v[64:67], v[168:171], v[192:195], v[64:67]\n\t"
        "v_mfma_f32_16x16x32_bf16 v[64:67], v[184:187], v[196:199], v[64:67]\n\t"
        "v_mfma_f32_16x16x32_bf16 v[68:71], v[184:187], v[204:207], v[68:71]\n\t"
        "v_mfma_f32_16x16x32_bf16 v[68:71], v[168:171], v[200:203], v[68:71]\n\t"
        "s_waitcnt lgkmcnt(3)\n\t"
        "v_mfma_f32_16x16x32_bf16 v[100:103], v[172:175], v[200:203], v[100:103]\n\t"
        "v_mfma_f32_16x16x32_bf16 v[100:103], v[188:191], v[204:207], v[100:103]\n\t"
        "v_mfma_f32_16x16x32_bf16 v[96:99], v[188:191], v[196:199], v[96:99]\n\t"
        "v_mfma_f32_16x16x32_bf16 v[96:99], v[172:175], v[192:195], v[96:99]\n\t"
        ".Lwp12:\n\t"
        "s_cbranch_execz .Lwp99\n\t"
        "v_mfma_f32_16x16x32_bf16 v[8:11], v[160:163], v[208:211], v[8:11]\n\t"
        "s_waitcnt lgkmcnt(0)\n\t"
        "v_mfma_f32_16x16x32_bf16 v[8:11], v[176:179], v[212:215], v[8:11]\n\t"
        "ds_read_b128 v[192:195], v234 offset:1024\n\t"
        "v_mfma_f32_16x16x32_bf16 v[12:15], v[176:179], v[220:223], v[12:15]\n\t"
        "v_mfma_f32_16x16x32_bf16 v[12:15], v[160:163], v[216:219], v[12:15]\n\t"
        "ds_read_b128 v[196:199], v234 offset:9216\n\t"
        "v_mfma_f32_16x16x32_bf16 v[44:47], v[164:167], v[216:219], v[44:47]\n\t"
        "v_mfma_f32_16x16x32_bf16 v[44:47], v[180:183], v[220:223], v[44:47]\n\t"
        "ds_read_b128 v[204:207], v234 offset:9472\n\t"
        "v_mfma_f32_16x16x32_bf16 v[40:43], v[180:183], v[212:215], v[40:43]\n\t"
        "v_mfma_f32_16x16x32_bf16 v[40:43], v[164:167], v[208:211], v[40:43]\n\t"
        "ds_read_b128 v[200:203], v234 offset:1280\n\t"
        "v_mfma_f32_16x16x32_bf16 v[72:75], v[168:171], v[208:211], v[72:75]\n\t"
        "v_mfma_f32_16x16x32_bf16 v[72:75], v[184:187], v[212:215], v[72:75]\n\t"
        "v_mfma_f32_16x16x32_bf16 v[76:79], v[184:187], v[220:223], v[76:79]\n\t"
        "v_mfma_f32_16x16x32_bf16 v[76:79], v[168:171], v[216:219], v[76:79]\n\t"
        "v_mfma_f32_16x16x32_bf16 v[108:111], v[172:175], v[216:219], v[108:111]\n\t"
        "v_mfma_f32_16x16x32_bf16 v[108:111], v[188:191], v[220:223], v[108:111]\n\t"
        "v_mfma_f32_16x16x32_bf16 v[104:107], v[188:191], v[212:215], v[104:107]\n\t"
        "v_mfma_f32_16x16x32_bf16 v[104:107], v[172:175], v[208:211], v[104:107]\n\t"
        "s_waitcnt lgkmcnt(0)\n\t"
        "v_mfma_f32_16x16x32_bf16 v[16:19], v[160:163], v[192:195], v[16:19]\n\t"
        "v_mfma_f32_16x16x32_bf16 v[16:19], v[176:179], v[196:199], v[16:19]\n\t"
        "ds_read_b128 v[208:211], v234 offset:1536\n\t"
        "v_mfma_f32_16x16x32_bf16 v[20:23], v[176:179], v[204:207], v[20:23]\n\t"
        "v_mfma_f32_16x16x32_bf16 v[20:23], v[160:163], v[200:203], v[20:23]\n\t"
        "ds_read_b128 v[212:215], v234 offset:9728\n\t"
        "v_mfma_f32_16x16x32_bf16 v[52:55], v[164:167], v[200:203], v[52:55]\n\t"
        "v_mfma_f32_16x16x32_bf16 v[52:55], v[180:183], v[204:207], v[52:55]\n\t"
        "ds_read_b128 v[220:223], v234 offset:9984\n\t"
        "v_mfma_f32_16x16x32_bf16 v[48:51], v[180:183], v[196:199], v[48:51]\n\t"
        "v_mfma_f32_16x16x32_bf16 v[48:51], v[164:167], v[192:195], v[48:51]\n\t"
        "ds_read_b128 v[216:219], v234 offset:1792\n\t"
        "s_add_u32 s60, s60, 32768\n\t"
        "v_mfma_f32_16x16x32_bf16 v[80:83], v[168:171], v[192:195], v[80:83]\n\t"
        "s_cmp_eq_u32 s60, 98304\n\t"
        "s_cselect_b32 s60, 0, s60\n\t"
        "v_mfma_f32_16x16x32_bf16 v[80:83], v[184:187], v[196:199], v[80:83]\n\t"
        "v_add_u32 v232, s60, %[xb0]\n\t"
        "s_lshr_b32 s61, s60, 1\n\t"
        "v_mfma_f32_16x16x32_bf16 v[84:87], v[184:187], v[204:207], v[84:87]\n\t"
        "v_add_u32 v233, s60, %[xb1]\n\t"
        "v_add_u32 v234, s61, %[ba]\n\t"
        "v_mfma_f32_16x16x32_bf16 v[84:87], v[168:171], v[200:203], v[84:87]\n\t"
        "v_mfma_f32_16x16x32_bf16 v[116:119], v[172:175], v[200:203], v[116:119]\n\t"
        "v_mfma_f32_16x16x32_bf16 v[116:119], v[188:191], v[204:207], v[116:119]\n\t"
        "s_waitcnt lgkmcnt(0)\n\t"
        "v_mfma_f32_16x16x32_bf16 v[112:115], v[188:191], v[196:199], v[112:115]\n\t"
        "v_mfma_f32_16x16x32_bf16 v[112:115], v[172:175], v[192:195], v[112:115]\n\t"
        "s_barrier\n\t"
        "v_mfma_f32_16x16x32_bf16 v[24:27], v[160:163], v[208:211], v[24:27]\n\t"
        "v_mfma_f32_16x16x32_bf16 v[24:27], v[176:179], v[212:215], v[24:27]\n\t"
        "ds_read_b128 v[192:195], v234 offset:0\n\t"
        "ds_read_b128 v[128:131], v232 offset:0\n\t"
        "v_mfma_f32_16x16x32_bf16 v[28:31], v[176:179], v[220:223], v[28:31]\n\t"
        "v_mfma_f32_16x16x32_bf16 v[28:31], v[160:163], v[216:219], v[28:31]\n\t"
        "ds_read_b128 v[196:199], v234 offset:8192\n\t"
        "ds_read_b128 v[144:147], v233 offset:0\n\t"
        "v_mfma_f32_16x16x32_bf16 v[60:63], v[164:167], v[216:219], v[60:63]\n\t"
        "v_mfma_f32_16x16x32_bf16 v[60:63], v[180:183], v[220:223], v[60:63]\n\t"
        "ds_read_b128 v[204:207], v234 offset:8448\n\t"
        "v_mfma_f32_16x16x32_bf16 v[56:59], v[180:183], v[212:215], v[56:59]\n\t"
        "v_mfma_f32_16x16x32_bf16 v[56:59], v[164:167], v[208:211], v[56:59]\n\t"
        "ds_read_b128 v[200:203], v234 offset:256\n\t"
        "v_mfma_f32_16x16x32_bf16 v[88:91], v[168:171], v[208:211], v[88:91]\n\t"
        "v_mfma_f32_16x16x32_bf16 v[88:91], v[184:187], v[212:215], v[88:91]\n\t"
        "ds_read_b128 v[132:135], v232 offset:2048\n\t"
        "v_mfma_f32_16x16x32_bf16 v[92:95], v[184:187], v[220:223], v[92:95]\n\t"
        "v_mfma_f32_16x16x32_bf16 v[92:95], v[168:171], v[216:219], v[92:95]\n\t"
        "ds_read_b128 v[148:151], v233 offset:2048\n\t"
        "v_mfma_f32_16x16x32_bf16 v[124:127], v[172:175], v[216:219], v[124:127]\n\t"
        "v_mfma_f32_16x16x32_bf16 v[124:127], v[188:191], v[220:223], v[124:127]\n\t"
        "ds_read_b128 v[136:139], v232 offset:4096\n\t"
        "v_mfma_f32_16x16x32_bf16 v[120:123], v[188:191], v[212:215], v[120:123]\n\t"
        "v_mfma_f32_16x16x32_bf16 v[120:123], v[172:175], v[208:211], v[120:123]\n\t"
        "ds_read_b128 v[152:155], v233 offset:4096\n\t"
        "s_waitcnt lgkmcnt(4)\n\t"
        "v_mfma_f32_16x16x32_bf16 v[0:3], v[128:131], v[192:195], v[0:3]\n\t"
        "v_mfma_f32_16x16x32_bf16 v[0:3], v[144:147], v[196:199], v[0:3]\n\t"
        "ds_read_b128 v[140:143], v232 offset:6144\n\t"
        "ds_read_b128 v[208:211], v234 offset:512\n\t"
        "v_mfma_f32_16x16x32_bf16 v[4:7], v[144:147], v[204:207], v[4:7]\n\t"
        "v_mfma_f32_16x16x32_bf16 v[4:7], v[128:131], v[200:203], v[4:7]\n\t"
        "ds_read_b128 v[156:159], v233 offset:6144\n\t"
        "s_waitcnt lgkmcnt(5)\n\t"
        "v_mfma_f32_16x16x32_bf16 v[36:39], v[132:135], v[200:203], v[36:39]\n\t"
        "ds_read_b128 v[212:215], v234 offset:8704\n\t"
        "v_mfma_f32_16x16x32_bf16 v[36:39], v[148:151], v[204:207], v[36:39]\n\t"
        "ds_read_b128 v[220:223], v234 offset:8960\n\t"
        "v_mfma_f32_16x16x32_bf16 v[32:35], v[148:151], v[196:199], v[32:35]\n\t"
        "v_mfma_f32_16x16x32_bf16 v[32:35], v[132:135], v[192:195], v[32:35]\n\t"
        "ds_read_b128 v[216:219], v234 offset:768\n\t"
        "s_waitcnt lgkmcnt(6)\n\t"
        "v_mfma_f32_16x16x32_bf16 v[64:67], v[136:139], v[192:195], v[64:67]\n\t"
        "v_mfma_f32_16x16x32_bf16 v[64:67], v[152:155], v[196:199], v[64:67]\n\t"
        "v_mfma_f32_16x16x32_bf16 v[68:71], v[152:155], v[204:207], v[68:71]\n\t"
        "v_mfma_f32_16x16x32_bf16 v[68:71], v[136:139], v[200:203], v[68:71]\n\t"
        "s_waitcnt lgkmcnt(3)\n\t"
        "v_mfma_f32_16x16x32_bf16 v[100:103], v[140:143], v[200:203], v[100:103]\n\t"
        "v_mfma_f32_16x16x32_bf16 v[100:103], v[156:159], v[204:207], v[100:103]\n\t"
        "v_mfma_f32_16x16x32_bf16 v[96:99], v[156:159], v[196:199], v[96:99]\n\t"
        "v_mfma_f32_16x16x32_bf16 v[96:99], v[140:143], v[192:195], v[96:99]\n\t"
        ".Lwp13:\n\t"
        "s_cbranch_execz .Lwp99\n\t"
        "v_mfma_f32_16x16x32_bf16 v[8:11], v[128:131], v[208:211], v[8:11]\n\t"
        "s_waitcnt lgkmcnt(0)\n\t"
        "v_mfma_f32_16x16x32_bf16 v[8:11], v[144:147], v[212:215], v[8:11]\n\t"
        "ds_read_b128 v[192:195], v234 offset:1024\n\t"
        "v_mfma_f32_16x16x32_bf16 v[12:15], v[144:147], v[220:223], v[12:15]\n\t"
        "v_mfma_f32_16x16x32_bf16 v[12:15], v[128:131], v[216:219], v[12:15]\n\t"
        "ds_read_b128 v[196:199], v234 offset:9216\n\t"
        "v_mfma_f32_16x16x32_bf16 v[44:47], v[132:135], v[216:219], v[44:47]\n\t"
        "v_mfma_f32_16x16x32_bf16 v[44:47], v[148:151], v[220:223], v[44:47]\n\t"
        "ds_read_b128 v[204:207], v234 offset:9472\n\t"
        "v_mfma_f32_16x16x32_bf16 v[40:43], v[148:151], v[212:215], v[40:43]\n\t"
        "v_mfma_f32_16x16x32_bf16 v[40:43], v[132:135], v[208:211], v[40:43]\n\t"
        "ds_read_b128 v[200:203], v234 offset:1280\n\t"
        "v_mfma_f32_16x16x32_bf16 v[72:75], v[136:139], v[208:211], v[72:75]\n\t"
        "v_mfma_f32_16x16x32_bf16 v[72:75], v[152:155], v[212:215], v[72:75]\n\t"
        "v_mfma_f32_16x16x32_bf16 v[76:79], v[152:155], v[220:223], v[76:79]\n\t"
        "v_mfma_f32_16x16x32_bf16 v[76:79], v[136:139], v[216:219], v[76:79]\n\t"
        "v_mfma_f32_16x16x32_bf16 v[108:111], v[140:143], v[216:219], v[108:111]\n\t"
        "v_mfma_f32_16x16x32_bf16 v[108:111], v[156:159], v[220:223], v[108:111]\n\t"
        "v_mfma_f32_16x16x32_bf16 v[104:107], v[156:159], v[212:215], v[104:107]\n\t"
        "v_mfma_f32_16x16x32_bf16 v[104:107], v[140:143], v[208:211], v[104:107]\n\t"
        "s_waitcnt lgkmcnt(0)\n\t"
        "v_mfma_f32_16x16x32_bf16 v[16:19], v[128:131], v[192:195], v[16:19]\n\t"
        "v_mfma_f32_16x16x32_bf16 v[16:19], v[144:147], v[196:199], v[16:19]\n\t"
        "ds_read_b128 v[208:211], v234 offset:1536\n\t"
        "v_mfma_f32_16x16x32_bf16 v[20:23], v[144:147], v[204:207], v[20:23]\n\t"
        "v_mfma_f32_16x16x32_bf16 v[20:23], v[128:131], v[200:203], v[20:23]\n\t"
        "ds_read_b128 v[212:215], v234 offset:9728\n\t"
        "v_mfma_f32_16x16x32_bf16 v[52:55], v[132:135], v[200:203], v[52:55]\n\t"
        "v_mfma_f32_16x16x32_bf16 v[52:55], v[148:151], v[204:207], v[52:55]\n\t"
        "ds_read_b128 v[220:223], v234 offset:9984\n\t"
        "v_mfma_f32_16x16x32_bf16 v[48:51], v[148:151], v[196:199], v[48:51]\n\t"
        "v_mfma_f32_16x16x32_bf16 v[48:51], v[132:135], v[192:195], v[48:51]\n\t"
        "ds_read_b128 v[216:219], v234 offset:1792\n\t"
        "s_add_u32 s60, s60, 32768\n\t"
        "v_mfma_f32_16x16x32_bf16 v[80:83], v[136:139], v[192:195], v[80:83]\n\t"
        "s_cmp_eq_u32 s60, 98304\n\t"
        "s_cselect_b32 s60, 0, s60\n\t"
        "v_mfma_f32_16x16x32_bf16 v[80:83], v[152:155], v[196:199], v[80:83]\n\t"
        "v_add_u32 v232, s60, %[xb0]\n\t"
        "s_lshr_b32 s61, s60, 1\n\t"
        "v_mfma_f32_16x16x32_bf16 v[84:87], v[152:155], v[204:207], v[84:87]\n\t"
        "v_add_u32 v233, s60, %[xb1]\n\t"
        "v_add_u32 v234, s61, %[ba]\n\t"
        "v_mfma_f32_16x16x32_bf16 v[84:87], v[136:139], v[200:203], v[84:87]\n\t"
        "s_cmp_lg_u32 s62, -1\n\t"
        "s_cbranch_scc1 .Lcloop\n\t"
        "v_mfma_f32_16x16x32_bf16 v[116:119], v[140:143], v[200:203], v[116:119]\n\t"
        "v_mfma_f32_16x16x32_bf16 v[116:119], v[156:159], v[204:207], v[116:119]\n\t"
        "s_waitcnt lgkmcnt(0)\n\t"
        "v_mfma_f32_16x16x32_bf16 v[112:115], v[156:159], v[196:199], v[112:115]\n\t"
        "v_mfma_f32_16x16x32_bf16 v[112:115], v[140:143], v[192:195], v[112:115]\n\t"
        "s_barrier\n\t"
        "v_mfma_f32_16x16x32_bf16 v[24:27], v[128:131], v[208:211], v[24:27]\n\t"
        "s_setprio 2\n\t"
        ".Lwp14:\n\t"
        "s_cbranch_execz .Lwp15\n\t"
        "v_mfma_f32_16x16x32_bf16 v[24:27], v[144:147], v[212:215], v[24:27]\n\t"
        "ds_read_b128 v[160:163], %[ba] offset:16384\n\t"
        "ds_read_b128 v[164:167], v232 offset:0\n\t"
        "v_mfma_f32_16x16x32_bf16 v[28:31], v[144:147], v[220:223], v[28:31]\n\t"
        "v_mfma_f32_16x16x32_bf16 v[28:31], v[128:131], v[216:219], v[28:31]\n\t"
        "ds_read_b128 v[144:147], %[ba] offset:24576\n\t"
        "ds_read_b128 v[168:171], v233 offset:0\n\t"
        "v_mfma_f32_16x16x32_bf16 v[60:63], v[132:135], v[216:219], v[60:63]\n\t"
        "v_mfma_f32_16x16x32_bf16 v[60:63], v[148:151], v[220:223], v[60:63]\n\t"
        "ds_read_b128 v[128:131], %[ba] offset:24832\n\t"
        "ds_read_b128 v[172:175], %[ba] offset:16640\n\t"
        "v_mfma_f32_16x16x32_bf16 v[56:59], v[148:151], v[212:215], v[56:59]\n\t"
        "ds_read_b128 v[176:179], v232 offset:2048\n\t"
        "v_mfma_f32_16x16x32_bf16 v[56:59], v[132:135], v[208:211], v[56:59]\n\t"
        "ds_read_b128 v[148:151], v233 offset:2048\n\t"
        "v_mfma_f32_16x16x32_bf16 v[88:91], v[136:139], v[208:211], v[88:91]\n\t"
        "v_mfma_f32_16x16x32_bf16 v[88:91], v[152:155], v[212:215], v[88:91]\n\t"
        "ds_read_b128 v[132:135], v232 offset:4096\n\t"
        "v_mfma_f32_16x16x32_bf16 v[92:95], v[152:155], v[220:223], v[92:95]\n\t"
        "v_mfma_f32_16x16x32_bf16 v[92:95], v[136:139], v[216:219], v[92:95]\n\t"
        "ds_read_b128 v[152:155], v233 offset:4096\n\t"
        "v_mfma_f32_16x16x32_bf16 v[124:127], v[140:143], v[216:219], v[124:127]\n\t"
        "v_mfma_f32_16x16x32_bf16 v[124:127], v[156:159], v[220:223], v[124:127]\n\t"
        "ds_read_b128 v[136:139], v232 offset:6144\n\t"
        "v_mfma_f32_16x16x32_bf16 v[120:123], v[156:159], v[212:215], v[120:123]\n\t"
        "v_mfma_f32_16x16x32_bf16 v[120:123], v[140:143], v[208:211], v[120:123]\n\t"
        "ds_read_b128 v[156:159], v233 offset:6144\n\t"
        "s_waitcnt lgkmcnt(4)\n\t"
        "v_mfma_f32_16x16x32_bf16 v[0:3], v[164:167], v[160:163], v[0:3]\n\t"
        "v_mfma_f32_16x16x32_bf16 v[0:3], v[168:171], v[144:147], v[0:3]\n\t"
        "ds_read_b128 v[140:143], %[ba] offset:16896\n\t"
        "v_mfma_f32_16x16x32_bf16 v[4:7], v[168:171], v[128:131], v[4:7]\n\t"
        "v_mfma_f32_16x16x32_bf16 v[4:7], v[164:167], v[172:175], v[4:7]\n\t"
        "ds_read_b128 v[180:183], %[ba] offset:25088\n\t"
        "v_mfma_f32_16x16x32_bf16 v[36:39], v[176:179], v[172:175], v[36:39]\n\t"
        "v_mfma_f32_16x16x32_bf16 v[36:39], v[148:151], v[128:131], v[36:39]\n\t"
        "ds_read_b128 v[184:187], %[ba] offset:25344\n\t"
        "v_mfma_f32_16x16x32_bf16 v[32:35], v[148:151], v[144:147], v[32:35]\n\t"
        "v_mfma_f32_16x16x32_bf16 v[32:35], v[176:179], v[160:163], v[32:35]\n\t"
        "ds_read_b128 v[188:191], %[ba] offset:17152\n\t"
        "s_waitcnt lgkmcnt(4)\n\t"
        "v_mfma_f32_16x16x32_bf16 v[64:67], v[132:135], v[160:163], v[64:67]\n\t"
        "v_mfma_f32_16x16x32_bf16 v[64:67], v[152:155], v[144:147], v[64:67]\n\t"
        "ds_read_b128 v[192:195], %[ba] offset:17408\n\t"
        "v_mfma_f32_16x16x32_bf16 v[68:71], v[152:155], v[128:131], v[68:71]\n\t"
        "v_mfma_f32_16x16x32_bf16 v[68:71], v[132:135], v[172:175], v[68:71]\n\t"
        "ds_read_b128 v[196:199], v232 offset:32768\n\t"
        "v_mfma_f32_16x16x32_bf16 v[100:103], v[136:139], v[172:175], v[100:103]\n\t"
        "v_mfma_f32_16x16x32_bf16 v[100:103], v[156:159], v[128:131], v[100:103]\n\t"
        "ds_read_b128 v[172:175], %[ba] offset:25600\n\t"
        "v_mfma_f32_16x16x32_bf16 v[96:99], v[156:159], v[144:147], v[96:99]\n\t"
        "v_mfma_f32_16x16x32_bf16 v[96:99], v[136:139], v[160:163], v[96:99]\n\t"
        "ds_read_b128 v[128:131], v233 offset:32768\n\t"
        "s_waitcnt lgkmcnt(4)\n\t"
        "v_mfma_f32_16x16x32_bf16 v[8:11], v[164:167], v[140:143], v[8:11]\n\t"
        "v_mfma_f32_16x16x32_bf16 v[8:11], v[168:171], v[180:183], v[8:11]\n\t"
        "ds_read_b128 v[144:147], %[ba] offset:25856\n\t"
        "v_mfma_f32_16x16x32_bf16 v[12:15], v[168:171], v[184:187], v[12:15]\n\t"
        "v_mfma_f32_16x16x32_bf16 v[12:15], v[164:167], v[188:191], v[12:15]\n\t"
        "ds_read_b128 v[160:163], %[ba] offset:17664\n\t"
        "v_mfma_f32_16x16x32_bf16 v[44:47], v[176:179], v[188:191], v[44:47]\n\t"
        "v_mfma_f32_16x16x32_bf16 v[44:47], v[148:151], v[184:187], v[44:47]\n\t"
        "ds_read_b128 v[164:167], %[ba] offset:17920\n\t"
        "ds_read_b128 v[168:171], %[ba] offset:26112\n\t"
        "v_mfma_f32_16x16x32_bf16 v[40:43], v[148:151], v[180:183], v[40:43]\n\t"
        "v_mfma_f32_16x16x32_bf16 v[40:43], v[176:179], v[140:143], v[40:43]\n\t"
        "ds_read_b128 v[148:151], %[ba] offset:26368\n\t"
        "v_mfma_f32_16x16x32_bf16 v[72:75], v[132:135], v[140:143], v[72:75]\n\t"
        "v_mfma_f32_16x16x32_bf16 v[72:75], v[152:155], v[180:183], v[72:75]\n\t"
        "ds_read_b128 v[176:179], %[ba] offset:18176\n\t"
        "v_mfma_f32_16x16x32_bf16 v[76:79], v[152:155], v[184:187], v[76:79]\n\t"
        "v_mfma_f32_16x16x32_bf16 v[76:79], v[132:135], v[188:191], v[76:79]\n\t"
        "ds_read_b128 v[152:155], v232 offset:34816\n\t"
        "v_mfma_f32_16x16x32_bf16 v[108:111], v[136:139], v[188:191], v[108:111]\n\t"
        "v_mfma_f32_16x16x32_bf16 v[108:111], v[156:159], v[184:187], v[108:111]\n\t"
        "ds_read_b128 v[132:135], v233 offset:34816\n\t"
        "v_mfma_f32_16x16x32_bf16 v[104:107], v[156:159], v[180:183], v[104:107]\n\t"
        "v_mfma_f32_16x16x32_bf16 v[104:107], v[136:139], v[140:143], v[104:107]\n\t"
        "s_waitcnt lgkmcnt(3)\n\t"
        "v_mfma_f32_16x16x32_bf16 v[0:3], v[196:199], v[192:195], v[0:3]\n\t"
        "v_mfma_f32_16x16x32_bf16 v[0:3], v[128:131], v[172:175], v[0:3]\n\t"
        "v_mfma_f32_16x16x32_bf16 v[4:7], v[128:131], v[144:147], v[4:7]\n\t"
        "v_mfma_f32_16x16x32_bf16 v[4:7], v[196:199], v[160:163], v[4:7]\n\t"
        "ds_read_b128 v[136:139], v232 offset:36864\n\t"
        "v_mfma_f32_16x16x32_bf16 v[8:11], v[196:199], v[164:167], v[8:11]\n\t"
        "v_mfma_f32_16x16x32_bf16 v[8:11], v[128:131], v[168:171], v[8:11]\n\t"
        "ds_read_b128 v[140:143], v233 offset:36864\n\t"
        "v_mfma_f32_16x16x32_bf16 v[12:15], v[128:131], v[148:151], v[12:15]\n\t"
        "s_waitcnt lgkmcnt(2)\n\t"
        "v_mfma_f32_16x16x32_bf16 v[12:15], v[196:199], v[176:179], v[12:15]\n\t"
        "v_mfma_f32_16x16x32_bf16 v[32:35], v[152:155], v[192:195], v[32:35]\n\t"
        "v_mfma_f32_16x16x32_bf16 v[32:35], v[132:135], v[172:175], v[32:35]\n\t"
        "ds_read_b128 v[128:131], v232 offset:38912\n\t"
        "v_mfma_f32_16x16x32_bf16 v[36:39], v[132:135], v[144:147], v[36:39]\n\t"
        "v_mfma_f32_16x16x32_bf16 v[36:39], v[152:155], v[160:163], v[36:39]\n\t"
        "ds_read_b128 v[156:159], v233 offset:38912\n\t"
        "v_mfma_f32_16x16x32_bf16 v[40:43], v[152:155], v[164:167], v[40:43]\n\t"
        "v_mfma_f32_16x16x32_bf16 v[40:43], v[132:135], v[168:171], v[40:43]\n\t"
        "v_mfma_f32_16x16x32_bf16 v[44:47], v[132:135], v[148:151], v[44:47]\n\t"
        "v_mfma_f32_16x16x32_bf16 v[44:47], v[152:155], v[176:179], v[44:47]\n\t"
        "s_waitcnt lgkmcnt(0)\n\t"
        "s_barrier\n\t"
        "v_mfma_f32_16x16x32_bf16 v[64:67], v[136:139], v[192:195], v[64:67]\n\t"
        ".Lwp15:\n\t"
        "s_cbranch_execz .Lwp97\n\t"
        "v_mfma_f32_16x16x32_bf16 v[64:67], v[140:143], v[172:175], v[64:67]\n\t"
        "ds_read_b128 v[132:135], %[ba] offset:32768\n\t"
        "ds_read_b128 v[152:155], %[xb0] offset:0\n\t"
        "v_mfma_f32_16x16x32_bf16 v[68:71], v[140:143], v[144:147], v[68:71]\n\t"
        "v_mfma_f32_16x16x32_bf16 v[68:71], v[136:139], v[160:163], v[68:71]\n\t"
        "ds_read_b128 v[180:183], %[ba] offset:40960\n\t"
        "ds_read_b128 v[184:187], %[xb1] offset:0\n\t"
        "v_mfma_f32_16x16x32_bf16 v[72:75], v[136:139], v[164:167], v[72:75]\n\t"
        "v_mfma_f32_16x16x32_bf16 v[72:75], v[140:143], v[168:171], v[72:75]\n\t"
        "ds_read_b128 v[188:191], %[ba] offset:41216\n\t"
        "ds_read_b128 v[196:199], %[ba] offset:33024\n\t"
        "v_mfma_f32_16x16x32_bf16 v[76:79], v[140:143], v[148:151], v[76:79]\n\t"
        "v_mfma_f32_16x16x32_bf16 v[76:79], v[136:139], v[176:179], v[76:79]\n\t"
        "ds_read_b128 v[140:143], %[ba] offset:33280\n\t"
        "s_waitcnt lgkmcnt(5)\n\t"
        "v_mfma_f32_16x16x32_bf16 v[0:3], v[152:155], v[132:135], v[0:3]\n\t"
        "ds_read_b128 v[136:139], %[ba] offset:41472\n\t"
        "s_waitcnt lgkmcnt(4)\n\t"
        "v_mfma_f32_16x16x32_bf16 v[0:3], v[184:187], v[180:183], v[0:3]\n\t"
        "ds_read_b128 v[200:203], %[ba] offset:41728\n\t"
        "s_waitcnt lgkmcnt(4)\n\t"
        "v_mfma_f32_16x16x32_bf16 v[4:7], v[184:187], v[188:191], v[4:7]\n\t"
        "ds_read_b128 v[204:207], %[ba] offset:33536\n\t"
        "s_waitcnt lgkmcnt(4)\n\t"
        "v_mfma_f32_16x16x32_bf16 v[4:7], v[152:155], v[196:199], v[4:7]\n\t"
        "ds_read_b128 v[208:211], %[xb0] offset:2048\n\t"
        "s_waitcnt lgkmcnt(4)\n\t"
        "v_mfma_f32_16x16x32_bf16 v[8:11], v[152:155], v[140:143], v[8:11]\n\t"
        "s_waitcnt lgkmcnt(3)\n\t"
        "v_mfma_f32_16x16x32_bf16 v[8:11], v[184:187], v[136:139], v[8:11]\n\t"
        "ds_read_b128 v[212:215], %[xb1] offset:2048\n\t"
        "s_waitcnt lgkmcnt(3)\n\t"
        "v_mfma_f32_16x16x32_bf16 v[12:15], v[184:187], v[200:203], v[12:15]\n\t"
        "s_waitcnt lgkmcnt(2)\n\t"
        "v_mfma_f32_16x16x32_bf16 v[12:15], v[152:155], v[204:207], v[12:15]\n\t"
        "v_mfma_f32_16x16x32_bf16 v[96:99], v[128:131], v[192:195], v[96:99]\n\t"
        "v_mfma_f32_16x16x32_bf16 v[96:99], v[156:159], v[172:175], v[96:99]\n\t"
        "ds_read_b128 v[152:155], %[xb0] offset:4096\n\t"
        "v_cvt_pk_bf16_f32 v224, v0, v4\n\t"
        "v_mfma_f32_16x16x32_bf16 v[100:103], v[156:159], v[144:147], v[100:103]\n\t"
        "v_mfma_f32_16x16x32_bf16 v[100:103], v[128:131], v[160:163], v[100:103]\n\t"
        "ds_read_b128 v[144:147], %[xb1] offset:4096\n\t"
        "v_cvt_pk_bf16_f32 v225, v8, v12\n\t"
        "v_mfma_f32_16x16x32_bf16 v[104:107], v[128:131], v[164:167], v[104:107]\n\t"
        "global_store_dwordx2 %[oo0], v[224:225], %[ob0] offset:0 nt\n\t"
        "v_mfma_f32_16x16x32_bf16 v[104:107], v[156:159], v[168:171], v[104:107]\n\t"
        "ds_read_b128 v[160:163], %[xb0] offset:6144\n\t"
        "v_cvt_pk_bf16_f32 v227, v9, v13\n\t"
        "v_mfma_f32_16x16x32_bf16 v[108:111], v[156:159], v[148:151], v[108:111]\n\t"
        "v_cvt_pk_bf16_f32 v226, v1, v5\n\t"
        "v_mfma_f32_16x16x32_bf16 v[108:111], v[128:131], v[176:179], v[108:111]\n\t"
        "ds_read_b128 v[148:151], %[xb1] offset:6144\n\t"
        "s_waitcnt lgkmcnt(4)\n\t"
        "v_mfma_f32_16x16x32_bf16 v[32:35], v[208:211], v[132:135], v[32:35]\n\t"
        "global_store_dwordx2 %[oo1], v[226:227], %[ob0] offset:0 nt\n\t"
        "v_mfma_f32_16x16x32_bf16 v[32:35], v[212:215], v[180:183], v[32:35]\n\t"
        "ds_read_b128 v[128:131], v232 offset:0\n\t"
        "v_cvt_pk_bf16_f32 v229, v10, v14\n\t"
        "v_mfma_f32_16x16x32_bf16 v[36:39], v[212:215], v[188:191], v[36:39]\n\t"
        "v_cvt_pk_bf16_f32 v228, v2, v6\n\t"
        "v_mfma_f32_16x16x32_bf16 v[36:39], v[208:211], v[196:199], v[36:39]\n\t"
        "ds_read_b128 v[156:159], %[ba] offset:33792\n\t"
        "global_store_dwordx2 %[oo2], v[228:229], %[ob0] offset:0 nt\n\t"
        "v_mfma_f32_16x16x32_bf16 v[40:43], v[208:211], v[140:143], v[40:43]\n\t"
        "v_mfma_f32_16x16x32_bf16 v[40:43], v[212:215], v[136:139], v[40:43]\n\t"
        "ds_read_b128 v[164:167], v233 offset:0\n\t"
        "v_cvt_pk_bf16_f32 v231, v11, v15\n\t"
        "v_mfma_f32_16x16x32_bf16 v[44:47], v[212:215], v[200:203], v[44:47]\n\t"
        "v_cvt_pk_bf16_f32 v230, v3, v7\n\t"
        "v_mfma_f32_16x16x32_bf16 v[44:47], v[208:211], v[204:207], v[44:47]\n\t"
        "ds_read_b128 v[168:171], %[ba] offset:41984\n\t"
        "s_waitcnt lgkmcnt(4)\n\t"
        "v_mfma_f32_16x16x32_bf16 v[64:67], v[152:155], v[132:135], v[64:67]\n\t"
        "global_store_dwordx2 %[oo3], v[230:231], %[ob0] offset:0 nt\n\t"
        "v_mfma_f32_16x16x32_bf16 v[64:67], v[144:147], v[180:183], v[64:67]\n\t"
        "ds_read_b128 v[172:175], %[ba] offset:42240\n\t"
        "v_cvt_pk_bf16_f32 v224, v32, v36\n\t"
        "v_mfma_f32_16x16x32_bf16 v[68:71], v[144:147], v[188:191], v[68:71]\n\t"
        "v_mfma_f32_16x16x32_bf16 v[68:71], v[152:155], v[196:199], v[68:71]\n\t"
        "ds_read_b128 v[176:179], %[ba] offset:34048\n\t"
        "v_cvt_pk_bf16_f32 v225, v40, v44\n\t"
        "v_mfma_f32_16x16x32_bf16 v[72:75], v[152:155], v[140:143], v[72:75]\n\t"
        "global_store_dwordx2 %[oo0], v[224:225], %[ob1] offset:0 nt\n\t"
        "v_mfma_f32_16x16x32_bf16 v[72:75], v[144:147], v[136:139], v[72:75]\n\t"
        "ds_read_b128 v[184:187], %[ba] offset:34304\n\t"
        "ds_read_b128 v[192:195], %[ba] offset:42496\n\t"
        "v_mfma_f32_16x16x32_bf16 v[76:79], v[144:147], v[200:203], v[76:79]\n\t"
        "v_cvt_pk_bf16_f32 v227, v41, v45\n\t"
        "v_cvt_pk_bf16_f32 v226, v33, v37\n\t"
        "v_mfma_f32_16x16x32_bf16 v[76:79], v[152:155], v[204:207], v[76:79]\n\t"
        "ds_read_b128 v[144:147], %[ba] offset:42752\n\t"
        "global_store_dwordx2 %[oo1], v[226:227], %[ob1] offset:0 nt\n\t"
        "v_mfma_f32_16x16x32_bf16 v[96:99], v[160:163], v[132:135], v[96:99]\n\t"
        ".Lwp16:\n\t"
        "s_cbranch_execz .Lwp17\n\t"
        "v_mfma_f32_16x16x32_bf16 v[96:99], v[148:151], v[180:183], v[96:99]\n\t"
        "ds_read_b128 v[132:135], %[ba] offset:34560\n\t"
        "v_cvt_pk_bf16_f32 v229, v42, v46\n\t"
        "v_mfma_f32_16x16x32_bf16 v[100:103], v[148:151], v[188:191], v[100:103]\n\t"
        "v_cvt_pk_bf16_f32 v228, v34, v38\n\t"
        "v_mfma_f32_16x16x32_bf16 v[100:103], v[160:163], v[196:199], v[100:103]\n\t"
        "ds_read_b128 v[152:155], v232 offset:2048\n\t"
        "global_store_dwordx2 %[oo2], v[228:229], %[ob1] offset:0 nt\n\t"
        "v_mfma_f32_16x16x32_bf16 v[104:107], v[160:163], v[140:143], v[104:107]\n\t"
        "v_mfma_f32_16x16x32_bf16 v[104:107], v[148:151], v[136:139], v[104:107]\n\t"
        "ds_read_b128 v[140:143], v233 offset:2048\n\t"
        "v_cvt_pk_bf16_f32 v231, v43, v47\n\t"
        "v_mfma_f32_16x16x32_bf16 v[108:111], v[148:151], v[200:203], v[108:111]\n\t"
        "v_cvt_pk_bf16_f32 v230, v35, v39\n\t"
        "v_mfma_f32_16x16x32_bf16 v[108:111], v[160:163], v[204:207], v[108:111]\n\t"
        "global_store_dwordx2 %[oo3], v[230:231], %[ob1] offset:0 nt\n\t"
        "s_waitcnt lgkmcnt(3)\n\t"
        "v_mfma_f32_16x16x32_bf16 v[16:19], v[128:131], v[156:159], v[16:19]\n\t"
        "v_mfma_f32_16x16x32_bf16 v[16:19], v[164:167], v[168:171], v[16:19]\n\t"
        "ds_read_b128 v[136:139], v232 offset:4096\n\t"
        "v_cvt_pk_bf16_f32 v225, v72, v76\n\t"
        "v_mfma_f32_16x16x32_bf16 v[20:23], v[164:167], v[172:175], v[20:23]\n\t"
        "v_cvt_pk_bf16_f32 v224, v64, v68\n\t"
        "v_mfma_f32_16x16x32_bf16 v[20:23], v[128:131], v[176:179], v[20:23]\n\t"
        "ds_read_b128 v[148:151], v233 offset:4096\n\t"
        "global_store_dwordx2 %[oo0], v[224:225], %[ob2] offset:0 nt\n\t"
        "v_mfma_f32_16x16x32_bf16 v[24:27], v[128:131], v[184:187], v[24:27]\n\t"
        "v_mfma_f32_16x16x32_bf16 v[24:27], v[164:167], v[192:195], v[24:27]\n\t"
        "ds_read_b128 v[160:163], v232 offset:6144\n\t"
        "v_cvt_pk_bf16_f32 v227, v73, v77\n\t"
        "v_mfma_f32_16x16x32_bf16 v[28:31], v[164:167], v[144:147], v[28:31]\n\t"
        "v_cvt_pk_bf16_f32 v226, v65, v69\n\t"
        "s_waitcnt lgkmcnt(3)\n\t"
        "v_mfma_f32_16x16x32_bf16 v[28:31], v[128:131], v[132:135], v[28:31]\n\t"
        "ds_read_b128 v[164:167], v233 offset:6144\n\t"
        "global_store_dwordx2 %[oo1], v[226:227], %[ob2] offset:0 nt\n\t"
        "v_mfma_f32_16x16x32_bf16 v[48:51], v[152:155], v[156:159], v[48:51]\n\t"
        "v_mfma_f32_16x16x32_bf16 v[48:51], v[140:143], v[168:171], v[48:51]\n\t"
        "ds_read_b128 v[128:131], v232 offset:32768\n\t"
        "v_cvt_pk_bf16_f32 v229, v74, v78\n\t"
        "v_mfma_f32_16x16x32_bf16 v[52:55], v[140:143], v[172:175], v[52:55]\n\t"
        "v_cvt_pk_bf16_f32 v228, v66, v70\n\t"
        "v_mfma_f32_16x16x32_bf16 v[52:55], v[152:155], v[176:179], v[52:55]\n\t"
        "ds_read_b128 v[180:183], v233 offset:32768\n\t"
        "ds_read_b128 v[188:191], %[xb0] offset:0\n\t"
        "v_mfma_f32_16x16x32_bf16 v[56:59], v[152:155], v[184:187], v[56:59]\n\t"
        "global_store_dwordx2 %[oo2], v[228:229], %[ob2] offset:0 nt\n\t"
        "v_mfma_f32_16x16x32_bf16 v[56:59], v[140:143], v[192:195], v[56:59]\n\t"
        "v_cvt_pk_bf16_f32 v231, v75, v79\n\t"
        "v_cvt_pk_bf16_f32 v230, v67, v71\n\t"
        "v_mfma_f32_16x16x32_bf16 v[60:63], v[140:143], v[144:147], v[60:63]\n\t"
        "v_mfma_f32_16x16x32_bf16 v[60:63], v[152:155], v[132:135], v[60:63]\n\t"
        "global_store_dwordx2 %[oo3], v[230:231], %[ob2] offset:0 nt\n\t"
        "s_barrier\n\t"
        "s_waitcnt lgkmcnt(3)\n\t"
        "v_mfma_f32_16x16x32_bf16 v[80:83], v[136:139], v[156:159], v[80:83]\n\t"
        "v_mfma_f32_16x16x32_bf16 v[80:83], v[148:151], v[168:171], v[80:83]\n\t"
        "ds_read_b128 v[140:143], %[ba] offset:0\n\t"
        "ds_read_b128 v[152:155], %[xb1] offset:0\n\t"
        "v_mfma_f32_16x16x32_bf16 v[84:87], v[148:151], v[172:175], v[84:87]\n\t"
        "v_cvt_pk_bf16_f32 v225, v104, v108\n\t"
        "v_cvt_pk_bf16_f32 v224, v96, v100\n\t"
        "v_mfma_f32_16x16x32_bf16 v[84:87], v[136:139], v[176:179], v[84:87]\n\t"
        ".Lwp17:\n\t"
        "s_cbranch_execz .Lwp18\n\t"
        "ds_read_b128 v[196:199], %[ba] offset:8192\n\t"
        "v_mfma_f32_16x16x32_bf16 v[88:91], v[136:139], v[184:187], v[88:91]\n\t"
        "ds_read_b128 v[200:203], %[ba] offset:1024\n\t"
        "ds_read_b128 v[204:207], %[ba] offset:9216\n\t"
        "v_mfma_f32_16x16x32_bf16 v[88:91], v[148:151], v[192:195], v[88:91]\n\t"
        "global_store_dwordx2 %[oo0], v[224:225], %[ob3] offset:0 nt\n\t"
        "ds_read_b128 v[208:211], %[ba] offset:9472\n\t"
        "v_mfma_f32_16x16x32_bf16 v[92:95], v[148:151], v[144:147], v[92:95]\n\t"
        "ds_read_b128 v[212:215], %[ba] offset:1280\n\t"
        "ds_read_b128 v[216:219], %[ba] offset:8448\n\t"
        "v_mfma_f32_16x16x32_bf16 v[92:95], v[136:139], v[132:135], v[92:95]\n\t"
        "v_cvt_pk_bf16_f32 v227, v105, v109\n\t"
        "v_cvt_pk_bf16_f32 v226, v97, v101\n\t"
        "v_mfma_f32_16x16x32_bf16 v[112:115], v[160:163], v[156:159], v[112:115]\n\t"
        "ds_read_b128 v[136:139], %[ba] offset:256\n\t"
        "ds_read_b128 v[148:151], %[ba] offset:512\n\t"
        "v_mfma_f32_16x16x32_bf16 v[112:115], v[164:167], v[168:171], v[112:115]\n\t"
        "ds_read_b128 v[156:159], %[ba] offset:8704\n\t"
        "global_store_dwordx2 %[oo1], v[226:227], %[ob3] offset:0 nt\n\t"
        "v_mfma_f32_16x16x32_bf16 v[116:119], v[164:167], v[172:175], v[116:119]\n\t"
        "ds_read_b128 v[168:171], %[ba] offset:1536\n\t"
        "ds_read_b128 v[220:223], %[ba] offset:9728\n\t"
        "v_mfma_f32_16x16x32_bf16 v[116:119], v[160:163], v[176:179], v[116:119]\n\t"
        "v_cvt_pk_bf16_f32 v229, v106, v110\n\t"
        "v_cvt_pk_bf16_f32 v228, v98, v102\n\t"
        "v_mfma_f32_16x16x32_bf16 v[120:123], v[160:163], v[184:187], v[120:123]\n\t"
        "ds_read_b128 v[172:175], %[ba] offset:9984\n\t"
        "ds_read_b128 v[176:179], %[ba] offset:1792\n\t"
        "v_mfma_f32_16x16x32_bf16 v[120:123], v[164:167], v[192:195], v[120:123]\n\t"
        "global_store_dwordx2 %[oo2], v[228:229], %[ob3] offset:0 nt\n\t"
        "ds_read_b128 v[184:187], %[ba] offset:8960\n\t"
        "v_mfma_f32_16x16x32_bf16 v[124:127], v[164:167], v[144:147], v[124:127]\n\t"
        "v_cvt_pk_bf16_f32 v231, v107, v111\n\t"
        "v_cvt_pk_bf16_f32 v230, v99, v103\n\t"
        "v_mfma_f32_16x16x32_bf16 v[124:127], v[160:163], v[132:135], v[124:127]\n\t"
        "ds_read_b128 v[144:147], %[ba] offset:768\n\t"
        "s_waitcnt lgkmcnt(9)\n\t"
        "v_mfma_f32_16x16x32_bf16 v[16:19], v[128:131], v[140:143], v[16:19]\n\t"
        "global_store_dwordx2 %[oo3], v[230:231], %[ob3] offset:0 nt\n\t"
        "v_mfma_f32_16x16x32_bf16 v[16:19], v[180:183], v[196:199], v[16:19]\n\t"
        "ds_read_b128 v[132:135], v232 offset:34816\n\t"
        "v_mfma_f32_16x16x32_bf16 v[16:19], v[188:191], v[200:203], v[16:19]\n\t"
        "v_mfma_f32_16x16x32_bf16 v[16:19], v[152:155], v[204:207], v[16:19]\n\t"
        "ds_read_b128 v[160:163], v233 offset:34816\n\t"
        "v_mfma_f32_16x16x32_bf16 v[20:23], v[152:155], v[208:211], v[20:23]\n\t"
        "v_mfma_f32_16x16x32_bf16 v[20:23], v[188:191], v[212:215], v[20:23]\n\t"
        "ds_read_b128 v[164:167], %[xb0] offset:2048\n\t"
        "v_mfma_f32_16x16x32_bf16 v[20:23], v[180:183], v[216:219], v[20:23]\n\t"
        "s_waitcnt lgkmcnt(4)\n\t"
        "v_mfma_f32_16x16x32_bf16 v[20:23], v[128:131], v[136:139], v[20:23]\n\t"
        "ds_read_b128 v[192:195], %[xb1] offset:2048\n\t"
        "v_mfma_f32_16x16x32_bf16 v[24:27], v[128:131], v[148:151], v[24:27]\n\t"
        "v_mfma_f32_16x16x32_bf16 v[24:27], v[180:183], v[156:159], v[24:27]\n\t"
        "v_mfma_f32_16x16x32_bf16 v[24:27], v[188:191], v[168:171], v[24:27]\n\t"
        "v_mfma_f32_16x16x32_bf16 v[24:27], v[152:155], v[220:223], v[24:27]\n\t"
        "v_mfma_f32_16x16x32_bf16 v[28:31], v[152:155], v[172:175], v[28:31]\n\t"
        "v_mfma_f32_16x16x32_bf16 v[28:31], v[188:191], v[176:179], v[28:31]\n\t"
        "v_mfma_f32_16x16x32_bf16 v[28:31], v[180:183], v[184:187], v[28:31]\n\t"
        "s_waitcnt lgkmcnt(1)\n\t"
        "v_mfma_f32_16x16x32_bf16 v[28:31], v[128:131], v[144:147], v[28:31]\n\t"
        "v_mfma_f32_16x16x32_bf16 v[48:51], v[132:135], v[140:143], v[48:51]\n\t"
        "v_mfma_f32_16x16x32_bf16 v[48:51], v[160:163], v[196:199], v[48:51]\n\t"
        "ds_read_b128 v[128:131], v232 offset:36864\n\t"
        "v_cvt_pk_bf16_f32 v224, v16, v20\n\t"
        "v_mfma_f32_16x16x32_bf16 v[48:51], v[164:167], v[200:203], v[48:51]\n\t"
        "s_waitcnt lgkmcnt(1)\n\t"
        "v_mfma_f32_16x16x32_bf16 v[48:51], v[192:195], v[204:207], v[48:51]\n\t"
        "ds_read_b128 v[152:155], v233 offset:36864\n\t"
        "v_cvt_pk_bf16_f32 v225, v24, v28\n\t"
        "v_mfma_f32_16x16x32_bf16 v[52:55], v[192:195], v[208:211], v[52:55]\n\t"
        "global_store_dwordx2 %[oo0], v[224:225], %[ob0] offset:128 nt\n\t"
        "v_mfma_f32_16x16x32_bf16 v[52:55], v[164:167], v[212:215], v[52:55]\n\t"
        "ds_read_b128 v[180:183], %[xb0] offset:4096\n\t"
        "v_cvt_pk_bf16_f32 v227, v25, v29\n\t"
        "v_mfma_f32_16x16x32_bf16 v[52:55], v[160:163], v[216:219], v[52:55]\n\t"
        "v_cvt_pk_bf16_f32 v226, v17, v21\n\t"
        "v_mfma_f32_16x16x32_bf16 v[52:55], v[132:135], v[136:139], v[52:55]\n\t"
        "ds_read_b128 v[188:191], %[xb1] offset:4096\n\t"
        "global_store_dwordx2 %[oo1], v[226:227], %[ob0] offset:128 nt\n\t"
        "v_mfma_f32_16x16x32_bf16 v[56:59], v[132:135], v[148:151], v[56:59]\n\t"
        "v_mfma_f32_16x16x32_bf16 v[56:59], v[160:163], v[156:159], v[56:59]\n\t"
        "v_cvt_pk_bf16_f32 v229, v26, v30\n\t"
        "v_cvt_pk_bf16_f32 v228, v18, v22\n\t"
        "v_mfma_f32_16x16x32_bf16 v[56:59], v[164:167], v[168:171], v[56:59]\n\t"
        "v_mfma_f32_16x16x32_bf16 v[56:59], v[192:195], v[220:223], v[56:59]\n\t"
        "global_store_dwordx2 %[oo2], v[228:229], %[ob0] offset:128 nt\n\t"
        "v_mfma_f32_16x16x32_bf16 v[60:63], v[192:195], v[172:175], v[60:63]\n\t"
        "v_mfma_f32_16x16x32_bf16 v[60:63], v[164:167], v[176:179], v[60:63]\n\t"
        "v_cvt_pk_bf16_f32 v231, v27, v31\n\t"
        "v_cvt_pk_bf16_f32 v230, v19, v23\n\t"
        "v_mfma_f32_16x16x32_bf16 v[60:63], v[160:163], v[184:187], v[60:63]\n\t"
        ".Lwp18:\n\t"
        "s_cbranch_execz .Lwp97\n\t"
        "v_mfma_f32_16x16x32_bf16 v[60:63], v[132:135], v[144:147], v[60:63]\n\t"
        "global_store_dwordx2 %[oo3], v[230:231], %[ob0] offset:128 nt\n\t"
        "s_waitcnt lgkmcnt(0)\n\t"
        "v_mfma_f32_16x16x32_bf16 v[80:83], v[128:131], v[140:143], v[80:83]\n\t"
        "v_mfma_f32_16x16x32_bf16 v[80:83], v[152:155], v[196:199], v[80:83]\n\t"
        "ds_read_b128 v[132:135], v232 offset:38912\n\t"
        "v_cvt_pk_bf16_f32 v224, v48, v52\n\t"
        "v_mfma_f32_16x16x32_bf16 v[80:83], v[180:183], v[200:203], v[80:83]\n\t"
        "v_mfma_f32_16x16x32_bf16 v[80:83], v[188:191], v[204:207], v[80:83]\n\t"
        "ds_read_b128 v[160:163], v233 offset:38912\n\t"
        "v_cvt_pk_bf16_f32 v225, v56, v60\n\t"
        "v_mfma_f32_16x16x32_bf16 v[84:87], v[188:191], v[208:211], v[84:87]\n\t"
        "global_store_dwordx2 %[oo0], v[224:225], %[ob1] offset:128 nt\n\t"
        "v_mfma_f32_16x16x32_bf16 v[84:87], v[180:183], v[212:215], v[84:87]\n\t"
        "ds_read_b128 v[164:167], %[xb0] offset:6144\n\t"
        "v_cvt_pk_bf16_f32 v227, v57, v61\n\t"
        "v_mfma_f32_16x16x32_bf16 v[84:87], v[152:155], v[216:219], v[84:87]\n\t"
        "v_cvt_pk_bf16_f32 v226, v49, v53\n\t"
        "v_mfma_f32_16x16x32_bf16 v[84:87], v[128:131], v[136:139], v[84:87]\n\t"
        "ds_read_b128 v[192:195], %[xb1] offset:6144\n\t"
        "global_store_dwordx2 %[oo1], v[226:227], %[ob1] offset:128 nt\n\t"
        "v_mfma_f32_16x16x32_bf16 v[88:91], v[128:131], v[148:151], v[88:91]\n\t"
        "v_mfma_f32_16x16x32_bf16 v[88:91], v[152:155], v[156:159], v[88:91]\n\t"
        "v_cvt_pk_bf16_f32 v229, v58, v62\n\t"
        "v_cvt_pk_bf16_f32 v228, v50, v54\n\t"
        "v_mfma_f32_16x16x32_bf16 v[88:91], v[180:183], v[168:171], v[88:91]\n\t"
        "v_mfma_f32_16x16x32_bf16 v[88:91], v[188:191], v[220:223], v[88:91]\n\t"
        "global_store_dwordx2 %[oo2], v[228:229], %[ob1] offset:128 nt\n\t"
        "v_mfma_f32_16x16x32_bf16 v[92:95], v[188:191], v[172:175], v[92:95]\n\t"
        "v_mfma_f32_16x16x32_bf16 v[92:95], v[180:183], v[176:179], v[92:95]\n\t"
        "v_cvt_pk_bf16_f32 v231, v59, v63\n\t"
        "v_cvt_pk_bf16_f32 v230, v51, v55\n\t"
        "v_mfma_f32_16x16x32_bf16 v[92:95], v[152:155], v[184:187], v[92:95]\n\t"
        "v_mfma_f32_16x16x32_bf16 v[92:95], v[128:131], v[144:147], v[92:95]\n\t"
        "global_store_dwordx2 %[oo3], v[230:231], %[ob1] offset:128 nt\n\t"
        "s_waitcnt lgkmcnt(0)\n\t"
        "v_mfma_f32_16x16x32_bf16 v[112:115], v[132:135], v[140:143], v[112:115]\n\t"
        "v_mfma_f32_16x16x32_bf16 v[112:115], v[160:163], v[196:199], v[112:115]\n\t"
        "v_cvt_pk_bf16_f32 v224, v80, v84\n\t"
        "v_mfma_f32_16x16x32_bf16 v[112:115], v[164:167], v[200:203], v[112:115]\n\t"
        "v_mfma_f32_16x16x32_bf16 v[112:115], v[192:195], v[204:207], v[112:115]\n\t"
        "v_cvt_pk_bf16_f32 v225, v88, v92\n\t"
        "global_store_dwordx2 %[oo0], v[224:225], %[ob2] offset:128 nt\n\t"
        "v_mfma_f32_16x16x32_bf16 v[116:119], v[192:195], v[208:211], v[116:119]\n\t"
        "v_mfma_f32_16x16x32_bf16 v[116:119], v[164:167], v[212:215], v[116:119]\n\t"
        "v_cvt_pk_bf16_f32 v227, v89, v93\n\t"
        "v_cvt_pk_bf16_f32 v226, v81, v85\n\t"
        "v_mfma_f32_16x16x32_bf16 v[116:119], v[160:163], v[216:219], v[116:119]\n\t"
        "v_mfma_f32_16x16x32_bf16 v[116:119], v[132:135], v[136:139], v[116:119]\n\t"
        "global_store_dwordx2 %[oo1], v[226:227], %[ob2] offset:128 nt\n\t"
        "v_mfma_f32_16x16x32_bf16 v[120:123], v[132:135], v[148:151], v[120:123]\n\t"
        "v_mfma_f32_16x16x32_bf16 v[120:123], v[160:163], v[156:159], v[120:123]\n\t"
        "v_cvt_pk_bf16_f32 v229, v90, v94\n\t"
        "v_cvt_pk_bf16_f32 v228, v82, v86\n\t"
        "v_mfma_f32_16x16x32_bf16 v[120:123], v[164:167], v[168:171], v[120:123]\n\t"
        "v_mfma_f32_16x16x32_bf16 v[120:123], v[192:195], v[220:223], v[120:123]\n\t"
        "global_store_dwordx2 %[oo2], v[228:229], %[ob2] offset:128 nt\n\t"
        "v_mfma_f32_16x16x32_bf16 v[124:127], v[192:195], v[172:175], v[124:127]\n\t"
        "v_mfma_f32_16x16x32_bf16 v[124:127], v[164:167], v[176:179], v[124:127]\n\t"
        "v_cvt_pk_bf16_f32 v230, v113, v117\n\t"
        "v_cvt_pk_bf16_f32 v224, v112, v116\n\t"
        "v_mfma_f32_16x16x32_bf16 v[124:127], v[160:163], v[184:187], v[124:127]\n\t"
        "v_cvt_pk_bf16_f32 v227, v91, v95\n\t"
        "v_cvt_pk_bf16_f32 v226, v83, v87\n\t"
        "v_mfma_f32_16x16x32_bf16 v[124:127], v[132:135], v[144:147], v[124:127]\n\t"
        "v_cvt_pk_bf16_f32 v228, v115, v119\n\t"
        "v_cvt_pk_bf16_f32 v128, v114, v118\n\t"
        "global_store_dwordx2 %[oo3], v[226:227], %[ob2] offset:128 nt\n\t"
        "s_nop 7\n\t"
        "v_cvt_pk_bf16_f32 v225, v120, v124\n\t"
        "global_store_dwordx2 %[oo0], v[224:225], %[ob3] offset:128 nt\n\t"
        "v_cvt_pk_bf16_f32 v231, v121, v125\n\t"
        "global_store_dwordx2 %[oo1], v[230:231], %[ob3] offset:128 nt\n\t"
        "v_cvt_pk_bf16_f32 v129, v122, v126\n\t"
        "global_store_dwordx2 %[oo2], v[128:129], %[ob3] offset:128 nt\n\t"
        "v_cvt_pk_bf16_f32 v229, v123, v127\n\t"
        "global_store_dwordx2 %[oo3], v[228:229], %[ob3] offset:128 nt\n\t"
        :
        : [xb0] "v"(xb0), [xb1] "v"(xb1), [ba] "v"(ba), [oo0] "v"(oo[0]), [oo1] "v"(oo[1]), [oo2] "v"(oo[2]), [oo3] "v"(oo[3]),
          [ob0] "s"(ob[0]), [ob1] "s"(ob[1]), [ob2] "s"(ob[2]), [ob3] "s"(ob[3]), [w] "s"(w)
        : "scc", "memory", "s60", "s61", "s62", "s63", "s64", "s65", "s66", "s67", "v0", "v1", "v2", "v3", "v4", "v5", "v6", "v7", "v8", "v9", "v10", "v11", "v12", "v13", "v14", "v15", "v16", "v17", "v18", "v19", "v20", "v21", "v22", "v23", "v24", "v25", "v26", "v27", "v28", "v29", "v30", "v31", "v32", "v33", "v34", "v35", "v36", "v37", "v38", "v39", "v40", "v41", "v42", "v43", "v44", "v45", "v46", "v47", "v48", "v49", "v50", "v51", "v52", "v53", "v54", "v55", "v56", "v57", "v58", "v59", "v60", "v61", "v62", "v63", "v64", "v65", "v66", "v67", "v68", "v69", "v70", "v71", "v72", "v73", "v74", "v75", "v76", "v77", "v78", "v79", "v80", "v81", "v82", "v83", "v84", "v85", "v86", "v87", "v88", "v89", "v90", "v91", "v92", "v93", "v94", "v95", "v96", "v97", "v98", "v99", "v100", "v101", "v102", "v103", "v104", "v105", "v106", "v107", "v108", "v109", "v110", "v111", "v112", "v113", "v114", "v115", "v116", "v117", "v118", "v119", "v120", "v121", "v122", "v123", "v124", "v125", "v126", "v127", "v128", "v129", "v130", "v131", "v132", "v133", "v134", "v135", "v136", "v137", "v138", "v139", "v140", "v141", "v142", "v143", "v144", "v145", "v146", "v147", "v148", "v149", "v150", "v151", "v152", "v153", "v154", "v155", "v156", "v157", "v158", "v159", "v160", "v161", "v162", "v163", "v164", "v165", "v166", "v167", "v168", "v169", "v170", "v171", "v172", "v173", "v174", "v175", "v176", "v177", "v178", "v179", "v180", "v181", "v182", "v183", "v184", "v185", "v186", "v187", "v188", "v189", "v190", "v191", "v192", "v193", "v194", "v195", "v196", "v197", "v198", "v199", "v200", "v201", "v202", "v203", "v204", "v205", "v206", "v207", "v208", "v209", "v210", "v211", "v212", "v213", "v214", "v215", "v216", "v217", "v218", "v219", "v220", "v221", "v222", "v223", "v224", "v225", "v226", "v227", "v228", "v229", "v230", "v231", "v232", "v233", "v234", "v235");
}

extern "C" void kernel_launch(void* const* d_in, const int* in_sizes, int n_in,
                              void* d_out, int out_size, void* d_ws, size_t ws_size,
                              hipStream_t stream) {
    const u16* x = (const u16*)d_in[0];
    const int* qw = (const int*)d_in[1];
    const u16* lut = (const u16*)d_in[2];
    u16* out = (u16*)d_out;
    (void)hipFuncSetAttribute((const void*)fused_kernel, hipFuncAttributeMaxDynamicSharedMemorySize, LDS_BYTES);
    fused_kernel<<<(M_ / BM) * (N_ / BN) + NPF, 512, LDS_BYTES, stream>>>(x, qw, lut, out, (char*)d_ws);
}
```

```cpp
#include <hip/hip_runtime.h>
#include <hip/hip_bf16.h>

typedef unsigned short u16;
typedef unsigned int u32;
using bf16x8 = __attribute__((ext_vector_type(8))) short;
using f32x4  = __attribute__((ext_vector_type(4))) float;
using i32x4  = __attribute__((ext_vector_type(4))) int;
using u32x2  = __attribute__((ext_vector_type(2))) unsigned int;
using u32x4  = __attribute__((ext_vector_type(4))) unsigned int;

constexpr int M_ = 2048, K_ = 1024, N_ = 3584;
constexpr int BM = 256, BN = 128;
constexpr int NPF = 32;
#define XSLEEP 4
#define SP0 3
#define NQ1 4
#define CPF 116
#define CPFB 32
#define PACE 0
#define VORD 1
constexpr int XS = BM * 64 * 2;
constexpr int BS = BN * 32 * 2;
constexpr int OFF_B = 3 * XS;
constexpr int WSL = 2 * BS;
constexpr int OFF_L = OFF_B + 3 * WSL;
constexpr int OFF_D = OFF_L + 16 * BN * 2;
constexpr int WS_F = 28 * 256, WS_W = 65536;
constexpr unsigned TW = 28 * 16384 + 0;
constexpr int SPIN_MAX = 1 << 16;
constexpr int LDS_BYTES = OFF_D + 1024;

typedef __bf16 bf16x2_t __attribute__((ext_vector_type(2)));
typedef float f32x2_t __attribute__((ext_vector_type(2)));
__device__ __forceinline__ u32 pack_bf16(float lo, float hi) {
    f32x2_t f = {lo, hi};
    bf16x2_t b = __builtin_convertvector(f, bf16x2_t);
    return __builtin_bit_cast(u32, b);
}

#define ROT0 0
#define OWN(mt, nt) (((mt) >> 1) ^ (ROT0 ? (nt) & 3 : 0))
#define TLO(mt, nt) ((0 && ((mt) & 1) ? 0x75631240u : 0x76532140u) ^ ((u32)OWN(mt, nt) * 0x11111111u))
#define THI(mt, nt) (0xFEDCBA98u ^ ((u32)(0) * 0x11111111u))
#define BAR() __builtin_amdgcn_s_barrier()
#define SB() __builtin_amdgcn_sched_barrier(0)
#define WAITQ(n, Qa) asm volatile("s_waitcnt vmcnt(" #n ") lgkmcnt(0)" : "+v"(Qa[0]), "+v"(Qa[1]), "+v"(Qa[2]), "+v"(Qa[3]), \
                                  "+v"(Qa[4]), "+v"(Qa[5]), "+v"(Qa[6]), "+v"(Qa[7]) :: "memory")
#define PBAR() BAR()
#define PAD(id, nx) asm volatile(".Lwp%c0:\n\ts_cbranch_execz .Lwp%c1" :: "i"(id), "i"(nx))

__global__ __launch_bounds__(512, 2) void fused_kernel(const u16* __restrict__ x_, const int* __restrict__ qw_,
                                                       const u16* __restrict__ lut_, u16* __restrict__ out_, char* ws_) {
    extern __shared__ __attribute__((aligned(16))) char smem[];
    typedef unsigned long u64x4 __attribute__((ext_vector_type(4)));
    u64x4 ka;
    unsigned long kp[5], kw;
    asm volatile("s_load_dwordx8 %0, %2, 0x0\n\ts_load_dwordx2 %1, %2, 0x20" : "=&s"(ka), "=&s"(kw) : "s"(__builtin_amdgcn_kernarg_segment_ptr()));
    {
        unsigned long pc0, ex;
        u32 co;
        asm volatile("s_getpc_b64 %0\n\t"
                     "s_cmp_lt_u32 %4, %5\n\t"
                     "s_cselect_b32 m0, %6, 0\n\t"
                     "v_cmp_gt_u32 vcc, m0, %3\n\t"
                     "s_and_b32 m0, %4, 24\n\t"
                     "s_lshl_b32 m0, m0, 4\n\t"
                     "v_lshl_add_u32 %2, %3, 9, m0\n\t"
                     "s_mov_b32 m0, %7\n\t"
                     "s_and_saveexec_b64 %1, vcc\n\t"
                     "global_load_lds_dword %2, %0\n\t"
                     "s_mov_b64 exec, %1"
                     : "=&s"(pc0), "=&s"(ex), "=&v"(co) : "v"(threadIdx.x), "s"(blockIdx.x), "i"(CPFB), "i"(CPF / 4), "i"(OFF_D) : "vcc", "scc", "memory");
    }
#define KWAIT() do { SB(); asm volatile("s_waitcnt lgkmcnt(0)\n\ts_mov_b64 %0, %5\n\ts_mov_b64 %1, %6\n\ts_mov_b64 %2, %7\n\ts_mov_b64 %3, %8\n\ts_mov_b64 %4, %9" \
                     : "=&s"(kp[0]), "=&s"(kp[1]), "=&s"(kp[2]), "=&s"(kp[3]), "=&s"(kp[4]) : "s"(ka[0]), "s"(ka[1]), "s"(ka[2]), "s"(ka[3]), "s"(kw) : "memory"); SB(); } while (0)
#define KPTR(i) (kp[i])
    const int tid = threadIdx.x;
    const int w = __builtin_amdgcn_readfirstlane(tid >> 6);
    const int lane = tid & 63;
    const int bid = (int)blockIdx.x - NPF;
    const int xcd = blockIdx.x & 7, idx = bid >> 3;
    if (bid < 0) {
        const int pw = (blockIdx.x >> 3) * 8 + w;
        KWAIT();
        const char* qb = (const char*)KPTR(1) + (xcd >> 1) * 896 * 4 + (long)((xcd & 1) * 2) * (64 * N_ * 4);
        const char* xb = (const char*)KPTR(0) + (long)(xcd & 1) * 1024 * 2048;
        const u32 dst = (u32)(size_t)(smem) + w * 256;
        {
            const int m = (xcd & 1) * 4 + (blockIdx.x >> 3);
            const u32 off = (u32)((m * 256 + (w & 3) * 64 + lane) * 2048 + ((TLO(m, 0) >> (w & 4)) & 15) * 128);
            if (0 && w >= 4) __builtin_amdgcn_s_sleep(0);
            asm volatile("s_mov_b32 m0, %2\n\ts_nop 0\n\tglobal_load_lds_dword %0, %1" :: "v"(off), "s"(KPTR(0)), "s"(dst) : "memory");
        }
#pragma unroll 1
        for (int G = pw; G < 168; G += 32) {
            int kind = 0, arg = 0, e = 0;
            if (G >= 0) { kind = 0; arg = 2; e = G - 0 + (xcd >> 1) * 4; }
            if (G >= 4) { kind = 0; arg = 3; e = G - 4 + (xcd >> 1) * 4; }
            if (G >= 8) { kind = 0; arg = 4; e = G - 8 + (xcd >> 1) * 4; }
            if (G >= 12) { kind = 1; arg = 8; e = G - 12; }
            if (G >= 32) { kind = 0; arg = 5; e = G - 32 + (xcd >> 1) * 4; }
            if (G >= 36) { kind = 1; arg = 8; e = G - 16; }
            if (G >= 64) { kind = 1; arg = 8; e = G - 16; }
            if (G >= 72) { kind = 0; arg = 6; e = G - 72 + (xcd >> 1) * 4; }
            if (G >= 76) { kind = 0; arg = 7; e = G - 76 + (xcd >> 1) * 4; }
            if (G >= 80) { kind = 1; arg = 12; e = G - 80; }
            if (G >= 96) { kind = 0; arg = 8; e = G - 96 + (xcd >> 1) * 4; }
            if (G >= 100) { kind = 0; arg = 9; e = G - 100 + (xcd >> 1) * 4; }
            if (G >= 104) { kind = 1; arg = 12; e = G - 88; }
            if (G >= 128) { kind = 0; arg = 10; e = G - 128 + (xcd >> 1) * 4; }
            if (G >= 132) { kind = 1; arg = 12; e = G - 92; }
            if (G >= 148) { kind = 0; arg = 11; e = G - 148 + (xcd >> 1) * 4; }
            if (G >= 152) { kind = 0; arg = 12; e = G - 152 + (xcd >> 1) * 4; }
            if (G >= 156) { kind = 0; arg = 13; e = G - 156 + (xcd >> 1) * 4; }
            if (G >= 160) { kind = 0; arg = 14; e = G - 160 + (xcd >> 1) * 4; }
            if (G >= 164) { kind = 0; arg = 15; e = G - 164 + (xcd >> 1) * 4; }
            asm volatile("s_waitcnt vmcnt(%0)" :: "i"(PACE) : "memory");
            if (kind == 2) __builtin_amdgcn_s_sleep(24);
            else if (kind) {
                const int L = e * 64 + lane;
                const int r = L / 28, c = L - r * 28;
                const u32 off = (u32)((arg * 64 + r) * (N_ * 4) + c * 128);
                asm volatile("s_mov_b32 m0, %2\n\ts_nop 0\n\tglobal_load_lds_dword %0, %1" :: "v"(off), "s"(qb), "s"(dst) : "memory");
            } else {
                const int t = ROT0 || arg >= 8 ? arg : (int)((TLO((xcd & 1) * 4 + (e >> 2), 0) >> (4 * arg)) & 15);
                const u32 off = (u32)((e * 64 + lane) * 2048 + t * 128);
                asm volatile("s_mov_b32 m0, %2\n\ts_nop 0\n\tglobal_load_lds_dword %0, %1" :: "v"(off), "s"(xb), "s"(dst) : "memory");
            }
        }
        asm volatile("s_waitcnt vmcnt(0)" ::: "memory");
        return;
    }
    const int mt = (xcd & 1) * 4 + (idx & 3);
    const int nt = (xcd >> 1) * 7 + (idx >> 2);
    const int row0 = mt * BM, col0 = nt * BN;

    if (__builtin_expect(w >= 4, 0)) {
        int lane;
        asm volatile("v_mbcnt_lo_u32_b32 %0, -1, 0\n\tv_mbcnt_hi_u32_b32 %0, -1, %0" : "=v"(lane));
        const int p = w - 4;
        const int l = lane & 31, h = lane >> 5;
        const int g = lane >> 4;
        u32 xoff[4];
#pragma unroll
        for (int i = 0; i < 4; ++i)
            xoff[i] = (u32)(((row0 + p * 64 + i * 8 + (lane >> 3)) * K_ + (((lane & 7) ^ ((i & 1) * 4 + g)) * 8)) * 2 - i * 1024);
        const u32 ldsx = (u32)(size_t)(smem) + p * 8192;
        auto issue_x1 = [&](int tile, int hh, int slotoff, int i) {
            const u16* base = (const u16*)KPTR(0) + (long)hh * 32 * K_ + tile * 64;
            const u32 dst = ldsx + slotoff + hh * 4096;
            if (i == 0) asm volatile("s_mov_b32 m0, %2\n\ts_nop 0\n\tglobal_load_lds_dwordx4 %0, %1" :: "v"(xoff[0]), "s"(base), "s"(dst) : "memory");
            else asm volatile("global_load_lds_dwordx4 %0, %1 offset:%2" :: "v"(xoff[i]), "s"(base), "i"(i * 1024) : "memory");
        };
        u32 qoff[8];
#pragma unroll
        for (int r = 0; r < 8; ++r) qoff[r] = (u32)(((p * 16 + h * 8 + r) * N_ + col0 + l * 4) * 4);
        auto issue_q1 = [&](int t, i32x4 (&Q)[8], int r) {
            const int* base = (const int*)KPTR(1) + (long)t * 64 * N_;
            asm volatile("global_load_dwordx4 %0, %1, %2 " : "=v"(Q[r]) : "v"(qoff[r]), "s"(base) : "memory");
        };
        u32 lutl = (u32)(size_t)(smem) + OFF_L + l * 4;
        u32 bdst = (u32)(size_t)(smem) + OFF_B + (p >> 1) * BS + ((2 * p + h) & 3) * 2048 + ((l >> 4) * 64 + (l & 15)) * 16;
        u32 V[4][8];
#define LUT1(Q, j, r) asm volatile("v_lshl_add_u32 %0, %1, 8, %2\n\tds_read_u16 %0, %0 offset:%3" : "=&v"(V[j][r]) \
                                   : "v"(Q[r][j]), "v"(lutl), "i"(((j) >> 1) * 128 + ((j) & 1) * 2) : "memory")
#define LUT4(Q, e) do { LUT1(Q, (e) >> 1, ((e) & 1) * 4); LUT1(Q, (e) >> 1, ((e) & 1) * 4 + 1); \
                        LUT1(Q, (e) >> 1, ((e) & 1) * 4 + 2); LUT1(Q, (e) >> 1, ((e) & 1) * 4 + 3); SB(); } while (0)
#define WRB(j, n) do { u32x4 b_; \
                       asm volatile("s_waitcnt lgkmcnt(" #n ")\n\t" \
                                    "v_lshl_or_b32 %0, %5, 16, %4\n\tv_lshl_or_b32 %1, %7, 16, %6\n\t" \
                                    "v_lshl_or_b32 %2, %9, 16, %8\n\tv_lshl_or_b32 %3, %11, 16, %10" \
                                    : "=&v"(b_[0]), "=&v"(b_[1]), "=&v"(b_[2]), "=&v"(b_[3]) \
                                    : "v"(V[j][0]), "v"(V[j][1]), "v"(V[j][2]), "v"(V[j][3]), "v"(V[j][4]), "v"(V[j][5]), "v"(V[j][6]), "v"(V[j][7]) : "memory"); \
                       asm volatile("ds_write_b128 %0, %1 offset:%2" :: "v"(bdst), "v"(b_), "i"((j) * 256) : "memory"); SB(); } while (0)
        const int i2 = OWN(mt, nt), h2 = mt & 1;
        const unsigned long tseq = ((unsigned long)THI(mt, nt) << 32) | TLO(mt, nt);
        const u32 ldsw = (u32)(size_t)(smem) + OFF_B + p * 4096;
        u32 wv = (u32)(nt * 16384 + p * 4096 + lane * 16);
        u32 wo = (u32)(nt * 16384 + p * 4096 + h * 2048 + (l >> 4) * 1024 + (l & 15) * 16);
        u32 po = (u32)(WS_F + (nt * 4 + p) * 256 + lane * 4);
        u32 fo0 = po + (u32)(i2 * 16 + h2 * 8);
        u32 no = (u32)(nt * 256 + (lane & 1) * 4);
        u32 nvl = no;
        KWAIT();
        const char* st1 = (const char*)KPTR(4) + WS_W + (long)(i2 + 4) * TW + h2 * 512;
        const char* qa1 = (const char*)KPTR(1);
        const int tb = 8 + (mt >> 1), kg = h2 * 4 + p;
        const char* qbg = (const char*)KPTR(1) + (long)(tb * 64 + kg * 8 - p * 16) * (N_ * 4);
        const char* stb = (const char*)KPTR(4) + WS_W + (long)tb * TW + (kg * 2048 - p * 4096);
        u32 fob = po + (u32)(((kg >> 1) - p) * 256 + tb * 16 + (kg & 1) * 8);
        asm volatile("" : "+v"(wo), "+v"(fo0), "+v"(fob), "+v"(wv), "+v"(po), "+v"(bdst), "+v"(lutl), "+v"(qoff[0]));
        asm volatile("" : "+v"(xoff[0]), "+v"(xoff[1]), "+v"(xoff[2]), "+v"(xoff[3]));
        asm volatile("" : "+s"(st1), "+s"(qa1), "+s"(qbg), "+s"(stb));
        {
            unsigned long ex_;
            asm volatile("s_mov_b64 %0, exec\n\ts_mov_b64 exec, 0\n\t"
                         "s_cmp_eq_u32 %1, 0\n\ts_cbranch_scc1 .Lwp30\n\t"
                         "s_cmp_eq_u32 %1, 1\n\ts_cbranch_scc1 .Lwp31\n\t"
                         "s_cmp_eq_u32 %1, 2\n\ts_cbranch_scc1 .Lwp32\n\t"
                         "s_branch .Lwp33\n\t"
                         ".Lwp98:\n\ts_mov_b64 exec, %0" : "=&s"(ex_) : "s"(p) : "scc", "memory");
        }
        asm volatile(
            "s_mov_b32 s73, 65536\n\t"
            "s_mov_b32 s72, 0\n\t"
            "s_mov_b64 s[84:85], %[ws]\n\t"
            "s_add_u32 s84, s84, 65536\n\t"
            "s_addc_u32 s85, s85, 0\n\t"
            "s_mov_b64 s[56:57], %[tseq]\n\t"
            "s_bfe_u32 s40, s56, 262144\n\t"
            "s_bfe_u32 s41, s56, 262148\n\t"
            "s_bfe_u32 s42, s56, 262152\n\t"
            "s_bfe_u32 s43, s56, 262156\n\t"
            "s_bfe_u32 s44, s56, 262160\n\t"
            "s_bfe_u32 s45, s56, 262164\n\t"
            "s_bfe_u32 s46, s56, 262168\n\t"
            "s_bfe_u32 s47, s56, 262172\n\t"
            "s_bfe_u32 s48, s57, 262144\n\t"
            "s_bfe_u32 s49, s57, 262148\n\t"
            "s_bfe_u32 s50, s57, 262152\n\t"
            "s_bfe_u32 s51, s57, 262156\n\t"
            "s_bfe_u32 s52, s57, 262160\n\t"
            "s_bfe_u32 s53, s57, 262164\n\t"
            "s_bfe_u32 s54, s57, 262168\n\t"
            "s_bfe_u32 s55, s57, 262172\n\t"
            "global_load_dword %[nvl], %[nvl], %[ws] sc1\n\t"
            "global_load_dword v192, %[po], %[ws] sc1\n\t"
            "s_barrier\n\t"
            ".Lwp30:\n\t"
            "s_cbranch_execz .Lwp98\n\t"
            "s_add_u32 m0, %[ldsx], 0\n\t"
            "s_lshl_b32 s76, s40, 7\n\t"
            "s_add_u32 s68, %[xpl], s76\n\t"
            "s_addc_u32 s69, %[xph], 0\n\t"
            "global_load_lds_dwordx4 %[x0], s[68:69] offset:0\n\t"
            "global_load_lds_dwordx4 %[x1], s[68:69] offset:1024\n\t"
            "global_load_lds_dwordx4 %[x2], s[68:69] offset:2048\n\t"
            "global_load_lds_dwordx4 %[x3], s[68:69] offset:3072\n\t"
            "s_add_u32 m0, %[ldsx], 4096\n\t"
            "s_lshl_b32 s76, s40, 7\n\t"
            "s_add_u32 s76, s76, 65536\n\t"
            "s_add_u32 s68, %[xpl], s76\n\t"
            "s_addc_u32 s69, %[xph], 0\n\t"
            "global_load_lds_dwordx4 %[x0], s[68:69] offset:0\n\t"
            "global_load_lds_dwordx4 %[x1], s[68:69] offset:1024\n\t"
            "global_load_lds_dwordx4 %[x2], s[68:69] offset:2048\n\t"
            "global_load_lds_dwordx4 %[x3], s[68:69] offset:3072\n\t"
            "s_mul_i32 s76, s41, 917504\n\t"
            "s_mov_b64 s[88:89], %[qa1]\n\t"
            "s_add_u32 s88, s88, s76\n\t"
            "s_addc_u32 s89, s89, 0\n\t"
            "global_load_dwordx4 v[128:131], %[qo], s[88:89] \n\t"
            "s_add_u32 s88, s88, 14336\n\t"
            "s_addc_u32 s89, s89, 0\n\t"
            "global_load_dwordx4 v[132:135], %[qo], s[88:89] \n\t"
            "s_add_u32 s88, s88, 14336\n\t"
            "s_addc_u32 s89, s89, 0\n\t"
            "global_load_dwordx4 v[136:139], %[qo], s[88:89] \n\t"
            "s_add_u32 s88, s88, 14336\n\t"
            "s_addc_u32 s89, s89, 0\n\t"
            "global_load_dwordx4 v[140:143], %[qo], s[88:89] \n\t"
            "s_add_u32 s88, s88, 14336\n\t"
            "s_addc_u32 s89, s89, 0\n\t"
            "global_load_dwordx4 v[144:147], %[qo], s[88:89] \n\t"
            "s_add_u32 s88, s88, 14336\n\t"
            "s_addc_u32 s89, s89, 0\n\t"
            "global_load_dwordx4 v[148:151], %[qo], s[88:89] \n\t"
            "s_add_u32 s88, s88, 14336\n\t"
            "s_addc_u32 s89, s89, 0\n\t"
            "global_load_dwordx4 v[152:155], %[qo], s[88:89] \n\t"
            "s_add_u32 s88, s88, 14336\n\t"
            "s_addc_u32 s89, s89, 0\n\t"
            "global_load_dwordx4 v[156:159], %[qo], s[88:89] \n\t"
            "s_waitcnt vmcnt(8)\n\t"
            "s_barrier\n\t"
            ".Lwp31:\n\t"
            "s_cbranch_execz .Lwp98\n\t"
            "s_lshl_b32 s76, %[h2], 9\n\t"
            "v_add_u32 v193, s76, %[bd]\n\t"
            "ds_read_b128 v[196:199], v193\n\t"
            "ds_read_b128 v[200:203], v193 offset:256\n\t"
            "s_mov_b64 s[70:71], %[st1]\n\t"
            "s_sub_u32 s70, s70, 1835008\n\t"
            "s_subb_u32 s71, s71, 0\n\t"
            "s_waitcnt lgkmcnt(0)\n\t"
            "global_store_dwordx4 %[wo], v[196:199], s[70:71] sc1\n\t"
            "global_store_dwordx4 %[wo], v[200:203], s[70:71] offset:256 sc1\n\t"
            "s_mov_b32 s76, 1664525\n\t"
            "v_mul_lo_u32 %[nvl], %[nvl], s76\n\t"
            "v_add_u32 %[nvl], 1013904223, %[nvl]\n\t"
            "s_setprio 1\n\t"
            "s_waitcnt vmcnt(9)\n\t"
            "v_lshl_add_u32 v160, v128, 8, %[lutl]\n\t"
            "ds_read_u16 v160, v160 offset:0\n\t"
            "v_lshl_add_u32 v168, v129, 8, %[lutl]\n\t"
            "ds_read_u16 v168, v168 offset:2\n\t"
            "v_lshl_add_u32 v176, v130, 8, %[lutl]\n\t"
            "ds_read_u16 v176, v176 offset:128\n\t"
            "v_lshl_add_u32 v184, v131, 8, %[lutl]\n\t"
            "ds_read_u16 v184, v184 offset:130\n\t"
            "s_add_u32 m0, %[ldsx], 32768\n\t"
            "s_lshl_b32 s76, s41, 7\n\t"
            "s_add_u32 s68, %[xpl], s76\n\t"
            "s_addc_u32 s69, %[xph], 0\n\t"
            "global_load_lds_dwordx4 %[x0], s[68:69] offset:0\n\t"
            "s_waitcnt vmcnt(9)\n\t"
            "v_lshl_add_u32 v161, v132, 8, %[lutl]\n\t"
            "ds_read_u16 v161, v161 offset:0\n\t"
            "v_lshl_add_u32 v169, v133, 8, %[lutl]\n\t"
            "ds_read_u16 v169, v169 offset:2\n\t"
            "v_lshl_add_u32 v177, v134, 8, %[lutl]\n\t"
            "ds_read_u16 v177, v177 offset:128\n\t"
            "v_lshl_add_u32 v185, v135, 8, %[lutl]\n\t"
            "ds_read_u16 v185, v185 offset:130\n\t"
            "global_load_lds_dwordx4 %[x1], s[68:69] offset:1024\n\t"
            "s_waitcnt vmcnt(9)\n\t"
            "v_lshl_add_u32 v162, v136, 8, %[lutl]\n\t"
            "ds_read_u16 v162, v162 offset:0\n\t"
            "v_lshl_add_u32 v170, v137, 8, %[lutl]\n\t"
            "ds_read_u16 v170, v170 offset:2\n\t"
            "v_lshl_add_u32 v178, v138, 8, %[lutl]\n\t"
            "ds_read_u16 v178, v178 offset:128\n\t"
            "v_lshl_add_u32 v186, v139, 8, %[lutl]\n\t"
            "ds_read_u16 v186, v186 offset:130\n\t"
            "global_load_lds_dwordx4 %[x2], s[68:69] offset:2048\n\t"
            "s_waitcnt vmcnt(9)\n\t"
            "v_lshl_add_u32 v163, v140, 8, %[lutl]\n\t"
            "ds_read_u16 v163, v163 offset:0\n\t"
            "v_lshl_add_u32 v171, v141, 8, %[lutl]\n\t"
            "ds_read_u16 v171, v171 offset:2\n\t"
            "v_lshl_add_u32 v179, v142, 8, %[lutl]\n\t"
            "ds_read_u16 v179, v179 offset:128\n\t"
            "v_lshl_add_u32 v187, v143, 8, %[lutl]\n\t"
            "ds_read_u16 v187, v187 offset:130\n\t"
            "global_load_lds_dwordx4 %[x3], s[68:69] offset:3072\n\t"
            "s_waitcnt vmcnt(9)\n\t"
            "v_lshl_add_u32 v164, v144, 8, %[lutl]\n\t"
            "ds_read_u16 v164, v164 offset:0\n\t"
            "v_lshl_add_u32 v172, v145, 8, %[lutl]\n\t"
            "ds_read_u16 v172, v172 offset:2\n\t"
            "v_lshl_add_u32 v180, v146, 8, %[lutl]\n\t"
            "ds_read_u16 v180, v180 offset:128\n\t"
            "v_lshl_add_u32 v188, v147, 8, %[lutl]\n\t"
            "ds_read_u16 v188, v188 offset:130\n\t"
            "s_add_u32 m0, %[ldsx], 36864\n\t"
            "s_lshl_b32 s76, s41, 7\n\t"
            "s_add_u32 s76, s76, 65536\n\t"
            "s_add_u32 s68, %[xpl], s76\n\t"
            "s_addc_u32 s69, %[xph], 0\n\t"
            "global_load_lds_dwordx4 %[x0], s[68:69] offset:0\n\t"
            "s_waitcnt vmcnt(9)\n\t"
            "v_lshl_add_u32 v165, v148, 8, %[lutl]\n\t"
            "ds_read_u16 v165, v165 offset:0\n\t"
            "v_lshl_add_u32 v173, v149, 8, %[lutl]\n\t"
            "ds_read_u16 v173, v173 offset:2\n\t"
            "v_lshl_add_u32 v181, v150, 8, %[lutl]\n\t"
            "ds_read_u16 v181, v181 offset:128\n\t"
            "v_lshl_add_u32 v189, v151, 8, %[lutl]\n\t"
            "ds_read_u16 v189, v189 offset:130\n\t"
            "global_load_lds_dwordx4 %[x1], s[68:69] offset:1024\n\t"
            "s_waitcnt vmcnt(9)\n\t"
            "v_lshl_add_u32 v166, v152, 8, %[lutl]\n\t"
            "ds_read_u16 v166, v166 offset:0\n\t"
            "v_lshl_add_u32 v174, v153, 8, %[lutl]\n\t"
            "ds_read_u16 v174, v174 offset:2\n\t"
            "v_lshl_add_u32 v182, v154, 8, %[lutl]\n\t"
            "ds_read_u16 v182, v182 offset:128\n\t"
            "v_lshl_add_u32 v190, v155, 8, %[lutl]\n\t"
            "ds_read_u16 v190, v190 offset:130\n\t"
            "global_load_lds_dwordx4 %[x2], s[68:69] offset:2048\n\t"
            "s_waitcnt vmcnt(9)\n\t"
            "v_lshl_add_u32 v167, v156, 8, %[lutl]\n\t"
            "ds_read_u16 v167, v167 offset:0\n\t"
            "v_lshl_add_u32 v175, v157, 8, %[lutl]\n\t"
            "ds_read_u16 v175, v175 offset:2\n\t"
            "v_lshl_add_u32 v183, v158, 8, %[lutl]\n\t"
            "ds_read_u16 v183, v183 offset:128\n\t"
            "v_lshl_add_u32 v191, v159, 8, %[lutl]\n\t"
            "ds_read_u16 v191, v191 offset:130\n\t"
            "global_load_lds_dwordx4 %[x3], s[68:69] offset:3072\n\t"
            "s_setprio 0\n\t"
            "s_waitcnt vmcnt(8)\n\t"
            "s_mov_b64 exec, 3\n\t"
            "global_store_dword %[fo0], %[nvl], %[ws] offset:0 sc1\n\t"
            "s_mov_b64 exec, -1\n\t"
            "s_waitcnt lgkmcnt(3)\n\t"
            "v_lshl_or_b32 v128, v161, 16, v160\n\t"
            "v_lshl_or_b32 v129, v163, 16, v162\n\t"
            "v_lshl_or_b32 v130, v165, 16, v164\n\t"
            "v_lshl_or_b32 v131, v167, 16, v166\n\t"
            "ds_write_b128 %[bd], v[128:131] offset:16384\n\t"
            "s_waitcnt lgkmcnt(3)\n\t"
            "v_lshl_or_b32 v132, v169, 16, v168\n\t"
            "v_lshl_or_b32 v133, v171, 16, v170\n\t"
            "v_lshl_or_b32 v134, v173, 16, v172\n\t"
            "v_lshl_or_b32 v135, v175, 16, v174\n\t"
            "ds_write_b128 %[bd], v[132:135] offset:16640\n\t"
            "s_waitcnt lgkmcnt(3)\n\t"
            "v_lshl_or_b32 v136, v177, 16, v176\n\t"
            "v_lshl_or_b32 v137, v179, 16, v178\n\t"
            "v_lshl_or_b32 v138, v181, 16, v180\n\t"
            "v_lshl_or_b32 v139, v183, 16, v182\n\t"
            "ds_write_b128 %[bd], v[136:139] offset:16896\n\t"
            "s_waitcnt lgkmcnt(3)\n\t"
            "v_lshl_or_b32 v140, v185, 16, v184\n\t"
            "v_lshl_or_b32 v141, v187, 16, v186\n\t"
            "v_lshl_or_b32 v142, v189, 16, v188\n\t"
            "v_lshl_or_b32 v143, v191, 16, v190\n\t"
            "ds_write_b128 %[bd], v[140:143] offset:17152\n\t"
            "s_add_u32 m0, %[ldsx], 65536\n\t"
            "s_lshl_b32 s76, s42, 7\n\t"
            "s_add_u32 s68, %[xpl], s76\n\t"
            "s_addc_u32 s69, %[xph], 0\n\t"
            "global_load_lds_dwordx4 %[x0], s[68:69] offset:0\n\t"
            "global_load_lds_dwordx4 %[x1], s[68:69] offset:1024\n\t"
            "global_load_lds_dwordx4 %[x2], s[68:69] offset:2048\n\t"
            "global_load_lds_dwordx4 %[x3], s[68:69] offset:3072\n\t"
            "s_add_u32 m0, %[ldsx], 69632\n\t"
            "s_lshl_b32 s76, s42, 7\n\t"
            "s_add_u32 s76, s76, 65536\n\t"
            "s_add_u32 s68, %[xpl], s76\n\t"
            "s_addc_u32 s69, %[xph], 0\n\t"
            "global_load_lds_dwordx4 %[x0], s[68:69] offset:0\n\t"
            "global_load_lds_dwordx4 %[x1], s[68:69] offset:1024\n\t"
            "global_load_lds_dwordx4 %[x2], s[68:69] offset:2048\n\t"
            "global_load_lds_dwordx4 %[x3], s[68:69] offset:3072\n\t"
            "s_waitcnt vmcnt(9) lgkmcnt(0)\n\t"
            "s_barrier\n\t"
            ".Lwp32:\n\t"
            "s_cbranch_execz .Lwp98\n\t"
            "global_load_dword v192, %[po], %[ws] sc1\n\t"
            "s_add_u32 m0, %[ldsx], 0\n\t"
            "s_lshl_b32 s76, s43, 7\n\t"
            "s_add_u32 s68, %[xpl], s76\n\t"
            "s_addc_u32 s69, %[xph], 0\n\t"
            "global_load_lds_dwordx4 %[x0], s[68:69] offset:0\n\t"
            "global_load_lds_dwordx4 %[x1], s[68:69] offset:1024\n\t"
            "global_load_lds_dwordx4 %[x2], s[68:69] offset:2048\n\t"
            "global_load_lds_dwordx4 %[x3], s[68:69] offset:3072\n\t"
            "s_add_u32 m0, %[ldsx], 4096\n\t"
            "s_lshl_b32 s76, s43, 7\n\t"
            "s_add_u32 s76, s76, 65536\n\t"
            "s_add_u32 s68, %[xpl], s76\n\t"
            "s_addc_u32 s69, %[xph], 0\n\t"
            "global_load_lds_dwordx4 %[x0], s[68:69] offset:0\n\t"
            "global_load_lds_dwordx4 %[x1], s[68:69] offset:1024\n\t"
            "global_load_lds_dwordx4 %[x2], s[68:69] offset:2048\n\t"
            "global_load_lds_dwordx4 %[x3], s[68:69] offset:3072\n\t"
            "global_load_dword v195, %[po], %[ws] sc1\n\t"
            "s_waitcnt vmcnt(9)\n\t"
            "s_lshl_b32 s76, s42, 2\n\t"
            ".Lzc0:\n\t"
            "v_cmp_eq_u32 vcc, v192, %[nvl]\n\t"
            "s_lshr_b64 s[74:75], vcc, s76\n\t"
            "s_and_b32 s74, s74, 15\n\t"
            "s_cmp_eq_u32 s74, 15\n\t"
            "s_cbranch_scc1 .Lzd0\n\t"
            "s_cmp_eq_u32 s73, 0\n\t"
            "s_cbranch_scc1 .Lzd0\n\t"
            "s_sub_u32 s73, s73, 1\n\t"
            "s_cmp_eq_u32 s72, 1\n\t"
            "s_cbranch_scc1 .Lze0\n\t"
            "s_mov_b32 s72, 1\n\t"
            "s_waitcnt vmcnt(0)\n\t"
            "v_mov_b32 v192, v195\n\t"
            "s_branch .Lzc0\n\t"
            ".Lze0:\n\t"
            "global_load_dword v192, %[po], %[ws] sc1\n\t"
            "s_waitcnt vmcnt(0)\n\t"
            "v_mov_b32 v195, v192\n\t"
            "s_branch .Lzc0\n\t"
            ".Lzd0:\n\t"
            "s_add_u32 m0, %[ldsw], 32768\n\t"
            "s_mul_i32 s76, s42, 458752\n\t"
            "s_add_u32 s70, s84, s76\n\t"
            "s_addc_u32 s71, s85, 0\n\t"
            "global_load_lds_dwordx4 %[wv], s[70:71] offset:0 sc1\n\t"
            "global_load_lds_dwordx4 %[wv], s[70:71] offset:1024 sc1\n\t"
            "global_load_lds_dwordx4 %[wv], s[70:71] offset:2048 sc1\n\t"
            "global_load_lds_dwordx4 %[wv], s[70:71] offset:3072 sc1\n\t"
            "s_cmp_lg_u32 %[h2], 0\n\t"
            "s_cbranch_scc1 .Lzs1\n\t"
            "global_store_dwordx4 %[wo], v[128:131], %[st1] sc1\n\t"
            "global_store_dwordx4 %[wo], v[132:135], %[st1] offset:256 sc1\n\t"
            "s_branch .Lzt1\n\t"
            ".Lzs1:\n\t"
            "global_store_dwordx4 %[wo], v[136:139], %[st1] sc1\n\t"
            "global_store_dwordx4 %[wo], v[140:143], %[st1] offset:256 sc1\n\t"
            ".Lzt1:\n\t"
            "s_waitcnt vmcnt(6)\n\t"
            "v_mov_b32 v192, v195\n\t"
            "s_lshl_b32 s76, s43, 2\n\t"
            ".Lzc2:\n\t"
            "v_cmp_eq_u32 vcc, v192, %[nvl]\n\t"
            "s_lshr_b64 s[74:75], vcc, s76\n\t"
            "s_and_b32 s74, s74, 15\n\t"
            "s_cmp_eq_u32 s74, 15\n\t"
            "s_cbranch_scc1 .Lzd2\n\t"
            "s_cmp_eq_u32 s73, 0\n\t"
            "s_cbranch_scc1 .Lzd2\n\t"
            "s_sub_u32 s73, s73, 1\n\t"
            "global_load_dword v192, %[po], %[ws] sc1\n\t"
            "s_waitcnt vmcnt(0)\n\t"
            "s_branch .Lzc2\n\t"
            ".Lzd2:\n\t"
            "s_add_u32 m0, %[ldsw], 0\n\t"
            "s_mul_i32 s76, s43, 458752\n\t"
            "s_add_u32 s70, s84, s76\n\t"
            "s_addc_u32 s71, s85, 0\n\t"
            "global_load_lds_dwordx4 %[wv], s[70:71] offset:0 sc1\n\t"
            "global_load_lds_dwordx4 %[wv], s[70:71] offset:1024 sc1\n\t"
            "global_load_lds_dwordx4 %[wv], s[70:71] offset:2048 sc1\n\t"
            "global_load_lds_dwordx4 %[wv], s[70:71] offset:3072 sc1\n\t"
            "s_waitcnt vmcnt(6)\n\t"
            "s_barrier\n\t"
            ".Lwp33:\n\t"
            "s_cbranch_execz .Lwp98\n\t"
            "s_lshl_b32 s76, s44, 2\n\t"
            ".Lzc3:\n\t"
            "v_cmp_eq_u32 vcc, v192, %[nvl]\n\t"
            "s_lshr_b64 s[74:75], vcc, s76\n\t"
            "s_and_b32 s74, s74, 15\n\t"
            "s_cmp_eq_u32 s74, 15\n\t"
            "s_cbranch_scc1 .Lzd3\n\t"
            "s_cmp_eq_u32 s73, 0\n\t"
            "s_cbranch_scc1 .Lzd3\n\t"
            "s_sub_u32 s73, s73, 1\n\t"
            "global_load_dword v192, %[po], %[ws] sc1\n\t"
            "s_waitcnt vmcnt(0)\n\t"
            "s_branch .Lzc3\n\t"
            ".Lzd3:\n\t"
            "s_add_u32 m0, %[ldsw], 16384\n\t"
            "s_mul_i32 s76, s44, 458752\n\t"
            "s_add_u32 s70, s84, s76\n\t"
            "s_addc_u32 s71, s85, 0\n\t"
            "global_load_lds_dwordx4 %[wv], s[70:71] offset:0 sc1\n\t"
            "global_load_lds_dwordx4 %[wv], s[70:71] offset:1024 sc1\n\t"
            "global_load_lds_dwordx4 %[wv], s[70:71] offset:2048 sc1\n\t"
            "global_load_lds_dwordx4 %[wv], s[70:71] offset:3072 sc1\n\t"
            "s_waitcnt vmcnt(8)\n\t"
            "s_mov_b64 exec, 3\n\t"
            "global_store_dword %[fo0], %[nvl], %[ws] offset:64 sc1\n\t"
            "s_mov_b64 exec, -1\n\t"
            "v_mbcnt_lo_u32_b32 v193, -1, 0\n\t"
            "v_mbcnt_hi_u32_b32 v193, -1, v193\n\t"
            "v_lshrrev_b32 v193, 5, v193\n\t"
            "v_mul_u32_u24 v194, 57344, v193\n\t"
            "v_sub_u32 %[qo], %[qo], v194\n\t"
            "v_mul_u32_u24 v194, 1792, v193\n\t"
            "v_sub_u32 %[wo], %[wo], v194\n\t"
            "s_mov_b64 s[88:89], %[qbg]\n\t"
            "global_load_dwordx4 v[204:207], %[qo], s[88:89] \n\t"
            "s_add_u32 s88, s88, 14336\n\t"
            "s_addc_u32 s89, s89, 0\n\t"
            "global_load_dwordx4 v[208:211], %[qo], s[88:89] \n\t"
            "s_add_u32 s88, s88, 14336\n\t"
            "s_addc_u32 s89, s89, 0\n\t"
            "global_load_dwordx4 v[212:215], %[qo], s[88:89] \n\t"
            "s_add_u32 s88, s88, 14336\n\t"
            "s_addc_u32 s89, s89, 0\n\t"
            "global_load_dwordx4 v[216:219], %[qo], s[88:89] \n\t"
            "s_add_u32 m0, %[ldsx], 32768\n\t"
            "s_lshl_b32 s76, s44, 7\n\t"
            "s_add_u32 s68, %[xpl], s76\n\t"
            "s_addc_u32 s69, %[xph], 0\n\t"
            "global_load_lds_dwordx4 %[x0], s[68:69] offset:0\n\t"
            "global_load_lds_dwordx4 %[x1], s[68:69] offset:1024\n\t"
            "global_load_lds_dwordx4 %[x2], s[68:69] offset:2048\n\t"
            "global_load_lds_dwordx4 %[x3], s[68:69] offset:3072\n\t"
            "s_add_u32 m0, %[ldsx], 36864\n\t"
            "s_lshl_b32 s76, s44, 7\n\t"
            "s_add_u32 s76, s76, 65536\n\t"
            "s_add_u32 s68, %[xpl], s76\n\t"
            "s_addc_u32 s69, %[xph], 0\n\t"
            "global_load_lds_dwordx4 %[x0], s[68:69] offset:0\n\t"
            "global_load_lds_dwordx4 %[x1], s[68:69] offset:1024\n\t"
            "global_load_lds_dwordx4 %[x2], s[68:69] offset:2048\n\t"
            "global_load_lds_dwordx4 %[x3], s[68:69] offset:3072\n\t"
            "s_waitcnt vmcnt(17)\n\t"
            "s_barrier\n\t"
            ".Lwp34:\n\t"
            "s_cbranch_execz .Lwp35\n\t"
            "global_load_dword v192, %[po], %[ws] sc1\n\t"
            "s_waitcnt vmcnt(9)\n\t"
            "s_setprio 0\n\t"
            "s_add_u32 m0, %[ldsx], 65536\n\t"
            "s_lshl_b32 s76, s45, 7\n\t"
            "s_add_u32 s68, %[xpl], s76\n\t"
            "s_addc_u32 s69, %[xph], 0\n\t"
            "global_load_lds_dwordx4 %[x0], s[68:69] offset:0\n\t"
            "v_lshl_add_u32 v160, v204, 8, %[lutl]\n\t"
            "ds_read_u16 v160, v160 offset:0\n\t"
            "v_lshl_add_u32 v161, v208, 8, %[lutl]\n\t"
            "ds_read_u16 v161, v161 offset:0\n\t"
            "global_load_lds_dwordx4 %[x1], s[68:69] offset:1024\n\t"
            "v_lshl_add_u32 v162, v212, 8, %[lutl]\n\t"
            "ds_read_u16 v162, v162 offset:0\n\t"
            "v_lshl_add_u32 v163, v216, 8, %[lutl]\n\t"
            "ds_read_u16 v163, v163 offset:0\n\t"
            "global_load_lds_dwordx4 %[x2], s[68:69] offset:2048\n\t"
            "v_lshl_add_u32 v168, v205, 8, %[lutl]\n\t"
            "ds_read_u16 v168, v168 offset:2\n\t"
            "v_lshl_add_u32 v169, v209, 8, %[lutl]\n\t"
            "ds_read_u16 v169, v169 offset:2\n\t"
            "global_load_lds_dwordx4 %[x3], s[68:69] offset:3072\n\t"
            "v_lshl_add_u32 v170, v213, 8, %[lutl]\n\t"
            "ds_read_u16 v170, v170 offset:2\n\t"
            "v_lshl_add_u32 v171, v217, 8, %[lutl]\n\t"
            "ds_read_u16 v171, v171 offset:2\n\t"
            "s_add_u32 m0, %[ldsx], 69632\n\t"
            "s_lshl_b32 s76, s45, 7\n\t"
            "s_add_u32 s76, s76, 65536\n\t"
            "s_add_u32 s68, %[xpl], s76\n\t"
            "s_addc_u32 s69, %[xph], 0\n\t"
            "global_load_lds_dwordx4 %[x0], s[68:69] offset:0\n\t"
            "v_lshl_add_u32 v176, v206, 8, %[lutl]\n\t"
            "ds_read_u16 v176, v176 offset:128\n\t"
            "v_lshl_add_u32 v177, v210, 8, %[lutl]\n\t"
            "ds_read_u16 v177, v177 offset:128\n\t"
            "global_load_lds_dwordx4 %[x1], s[68:69] offset:1024\n\t"
            "v_lshl_add_u32 v178, v214, 8, %[lutl]\n\t"
            "ds_read_u16 v178, v178 offset:128\n\t"
            "v_lshl_add_u32 v179, v218, 8, %[lutl]\n\t"
            "ds_read_u16 v179, v179 offset:128\n\t"
            "global_load_lds_dwordx4 %[x2], s[68:69] offset:2048\n\t"
            "v_lshl_add_u32 v184, v207, 8, %[lutl]\n\t"
            "ds_read_u16 v184, v184 offset:130\n\t"
            "v_lshl_add_u32 v185, v211, 8, %[lutl]\n\t"
            "ds_read_u16 v185, v185 offset:130\n\t"
            "global_load_lds_dwordx4 %[x3], s[68:69] offset:3072\n\t"
            "v_lshl_add_u32 v186, v215, 8, %[lutl]\n\t"
            "ds_read_u16 v186, v186 offset:130\n\t"
            "v_lshl_add_u32 v187, v219, 8, %[lutl]\n\t"
            "ds_read_u16 v187, v187 offset:130\n\t"
            "s_setprio 0\n\t"
            "s_waitcnt vmcnt(8)\n\t"
            "s_lshl_b32 s76, s45, 2\n\t"
            ".Lzc4:\n\t"
            "v_cmp_eq_u32 vcc, v192, %[nvl]\n\t"
            "s_lshr_b64 s[74:75], vcc, s76\n\t"
            "s_and_b32 s74, s74, 15\n\t"
            "s_cmp_eq_u32 s74, 15\n\t"
            "s_cbranch_scc1 .Lzd4\n\t"
            "s_cmp_eq_u32 s73, 0\n\t"
            "s_cbranch_scc1 .Lzd4\n\t"
            "s_sub_u32 s73, s73, 1\n\t"
            "global_load_dword v192, %[po], %[ws] sc1\n\t"
            "s_waitcnt vmcnt(0)\n\t"
            "s_branch .Lzc4\n\t"
            ".Lzd4:\n\t"
            "s_add_u32 m0, %[ldsw], 32768\n\t"
            "s_mul_i32 s76, s45, 458752\n\t"
            "s_add_u32 s70, s84, s76\n\t"
            "s_addc_u32 s71, s85, 0\n\t"
            "global_load_lds_dwordx4 %[wv], s[70:71] offset:0 sc1\n\t"
            "global_load_lds_dwordx4 %[wv], s[70:71] offset:1024 sc1\n\t"
            "global_load_lds_dwordx4 %[wv], s[70:71] offset:2048 sc1\n\t"
            "global_load_lds_dwordx4 %[wv], s[70:71] offset:3072 sc1\n\t"
            "s_waitcnt lgkmcnt(8)\n\t"
            "v_lshl_or_b32 v220, v161, 16, v160\n\t"
            "v_lshl_or_b32 v221, v163, 16, v162\n\t"
            "v_lshl_or_b32 v222, v169, 16, v168\n\t"
            "v_lshl_or_b32 v223, v171, 16, v170\n\t"
            "s_waitcnt lgkmcnt(0)\n\t"
            "v_lshl_or_b32 v224, v177, 16, v176\n\t"
            "v_lshl_or_b32 v225, v179, 16, v178\n\t"
            "v_lshl_or_b32 v226, v185, 16, v184\n\t"
            "v_lshl_or_b32 v227, v187, 16, v186\n\t"
            "s_nop 0\n\t"
            "v_permlane32_swap_b32 v220, v222\n\t"
            "v_permlane32_swap_b32 v221, v223\n\t"
            "v_permlane32_swap_b32 v224, v226\n\t"
            "v_permlane32_swap_b32 v225, v227\n\t"
            "s_mov_b64 s[88:89], %[stb]\n\t"
            "s_nop 0\n\t"
            "global_store_dwordx4 %[wo], v[220:223], s[88:89] offset:0 sc1\n\t"
            "global_store_dwordx4 %[wo], v[224:227], s[88:89] offset:512 sc1\n\t"
            "s_barrier\n\t"
            ".Lwp35:\n\t"
            "s_cbranch_execz .Lwp36\n\t"
            "s_lshl_b32 s76, s46, 2\n\t"
            ".Lzc5:\n\t"
            "v_cmp_eq_u32 vcc, v192, %[nvl]\n\t"
            "s_lshr_b64 s[74:75], vcc, s76\n\t"
            "s_and_b32 s74, s74, 15\n\t"
            "s_cmp_eq_u32 s74, 15\n\t"
            "s_cbranch_scc1 .Lzd5\n\t"
            "s_cmp_eq_u32 s73, 0\n\t"
            "s_cbranch_scc1 .Lzd5\n\t"
            "s_sub_u32 s73, s73, 1\n\t"
            "global_load_dword v192, %[po], %[ws] sc1\n\t"
            "s_waitcnt vmcnt(0)\n\t"
            "s_branch .Lzc5\n\t"
            ".Lzd5:\n\t"
            "s_add_u32 m0, %[ldsw], 0\n\t"
            "s_mul_i32 s76, s46, 458752\n\t"
            "s_add_u32 s70, s84, s76\n\t"
            "s_addc_u32 s71, s85, 0\n\t"
            "global_load_lds_dwordx4 %[wv], s[70:71] offset:0 sc1\n\t"
            "global_load_lds_dwordx4 %[wv], s[70:71] offset:1024 sc1\n\t"
            "global_load_lds_dwordx4 %[wv], s[70:71] offset:2048 sc1\n\t"
            "global_load_lds_dwordx4 %[wv], s[70:71] offset:3072 sc1\n\t"
            "s_add_u32 m0, %[ldsx], 0\n\t"
            "s_lshl_b32 s76, s46, 7\n\t"
            "s_add_u32 s68, %[xpl], s76\n\t"
            "s_addc_u32 s69, %[xph], 0\n\t"
            "global_load_lds_dwordx4 %[x0], s[68:69] offset:0\n\t"
            "global_load_lds_dwordx4 %[x1], s[68:69] offset:1024\n\t"
            "global_load_lds_dwordx4 %[x2], s[68:69] offset:2048\n\t"
            "global_load_lds_dwordx4 %[x3], s[68:69] offset:3072\n\t"
            "s_add_u32 m0, %[ldsx], 4096\n\t"
            "s_lshl_b32 s76, s46, 7\n\t"
            "s_add_u32 s76, s76, 65536\n\t"
            "s_add_u32 s68, %[xpl], s76\n\t"
            "s_addc_u32 s69, %[xph], 0\n\t"
            "global_load_lds_dwordx4 %[x0], s[68:69] offset:0\n\t"
            "global_load_lds_dwordx4 %[x1], s[68:69] offset:1024\n\t"
            "global_load_lds_dwordx4 %[x2], s[68:69] offset:2048\n\t"
            "global_load_lds_dwordx4 %[x3], s[68:69] offset:3072\n\t"
            "s_waitcnt vmcnt(12)\n\t"
            "s_mov_b64 exec, 3\n\t"
            "global_store_dword %[fob], %[nvl], %[ws] offset:0 sc1\n\t"
            "s_mov_b64 exec, -1\n\t"
            "s_barrier\n\t"
            "s_lshl_b32 s76, s47, 2\n\t"
            ".Lzc6:\n\t"
            "v_cmp_eq_u32 vcc, v192, %[nvl]\n\t"
            "s_lshr_b64 s[74:75], vcc, s76\n\t"
            "s_and_b32 s74, s74, 15\n\t"
            "s_cmp_eq_u32 s74, 15\n\t"
            "s_cbranch_scc1 .Lzd6\n\t"
            "s_cmp_eq_u32 s73, 0\n\t"
            "s_cbranch_scc1 .Lzd6\n\t"
            "s_sub_u32 s73, s73, 1\n\t"
            "global_load_dword v192, %[po], %[ws] sc1\n\t"
            "s_waitcnt vmcnt(0)\n\t"
            "s_branch .Lzc6\n\t"
            ".Lzd6:\n\t"
            "s_add_u32 m0, %[ldsw], 16384\n\t"
            "s_mul_i32 s76, s47, 458752\n\t"
            "s_add_u32 s70, s84, s76\n\t"
            "s_addc_u32 s71, s85, 0\n\t"
            "global_load_lds_dwordx4 %[wv], s[70:71] offset:0 sc1\n\t"
            "global_load_lds_dwordx4 %[wv], s[70:71] offset:1024 sc1\n\t"
            "global_load_lds_dwordx4 %[wv], s[70:71] offset:2048 sc1\n\t"
            "global_load_lds_dwordx4 %[wv], s[70:71] offset:3072 sc1\n\t"
            "s_add_u32 m0, %[ldsx], 32768\n\t"
            "s_lshl_b32 s76, s47, 7\n\t"
            "s_add_u32 s68, %[xpl], s76\n\t"
            "s_addc_u32 s69, %[xph], 0\n\t"
            "global_load_lds_dwordx4 %[x0], s[68:69] offset:0\n\t"
            "global_load_lds_dwordx4 %[x1], s[68:69] offset:1024\n\t"
            "global_load_lds_dwordx4 %[x2], s[68:69] offset:2048\n\t"
            "global_load_lds_dwordx4 %[x3], s[68:69] offset:3072\n\t"
            "s_add_u32 m0, %[ldsx], 36864\n\t"
            "s_lshl_b32 s76, s47, 7\n\t"
            "s_add_u32 s76, s76, 65536\n\t"
            "s_add_u32 s68, %[xpl], s76\n\t"
            "s_addc_u32 s69, %[xph], 0\n\t"
            "global_load_lds_dwordx4 %[x0], s[68:69] offset:0\n\t"
            "global_load_lds_dwordx4 %[x1], s[68:69] offset:1024\n\t"
            "global_load_lds_dwordx4 %[x2], s[68:69] offset:2048\n\t"
            "global_load_lds_dwordx4 %[x3], s[68:69] offset:3072\n\t"
            "s_mov_b64 s[88:89], %[qbg]\n\t"
            "s_add_u32 s88, s88, 3670016\n\t"
            "s_addc_u32 s89, s89, 0\n\t"
            "global_load_dwordx4 v[204:207], %[qo], s[88:89] \n\t"
            "s_add_u32 s88, s88, 14336\n\t"
            "s_addc_u32 s89, s89, 0\n\t"
            "global_load_dwordx4 v[208:211], %[qo], s[88:89] \n\t"
            "s_add_u32 s88, s88, 14336\n\t"
            "s_addc_u32 s89, s89, 0\n\t"
            "global_load_dwordx4 v[212:215], %[qo], s[88:89] \n\t"
            "s_add_u32 s88, s88, 14336\n\t"
            "s_addc_u32 s89, s89, 0\n\t"
            "global_load_dwordx4 v[216:219], %[qo], s[88:89] \n\t"
            "s_waitcnt vmcnt(17)\n\t"
            "s_barrier\n\t"
            ".Lwp36:\n\t"
            "s_cbranch_execz .Lwp37\n\t"
            "global_load_dword v192, %[po], %[ws] sc1\n\t"
            "s_add_u32 m0, %[ldsx], 65536\n\t"
            "s_lshl_b32 s76, s48, 7\n\t"
            "s_add_u32 s68, %[xpl], s76\n\t"
            "s_addc_u32 s69, %[xph], 0\n\t"
            "global_load_lds_dwordx4 %[x0], s[68:69] offset:0\n\t"
            "global_load_lds_dwordx4 %[x1], s[68:69] offset:1024\n\t"
            "global_load_lds_dwordx4 %[x2], s[68:69] offset:2048\n\t"
            "global_load_lds_dwordx4 %[x3], s[68:69] offset:3072\n\t"
            "s_add_u32 m0, %[ldsx], 69632\n\t"
            "s_lshl_b32 s76, s48, 7\n\t"
            "s_add_u32 s76, s76, 65536\n\t"
            "s_add_u32 s68, %[xpl], s76\n\t"
            "s_addc_u32 s69, %[xph], 0\n\t"
            "global_load_lds_dwordx4 %[x0], s[68:69] offset:0\n\t"
            "global_load_lds_dwordx4 %[x1], s[68:69] offset:1024\n\t"
            "global_load_lds_dwordx4 %[x2], s[68:69] offset:2048\n\t"
            "global_load_lds_dwordx4 %[x3], s[68:69] offset:3072\n\t"
            "s_waitcnt vmcnt(8)\n\t"
            "s_lshl_b32 s76, s48, 2\n\t"
            ".Lzc7:\n\t"
            "v_cmp_eq_u32 vcc, v192, %[nvl]\n\t"
            "s_lshr_b64 s[74:75], vcc, s76\n\t"
            "s_and_b32 s74, s74, 15\n\t"
            "s_cmp_eq_u32 s74, 15\n\t"
            "s_cbranch_scc1 .Lzd7\n\t"
            "s_cmp_eq_u32 s73, 0\n\t"
            "s_cbranch_scc1 .Lzd7\n\t"
            "s_sub_u32 s73, s73, 1\n\t"
            "global_load_dword v192, %[po], %[ws] sc1\n\t"
            "s_waitcnt vmcnt(0)\n\t"
            "s_branch .Lzc7\n\t"
            ".Lzd7:\n\t"
            "s_add_u32 m0, %[ldsw], 32768\n\t"
            "s_mul_i32 s76, s48, 458752\n\t"
            "s_add_u32 s70, s84, s76\n\t"
            "s_addc_u32 s71, s85, 0\n\t"
            "global_load_lds_dwordx4 %[wv], s[70:71] offset:0 sc1\n\t"
            "global_load_lds_dwordx4 %[wv], s[70:71] offset:1024 sc1\n\t"
            "global_load_lds_dwordx4 %[wv], s[70:71] offset:2048 sc1\n\t"
            "global_load_lds_dwordx4 %[wv], s[70:71] offset:3072 sc1\n\t"
            "s_barrier\n\t"
            ".Lwp37:\n\t"
            "s_cbranch_execz .Lwp97\n\t"
            "s_lshl_b32 s76, s49, 2\n\t"
            ".Lzc8:\n\t"
            "v_cmp_eq_u32 vcc, v192, %[nvl]\n\t"
            "s_lshr_b64 s[74:75], vcc, s76\n\t"
            "s_and_b32 s74, s74, 15\n\t"
            "s_cmp_eq_u32 s74, 15\n\t"
            "s_cbranch_scc1 .Lzd8\n\t"
            "s_cmp_eq_u32 s73, 0\n\t"
            "s_cbranch_scc1 .Lzd8\n\t"
            "s_sub_u32 s73, s73, 1\n\t"
            "global_load_dword v192, %[po], %[ws] sc1\n\t"
            "s_waitcnt vmcnt(0)\n\t"
            "s_branch .Lzc8\n\t"
            ".Lzd8:\n\t"
            "s_add_u32 m0, %[ldsw], 0\n\t"
            "s_mul_i32 s76, s49, 458752\n\t"
            "s_add_u32 s70, s84, s76\n\t"
            "s_addc_u32 s71, s85, 0\n\t"
            "global_load_lds_dwordx4 %[wv], s[70:71] offset:0 sc1\n\t"
            "global_load_lds_dwordx4 %[wv], s[70:71] offset:1024 sc1\n\t"
            "global_load_lds_dwordx4 %[wv], s[70:71] offset:2048 sc1\n\t"
            "global_load_lds_dwordx4 %[wv], s[70:71] offset:3072 sc1\n\t"
            "s_setprio 0\n\t"
            "s_add_u32 m0, %[ldsx], 0\n\t"
            "s_lshl_b32 s76, s49, 7\n\t"
            "s_add_u32 s68, %[xpl], s76\n\t"
            "s_addc_u32 s69, %[xph], 0\n\t"
            "global_load_lds_dwordx4 %[x0], s[68:69] offset:0\n\t"
            "v_lshl_add_u32 v160, v204, 8, %[lutl]\n\t"
            "ds_read_u16 v160, v160 offset:0\n\t"
            "v_lshl_add_u32 v161, v208, 8, %[lutl]\n\t"
            "ds_read_u16 v161, v161 offset:0\n\t"
            "global_load_lds_dwordx4 %[x1], s[68:69] offset:1024\n\t"
            "v_lshl_add_u32 v162, v212, 8, %[lutl]\n\t"
            "ds_read_u16 v162, v162 offset:0\n\t"
            "v_lshl_add_u32 v163, v216, 8, %[lutl]\n\t"
            "ds_read_u16 v163, v163 offset:0\n\t"
            "global_load_lds_dwordx4 %[x2], s[68:69] offset:2048\n\t"
            "v_lshl_add_u32 v168, v205, 8, %[lutl]\n\t"
            "ds_read_u16 v168, v168 offset:2\n\t"
            "v_lshl_add_u32 v169, v209, 8, %[lutl]\n\t"
            "ds_read_u16 v169, v169 offset:2\n\t"
            "global_load_lds_dwordx4 %[x3], s[68:69] offset:3072\n\t"
            "v_lshl_add_u32 v170, v213, 8, %[lutl]\n\t"
            "ds_read_u16 v170, v170 offset:2\n\t"
            "v_lshl_add_u32 v171, v217, 8, %[lutl]\n\t"
            "ds_read_u16 v171, v171 offset:2\n\t"
            "s_add_u32 m0, %[ldsx], 4096\n\t"
            "s_lshl_b32 s76, s49, 7\n\t"
            "s_add_u32 s76, s76, 65536\n\t"
            "s_add_u32 s68, %[xpl], s76\n\t"
            "s_addc_u32 s69, %[xph], 0\n\t"
            "global_load_lds_dwordx4 %[x0], s[68:69] offset:0\n\t"
            "v_lshl_add_u32 v176, v206, 8, %[lutl]\n\t"
            "ds_read_u16 v176, v176 offset:128\n\t"
            "v_lshl_add_u32 v177, v210, 8, %[lutl]\n\t"
            "ds_read_u16 v177, v177 offset:128\n\t"
            "global_load_lds_dwordx4 %[x1], s[68:69] offset:1024\n\t"
            "v_lshl_add_u32 v178, v214, 8, %[lutl]\n\t"
            "ds_read_u16 v178, v178 offset:128\n\t"
            "v_lshl_add_u32 v179, v218, 8, %[lutl]\n\t"
            "ds_read_u16 v179, v179 offset:128\n\t"
            "global_load_lds_dwordx4 %[x2], s[68:69] offset:2048\n\t"
            "v_lshl_add_u32 v184, v207, 8, %[lutl]\n\t"
            "ds_read_u16 v184, v184 offset:130\n\t"
            "v_lshl_add_u32 v185, v211, 8, %[lutl]\n\t"
            "ds_read_u16 v185, v185 offset:130\n\t"
            "global_load_lds_dwordx4 %[x3], s[68:69] offset:3072\n\t"
            "v_lshl_add_u32 v186, v215, 8, %[lutl]\n\t"
            "ds_read_u16 v186, v186 offset:130\n\t"
            "v_lshl_add_u32 v187, v219, 8, %[lutl]\n\t"
            "ds_read_u16 v187, v187 offset:130\n\t"
            "s_setprio 0\n\t"
            "s_waitcnt lgkmcnt(8)\n\t"
            "v_lshl_or_b32 v220, v161, 16, v160\n\t"
            "v_lshl_or_b32 v221, v163, 16, v162\n\t"
            "v_lshl_or_b32 v222, v169, 16, v168\n\t"
            "v_lshl_or_b32 v223, v171, 16, v170\n\t"
            "s_waitcnt lgkmcnt(0)\n\t"
            "v_lshl_or_b32 v224, v177, 16, v176\n\t"
            "v_lshl_or_b32 v225, v179, 16, v178\n\t"
            "v_lshl_or_b32 v226, v185, 16, v184\n\t"
            "v_lshl_or_b32 v227, v187, 16, v186\n\t"
            "s_nop 0\n\t"
            "v_permlane32_swap_b32 v220, v222\n\t"
            "v_permlane32_swap_b32 v221, v223\n\t"
            "v_permlane32_swap_b32 v224, v226\n\t"
            "v_permlane32_swap_b32 v225, v227\n\t"
            "s_mov_b64 s[88:89], %[stb]\n\t"
            "s_add_u32 s88, s88, 1835008\n\t"
            "s_addc_u32 s89, s89, 0\n\t"
            "global_store_dwordx4 %[wo], v[220:223], s[88:89] offset:0 sc1\n\t"
            "global_store_dwordx4 %[wo], v[224:227], s[88:89] offset:512 sc1\n\t"
            "s_waitcnt vmcnt(14)\n\t"
            "s_barrier\n\t"
            ".Lwp38:\n\t"
            "s_cbranch_execz .Lwp39\n\t"
            "s_lshl_b32 s76, s50, 2\n\t"
            ".Lzc9:\n\t"
            "v_cmp_eq_u32 vcc, v192, %[nvl]\n\t"
            "s_lshr_b64 s[74:75], vcc, s76\n\t"
            "s_and_b32 s74, s74, 15\n\t"
            "s_cmp_eq_u32 s74, 15\n\t"
            "s_cbranch_scc1 .Lzd9\n\t"
            "s_cmp_eq_u32 s73, 0\n\t"
            "s_cbranch_scc1 .Lzd9\n\t"
            "s_sub_u32 s73, s73, 1\n\t"
            "global_load_dword v192, %[po], %[ws] sc1\n\t"
            "s_waitcnt vmcnt(0)\n\t"
            "s_branch .Lzc9\n\t"
            ".Lzd9:\n\t"
            "s_add_u32 m0, %[ldsw], 16384\n\t"
            "s_mul_i32 s76, s50, 458752\n\t"
            "s_add_u32 s70, s84, s76\n\t"
            "s_addc_u32 s71, s85, 0\n\t"
            "global_load_lds_dwordx4 %[wv], s[70:71] offset:0 sc1\n\t"
            "global_load_lds_dwordx4 %[wv], s[70:71] offset:1024 sc1\n\t"
            "global_load_lds_dwordx4 %[wv], s[70:71] offset:2048 sc1\n\t"
            "global_load_lds_dwordx4 %[wv], s[70:71] offset:3072 sc1\n\t"
            "s_add_u32 m0, %[ldsx], 32768\n\t"
            "s_lshl_b32 s76, s50, 7\n\t"
            "s_add_u32 s68, %[xpl], s76\n\t"
            "s_addc_u32 s69, %[xph], 0\n\t"
            "global_load_lds_dwordx4 %[x0], s[68:69] offset:0\n\t"
            "global_load_lds_dwordx4 %[x1], s[68:69] offset:1024\n\t"
            "global_load_lds_dwordx4 %[x2], s[68:69] offset:2048\n\t"
            "global_load_lds_dwordx4 %[x3], s[68:69] offset:3072\n\t"
            "s_add_u32 m0, %[ldsx], 36864\n\t"
            "s_lshl_b32 s76, s50, 7\n\t"
            "s_add_u32 s76, s76, 65536\n\t"
            "s_add_u32 s68, %[xpl], s76\n\t"
            "s_addc_u32 s69, %[xph], 0\n\t"
            "global_load_lds_dwordx4 %[x0], s[68:69] offset:0\n\t"
            "global_load_lds_dwordx4 %[x1], s[68:69] offset:1024\n\t"
            "global_load_lds_dwordx4 %[x2], s[68:69] offset:2048\n\t"
            "global_load_lds_dwordx4 %[x3], s[68:69] offset:3072\n\t"
            "s_waitcnt vmcnt(12)\n\t"
            "s_mov_b64 exec, 3\n\t"
            "global_store_dword %[fob], %[nvl], %[ws] offset:64 sc1\n\t"
            "s_mov_b64 exec, -1\n\t"
            "s_barrier\n\t"
            "s_lshl_b32 s76, s51, 2\n\t"
            ".Lzc10:\n\t"
            "v_cmp_eq_u32 vcc, v192, %[nvl]\n\t"
            "s_lshr_b64 s[74:75], vcc, s76\n\t"
            "s_and_b32 s74, s74, 15\n\t"
            "s_cmp_eq_u32 s74, 15\n\t"
            "s_cbranch_scc1 .Lzd10\n\t"
            "s_cmp_eq_u32 s73, 0\n\t"
            "s_cbranch_scc1 .Lzd10\n\t"
            "s_sub_u32 s73, s73, 1\n\t"
            "global_load_dword v192, %[po], %[ws] sc1\n\t"
            "s_waitcnt vmcnt(0)\n\t"
            "s_branch .Lzc10\n\t"
            ".Lzd10:\n\t"
            "s_add_u32 m0, %[ldsw], 32768\n\t"
            "s_mul_i32 s76, s51, 458752\n\t"
            "s_add_u32 s70, s84, s76\n\t"
            "s_addc_u32 s71, s85, 0\n\t"
            "global_load_lds_dwordx4 %[wv], s[70:71] offset:0 sc1\n\t"
            "global_load_lds_dwordx4 %[wv], s[70:71] offset:1024 sc1\n\t"
            "global_load_lds_dwordx4 %[wv], s[70:71] offset:2048 sc1\n\t"
            "global_load_lds_dwordx4 %[wv], s[70:71] offset:3072 sc1\n\t"
            "s_add_u32 m0, %[ldsx], 65536\n\t"
            "s_lshl_b32 s76, s51, 7\n\t"
            "s_add_u32 s68, %[xpl], s76\n\t"
            "s_addc_u32 s69, %[xph], 0\n\t"
            "global_load_lds_dwordx4 %[x0], s[68:69] offset:0\n\t"
            "global_load_lds_dwordx4 %[x1], s[68:69] offset:1024\n\t"
            "global_load_lds_dwordx4 %[x2], s[68:69] offset:2048\n\t"
            "global_load_lds_dwordx4 %[x3], s[68:69] offset:3072\n\t"
            "s_add_u32 m0, %[ldsx], 69632\n\t"
            "s_lshl_b32 s76, s51, 7\n\t"
            "s_add_u32 s76, s76, 65536\n\t"
            "s_add_u32 s68, %[xpl], s76\n\t"
            "s_addc_u32 s69, %[xph], 0\n\t"
            "global_load_lds_dwordx4 %[x0], s[68:69] offset:0\n\t"
            "global_load_lds_dwordx4 %[x1], s[68:69] offset:1024\n\t"
            "global_load_lds_dwordx4 %[x2], s[68:69] offset:2048\n\t"
            "global_load_lds_dwordx4 %[x3], s[68:69] offset:3072\n\t"
            "s_waitcnt vmcnt(13)\n\t"
            "s_barrier\n\t"
            ".Lwp39:\n\t"
            "s_cbranch_execz .Lwp40\n\t"
            "global_load_dword v192, %[po], %[ws] sc1\n\t"
            "s_add_u32 m0, %[ldsx], 0\n\t"
            "s_lshl_b32 s76, s52, 7\n\t"
            "s_add_u32 s68, %[xpl], s76\n\t"
            "s_addc_u32 s69, %[xph], 0\n\t"
            "global_load_lds_dwordx4 %[x0], s[68:69] offset:0\n\t"
            "global_load_lds_dwordx4 %[x1], s[68:69] offset:1024\n\t"
            "global_load_lds_dwordx4 %[x2], s[68:69] offset:2048\n\t"
            "global_load_lds_dwordx4 %[x3], s[68:69] offset:3072\n\t"
            "s_add_u32 m0, %[ldsx], 4096\n\t"
            "s_lshl_b32 s76, s52, 7\n\t"
            "s_add_u32 s76, s76, 65536\n\t"
            "s_add_u32 s68, %[xpl], s76\n\t"
            "s_addc_u32 s69, %[xph], 0\n\t"
            "global_load_lds_dwordx4 %[x0], s[68:69] offset:0\n\t"
            "global_load_lds_dwordx4 %[x1], s[68:69] offset:1024\n\t"
            "global_load_lds_dwordx4 %[x2], s[68:69] offset:2048\n\t"
            "global_load_lds_dwordx4 %[x3], s[68:69] offset:3072\n\t"
            "s_waitcnt vmcnt(8)\n\t"
            "s_lshl_b32 s76, s52, 2\n\t"
            ".Lzc11:\n\t"
            "v_cmp_eq_u32 vcc, v192, %[nvl]\n\t"
            "s_lshr_b64 s[74:75], vcc, s76\n\t"
            "s_and_b32 s74, s74, 15\n\t"
            "s_cmp_eq_u32 s74, 15\n\t"
            "s_cbranch_scc1 .Lzd11\n\t"
            "s_cmp_eq_u32 s73, 0\n\t"
            "s_cbranch_scc1 .Lzd11\n\t"
            "s_sub_u32 s73, s73, 1\n\t"
            "global_load_dword v192, %[po], %[ws] sc1\n\t"
            "s_waitcnt vmcnt(0)\n\t"
            "s_branch .Lzc11\n\t"
            ".Lzd11:\n\t"
            "s_add_u32 m0, %[ldsw], 0\n\t"
            "s_mul_i32 s76, s52, 458752\n\t"
            "s_add_u32 s70, s84, s76\n\t"
            "s_addc_u32 s71, s85, 0\n\t"
            "global_load_lds_dwordx4 %[wv], s[70:71] offset:0 sc1\n\t"
            "global_load_lds_dwordx4 %[wv], s[70:71] offset:1024 sc1\n\t"
            "global_load_lds_dwordx4 %[wv], s[70:71] offset:2048 sc1\n\t"
            "global_load_lds_dwordx4 %[wv], s[70:71] offset:3072 sc1\n\t"
            "s_barrier\n\t"
            "s_mov_b32 s76, 48\n\t"
            ".Lzc12:\n\t"
            "v_cmp_eq_u32 vcc, v192, %[nvl]\n\t"
            "s_lshr_b64 s[74:75], vcc, s76\n\t"
            "s_and_b32 s74, s74, 65535\n\t"
            "s_cmp_eq_u32 s74, 65535\n\t"
            "s_cbranch_scc1 .Lzd12\n\t"
            "s_cmp_eq_u32 s73, 0\n\t"
            "s_cbranch_scc1 .Lzd12\n\t"
            "s_sub_u32 s73, s73, 1\n\t"
            "global_load_dword v192, %[po], %[ws] sc1\n\t"
            "s_waitcnt vmcnt(0)\n\t"
            "s_branch .Lzc12\n\t"
            ".Lzd12:\n\t"
            "s_add_u32 m0, %[ldsw], 16384\n\t"
            "s_mul_i32 s76, s53, 458752\n\t"
            "s_add_u32 s70, s84, s76\n\t"
            "s_addc_u32 s71, s85, 0\n\t"
            "s_mul_i32 s76, s54, 458752\n\t"
            "s_sub_u32 s76, s76, 1024\n\t"
            "s_add_u32 s86, s84, s76\n\t"
            "s_addc_u32 s87, s85, 0\n\t"
            "global_load_lds_dwordx4 %[wv], s[70:71] offset:0 sc1\n\t"
            "global_load_lds_dwordx4 %[wv], s[86:87] offset:1024 sc1\n\t"
            "global_load_lds_dwordx4 %[wv], s[70:71] offset:2048 sc1\n\t"
            "global_load_lds_dwordx4 %[wv], s[86:87] offset:3072 sc1\n\t"
            "s_add_u32 m0, %[ldsx], 32768\n\t"
            "s_lshl_b32 s76, s53, 7\n\t"
            "s_add_u32 s68, %[xpl], s76\n\t"
            "s_addc_u32 s69, %[xph], 0\n\t"
            "global_load_lds_dwordx4 %[x0], s[68:69] offset:0\n\t"
            "global_load_lds_dwordx4 %[x1], s[68:69] offset:1024\n\t"
            "global_load_lds_dwordx4 %[x2], s[68:69] offset:2048\n\t"
            "global_load_lds_dwordx4 %[x3], s[68:69] offset:3072\n\t"
            "s_add_u32 m0, %[ldsx], 36864\n\t"
            "s_lshl_b32 s76, s53, 7\n\t"
            "s_add_u32 s76, s76, 65536\n\t"
            "s_add_u32 s68, %[xpl], s76\n\t"
            "s_addc_u32 s69, %[xph], 0\n\t"
            "global_load_lds_dwordx4 %[x0], s[68:69] offset:0\n\t"
            "global_load_lds_dwordx4 %[x1], s[68:69] offset:1024\n\t"
            "global_load_lds_dwordx4 %[x2], s[68:69] offset:2048\n\t"
            "global_load_lds_dwordx4 %[x3], s[68:69] offset:3072\n\t"
            "s_waitcnt vmcnt(12)\n\t"
            "s_barrier\n\t"
            ".Lwp40:\n\t"
            "s_cbranch_execz .Lwp97\n\t"
            "s_add_u32 m0, %[ldsx], 65536\n\t"
            "s_lshl_b32 s76, s54, 7\n\t"
            "s_add_u32 s68, %[xpl], s76\n\t"
            "s_addc_u32 s69, %[xph], 0\n\t"
            "global_load_lds_dwordx4 %[x0], s[68:69] offset:0\n\t"
            "global_load_lds_dwordx4 %[x1], s[68:69] offset:1024\n\t"
            "global_load_lds_dwordx4 %[x2], s[68:69] offset:2048\n\t"
            "global_load_lds_dwordx4 %[x3], s[68:69] offset:3072\n\t"
            "s_add_u32 m0, %[ldsx], 69632\n\t"
            "s_lshl_b32 s76, s54, 7\n\t"
            "s_add_u32 s76, s76, 65536\n\t"
            "s_add_u32 s68, %[xpl], s76\n\t"
            "s_addc_u32 s69, %[xph], 0\n\t"
            "global_load_lds_dwordx4 %[x0], s[68:69] offset:0\n\t"
            "global_load_lds_dwordx4 %[x1], s[68:69] offset:1024\n\t"
            "global_load_lds_dwordx4 %[x2], s[68:69] offset:2048\n\t"
            "global_load_lds_dwordx4 %[x3], s[68:69] offset:3072\n\t"
            "s_add_u32 m0, %[ldsw], 32768\n\t"
            "s_mul_i32 s76, s55, 458752\n\t"
            "s_add_u32 s70, s84, s76\n\t"
            "s_addc_u32 s71, s85, 0\n\t"
            "s_mul_i32 s76, s53, 458752\n\t"
            "s_add_u32 s86, s84, s76\n\t"
            "s_addc_u32 s87, s85, 0\n\t"
            "global_load_lds_dwordx4 %[wv], s[70:71] offset:0 sc1\n\t"
            "global_load_lds_dwordx4 %[wv], s[86:87] offset:1024 sc1\n\t"
            "global_load_lds_dwordx4 %[wv], s[70:71] offset:2048 sc1\n\t"
            "global_load_lds_dwordx4 %[wv], s[86:87] offset:3072 sc1\n\t"
            "s_waitcnt vmcnt(4)\n\t"
            "s_barrier\n\t"
            "s_add_u32 m0, %[ldsx], 0\n\t"
            "s_lshl_b32 s76, s55, 7\n\t"
            "s_add_u32 s68, %[xpl], s76\n\t"
            "s_addc_u32 s69, %[xph], 0\n\t"
            "global_load_lds_dwordx4 %[x0], s[68:69] offset:0\n\t"
            "global_load_lds_dwordx4 %[x1], s[68:69] offset:1024\n\t"
            "global_load_lds_dwordx4 %[x2], s[68:69] offset:2048\n\t"
            "global_load_lds_dwordx4 %[x3], s[68:69] offset:3072\n\t"
            "s_add_u32 m0, %[ldsx], 4096\n\t"
            "s_lshl_b32 s76, s55, 7\n\t"
            "s_add_u32 s76, s76, 65536\n\t"
            "s_add_u32 s68, %[xpl], s76\n\t"
            "s_addc_u32 s69, %[xph], 0\n\t"
            "global_load_lds_dwordx4 %[x0], s[68:69] offset:0\n\t"
            "global_load_lds_dwordx4 %[x1], s[68:69] offset:1024\n\t"
            "global_load_lds_dwordx4 %[x2], s[68:69] offset:2048\n\t"
            "global_load_lds_dwordx4 %[x3], s[68:69] offset:3072\n\t"
            "s_add_u32 m0, %[ldsw], 0\n\t"
            "s_mul_i32 s76, s54, 458752\n\t"
            "s_add_u32 s76, s76, 1024\n\t"
            "s_add_u32 s70, s84, s76\n\t"
            "s_addc_u32 s71, s85, 0\n\t"
            "s_mul_i32 s76, s55, 458752\n\t"
            "s_add_u32 s86, s84, s76\n\t"
            "s_addc_u32 s87, s85, 0\n\t"
            "global_load_lds_dwordx4 %[wv], s[70:71] offset:0 sc1\n\t"
            "global_load_lds_dwordx4 %[wv], s[86:87] offset:1024 sc1\n\t"
            "global_load_lds_dwordx4 %[wv], s[70:71] offset:2048 sc1\n\t"
            "global_load_lds_dwordx4 %[wv], s[86:87] offset:3072 sc1\n\t"
            "s_waitcnt vmcnt(4)\n\t"
            "s_barrier\n\t"
            ".Lwp41:\n\t"
            "s_cbranch_execz .Lwp14\n\t"
            "s_mov_b32 s76, 16\n\t"
            ".Lzc13:\n\t"
            "v_cmp_eq_u32 vcc, v192, %[nvl]\n\t"
            "s_lshr_b64 s[74:75], vcc, s76\n\t"
            "s_and_b32 s74, s74, 65535\n\t"
            "s_cmp_eq_u32 s74, 65535\n\t"
            "s_cbranch_scc1 .Lzd13\n\t"
            "s_cmp_eq_u32 s73, 0\n\t"
            "s_cbranch_scc1 .Lzd13\n\t"
            "s_sub_u32 s73, s73, 1\n\t"
            "global_load_dword v192, %[po], %[ws] sc1\n\t"
            "s_waitcnt vmcnt(0)\n\t"
            "s_branch .Lzc13\n\t"
            ".Lzd13:\n\t"
            "s_waitcnt vmcnt(0)\n\t"
            "s_barrier\n\t"
            : [nvl] "+v"(nvl), [qo] "+v"(qoff[0]), [wo] "+v"(wo)
            : [x0] "v"(xoff[0]), [x1] "v"(xoff[1]), [x2] "v"(xoff[2]), [x3] "v"(xoff[3]), [wv] "v"(wv), [po] "v"(po),
              [lutl] "v"(lutl), [fo0] "v"(fo0), [fob] "v"(fob), [bd] "v"(bdst),
              [xpl] "s"((u32)KPTR(0)), [xph] "s"((u32)(KPTR(0) >> 32)), [ws] "s"(KPTR(4)),
              [qbg] "s"(qbg), [qa1] "s"(qa1), [tseq] "s"(tseq), [p] "s"(p), [h2] "s"(h2), [ldsx] "s"(ldsx), [ldsw] "s"(ldsw), [st1] "s"(st1), [stb] "s"(stb)
            : "vcc", "scc", "memory", "s40", "s41", "s42", "s43", "s44", "s45", "s46", "s47", "s48", "s49", "s50", "s51", "s52", "s53", "s54", "s55", "s56", "s57", "s58", "s59", "s60", "s61", "s62", "s63", "s64", "s65", "s66", "s67", "s68", "s69", "s70", "s71", "s72", "s73", "s74", "s75", "s76", "s77", "s78", "s79", "s80", "s81", "s82", "s83", "s84", "s85", "s86", "s87", "s88", "s89", "v128", "v129", "v130", "v131", "v132", "v133", "v134", "v135", "v136", "v137", "v138", "v139", "v140", "v141", "v142", "v143", "v144", "v145", "v146", "v147", "v148", "v149", "v150", "v151", "v152", "v153", "v154", "v155", "v156", "v157", "v158", "v159", "v160", "v161", "v162", "v163", "v164", "v165", "v166", "v167", "v168", "v169", "v170", "v171", "v172", "v173", "v174", "v175", "v176", "v177", "v178", "v179", "v180", "v181", "v182", "v183", "v184", "v185", "v186", "v187", "v188", "v189", "v190", "v191", "v192", "v193", "v194", "v195", "v196", "v197", "v198", "v199", "v200", "v201", "v202", "v203", "v204", "v205", "v206", "v207", "v208", "v209", "v210", "v211", "v212", "v213", "v214", "v215", "v216", "v217", "v218", "v219", "v220", "v221", "v222", "v223", "v224", "v225", "v226", "v227", "v228", "v229", "v230", "v231", "v232", "v233", "v234", "v235");
        if (mt == 0 && p == 0 && lane < 2) asm volatile("global_store_dword %0, %1, %2 sc1" :: "v"(no), "v"(nvl), "s"(KPTR(4)) : "memory");
        return;
    }

    u32 oo[4];
    unsigned long ob[4];

    int xb0 = 0;
    int xb1 = 0;
    int ba;
#define LDX(a) (*(const bf16x8*)(smem + (a)))
#define MP(i, j, x0, w0, x1, w1) do { asm volatile("v_mfma_f32_16x16x32_bf16 %0, %1, %2, %0\n\tv_mfma_f32_16x16x32_bf16 %0, %3, %4, %0" \
                                      : "+v"(acc[i][j]) : "v"(x0), "v"(w0), "v"(x1), "v"(w1)); SB(); } while (0)
#define LD(d, a) do { d = LDX(a); SB(); } while (0)
#define CBAR() do { asm volatile("s_waitcnt lgkmcnt(0)" ::: "memory"); BAR(); asm volatile("" ::: "memory"); } while (0)

    {
        u32 V[4][8];
        i32x4 Q0[8];
#define PK(b_, j, n) asm volatile("s_waitcnt lgkmcnt(" #n ")\n\t" \
                                    "v_lshl_or_b32 %0, %5, 16, %4\n\tv_lshl_or_b32 %1, %7, 16, %6\n\t" \
                                    "v_lshl_or_b32 %2, %9, 16, %8\n\tv_lshl_or_b32 %3, %11, 16, %10" \
                                    : "=&v"(b_[0]), "=&v"(b_[1]), "=&v"(b_[2]), "=&v"(b_[3]) \
                                    : "v"(V[j][0]), "v"(V[j][1]), "v"(V[j][2]), "v"(V[j][3]), "v"(V[j][4]), "v"(V[j][5]), "v"(V[j][6]), "v"(V[j][7]) : "memory")
#define WR(b_, j) asm volatile("ds_write_b128 %0, %1 offset:%2" :: "v"(bdst), "v"(b_), "i"((j) * 256) : "memory")
        {
            const u32 l = tid & 31, h = (tid >> 5) & 1;
            const u32 lo = l * 8 + h * 4, qv = h * (8 * N_ * 4) + l * 16;
            const u32 ls = (u32)((w * 4 * N_ + col0) * 2), qs = (u32)(((OWN(mt, nt) * 64 + w * 16) * N_ + col0) * 4);
            const u32 ld = (u32)(size_t)(smem) + OFF_L + w * 1024;
            asm volatile("s_mov_b32 m0, %[ld]\n\t"
                         "s_waitcnt lgkmcnt(0)\n\t"
                         "s_mov_b64 s[60:61], %[lp]\n\ts_add_u32 s60, s60, %[ls]\n\ts_addc_u32 s61, s61, 0\n\t"
                         "s_mov_b64 s[62:63], %[qp]\n\ts_add_u32 s62, s62, %[qs]\n\ts_addc_u32 s63, s63, 0\n\t"
                         "global_load_lds_dword %[lo], s[60:61] offset:0\n\t"
                         "s_add_u32 s60, s60, 6912\n\ts_addc_u32 s61, s61, 0\n\t"
                         "global_load_lds_dword %[lo], s[60:61] offset:256\n\t"
                         "s_add_u32 s60, s60, 6912\n\ts_addc_u32 s61, s61, 0\n\t"
                         "global_load_lds_dword %[lo], s[60:61] offset:512\n\t"
                         "s_add_u32 s60, s60, 6912\n\ts_addc_u32 s61, s61, 0\n\t"
                         "global_load_lds_dword %[lo], s[60:61] offset:768\n\t"
                         "global_load_dwordx4 %[q0], %[qv], s[62:63]\n\t"
                         "s_add_u32 s62, s62, 14336\n\ts_addc_u32 s63, s63, 0\n\t"
                         "global_load_dwordx4 %[q1], %[qv], s[62:63]\n\t"
                         "s_add_u32 s62, s62, 14336\n\ts_addc_u32 s63, s63, 0\n\t"
                         "global_load_dwordx4 %[q2], %[qv], s[62:63]\n\t"
                         "s_add_u32 s62, s62, 14336\n\ts_addc_u32 s63, s63, 0\n\t"
                         "global_load_dwordx4 %[q3], %[qv], s[62:63]\n\t"
                         "s_add_u32 s62, s62, 14336\n\ts_addc_u32 s63, s63, 0\n\t"
                         "global_load_dwordx4 %[q4], %[qv], s[62:63]\n\t"
                         "s_add_u32 s62, s62, 14336\n\ts_addc_u32 s63, s63, 0\n\t"
                         "global_load_dwordx4 %[q5], %[qv], s[62:63]\n\t"
                         "s_add_u32 s62, s62, 14336\n\ts_addc_u32 s63, s63, 0\n\t"
                         "global_load_dwordx4 %[q6], %[qv], s[62:63]\n\t"
                         "s_add_u32 s62, s62, 14336\n\ts_addc_u32 s63, s63, 0\n\t"
                         "global_load_dwordx4 %[q7], %[qv], s[62:63]\n\t"
                         : [q0] "=&v"(Q0[0]), [q1] "=&v"(Q0[1]), [q2] "=&v"(Q0[2]), [q3] "=&v"(Q0[3]),
                           [q4] "=&v"(Q0[4]), [q5] "=&v"(Q0[5]), [q6] "=&v"(Q0[6]), [q7] "=&v"(Q0[7])
                         : [lo] "v"(lo), [qv] "v"(qv), [ls] "s"(ls), [qs] "s"(qs), [ld] "s"(ld), [lp] "s"(ka[2]), [qp] "s"(ka[1])
                         : "s60", "s61", "s62", "s63", "scc", "memory");
            SB();
        }
        KWAIT();
        int ln = lane;
        asm volatile("" : "+v"(ln));
        const int c = ln & 15, g = ln >> 4, l = ln & 31, h = ln >> 5;
        xb0 = (w * 64 + c) * 128 + ((g ^ (c >> 1)) * 16);
        xb1 = xb0 ^ 64;
        ba = OFF_B + g * 2048 + c * 16;
        u32 lutl = (u32)(size_t)(smem) + OFF_L + l * 4;
        u32 bdst = (u32)(size_t)(smem) + OFF_B + w * 4096 + h * 2048 + ((l >> 4) * 64 + (l & 15)) * 16;
#pragma unroll
        for (int r = 0; r < 4; ++r) {
            oo[r] = (u32)(((row0 + w * 64 + g * 4 + r) * N_ + col0 + c * 4) * 2);
            ob[r] = KPTR(3) + (unsigned long)(r * 16 * N_ * 2);
            asm volatile("" : "+v"(oo[r]), "+s"(ob[r]));
        }
        asm volatile("" : "+v"(xb0), "+v"(xb1), "+v"(ba));
        asm volatile("" : "+v"(bdst), "+v"(lutl));
        {
            unsigned long ex_;
            asm volatile("s_mov_b64 %0, exec\n\ts_mov_b64 exec, 0\n\t"
                         "s_cmp_eq_u32 %1, 0\n\ts_cbranch_scc1 .Lwp20\n\t"
                         "s_cmp_eq_u32 %1, 1\n\ts_cbranch_scc1 .Lwp11\n\t"
                         "s_cmp_eq_u32 %1, 2\n\ts_cbranch_scc1 .Lwp12\n\t"
                         "s_branch .Lwp13\n\t"
                         ".Lwp99:\n\ts_mov_b64 exec, %0" : "=&s"(ex_) : "s"(w) : "scc", "memory");
        }
        asm volatile("s_waitcnt vmcnt(8)" ::: "memory");
        BAR();
        PAD(20, 10);
        asm volatile("s_waitcnt vmcnt(7)" : "+v"(Q0[0]) :: "memory"); LUT1(Q0, 0, 0); LUT1(Q0, 1, 0); LUT1(Q0, 2, 0); LUT1(Q0, 3, 0); SB();
        asm volatile("s_waitcnt vmcnt(6)" : "+v"(Q0[1]) :: "memory"); LUT1(Q0, 0, 1); LUT1(Q0, 1, 1); LUT1(Q0, 2, 1); LUT1(Q0, 3, 1); SB();
        asm volatile("s_waitcnt vmcnt(5)" : "+v"(Q0[2]) :: "memory"); LUT1(Q0, 0, 2); LUT1(Q0, 1, 2); LUT1(Q0, 2, 2); LUT1(Q0, 3, 2); SB();
        asm volatile("s_waitcnt vmcnt(4)" : "+v"(Q0[3]) :: "memory"); LUT1(Q0, 0, 3); LUT1(Q0, 1, 3); LUT1(Q0, 2, 3); LUT1(Q0, 3, 3); SB();
        asm volatile("s_waitcnt vmcnt(3)" : "+v"(Q0[4]) :: "memory"); LUT1(Q0, 0, 4); LUT1(Q0, 1, 4); LUT1(Q0, 2, 4); LUT1(Q0, 3, 4); SB();
        asm volatile("s_waitcnt vmcnt(2)" : "+v"(Q0[5]) :: "memory"); LUT1(Q0, 0, 5); LUT1(Q0, 1, 5); LUT1(Q0, 2, 5); LUT1(Q0, 3, 5); SB();
        asm volatile("s_waitcnt vmcnt(1)" : "+v"(Q0[6]) :: "memory"); LUT1(Q0, 0, 6); LUT1(Q0, 1, 6); LUT1(Q0, 2, 6); LUT1(Q0, 3, 6); SB();
        asm volatile("s_waitcnt vmcnt(0)" : "+v"(Q0[7]) :: "memory"); LUT1(Q0, 0, 7); LUT1(Q0, 1, 7); LUT1(Q0, 2, 7); LUT1(Q0, 3, 7); SB();
        {   u32x4 b0, b1, b2, b3;
            PK(b0, 0, 3); WR(b0, 0); PK(b1, 1, 3); WR(b1, 1); PK(b2, 2, 3); WR(b2, 2); PK(b3, 3, 3); WR(b3, 3);
            SB();
        }
        asm volatile("s_waitcnt lgkmcnt(0)" ::: "memory");
        BAR();
    }
    asm volatile(
        "v_mov_b32 v232, %[xb0]\n\t"
        "v_mov_b32 v233, %[xb1]\n\t"
        "v_mov_b32 v234, %[ba]\n\t"
        ".Lwp10:\n\t"
        "s_cbranch_execz .Lwp99\n\t"
        "ds_read_b128 v[192:195], v234 offset:0\n\t"
        "ds_read_b128 v[128:131], v232 offset:0\n\t"
        "ds_read_b128 v[196:199], v234 offset:8192\n\t"
        "ds_read_b128 v[144:147], v233 offset:0\n\t"
        "ds_read_b128 v[204:207], v234 offset:8448\n\t"
        "ds_read_b128 v[200:203], v234 offset:256\n\t"
        "ds_read_b128 v[132:135], v232 offset:2048\n\t"
        "ds_read_b128 v[148:151], v233 offset:2048\n\t"
        "s_waitcnt lgkmcnt(6)\n\t"
        "v_mfma_f32_16x16x32_bf16 v[0:3], v[128:131], v[192:195], 0\n\t"
        "s_waitcnt lgkmcnt(4)\n\t"
        "v_mfma_f32_16x16x32_bf16 v[0:3], v[144:147], v[196:199], v[0:3]\n\t"
        "ds_read_b128 v[136:139], v232 offset:4096\n\t"
        "s_waitcnt lgkmcnt(4)\n\t"
        "v_mfma_f32_16x16x32_bf16 v[4:7], v[144:147], v[204:207], 0\n\t"
        "s_waitcnt lgkmcnt(3)\n\t"
        "v_mfma_f32_16x16x32_bf16 v[4:7], v[128:131], v[200:203], v[4:7]\n\t"
        "ds_read_b128 v[152:155], v233 offset:4096\n\t"
        "s_waitcnt lgkmcnt(3)\n\t"
        "v_mfma_f32_16x16x32_bf16 v[36:39], v[132:135], v[200:203], 0\n\t"
        "s_waitcnt lgkmcnt(2)\n\t"
        "v_mfma_f32_16x16x32_bf16 v[36:39], v[148:151], v[204:207], v[36:39]\n\t"
        "ds_read_b128 v[140:143], v232 offset:6144\n\t"
        "v_mfma_f32_16x16x32_bf16 v[32:35], v[148:151], v[196:199], 0\n\t"
        "v_mfma_f32_16x16x32_bf16 v[32:35], v[132:135], v[192:195], v[32:35]\n\t"
        "ds_read_b128 v[156:159], v233 offset:6144\n\t"
        "s_waitcnt lgkmcnt(3)\n\t"
        "v_mfma_f32_16x16x32_bf16 v[64:67], v[136:139], v[192:195], 0\n\t"
        "s_waitcnt lgkmcnt(2)\n\t"
        "v_mfma_f32_16x16x32_bf16 v[64:67], v[152:155], v[196:199], v[64:67]\n\t"
        "ds_read_b128 v[208:211], v234 offset:512\n\t"
        "v_mfma_f32_16x16x32_bf16 v[68:71], v[152:155], v[204:207], 0\n\t"
        "v_mfma_f32_16x16x32_bf16 v[68:71], v[136:139], v[200:203], v[68:71]\n\t"
        "ds_read_b128 v[212:215], v234 offset:8704\n\t"
        "s_waitcnt lgkmcnt(3)\n\t"
        "v_mfma_f32_16x16x32_bf16 v[100:103], v[140:143], v[200:203], 0\n\t"
        "s_waitcnt lgkmcnt(2)\n\t"
        "v_mfma_f32_16x16x32_bf16 v[100:103], v[156:159], v[204:207], v[100:103]\n\t"
        "ds_read_b128 v[220:223], v234 offset:8960\n\t"
        "v_mfma_f32_16x16x32_bf16 v[96:99], v[156:159], v[196:199], 0\n\t"
        "v_mfma_f32_16x16x32_bf16 v[96:99], v[140:143], v[192:195], v[96:99]\n\t"
        "ds_read_b128 v[216:219], v234 offset:768\n\t"
        "s_waitcnt lgkmcnt(3)\n\t"
        "v_mfma_f32_16x16x32_bf16 v[8:11], v[128:131], v[208:211], 0\n\t"
        "s_waitcnt lgkmcnt(2)\n\t"
        "v_mfma_f32_16x16x32_bf16 v[8:11], v[144:147], v[212:215], v[8:11]\n\t"
        "ds_read_b128 v[192:195], v234 offset:1024\n\t"
        "s_waitcnt lgkmcnt(2)\n\t"
        "v_mfma_f32_16x16x32_bf16 v[12:15], v[144:147], v[220:223], 0\n\t"
        "s_waitcnt lgkmcnt(1)\n\t"
        "v_mfma_f32_16x16x32_bf16 v[12:15], v[128:131], v[216:219], v[12:15]\n\t"
        "ds_read_b128 v[196:199], v234 offset:9216\n\t"
        "v_mfma_f32_16x16x32_bf16 v[44:47], v[132:135], v[216:219], 0\n\t"
        "v_mfma_f32_16x16x32_bf16 v[44:47], v[148:151], v[220:223], v[44:47]\n\t"
        "ds_read_b128 v[204:207], v234 offset:9472\n\t"
        "v_mfma_f32_16x16x32_bf16 v[40:43], v[148:151], v[212:215], 0\n\t"
        "v_mfma_f32_16x16x32_bf16 v[40:43], v[132:135], v[208:211], v[40:43]\n\t"
        "ds_read_b128 v[200:203], v234 offset:1280\n\t"
        "v_mfma_f32_16x16x32_bf16 v[72:75], v[136:139], v[208:211], 0\n\t"
        "v_mfma_f32_16x16x32_bf16 v[72:75], v[152:155], v[212:215], v[72:75]\n\t"
        "v_mfma_f32_16x16x32_bf16 v[76:79], v[152:155], v[220:223], 0\n\t"
        "v_mfma_f32_16x16x32_bf16 v[76:79], v[136:139], v[216:219], v[76:79]\n\t"
        "v_mfma_f32_16x16x32_bf16 v[108:111], v[140:143], v[216:219], 0\n\t"
        "v_mfma_f32_16x16x32_bf16 v[108:111], v[156:159], v[220:223], v[108:111]\n\t"
        "v_mfma_f32_16x16x32_bf16 v[104:107], v[156:159], v[212:215], 0\n\t"
        "v_mfma_f32_16x16x32_bf16 v[104:107], v[140:143], v[208:211], v[104:107]\n\t"
        "s_waitcnt lgkmcnt(0)\n\t"
        "v_mfma_f32_16x16x32_bf16 v[16:19], v[128:131], v[192:195], 0\n\t"
        "v_mfma_f32_16x16x32_bf16 v[16:19], v[144:147], v[196:199], v[16:19]\n\t"
        "ds_read_b128 v[208:211], v234 offset:1536\n\t"
        "v_mfma_f32_16x16x32_bf16 v[20:23], v[144:147], v[204:207], 0\n\t"
        "v_mfma_f32_16x16x32_bf16 v[20:23], v[128:131], v[200:203], v[20:23]\n\t"
        "ds_read_b128 v[212:215], v234 offset:9728\n\t"
        "v_mfma_f32_16x16x32_bf16 v[52:55], v[132:135], v[200:203], 0\n\t"
        "v_mfma_f32_16x16x32_bf16 v[52:55], v[148:151], v[204:207], v[52:55]\n\t"
        "ds_read_b128 v[220:223], v234 offset:9984\n\t"
        "v_mfma_f32_16x16x32_bf16 v[48:51], v[148:151], v[196:199], 0\n\t"
        "v_mfma_f32_16x16x32_bf16 v[48:51], v[132:135], v[192:195], v[48:51]\n\t"
        "ds_read_b128 v[216:219], v234 offset:1792\n\t"
        "v_mfma_f32_16x16x32_bf16 v[80:83], v[136:139], v[192:195], 0\n\t"
        "v_mfma_f32_16x16x32_bf16 v[80:83], v[152:155], v[196:199], v[80:83]\n\t"
        "v_mfma_f32_16x16x32_bf16 v[84:87], v[152:155], v[204:207], 0\n\t"
        "v_mfma_f32_16x16x32_bf16 v[84:87], v[136:139], v[200:203], v[84:87]\n\t"
        "s_mov_b32 s60, 32768\n\t"
        "s_mov_b32 s62, 5\n\t"
        "v_add_u32 v232, 32768, %[xb0]\n\t"
        "v_add_u32 v233, 32768, %[xb1]\n\t"
        "v_add_u32 v234, 16384, %[ba]\n\t"
        "v_mov_b64 v[24:25], 0\n\t"
        "v_mov_b64 v[26:27], 0\n\t"
        "v_mov_b64 v[28:29], 0\n\t"
        "v_mov_b64 v[30:31], 0\n\t"
        "v_mov_b64 v[56:57], 0\n\t"
        "v_mov_b64 v[58:59], 0\n\t"
        "v_mov_b64 v[60:61], 0\n\t"
        "v_mov_b64 v[62:63], 0\n\t"
        "v_mov_b64 v[88:89], 0\n\t"
        "v_mov_b64 v[90:91], 0\n\t"
        "v_mov_b64 v[92:93], 0\n\t"
        "v_mov_b64 v[94:95], 0\n\t"
        "v_mov_b64 v[120:121], 0\n\t"
        "v_mov_b64 v[122:123], 0\n\t"
        "v_mov_b64 v[124:125], 0\n\t"
        "v_mov_b64 v[126:127], 0\n\t"
        "v_mov_b64 v[116:117], 0\n\t"
        "v_mov_b64 v[118:119], 0\n\t"
        "v_mov_b64 v[112:113], 0\n\t"
        "v_mov_b64 v[114:115], 0\n\t"
        "s_nop 1\n\t"
        "s_mov_b64 exec, 0\n\t"
        "s_cmp_eq_u32 %[w], 0\n\t"
        "s_cbranch_scc1 .Lwp34\n\t"
        "s_cmp_eq_u32 %[w], 1\n\t"
        "s_cbranch_scc1 .Lwp38\n\t"
        "s_cmp_eq_u32 %[w], 2\n\t"
        "s_cbranch_scc1 .Lwp41\n\t"
        "s_branch .Lwp16\n\t"
        ".Lwp97:\n\t"
        "s_mov_b64 exec, -1\n\t"
        ".Lcloop:\n\t"
        "v_mfma_f32_16x16x32_bf16 v[116:119], v[140:143], v[200:203], v[116:119]\n\t"
        ".Lwp11:\n\t"
        "s_cbranch_execz .Lwp99\n\t"
        "s_sub_u32 s62, s62, 1\n\t"
        "v_mfma_f32_16x16x32_bf16 v[116:119], v[156:159], v[204:207], v[116:119]\n\t"
        "s_waitcnt lgkmcnt(0)\n\t"
        "v_mfma_f32_16x16x32_bf16 v[112:115], v[156:159], v[196:199], v[112:115]\n\t"
        "v_mfma_f32_16x16x32_bf16 v[112:115], v[140:143], v[192:195], v[112:115]\n\t"
        "s_barrier\n\t"
        "v_mfma_f32_16x16x32_bf16 v[24:27], v[128:131], v[208:211], v[24:27]\n\t"
        "v_mfma_f32_16x16x32_bf16 v[24:27], v[144:147], v[212:215], v[24:27]\n\t"
        "ds_read_b128 v[192:195], v234 offset:0\n\t"
        "ds_read_b128 v[160:163], v232 offset:0\n\t"
        "v_mfma_f32_16x16x32_bf16 v[28:31], v[144:147], v[220:223], v[28:31]\n\t"
        "v_mfma_f32_16x16x32_bf16 v[28:31], v[128:131], v[216:219], v[28:31]\n\t"
        "ds_read_b128 v[196:199], v234 offset:8192\n\t"
        "ds_read_b128 v[176:179], v233 offset:0\n\t"
        "v_mfma_f32_16x16x32_bf16 v[60:63], v[132:135], v[216:219], v[60:63]\n\t"
        "v_mfma_f32_16x16x32_bf16 v[60:63], v[148:151], v[220:223], v[60:63]\n\t"
        "ds_read_b128 v[204:207], v234 offset:8448\n\t"
        "v_mfma_f32_16x16x32_bf16 v[56:59], v[148:151], v[212:215], v[56:59]\n\t"
        "v_mfma_f32_16x16x32_bf16 v[56:59], v[132:135], v[208:211], v[56:59]\n\t"
        "ds_read_b128 v[200:203], v234 offset:256\n\t"
        "v_mfma_f32_16x16x32_bf16 v[88:91], v[136:139], v[208:211], v[88:91]\n\t"
        "v_mfma_f32_16x16x32_bf16 v[88:91], v[152:155], v[212:215], v[88:91]\n\t"
        "ds_read_b128 v[164:167], v232 offset:2048\n\t"
        "v_mfma_f32_16x16x32_bf16 v[92:95], v[152:155], v[220:223], v[92:95]\n\t"
        "v_mfma_f32_16x16x32_bf16 v[92:95], v[136:139], v[216:219], v[92:95]\n\t"
        "ds_read_b128 v[180:183], v233 offset:2048\n\t"
        "v_mfma_f32_16x16x32_bf16 v[124:127], v[140:143], v[216:219], v[124:127]\n\t"
        "v_mfma_f32_16x16x32_bf16 v[124:127], v[156:159], v[220:223], v[124:127]\n\t"
        "ds_read_b128 v[168:171], v232 offset:4096\n\t"
        "v_mfma_f32_16x16x32_bf16 v[120:123], v[156:159], v[212:215], v[120:123]\n\t"
        "v_mfma_f32_16x16x32_bf16 v[120:123], v[140:143], v[208:211], v[120:123]\n\t"
        "ds_read_b128 v[184:187], v233 offset:4096\n\t"
        "s_waitcnt lgkmcnt(4)\n\t"
        "v_mfma_f32_16x16x32_bf16 v[0:3], v[160:163], v[192:195], v[0:3]\n\t"
        "v_mfma_f32_16x16x32_bf16 v[0:3], v[176:179], v[196:199], v[0:3]\n\t"
        "ds_read_b128 v[172:175], v232 offset:6144\n\t"
        "ds_read_b128 v[208:211], v234 offset:512\n\t"
        "v_mfma_f32_16x16x32_bf16 v[4:7], v[176:179], v[204:207], v[4:7]\n\t"
        "v_mfma_f32_16x16x32_bf16 v[4:7], v[160:163], v[200:203], v[4:7]\n\t"
        "ds_read_b128 v[188:191], v233 offset:6144\n\t"
        "s_waitcnt lgkmcnt(5)\n\t"
        "v_mfma_f32_16x16x32_bf16 v[36:39], v[164:167], v[200:203], v[36:39]\n\t"
        "ds_read_b128 v[212:215], v234 offset:8704\n\t"
        "v_mfma_f32_16x16x32_bf16 v[36:39], v[180:183], v[204:207], v[36:39]\n\t"
        "ds_read_b128 v[220:223], v234 offset:8960\n\t"
        "v_mfma_f32_16x16x32_bf16 v[32:35], v[180:183], v[196:199], v[32:35]\n\t"
        "v_mfma_f32_16x16x32_bf16 v[32:35], v[164:167], v[192:195], v[32:35]\n\t"
        "ds_read_b128 v[216:219], v234 offset:768\n\t"
        "s_waitcnt lgkmcnt(6)\n\t"
        "v_mfma_f32_16x16x32_bf16 v[64:67], v[168:171], v[192:195], v[64:67]\n\t"
        "v_mfma_f32_16x16x32_bf16 v[64:67], v[184:187], v[196:199], v[64:67]\n\t"
        "v_mfma_f32_16x16x32_bf16 v[68:71], v[184:187], v[204:207], v[68:71]\n\t"
        "v_mfma_f32_16x16x32_bf16 v[68:71], v[168:171], v[200:203], v[68:71]\n\t"
        "s_waitcnt lgkmcnt(3)\n\t"
        "v_mfma_f32_16x16x32_bf16 v[100:103], v[172:175], v[200:203], v[100:103]\n\t"
        "v_mfma_f32_16x16x32_bf16 v[100:103], v[188:191], v[204:207], v[100:103]\n\t"
        "v_mfma_f32_16x16x32_bf16 v[96:99], v[188:191], v[196:199], v[96:99]\n\t"
        "v_mfma_f32_16x16x32_bf16 v[96:99], v[172:175], v[192:195], v[96:99]\n\t"
        ".Lwp12:\n\t"
        "s_cbranch_execz .Lwp99\n\t"
        "v_mfma_f32_16x16x32_bf16 v[8:11], v[160:163], v[208:211], v[8:11]\n\t"
        "s_waitcnt lgkmcnt(0)\n\t"
        "v_mfma_f32_16x16x32_bf16 v[8:11], v[176:179], v[212:215], v[8:11]\n\t"
        "ds_read_b128 v[192:195], v234 offset:1024\n\t"
        "v_mfma_f32_16x16x32_bf16 v[12:15], v[176:179], v[220:223], v[12:15]\n\t"
        "v_mfma_f32_16x16x32_bf16 v[12:15], v[160:163], v[216:219], v[12:15]\n\t"
        "ds_read_b128 v[196:199], v234 offset:9216\n\t"
        "v_mfma_f32_16x16x32_bf16 v[44:47], v[164:167], v[216:219], v[44:47]\n\t"
        "v_mfma_f32_16x16x32_bf16 v[44:47], v[180:183], v[220:223], v[44:47]\n\t"
        "ds_read_b128 v[204:207], v234 offset:9472\n\t"
        "v_mfma_f32_16x16x32_bf16 v[40:43], v[180:183], v[212:215], v[40:43]\n\t"
        "v_mfma_f32_16x16x32_bf16 v[40:43], v[164:167], v[208:211], v[40:43]\n\t"
        "ds_read_b128 v[200:203], v234 offset:1280\n\t"
        "v_mfma_f32_16x16x32_bf16 v[72:75], v[168:171], v[208:211], v[72:75]\n\t"
        "v_mfma_f32_16x16x32_bf16 v[72:75], v[184:187], v[212:215], v[72:75]\n\t"
        "v_mfma_f32_16x16x32_bf16 v[76:79], v[184:187], v[220:223], v[76:79]\n\t"
        "v_mfma_f32_16x16x32_bf16 v[76:79], v[168:171], v[216:219], v[76:79]\n\t"
        "v_mfma_f32_16x16x32_bf16 v[108:111], v[172:175], v[216:219], v[108:111]\n\t"
        "v_mfma_f32_16x16x32_bf16 v[108:111], v[188:191], v[220:223], v[108:111]\n\t"
        "v_mfma_f32_16x16x32_bf16 v[104:107], v[188:191], v[212:215], v[104:107]\n\t"
        "v_mfma_f32_16x16x32_bf16 v[104:107], v[172:175], v[208:211], v[104:107]\n\t"
        "s_waitcnt lgkmcnt(0)\n\t"
        "v_mfma_f32_16x16x32_bf16 v[16:19], v[160:163], v[192:195], v[16:19]\n\t"
        "v_mfma_f32_16x16x32_bf16 v[16:19], v[176:179], v[196:199], v[16:19]\n\t"
        "ds_read_b128 v[208:211], v234 offset:1536\n\t"
        "v_mfma_f32_16x16x32_bf16 v[20:23], v[176:179], v[204:207], v[20:23]\n\t"
        "v_mfma_f32_16x16x32_bf16 v[20:23], v[160:163], v[200:203], v[20:23]\n\t"
        "ds_read_b128 v[212:215], v234 offset:9728\n\t"
        "v_mfma_f32_16x16x32_bf16 v[52:55], v[164:167], v[200:203], v[52:55]\n\t"
        "v_mfma_f32_16x16x32_bf16 v[52:55], v[180:183], v[204:207], v[52:55]\n\t"
        "ds_read_b128 v[220:223], v234 offset:9984\n\t"
        "v_mfma_f32_16x16x32_bf16 v[48:51], v[180:183], v[196:199], v[48:51]\n\t"
        "v_mfma_f32_16x16x32_bf16 v[48:51], v[164:167], v[192:195], v[48:51]\n\t"
        "ds_read_b128 v[216:219], v234 offset:1792\n\t"
        "s_add_u32 s60, s60, 32768\n\t"
        "v_mfma_f32_16x16x32_bf16 v[80:83], v[168:171], v[192:195], v[80:83]\n\t"
        "s_cmp_eq_u32 s60, 98304\n\t"
        "s_cselect_b32 s60, 0, s60\n\t"
        "v_mfma_f32_16x16x32_bf16 v[80:83], v[184:187], v[196:199], v[80:83]\n\t"
        "v_add_u32 v232, s60, %[xb0]\n\t"
        "s_lshr_b32 s61, s60, 1\n\t"
        "v_mfma_f32_16x16x32_bf16 v[84:87], v[184:187], v[204:207], v[84:87]\n\t"
        "v_add_u32 v233, s60, %[xb1]\n\t"
        "v_add_u32 v234, s61, %[ba]\n\t"
        "v_mfma_f32_16x16x32_bf16 v[84:87], v[168:171], v[200:203], v[84:87]\n\t"
        "v_mfma_f32_16x16x32_bf16 v[116:119], v[172:175], v[200:203], v[116:119]\n\t"
        "v_mfma_f32_16x16x32_bf16 v[116:119], v[188:191], v[204:207], v[116:119]\n\t"
        "s_waitcnt lgkmcnt(0)\n\t"
        "v_mfma_f32_16x16x32_bf16 v[112:115], v[188:191], v[196:199], v[112:115]\n\t"
        "v_mfma_f32_16x16x32_bf16 v[112:115], v[172:175], v[192:195], v[112:115]\n\t"
        "s_barrier\n\t"
        "v_mfma_f32_16x16x32_bf16 v[24:27], v[160:163], v[208:211], v[24:27]\n\t"
        "v_mfma_f32_16x16x32_bf16 v[24:27], v[176:179], v[212:215], v[24:27]\n\t"
        "ds_read_b128 v[192:195], v234 offset:0\n\t"
        "ds_read_b128 v[128:131], v232 offset:0\n\t"
        "v_mfma_f32_16x16x32_bf16 v[28:31], v[176:179], v[220:223], v[28:31]\n\t"
        "v_mfma_f32_16x16x32_bf16 v[28:31], v[160:163], v[216:219], v[28:31]\n\t"
        "ds_read_b128 v[196:199], v234 offset:8192\n\t"
        "ds_read_b128 v[144:147], v233 offset:0\n\t"
        "v_mfma_f32_16x16x32_bf16 v[60:63], v[164:167], v[216:219], v[60:63]\n\t"
        "v_mfma_f32_16x16x32_bf16 v[60:63], v[180:183], v[220:223], v[60:63]\n\t"
        "ds_read_b128 v[204:207], v234 offset:8448\n\t"
        "v_mfma_f32_16x16x32_bf16 v[56:59], v[180:183], v[212:215], v[56:59]\n\t"
        "v_mfma_f32_16x16x32_bf16 v[56:59], v[164:167], v[208:211], v[56:59]\n\t"
        "ds_read_b128 v[200:203], v234 offset:256\n\t"
        "v_mfma_f32_16x16x32_bf16 v[88:91], v[168:171], v[208:211], v[88:91]\n\t"
        "v_mfma_f32_16x16x32_bf16 v[88:91], v[184:187], v[212:215], v[88:91]\n\t"
        "ds_read_b128 v[132:135], v232 offset:2048\n\t"
        "v_mfma_f32_16x16x32_bf16 v[92:95], v[184:187], v[220:223], v[92:95]\n\t"
        "v_mfma_f32_16x16x32_bf16 v[92:95], v[168:171], v[216:219], v[92:95]\n\t"
        "ds_read_b128 v[148:151], v233 offset:2048\n\t"
        "v_mfma_f32_16x16x32_bf16 v[124:127], v[172:175], v[216:219], v[124:127]\n\t"
        "v_mfma_f32_16x16x32_bf16 v[124:127], v[188:191], v[220:223], v[124:127]\n\t"
        "ds_read_b128 v[136:139], v232 offset:4096\n\t"
        "v_mfma_f32_16x16x32_bf16 v[120:123], v[188:191], v[212:215], v[120:123]\n\t"
        "v_mfma_f32_16x16x32_bf16 v[120:123], v[172:175], v[208:211], v[120:123]\n\t"
        "ds_read_b128 v[152:155], v233 offset:4096\n\t"
        "s_waitcnt lgkmcnt(4)\n\t"
        "v_mfma_f32_16x16x32_bf16 v[0:3], v[128:131], v[192:195], v[0:3]\n\t"
        "v_mfma_f32_16x16x32_bf16 v[0:3], v[144:147], v[196:199], v[0:3]\n\t"
        "ds_read_b128 v[140:143], v232 offset:6144\n\t"
        "ds_read_b128 v[208:211], v234 offset:512\n\t"
        "v_mfma_f32_16x16x32_bf16 v[4:7], v[144:147], v[204:207], v[4:7]\n\t"
        "v_mfma_f32_16x16x32_bf16 v[4:7], v[128:131], v[200:203], v[4:7]\n\t"
        "ds_read_b128 v[156:159], v233 offset:6144\n\t"
        "s_waitcnt lgkmcnt(5)\n\t"
        "v_mfma_f32_16x16x32_bf16 v[36:39], v[132:135], v[200:203], v[36:39]\n\t"
        "ds_read_b128 v[212:215], v234 offset:8704\n\t"
        "v_mfma_f32_16x16x32_bf16 v[36:39], v[148:151], v[204:207], v[36:39]\n\t"
        "ds_read_b128 v[220:223], v234 offset:8960\n\t"
        "v_mfma_f32_16x16x32_bf16 v[32:35], v[148:151], v[196:199], v[32:35]\n\t"
        "v_mfma_f32_16x16x32_bf16 v[32:35], v[132:135], v[192:195], v[32:35]\n\t"
        "ds_read_b128 v[216:219], v234 offset:768\n\t"
        "s_waitcnt lgkmcnt(6)\n\t"
        "v_mfma_f32_16x16x32_bf16 v[64:67], v[136:139], v[192:195], v[64:67]\n\t"
        "v_mfma_f32_16x16x32_bf16 v[64:67], v[152:155], v[196:199], v[64:67]\n\t"
        "v_mfma_f32_16x16x32_bf16 v[68:71], v[152:155], v[204:207], v[68:71]\n\t"
        "v_mfma_f32_16x16x32_bf16 v[68:71], v[136:139], v[200:203], v[68:71]\n\t"
        "s_waitcnt lgkmcnt(3)\n\t"
        "v_mfma_f32_16x16x32_bf16 v[100:103], v[140:143], v[200:203], v[100:103]\n\t"
        "v_mfma_f32_16x16x32_bf16 v[100:103], v[156:159], v[204:207], v[100:103]\n\t"
        "v_mfma_f32_16x16x32_bf16 v[96:99], v[156:159], v[196:199], v[96:99]\n\t"
        "v_mfma_f32_16x16x32_bf16 v[96:99], v[140:143], v[192:195], v[96:99]\n\t"
        ".Lwp13:\n\t"
        "s_cbranch_execz .Lwp99\n\t"
        "v_mfma_f32_16x16x32_bf16 v[8:11], v[128:131], v[208:211], v[8:11]\n\t"
        "s_waitcnt lgkmcnt(0)\n\t"
        "v_mfma_f32_16x16x32_bf16 v[8:11], v[144:147], v[212:215], v[8:11]\n\t"
        "ds_read_b128 v[192:195], v234 offset:1024\n\t"
        "v_mfma_f32_16x16x32_bf16 v[12:15], v[144:147], v[220:223], v[12:15]\n\t"
        "v_mfma_f32_16x16x32_bf16 v[12:15], v[128:131], v[216:219], v[12:15]\n\t"
        "ds_read_b128 v[196:199], v234 offset:9216\n\t"
        "v_mfma_f32_16x16x32_bf16 v[44:47], v[132:135], v[216:219], v[44:47]\n\t"
        "v_mfma_f32_16x16x32_bf16 v[44:47], v[148:151], v[220:223], v[44:47]\n\t"
        "ds_read_b128 v[204:207], v234 offset:9472\n\t"
        "v_mfma_f32_16x16x32_bf16 v[40:43], v[148:151], v[212:215], v[40:43]\n\t"
        "v_mfma_f32_16x16x32_bf16 v[40:43], v[132:135], v[208:211], v[40:43]\n\t"
        "ds_read_b128 v[200:203], v234 offset:1280\n\t"
        "v_mfma_f32_16x16x32_bf16 v[72:75], v[136:139], v[208:211], v[72:75]\n\t"
        "v_mfma_f32_16x16x32_bf16 v[72:75], v[152:155], v[212:215], v[72:75]\n\t"
        "v_mfma_f32_16x16x32_bf16 v[76:79], v[152:155], v[220:223], v[76:79]\n\t"
        "v_mfma_f32_16x16x32_bf16 v[76:79], v[136:139], v[216:219], v[76:79]\n\t"
        "v_mfma_f32_16x16x32_bf16 v[108:111], v[140:143], v[216:219], v[108:111]\n\t"
        "v_mfma_f32_16x16x32_bf16 v[108:111], v[156:159], v[220:223], v[108:111]\n\t"
        "v_mfma_f32_16x16x32_bf16 v[104:107], v[156:159], v[212:215], v[104:107]\n\t"
        "v_mfma_f32_16x16x32_bf16 v[104:107], v[140:143], v[208:211], v[104:107]\n\t"
        "s_waitcnt lgkmcnt(0)\n\t"
        "v_mfma_f32_16x16x32_bf16 v[16:19], v[128:131], v[192:195], v[16:19]\n\t"
        "v_mfma_f32_16x16x32_bf16 v[16:19], v[144:147], v[196:199], v[16:19]\n\t"
        "ds_read_b128 v[208:211], v234 offset:1536\n\t"
        "v_mfma_f32_16x16x32_bf16 v[20:23], v[144:147], v[204:207], v[20:23]\n\t"
        "v_mfma_f32_16x16x32_bf16 v[20:23], v[128:131], v[200:203], v[20:23]\n\t"
        "ds_read_b128 v[212:215], v234 offset:9728\n\t"
        "v_mfma_f32_16x16x32_bf16 v[52:55], v[132:135], v[200:203], v[52:55]\n\t"
        "v_mfma_f32_16x16x32_bf16 v[52:55], v[148:151], v[204:207], v[52:55]\n\t"
        "ds_read_b128 v[220:223], v234 offset:9984\n\t"
        "v_mfma_f32_16x16x32_bf16 v[48:51], v[148:151], v[196:199], v[48:51]\n\t"
        "v_mfma_f32_16x16x32_bf16 v[48:51], v[132:135], v[192:195], v[48:51]\n\t"
        "ds_read_b128 v[216:219], v234 offset:1792\n\t"
        "s_add_u32 s60, s60, 32768\n\t"
        "v_mfma_f32_16x16x32_bf16 v[80:83], v[136:139], v[192:195], v[80:83]\n\t"
        "s_cmp_eq_u32 s60, 98304\n\t"
        "s_cselect_b32 s60, 0, s60\n\t"
        "v_mfma_f32_16x16x32_bf16 v[80:83], v[152:155], v[196:199], v[80:83]\n\t"
        "v_add_u32 v232, s60, %[xb0]\n\t"
        "s_lshr_b32 s61, s60, 1\n\t"
        "v_mfma_f32_16x16x32_bf16 v[84:87], v[152:155], v[204:207], v[84:87]\n\t"
        "v_add_u32 v233, s60, %[xb1]\n\t"
        "v_add_u32 v234, s61, %[ba]\n\t"
        "v_mfma_f32_16x16x32_bf16 v[84:87], v[136:139], v[200:203], v[84:87]\n\t"
        "s_cmp_lg_u32 s62, -1\n\t"
        "s_cbranch_scc1 .Lcloop\n\t"
        "v_mfma_f32_16x16x32_bf16 v[116:119], v[140:143], v[200:203], v[116:119]\n\t"
        "v_mfma_f32_16x16x32_bf16 v[116:119], v[156:159], v[204:207], v[116:119]\n\t"
        "s_waitcnt lgkmcnt(0)\n\t"
        "v_mfma_f32_16x16x32_bf16 v[112:115], v[156:159], v[196:199], v[112:115]\n\t"
        "v_mfma_f32_16x16x32_bf16 v[112:115], v[140:143], v[192:195], v[112:115]\n\t"
        "s_barrier\n\t"
        "v_mfma_f32_16x16x32_bf16 v[24:27], v[128:131], v[208:211], v[24:27]\n\t"
        "s_setprio 2\n\t"
        ".Lwp14:\n\t"
        "s_cbranch_execz .Lwp15\n\t"
        "v_mfma_f32_16x16x32_bf16 v[24:27], v[144:147], v[212:215], v[24:27]\n\t"
        "ds_read_b128 v[160:163], %[ba] offset:16384\n\t"
        "ds_read_b128 v[164:167], v232 offset:0\n\t"
        "v_mfma_f32_16x16x32_bf16 v[28:31], v[144:147], v[220:223], v[28:31]\n\t"
        "v_mfma_f32_16x16x32_bf16 v[28:31], v[128:131], v[216:219], v[28:31]\n\t"
        "ds_read_b128 v[144:147], %[ba] offset:24576\n\t"
        "ds_read_b128 v[168:171], v233 offset:0\n\t"
        "v_mfma_f32_16x16x32_bf16 v[60:63], v[132:135], v[216:219], v[60:63]\n\t"
        "v_mfma_f32_16x16x32_bf16 v[60:63], v[148:151], v[220:223], v[60:63]\n\t"
        "ds_read_b128 v[128:131], %[ba] offset:24832\n\t"
        "ds_read_b128 v[172:175], %[ba] offset:16640\n\t"
        "v_mfma_f32_16x16x32_bf16 v[56:59], v[148:151], v[212:215], v[56:59]\n\t"
        "ds_read_b128 v[176:179], v232 offset:2048\n\t"
        "v_mfma_f32_16x16x32_bf16 v[56:59], v[132:135], v[208:211], v[56:59]\n\t"
        "ds_read_b128 v[148:151], v233 offset:2048\n\t"
        "v_mfma_f32_16x16x32_bf16 v[88:91], v[136:139], v[208:211], v[88:91]\n\t"
        "v_mfma_f32_16x16x32_bf16 v[88:91], v[152:155], v[212:215], v[88:91]\n\t"
        "ds_read_b128 v[132:135], v232 offset:4096\n\t"
        "v_mfma_f32_16x16x32_bf16 v[92:95], v[152:155], v[220:223], v[92:95]\n\t"
        "v_mfma_f32_16x16x32_bf16 v[92:95], v[136:139], v[216:219], v[92:95]\n\t"
        "ds_read_b128 v[152:155], v233 offset:4096\n\t"
        "v_mfma_f32_16x16x32_bf16 v[124:127], v[140:143], v[216:219], v[124:127]\n\t"
        "v_mfma_f32_16x16x32_bf16 v[124:127], v[156:159], v[220:223], v[124:127]\n\t"
        "ds_read_b128 v[136:139], v232 offset:6144\n\t"
        "v_mfma_f32_16x16x32_bf16 v[120:123], v[156:159], v[212:215], v[120:123]\n\t"
        "v_mfma_f32_16x16x32_bf16 v[120:123], v[140:143], v[208:211], v[120:123]\n\t"
        "ds_read_b128 v[156:159], v233 offset:6144\n\t"
        "s_waitcnt lgkmcnt(4)\n\t"
        "v_mfma_f32_16x16x32_bf16 v[0:3], v[164:167], v[160:163], v[0:3]\n\t"
        "v_mfma_f32_16x16x32_bf16 v[0:3], v[168:171], v[144:147], v[0:3]\n\t"
        "ds_read_b128 v[140:143], %[ba] offset:16896\n\t"
        "v_mfma_f32_16x16x32_bf16 v[4:7], v[168:171], v[128:131], v[4:7]\n\t"
        "v_mfma_f32_16x16x32_bf16 v[4:7], v[164:167], v[172:175], v[4:7]\n\t"
        "ds_read_b128 v[180:183], %[ba] offset:25088\n\t"
        "v_mfma_f32_16x16x32_bf16 v[36:39], v[176:179], v[172:175], v[36:39]\n\t"
        "v_mfma_f32_16x16x32_bf16 v[36:39], v[148:151], v[128:131], v[36:39]\n\t"
        "ds_read_b128 v[184:187], %[ba] offset:25344\n\t"
        "v_mfma_f32_16x16x32_bf16 v[32:35], v[148:151], v[144:147], v[32:35]\n\t"
        "v_mfma_f32_16x16x32_bf16 v[32:35], v[176:179], v[160:163], v[32:35]\n\t"
        "ds_read_b128 v[188:191], %[ba] offset:17152\n\t"
        "s_waitcnt lgkmcnt(4)\n\t"
        "v_mfma_f32_16x16x32_bf16 v[64:67], v[132:135], v[160:163], v[64:67]\n\t"
        "v_mfma_f32_16x16x32_bf16 v[64:67], v[152:155], v[144:147], v[64:67]\n\t"
        "ds_read_b128 v[192:195], %[ba] offset:17408\n\t"
        "v_mfma_f32_16x16x32_bf16 v[68:71], v[152:155], v[128:131], v[68:71]\n\t"
        "v_mfma_f32_16x16x32_bf16 v[68:71], v[132:135], v[172:175], v[68:71]\n\t"
        "ds_read_b128 v[196:199], v232 offset:32768\n\t"
        "v_mfma_f32_16x16x32_bf16 v[100:103], v[136:139], v[172:175], v[100:103]\n\t"
        "v_mfma_f32_16x16x32_bf16 v[100:103], v[156:159], v[128:131], v[100:103]\n\t"
        "ds_read_b128 v[172:175], %[ba] offset:25600\n\t"
        "v_mfma_f32_16x16x32_bf16 v[96:99], v[156:159], v[144:147], v[96:99]\n\t"
        "v_mfma_f32_16x16x32_bf16 v[96:99], v[136:139], v[160:163], v[96:99]\n\t"
        "ds_read_b128 v[128:131], v233 offset:32768\n\t"
        "s_waitcnt lgkmcnt(4)\n\t"
        "v_mfma_f32_16x16x32_bf16 v[8:11], v[164:167], v[140:143], v[8:11]\n\t"
        "v_mfma_f32_16x16x32_bf16 v[8:11], v[168:171], v[180:183], v[8:11]\n\t"
        "ds_read_b128 v[144:147], %[ba] offset:25856\n\t"
        "v_mfma_f32_16x16x32_bf16 v[12:15], v[168:171], v[184:187], v[12:15]\n\t"
        "v_mfma_f32_16x16x32_bf16 v[12:15], v[164:167], v[188:191], v[12:15]\n\t"
        "ds_read_b128 v[160:163], %[ba] offset:17664\n\t"
        "v_mfma_f32_16x16x32_bf16 v[44:47], v[176:179], v[188:191], v[44:47]\n\t"
        "v_mfma_f32_16x16x32_bf16 v[44:47], v[148:151], v[184:187], v[44:47]\n\t"
        "ds_read_b128 v[164:167], %[ba] offset:17920\n\t"
        "ds_read_b128 v[168:171], %[ba] offset:26112\n\t"
        "v_mfma_f32_16x16x32_bf16 v[40:43], v[148:151], v[180:183], v[40:43]\n\t"
        "v_mfma_f32_16x16x32_bf16 v[40:43], v[176:179], v[140:143], v[40:43]\n\t"
        "ds_read_b128 v[148:151], %[ba] offset:26368\n\t"
        "v_mfma_f32_16x16x32_bf16 v[72:75], v[132:135], v[140:143], v[72:75]\n\t"
        "v_mfma_f32_16x16x32_bf16 v[72:75], v[152:155], v[180:183], v[72:75]\n\t"
        "ds_read_b128 v[176:179], %[ba] offset:18176\n\t"
        "v_mfma_f32_16x16x32_bf16 v[76:79], v[152:155], v[184:187], v[76:79]\n\t"
        "v_mfma_f32_16x16x32_bf16 v[76:79], v[132:135], v[188:191], v[76:79]\n\t"
        "ds_read_b128 v[152:155], v232 offset:34816\n\t"
        "v_mfma_f32_16x16x32_bf16 v[108:111], v[136:139], v[188:191], v[108:111]\n\t"
        "v_mfma_f32_16x16x32_bf16 v[108:111], v[156:159], v[184:187], v[108:111]\n\t"
        "ds_read_b128 v[132:135], v233 offset:34816\n\t"
        "v_mfma_f32_16x16x32_bf16 v[104:107], v[156:159], v[180:183], v[104:107]\n\t"
        "v_mfma_f32_16x16x32_bf16 v[104:107], v[136:139], v[140:143], v[104:107]\n\t"
        "s_waitcnt lgkmcnt(3)\n\t"
        "v_mfma_f32_16x16x32_bf16 v[0:3], v[196:199], v[192:195], v[0:3]\n\t"
        "v_mfma_f32_16x16x32_bf16 v[0:3], v[128:131], v[172:175], v[0:3]\n\t"
        "v_mfma_f32_16x16x32_bf16 v[4:7], v[128:131], v[144:147], v[4:7]\n\t"
        "v_mfma_f32_16x16x32_bf16 v[4:7], v[196:199], v[160:163], v[4:7]\n\t"
        "ds_read_b128 v[136:139], v232 offset:36864\n\t"
        "v_mfma_f32_16x16x32_bf16 v[8:11], v[196:199], v[164:167], v[8:11]\n\t"
        "v_mfma_f32_16x16x32_bf16 v[8:11], v[128:131], v[168:171], v[8:11]\n\t"
        "ds_read_b128 v[140:143], v233 offset:36864\n\t"
        "v_mfma_f32_16x16x32_bf16 v[12:15], v[128:131], v[148:151], v[12:15]\n\t"
        "s_waitcnt lgkmcnt(2)\n\t"
        "v_mfma_f32_16x16x32_bf16 v[12:15], v[196:199], v[176:179], v[12:15]\n\t"
        "v_mfma_f32_16x16x32_bf16 v[32:35], v[152:155], v[192:195], v[32:35]\n\t"
        "v_mfma_f32_16x16x32_bf16 v[32:35], v[132:135], v[172:175], v[32:35]\n\t"
        "ds_read_b128 v[128:131], v232 offset:38912\n\t"
        "v_mfma_f32_16x16x32_bf16 v[36:39], v[132:135], v[144:147], v[36:39]\n\t"
        "v_mfma_f32_16x16x32_bf16 v[36:39], v[152:155], v[160:163], v[36:39]\n\t"
        "ds_read_b128 v[156:159], v233 offset:38912\n\t"
        "v_mfma_f32_16x16x32_bf16 v[40:43], v[152:155], v[164:167], v[40:43]\n\t"
        "v_mfma_f32_16x16x32_bf16 v[40:43], v[132:135], v[168:171], v[40:43]\n\t"
        "v_mfma_f32_16x16x32_bf16 v[44:47], v[132:135], v[148:151], v[44:47]\n\t"
        "v_mfma_f32_16x16x32_bf16 v[44:47], v[152:155], v[176:179], v[44:47]\n\t"
        "s_waitcnt lgkmcnt(0)\n\t"
        "s_barrier\n\t"
        "v_mfma_f32_16x16x32_bf16 v[64:67], v[136:139], v[192:195], v[64:67]\n\t"
        ".Lwp15:\n\t"
        "s_cbranch_execz .Lwp97\n\t"
        "v_mfma_f32_16x16x32_bf16 v[64:67], v[140:143], v[172:175], v[64:67]\n\t"
        "ds_read_b128 v[132:135], %[ba] offset:32768\n\t"
        "ds_read_b128 v[152:155], %[xb0] offset:0\n\t"
        "v_mfma_f32_16x16x32_bf16 v[68:71], v[140:143], v[144:147], v[68:71]\n\t"
        "v_mfma_f32_16x16x32_bf16 v[68:71], v[136:139], v[160:163], v[68:71]\n\t"
        "ds_read_b128 v[180:183], %[ba] offset:40960\n\t"
        "ds_read_b128 v[184:187], %[xb1] offset:0\n\t"
        "v_mfma_f32_16x16x32_bf16 v[72:75], v[136:139], v[164:167], v[72:75]\n\t"
        "v_mfma_f32_16x16x32_bf16 v[72:75], v[140:143], v[168:171], v[72:75]\n\t"
        "ds_read_b128 v[188:191], %[ba] offset:41216\n\t"
        "ds_read_b128 v[196:199], %[ba] offset:33024\n\t"
        "v_mfma_f32_16x16x32_bf16 v[76:79], v[140:143], v[148:151], v[76:79]\n\t"
        "v_mfma_f32_16x16x32_bf16 v[76:79], v[136:139], v[176:179], v[76:79]\n\t"
        "ds_read_b128 v[140:143], %[ba] offset:33280\n\t"
        "s_waitcnt lgkmcnt(5)\n\t"
        "v_mfma_f32_16x16x32_bf16 v[0:3], v[152:155], v[132:135], v[0:3]\n\t"
        "ds_read_b128 v[136:139], %[ba] offset:41472\n\t"
        "s_waitcnt lgkmcnt(4)\n\t"
        "v_mfma_f32_16x16x32_bf16 v[0:3], v[184:187], v[180:183], v[0:3]\n\t"
        "ds_read_b128 v[200:203], %[ba] offset:41728\n\t"
        "s_waitcnt lgkmcnt(4)\n\t"
        "v_mfma_f32_16x16x32_bf16 v[4:7], v[184:187], v[188:191], v[4:7]\n\t"
        "ds_read_b128 v[204:207], %[ba] offset:33536\n\t"
        "s_waitcnt lgkmcnt(4)\n\t"
        "v_mfma_f32_16x16x32_bf16 v[4:7], v[152:155], v[196:199], v[4:7]\n\t"
        "ds_read_b128 v[208:211], %[xb0] offset:2048\n\t"
        "s_waitcnt lgkmcnt(4)\n\t"
        "v_mfma_f32_16x16x32_bf16 v[8:11], v[152:155], v[140:143], v[8:11]\n\t"
        "s_waitcnt lgkmcnt(3)\n\t"
        "v_mfma_f32_16x16x32_bf16 v[8:11], v[184:187], v[136:139], v[8:11]\n\t"
        "ds_read_b128 v[212:215], %[xb1] offset:2048\n\t"
        "s_waitcnt lgkmcnt(3)\n\t"
        "v_mfma_f32_16x16x32_bf16 v[12:15], v[184:187], v[200:203], v[12:15]\n\t"
        "s_waitcnt lgkmcnt(2)\n\t"
        "v_mfma_f32_16x16x32_bf16 v[12:15], v[152:155], v[204:207], v[12:15]\n\t"
        "v_mfma_f32_16x16x32_bf16 v[96:99], v[128:131], v[192:195], v[96:99]\n\t"
        "v_mfma_f32_16x16x32_bf16 v[96:99], v[156:159], v[172:175], v[96:99]\n\t"
        "ds_read_b128 v[152:155], %[xb0] offset:4096\n\t"
        "v_cvt_pk_bf16_f32 v224, v0, v4\n\t"
        "v_mfma_f32_16x16x32_bf16 v[100:103], v[156:159], v[144:147], v[100:103]\n\t"
        "v_mfma_f32_16x16x32_bf16 v[100:103], v[128:131], v[160:163], v[100:103]\n\t"
        "ds_read_b128 v[144:147], %[xb1] offset:4096\n\t"
        "v_cvt_pk_bf16_f32 v225, v8, v12\n\t"
        "v_mfma_f32_16x16x32_bf16 v[104:107], v[128:131], v[164:167], v[104:107]\n\t"
        "global_store_dwordx2 %[oo0], v[224:225], %[ob0] offset:0 nt\n\t"
        "v_mfma_f32_16x16x32_bf16 v[104:107], v[156:159], v[168:171], v[104:107]\n\t"
        "ds_read_b128 v[160:163], %[xb0] offset:6144\n\t"
        "v_cvt_pk_bf16_f32 v227, v9, v13\n\t"
        "v_mfma_f32_16x16x32_bf16 v[108:111], v[156:159], v[148:151], v[108:111]\n\t"
        "v_cvt_pk_bf16_f32 v226, v1, v5\n\t"
        "v_mfma_f32_16x16x32_bf16 v[108:111], v[128:131], v[176:179], v[108:111]\n\t"
        "ds_read_b128 v[148:151], %[xb1] offset:6144\n\t"
        "s_waitcnt lgkmcnt(4)\n\t"
        "v_mfma_f32_16x16x32_bf16 v[32:35], v[208:211], v[132:135], v[32:35]\n\t"
        "global_store_dwordx2 %[oo1], v[226:227], %[ob0] offset:0 nt\n\t"
        "v_mfma_f32_16x16x32_bf16 v[32:35], v[212:215], v[180:183], v[32:35]\n\t"
        "ds_read_b128 v[128:131], v232 offset:0\n\t"
        "v_cvt_pk_bf16_f32 v229, v10, v14\n\t"
        "v_mfma_f32_16x16x32_bf16 v[36:39], v[212:215], v[188:191], v[36:39]\n\t"
        "v_cvt_pk_bf16_f32 v228, v2, v6\n\t"
        "v_mfma_f32_16x16x32_bf16 v[36:39], v[208:211], v[196:199], v[36:39]\n\t"
        "ds_read_b128 v[156:159], %[ba] offset:33792\n\t"
        "global_store_dwordx2 %[oo2], v[228:229], %[ob0] offset:0 nt\n\t"
        "v_mfma_f32_16x16x32_bf16 v[40:43], v[208:211], v[140:143], v[40:43]\n\t"
        "v_mfma_f32_16x16x32_bf16 v[40:43], v[212:215], v[136:139], v[40:43]\n\t"
        "ds_read_b128 v[164:167], v233 offset:0\n\t"
        "v_cvt_pk_bf16_f32 v231, v11, v15\n\t"
        "v_mfma_f32_16x16x32_bf16 v[44:47], v[212:215], v[200:203], v[44:47]\n\t"
        "v_cvt_pk_bf16_f32 v230, v3, v7\n\t"
        "v_mfma_f32_16x16x32_bf16 v[44:47], v[208:211], v[204:207], v[44:47]\n\t"
        "ds_read_b128 v[168:171], %[ba] offset:41984\n\t"
        "s_waitcnt lgkmcnt(4)\n\t"
        "v_mfma_f32_16x16x32_bf16 v[64:67], v[152:155], v[132:135], v[64:67]\n\t"
        "global_store_dwordx2 %[oo3], v[230:231], %[ob0] offset:0 nt\n\t"
        "v_mfma_f32_16x16x32_bf16 v[64:67], v[144:147], v[180:183], v[64:67]\n\t"
        "ds_read_b128 v[172:175], %[ba] offset:42240\n\t"
        "v_cvt_pk_bf16_f32 v224, v32, v36\n\t"
        "v_mfma_f32_16x16x32_bf16 v[68:71], v[144:147], v[188:191], v[68:71]\n\t"
        "v_mfma_f32_16x16x32_bf16 v[68:71], v[152:155], v[196:199], v[68:71]\n\t"
        "ds_read_b128 v[176:179], %[ba] offset:34048\n\t"
        "v_cvt_pk_bf16_f32 v225, v40, v44\n\t"
        "v_mfma_f32_16x16x32_bf16 v[72:75], v[152:155], v[140:143], v[72:75]\n\t"
        "global_store_dwordx2 %[oo0], v[224:225], %[ob1] offset:0 nt\n\t"
        "v_mfma_f32_16x16x32_bf16 v[72:75], v[144:147], v[136:139], v[72:75]\n\t"
        "ds_read_b128 v[184:187], %[ba] offset:34304\n\t"
        "ds_read_b128 v[192:195], %[ba] offset:42496\n\t"
        "v_mfma_f32_16x16x32_bf16 v[76:79], v[144:147], v[200:203], v[76:79]\n\t"
        "v_cvt_pk_bf16_f32 v227, v41, v45\n\t"
        "v_cvt_pk_bf16_f32 v226, v33, v37\n\t"
        "v_mfma_f32_16x16x32_bf16 v[76:79], v[152:155], v[204:207], v[76:79]\n\t"
        "ds_read_b128 v[144:147], %[ba] offset:42752\n\t"
        "global_store_dwordx2 %[oo1], v[226:227], %[ob1] offset:0 nt\n\t"
        "v_mfma_f32_16x16x32_bf16 v[96:99], v[160:163], v[132:135], v[96:99]\n\t"
        ".Lwp16:\n\t"
        "s_cbranch_execz .Lwp17\n\t"
        "v_mfma_f32_16x16x32_bf16 v[96:99], v[148:151], v[180:183], v[96:99]\n\t"
        "ds_read_b128 v[132:135], %[ba] offset:34560\n\t"
        "v_cvt_pk_bf16_f32 v229, v42, v46\n\t"
        "v_mfma_f32_16x16x32_bf16 v[100:103], v[148:151], v[188:191], v[100:103]\n\t"
        "v_cvt_pk_bf16_f32 v228, v34, v38\n\t"
        "v_mfma_f32_16x16x32_bf16 v[100:103], v[160:163], v[196:199], v[100:103]\n\t"
        "ds_read_b128 v[152:155], v232 offset:2048\n\t"
        "global_store_dwordx2 %[oo2], v[228:229], %[ob1] offset:0 nt\n\t"
        "v_mfma_f32_16x16x32_bf16 v[104:107], v[160:163], v[140:143], v[104:107]\n\t"
        "v_mfma_f32_16x16x32_bf16 v[104:107], v[148:151], v[136:139], v[104:107]\n\t"
        "ds_read_b128 v[140:143], v233 offset:2048\n\t"
        "v_cvt_pk_bf16_f32 v231, v43, v47\n\t"
        "v_mfma_f32_16x16x32_bf16 v[108:111], v[148:151], v[200:203], v[108:111]\n\t"
        "v_cvt_pk_bf16_f32 v230, v35, v39\n\t"
        "v_mfma_f32_16x16x32_bf16 v[108:111], v[160:163], v[204:207], v[108:111]\n\t"
        "global_store_dwordx2 %[oo3], v[230:231], %[ob1] offset:0 nt\n\t"
        "s_waitcnt lgkmcnt(3)\n\t"
        "v_mfma_f32_16x16x32_bf16 v[16:19], v[128:131], v[156:159], v[16:19]\n\t"
        "v_mfma_f32_16x16x32_bf16 v[16:19], v[164:167], v[168:171], v[16:19]\n\t"
        "ds_read_b128 v[136:139], v232 offset:4096\n\t"
        "v_cvt_pk_bf16_f32 v225, v72, v76\n\t"
        "v_mfma_f32_16x16x32_bf16 v[20:23], v[164:167], v[172:175], v[20:23]\n\t"
        "v_cvt_pk_bf16_f32 v224, v64, v68\n\t"
        "v_mfma_f32_16x16x32_bf16 v[20:23], v[128:131], v[176:179], v[20:23]\n\t"
        "ds_read_b128 v[148:151], v233 offset:4096\n\t"
        "global_store_dwordx2 %[oo0], v[224:225], %[ob2] offset:0 nt\n\t"
        "v_mfma_f32_16x16x32_bf16 v[24:27], v[128:131], v[184:187], v[24:27]\n\t"
        "v_mfma_f32_16x16x32_bf16 v[24:27], v[164:167], v[192:195], v[24:27]\n\t"
        "ds_read_b128 v[160:163], v232 offset:6144\n\t"
        "v_cvt_pk_bf16_f32 v227, v73, v77\n\t"
        "v_mfma_f32_16x16x32_bf16 v[28:31], v[164:167], v[144:147], v[28:31]\n\t"
        "v_cvt_pk_bf16_f32 v226, v65, v69\n\t"
        "s_waitcnt lgkmcnt(3)\n\t"
        "v_mfma_f32_16x16x32_bf16 v[28:31], v[128:131], v[132:135], v[28:31]\n\t"
        "ds_read_b128 v[164:167], v233 offset:6144\n\t"
        "global_store_dwordx2 %[oo1], v[226:227], %[ob2] offset:0 nt\n\t"
        "v_mfma_f32_16x16x32_bf16 v[48:51], v[152:155], v[156:159], v[48:51]\n\t"
        "v_mfma_f32_16x16x32_bf16 v[48:51], v[140:143], v[168:171], v[48:51]\n\t"
        "ds_read_b128 v[128:131], v232 offset:32768\n\t"
        "v_cvt_pk_bf16_f32 v229, v74, v78\n\t"
        "v_mfma_f32_16x16x32_bf16 v[52:55], v[140:143], v[172:175], v[52:55]\n\t"
        "v_cvt_pk_bf16_f32 v228, v66, v70\n\t"
        "v_mfma_f32_16x16x32_bf16 v[52:55], v[152:155], v[176:179], v[52:55]\n\t"
        "ds_read_b128 v[180:183], v233 offset:32768\n\t"
        "ds_read_b128 v[188:191], %[xb0] offset:0\n\t"
        "v_mfma_f32_16x16x32_bf16 v[56:59], v[152:155], v[184:187], v[56:59]\n\t"
        "global_store_dwordx2 %[oo2], v[228:229], %[ob2] offset:0 nt\n\t"
        "v_mfma_f32_16x16x32_bf16 v[56:59], v[140:143], v[192:195], v[56:59]\n\t"
        "v_cvt_pk_bf16_f32 v231, v75, v79\n\t"
        "v_cvt_pk_bf16_f32 v230, v67, v71\n\t"
        "v_mfma_f32_16x16x32_bf16 v[60:63], v[140:143], v[144:147], v[60:63]\n\t"
        "v_mfma_f32_16x16x32_bf16 v[60:63], v[152:155], v[132:135], v[60:63]\n\t"
        "global_store_dwordx2 %[oo3], v[230:231], %[ob2] offset:0 nt\n\t"
        "s_barrier\n\t"
        "s_waitcnt lgkmcnt(3)\n\t"
        "v_mfma_f32_16x16x32_bf16 v[80:83], v[136:139], v[156:159], v[80:83]\n\t"
        "v_mfma_f32_16x16x32_bf16 v[80:83], v[148:151], v[168:171], v[80:83]\n\t"
        "ds_read_b128 v[140:143], %[ba] offset:0\n\t"
        "ds_read_b128 v[152:155], %[xb1] offset:0\n\t"
        "v_mfma_f32_16x16x32_bf16 v[84:87], v[148:151], v[172:175], v[84:87]\n\t"
        "v_cvt_pk_bf16_f32 v225, v104, v108\n\t"
        "v_cvt_pk_bf16_f32 v224, v96, v100\n\t"
        "v_mfma_f32_16x16x32_bf16 v[84:87], v[136:139], v[176:179], v[84:87]\n\t"
        ".Lwp17:\n\t"
        "s_cbranch_execz .Lwp18\n\t"
        "ds_read_b128 v[196:199], %[ba] offset:8192\n\t"
        "v_mfma_f32_16x16x32_bf16 v[88:91], v[136:139], v[184:187], v[88:91]\n\t"
        "ds_read_b128 v[200:203], %[ba] offset:1024\n\t"
        "ds_read_b128 v[204:207], %[ba] offset:9216\n\t"
        "v_mfma_f32_16x16x32_bf16 v[88:91], v[148:151], v[192:195], v[88:91]\n\t"
        "global_store_dwordx2 %[oo0], v[224:225], %[ob3] offset:0 nt\n\t"
        "ds_read_b128 v[208:211], %[ba] offset:9472\n\t"
        "v_mfma_f32_16x16x32_bf16 v[92:95], v[148:151], v[144:147], v[92:95]\n\t"
        "ds_read_b128 v[212:215], %[ba] offset:1280\n\t"
        "ds_read_b128 v[216:219], %[ba] offset:8448\n\t"
        "v_mfma_f32_16x16x32_bf16 v[92:95], v[136:139], v[132:135], v[92:95]\n\t"
        "v_cvt_pk_bf16_f32 v227, v105, v109\n\t"
        "v_cvt_pk_bf16_f32 v226, v97, v101\n\t"
        "v_mfma_f32_16x16x32_bf16 v[112:115], v[160:163], v[156:159], v[112:115]\n\t"
        "ds_read_b128 v[136:139], %[ba] offset:256\n\t"
        "ds_read_b128 v[148:151], %[ba] offset:512\n\t"
        "v_mfma_f32_16x16x32_bf16 v[112:115], v[164:167], v[168:171], v[112:115]\n\t"
        "ds_read_b128 v[156:159], %[ba] offset:8704\n\t"
        "global_store_dwordx2 %[oo1], v[226:227], %[ob3] offset:0 nt\n\t"
        "v_mfma_f32_16x16x32_bf16 v[116:119], v[164:167], v[172:175], v[116:119]\n\t"
        "ds_read_b128 v[168:171], %[ba] offset:1536\n\t"
        "ds_read_b128 v[220:223], %[ba] offset:9728\n\t"
        "v_mfma_f32_16x16x32_bf16 v[116:119], v[160:163], v[176:179], v[116:119]\n\t"
        "v_cvt_pk_bf16_f32 v229, v106, v110\n\t"
        "v_cvt_pk_bf16_f32 v228, v98, v102\n\t"
        "v_mfma_f32_16x16x32_bf16 v[120:123], v[160:163], v[184:187], v[120:123]\n\t"
        "ds_read_b128 v[172:175], %[ba] offset:9984\n\t"
        "ds_read_b128 v[176:179], %[ba] offset:1792\n\t"
        "v_mfma_f32_16x16x32_bf16 v[120:123], v[164:167], v[192:195], v[120:123]\n\t"
        "global_store_dwordx2 %[oo2], v[228:229], %[ob3] offset:0 nt\n\t"
        "ds_read_b128 v[184:187], %[ba] offset:8960\n\t"
        "v_mfma_f32_16x16x32_bf16 v[124:127], v[164:167], v[144:147], v[124:127]\n\t"
        "v_cvt_pk_bf16_f32 v231, v107, v111\n\t"
        "v_cvt_pk_bf16_f32 v230, v99, v103\n\t"
        "v_mfma_f32_16x16x32_bf16 v[124:127], v[160:163], v[132:135], v[124:127]\n\t"
        "ds_read_b128 v[144:147], %[ba] offset:768\n\t"
        "s_waitcnt lgkmcnt(9)\n\t"
        "v_mfma_f32_16x16x32_bf16 v[16:19], v[128:131], v[140:143], v[16:19]\n\t"
        "global_store_dwordx2 %[oo3], v[230:231], %[ob3] offset:0 nt\n\t"
        "v_mfma_f32_16x16x32_bf16 v[16:19], v[180:183], v[196:199], v[16:19]\n\t"
        "ds_read_b128 v[132:135], v232 offset:34816\n\t"
        "v_mfma_f32_16x16x32_bf16 v[16:19], v[188:191], v[200:203], v[16:19]\n\t"
        "v_mfma_f32_16x16x32_bf16 v[16:19], v[152:155], v[204:207], v[16:19]\n\t"
        "ds_read_b128 v[160:163], v233 offset:34816\n\t"
        "v_mfma_f32_16x16x32_bf16 v[20:23], v[152:155], v[208:211], v[20:23]\n\t"
        "v_mfma_f32_16x16x32_bf16 v[20:23], v[188:191], v[212:215], v[20:23]\n\t"
        "ds_read_b128 v[164:167], %[xb0] offset:2048\n\t"
        "v_mfma_f32_16x16x32_bf16 v[20:23], v[180:183], v[216:219], v[20:23]\n\t"
        "s_waitcnt lgkmcnt(4)\n\t"
        "v_mfma_f32_16x16x32_bf16 v[20:23], v[128:131], v[136:139], v[20:23]\n\t"
        "ds_read_b128 v[192:195], %[xb1] offset:2048\n\t"
        "v_mfma_f32_16x16x32_bf16 v[24:27], v[128:131], v[148:151], v[24:27]\n\t"
        "v_mfma_f32_16x16x32_bf16 v[24:27], v[180:183], v[156:159], v[24:27]\n\t"
        "v_mfma_f32_16x16x32_bf16 v[24:27], v[188:191], v[168:171], v[24:27]\n\t"
        "v_mfma_f32_16x16x32_bf16 v[24:27], v[152:155], v[220:223], v[24:27]\n\t"
        "v_mfma_f32_16x16x32_bf16 v[28:31], v[152:155], v[172:175], v[28:31]\n\t"
        "v_mfma_f32_16x16x32_bf16 v[28:31], v[188:191], v[176:179], v[28:31]\n\t"
        "v_mfma_f32_16x16x32_bf16 v[28:31], v[180:183], v[184:187], v[28:31]\n\t"
        "s_waitcnt lgkmcnt(1)\n\t"
        "v_mfma_f32_16x16x32_bf16 v[28:31], v[128:131], v[144:147], v[28:31]\n\t"
        "v_mfma_f32_16x16x32_bf16 v[48:51], v[132:135], v[140:143], v[48:51]\n\t"
        "v_mfma_f32_16x16x32_bf16 v[48:51], v[160:163], v[196:199], v[48:51]\n\t"
        "ds_read_b128 v[128:131], v232 offset:36864\n\t"
        "v_cvt_pk_bf16_f32 v224, v16, v20\n\t"
        "v_mfma_f32_16x16x32_bf16 v[48:51], v[164:167], v[200:203], v[48:51]\n\t"
        "s_waitcnt lgkmcnt(1)\n\t"
        "v_mfma_f32_16x16x32_bf16 v[48:51], v[192:195], v[204:207], v[48:51]\n\t"
        "ds_read_b128 v[152:155], v233 offset:36864\n\t"
        "v_cvt_pk_bf16_f32 v225, v24, v28\n\t"
        "v_mfma_f32_16x16x32_bf16 v[52:55], v[192:195], v[208:211], v[52:55]\n\t"
        "global_store_dwordx2 %[oo0], v[224:225], %[ob0] offset:128 nt\n\t"
        "v_mfma_f32_16x16x32_bf16 v[52:55], v[164:167], v[212:215], v[52:55]\n\t"
        "ds_read_b128 v[180:183], %[xb0] offset:4096\n\t"
        "v_cvt_pk_bf16_f32 v227, v25, v29\n\t"
        "v_mfma_f32_16x16x32_bf16 v[52:55], v[160:163], v[216:219], v[52:55]\n\t"
        "v_cvt_pk_bf16_f32 v226, v17, v21\n\t"
        "v_mfma_f32_16x16x32_bf16 v[52:55], v[132:135], v[136:139], v[52:55]\n\t"
        "ds_read_b128 v[188:191], %[xb1] offset:4096\n\t"
        "global_store_dwordx2 %[oo1], v[226:227], %[ob0] offset:128 nt\n\t"
        "v_mfma_f32_16x16x32_bf16 v[56:59], v[132:135], v[148:151], v[56:59]\n\t"
        "v_mfma_f32_16x16x32_bf16 v[56:59], v[160:163], v[156:159], v[56:59]\n\t"
        "v_cvt_pk_bf16_f32 v229, v26, v30\n\t"
        "v_cvt_pk_bf16_f32 v228, v18, v22\n\t"
        "v_mfma_f32_16x16x32_bf16 v[56:59], v[164:167], v[168:171], v[56:59]\n\t"
        "v_mfma_f32_16x16x32_bf16 v[56:59], v[192:195], v[220:223], v[56:59]\n\t"
        "global_store_dwordx2 %[oo2], v[228:229], %[ob0] offset:128 nt\n\t"
        "v_mfma_f32_16x16x32_bf16 v[60:63], v[192:195], v[172:175], v[60:63]\n\t"
        "v_mfma_f32_16x16x32_bf16 v[60:63], v[164:167], v[176:179], v[60:63]\n\t"
        "v_cvt_pk_bf16_f32 v231, v27, v31\n\t"
        "v_cvt_pk_bf16_f32 v230, v19, v23\n\t"
        "v_mfma_f32_16x16x32_bf16 v[60:63], v[160:163], v[184:187], v[60:63]\n\t"
        ".Lwp18:\n\t"
        "s_cbranch_execz .Lwp97\n\t"
        "v_mfma_f32_16x16x32_bf16 v[60:63], v[132:135], v[144:147], v[60:63]\n\t"
        "global_store_dwordx2 %[oo3], v[230:231], %[ob0] offset:128 nt\n\t"
        "s_waitcnt lgkmcnt(0)\n\t"
        "v_mfma_f32_16x16x32_bf16 v[80:83], v[128:131], v[140:143], v[80:83]\n\t"
        "v_mfma_f32_16x16x32_bf16 v[80:83], v[152:155], v[196:199], v[80:83]\n\t"
        "ds_read_b128 v[132:135], v232 offset:38912\n\t"
        "v_cvt_pk_bf16_f32 v224, v48, v52\n\t"
        "v_mfma_f32_16x16x32_bf16 v[80:83], v[180:183], v[200:203], v[80:83]\n\t"
        "v_mfma_f32_16x16x32_bf16 v[80:83], v[188:191], v[204:207], v[80:83]\n\t"
        "ds_read_b128 v[160:163], v233 offset:38912\n\t"
        "v_cvt_pk_bf16_f32 v225, v56, v60\n\t"
        "v_mfma_f32_16x16x32_bf16 v[84:87], v[188:191], v[208:211], v[84:87]\n\t"
        "global_store_dwordx2 %[oo0], v[224:225], %[ob1] offset:128 nt\n\t"
        "v_mfma_f32_16x16x32_bf16 v[84:87], v[180:183], v[212:215], v[84:87]\n\t"
        "ds_read_b128 v[164:167], %[xb0] offset:6144\n\t"
        "v_cvt_pk_bf16_f32 v227, v57, v61\n\t"
        "v_mfma_f32_16x16x32_bf16 v[84:87], v[152:155], v[216:219], v[84:87]\n\t"
        "v_cvt_pk_bf16_f32 v226, v49, v53\n\t"
        "v_mfma_f32_16x16x32_bf16 v[84:87], v[128:131], v[136:139], v[84:87]\n\t"
        "ds_read_b128 v[192:195], %[xb1] offset:6144\n\t"
        "global_store_dwordx2 %[oo1], v[226:227], %[ob1] offset:128 nt\n\t"
        "v_mfma_f32_16x16x32_bf16 v[88:91], v[128:131], v[148:151], v[88:91]\n\t"
        "v_mfma_f32_16x16x32_bf16 v[88:91], v[152:155], v[156:159], v[88:91]\n\t"
        "v_cvt_pk_bf16_f32 v229, v58, v62\n\t"
        "v_cvt_pk_bf16_f32 v228, v50, v54\n\t"
        "v_mfma_f32_16x16x32_bf16 v[88:91], v[180:183], v[168:171], v[88:91]\n\t"
        "v_mfma_f32_16x16x32_bf16 v[88:91], v[188:191], v[220:223], v[88:91]\n\t"
        "global_store_dwordx2 %[oo2], v[228:229], %[ob1] offset:128 nt\n\t"
        "v_mfma_f32_16x16x32_bf16 v[92:95], v[188:191], v[172:175], v[92:95]\n\t"
        "v_mfma_f32_16x16x32_bf16 v[92:95], v[180:183], v[176:179], v[92:95]\n\t"
        "v_cvt_pk_bf16_f32 v231, v59, v63\n\t"
        "v_cvt_pk_bf16_f32 v230, v51, v55\n\t"
        "v_mfma_f32_16x16x32_bf16 v[92:95], v[152:155], v[184:187], v[92:95]\n\t"
        "v_mfma_f32_16x16x32_bf16 v[92:95], v[128:131], v[144:147], v[92:95]\n\t"
        "global_store_dwordx2 %[oo3], v[230:231], %[ob1] offset:128 nt\n\t"
        "s_waitcnt lgkmcnt(0)\n\t"
        "v_mfma_f32_16x16x32_bf16 v[112:115], v[132:135], v[140:143], v[112:115]\n\t"
        "v_mfma_f32_16x16x32_bf16 v[112:115], v[160:163], v[196:199], v[112:115]\n\t"
        "v_cvt_pk_bf16_f32 v224, v80, v84\n\t"
        "v_mfma_f32_16x16x32_bf16 v[112:115], v[164:167], v[200:203], v[112:115]\n\t"
        "v_mfma_f32_16x16x32_bf16 v[112:115], v[192:195], v[204:207], v[112:115]\n\t"
        "v_cvt_pk_bf16_f32 v225, v88, v92\n\t"
        "global_store_dwordx2 %[oo0], v[224:225], %[ob2] offset:128 nt\n\t"
        "v_mfma_f32_16x16x32_bf16 v[116:119], v[192:195], v[208:211], v[116:119]\n\t"
        "v_mfma_f32_16x16x32_bf16 v[116:119], v[164:167], v[212:215], v[116:119]\n\t"
        "v_cvt_pk_bf16_f32 v227, v89, v93\n\t"
        "v_cvt_pk_bf16_f32 v226, v81, v85\n\t"
        "v_mfma_f32_16x16x32_bf16 v[116:119], v[160:163], v[216:219], v[116:119]\n\t"
        "v_mfma_f32_16x16x32_bf16 v[116:119], v[132:135], v[136:139], v[116:119]\n\t"
        "global_store_dwordx2 %[oo1], v[226:227], %[ob2] offset:128 nt\n\t"
        "v_mfma_f32_16x16x32_bf16 v[120:123], v[132:135], v[148:151], v[120:123]\n\t"
        "v_mfma_f32_16x16x32_bf16 v[120:123], v[160:163], v[156:159], v[120:123]\n\t"
        "v_cvt_pk_bf16_f32 v229, v90, v94\n\t"
        "v_cvt_pk_bf16_f32 v228, v82, v86\n\t"
        "v_mfma_f32_16x16x32_bf16 v[120:123], v[164:167], v[168:171], v[120:123]\n\t"
        "v_mfma_f32_16x16x32_bf16 v[120:123], v[192:195], v[220:223], v[120:123]\n\t"
        "global_store_dwordx2 %[oo2], v[228:229], %[ob2] offset:128 nt\n\t"
        "v_mfma_f32_16x16x32_bf16 v[124:127], v[192:195], v[172:175], v[124:127]\n\t"
        "v_mfma_f32_16x16x32_bf16 v[124:127], v[164:167], v[176:179], v[124:127]\n\t"
        "v_cvt_pk_bf16_f32 v230, v113, v117\n\t"
        "v_cvt_pk_bf16_f32 v224, v112, v116\n\t"
        "v_mfma_f32_16x16x32_bf16 v[124:127], v[160:163], v[184:187], v[124:127]\n\t"
        "v_cvt_pk_bf16_f32 v227, v91, v95\n\t"
        "v_cvt_pk_bf16_f32 v226, v83, v87\n\t"
        "v_mfma_f32_16x16x32_bf16 v[124:127], v[132:135], v[144:147], v[124:127]\n\t"
        "v_cvt_pk_bf16_f32 v228, v115, v119\n\t"
        "v_cvt_pk_bf16_f32 v128, v114, v118\n\t"
        "global_store_dwordx2 %[oo3], v[226:227], %[ob2] offset:128 nt\n\t"
        "s_nop 7\n\t"
        "v_cvt_pk_bf16_f32 v225, v120, v124\n\t"
        "global_store_dwordx2 %[oo0], v[224:225], %[ob3] offset:128 nt\n\t"
        "v_cvt_pk_bf16_f32 v231, v121, v125\n\t"
        "global_store_dwordx2 %[oo1], v[230:231], %[ob3] offset:128 nt\n\t"
        "v_cvt_pk_bf16_f32 v129, v122, v126\n\t"
        "global_store_dwordx2 %[oo2], v[128:129], %[ob3] offset:128 nt\n\t"
        "v_cvt_pk_bf16_f32 v229, v123, v127\n\t"
        "global_store_dwordx2 %[oo3], v[228:229], %[ob3] offset:128 nt\n\t"
        :
        : [xb0] "v"(xb0), [xb1] "v"(xb1), [ba] "v"(ba), [oo0] "v"(oo[0]), [oo1] "v"(oo[1]), [oo2] "v"(oo[2]), [oo3] "v"(oo[3]),
          [ob0] "s"(ob[0]), [ob1] "s"(ob[1]), [ob2] "s"(ob[2]), [ob3] "s"(ob[3]), [w] "s"(w)
        : "scc", "memory", "s60", "s61", "s62", "s63", "s64", "s65", "s66", "s67", "v0", "v1", "v2", "v3", "v4", "v5", "v6", "v7", "v8", "v9", "v10", "v11", "v12", "v13", "v14", "v15", "v16", "v17", "v18", "v19", "v20", "v21", "v22", "v23", "v24", "v25", "v26", "v27", "v28", "v29", "v30", "v31", "v32", "v33", "v34", "v35", "v36", "v37", "v38", "v39", "v40", "v41", "v42", "v43", "v44", "v45", "v46", "v47", "v48", "v49", "v50", "v51", "v52", "v53", "v54", "v55", "v56", "v57", "v58", "v59", "v60", "v61", "v62", "v63", "v64", "v65", "v66", "v67", "v68", "v69", "v70", "v71", "v72", "v73", "v74", "v75", "v76", "v77", "v78", "v79", "v80", "v81", "v82", "v83", "v84", "v85", "v86", "v87", "v88", "v89", "v90", "v91", "v92", "v93", "v94", "v95", "v96", "v97", "v98", "v99", "v100", "v101", "v102", "v103", "v104", "v105", "v106", "v107", "v108", "v109", "v110", "v111", "v112", "v113", "v114", "v115", "v116", "v117", "v118", "v119", "v120", "v121", "v122", "v123", "v124", "v125", "v126", "v127", "v128", "v129", "v130", "v131", "v132", "v133", "v134", "v135", "v136", "v137", "v138", "v139", "v140", "v141", "v142", "v143", "v144", "v145", "v146", "v147", "v148", "v149", "v150", "v151", "v152", "v153", "v154", "v155", "v156", "v157", "v158", "v159", "v160", "v161", "v162", "v163", "v164", "v165", "v166", "v167", "v168", "v169", "v170", "v171", "v172", "v173", "v174", "v175", "v176", "v177", "v178", "v179", "v180", "v181", "v182", "v183", "v184", "v185", "v186", "v187", "v188", "v189", "v190", "v191", "v192", "v193", "v194", "v195", "v196", "v197", "v198", "v199", "v200", "v201", "v202", "v203", "v204", "v205", "v206", "v207", "v208", "v209", "v210", "v211", "v212", "v213", "v214", "v215", "v216", "v217", "v218", "v219", "v220", "v221", "v222", "v223", "v224", "v225", "v226", "v227", "v228", "v229", "v230", "v231", "v232", "v233", "v234", "v235");
}

extern "C" void kernel_launch(void* const* d_in, const int* in_sizes, int n_in,
                              void* d_out, int out_size, void* d_ws, size_t ws_size,
                              hipStream_t stream) {
    const u16* x = (const u16*)d_in[0];
    const int* qw = (const int*)d_in[1];
    const u16* lut = (const u16*)d_in[2];
    u16* out = (u16*)d_out;
    (void)hipFuncSetAttribute((const void*)fused_kernel, hipFuncAttributeMaxDynamicSharedMemorySize, LDS_BYTES);
    fused_kernel<<<(M_ / BM) * (N_ / BN) + NPF, 512, LDS_BYTES, stream>>>(x, qw, lut, out, (char*)d_ws);
}
```

```cpp
#include <hip/hip_runtime.h>
#include <hip/hip_bf16.h>

typedef unsigned short u16;
typedef unsigned int u32;
using bf16x8 = __attribute__((ext_vector_type(8))) short;
using f32x4  = __attribute__((ext_vector_type(4))) float;
using i32x4  = __attribute__((ext_vector_type(4))) int;
using u32x2  = __attribute__((ext_vector_type(2))) unsigned int;
using u32x4  = __attribute__((ext_vector_type(4))) unsigned int;

constexpr int M_ = 2048, K_ = 1024, N_ = 3584;
constexpr int BM = 256, BN = 128;
constexpr int NPF = 32;
#define XSLEEP 4
#define SP0 3
#define NQ1 4
#define CPF 116
#define CPFB 32
#define PACE 0
#define VORD 1
constexpr int XS = BM * 64 * 2;
constexpr int BS = BN * 32 * 2;
constexpr int OFF_B = 3 * XS;
constexpr int WSL = 2 * BS;
constexpr int OFF_L = OFF_B + 3 * WSL;
constexpr int OFF_D = OFF_L + 16 * BN * 2;
constexpr int WS_F = 28 * 256, WS_W = 65536;
constexpr unsigned TW = 28 * 16384 + 0;
constexpr int SPIN_MAX = 1 << 16;
constexpr int LDS_BYTES = OFF_D + 1024;

typedef __bf16 bf16x2_t __attribute__((ext_vector_type(2)));
typedef float f32x2_t __attribute__((ext_vector_type(2)));
__device__ __forceinline__ u32 pack_bf16(float lo, float hi) {
    f32x2_t f = {lo, hi};
    bf16x2_t b = __builtin_convertvector(f, bf16x2_t);
    return __builtin_bit_cast(u32, b);
}

#define ROT0 0
#define OWN(mt, nt) (((mt) >> 1) ^ (ROT0 ? (nt) & 3 : 0))
#define TLO(mt, nt) ((0 && ((mt) & 1) ? 0x75631240u : 0x76532140u) ^ ((u32)OWN(mt, nt) * 0x11111111u))
#define THI(mt, nt) (0xFEDCBA98u ^ ((u32)(0) * 0x11111111u))
#define BAR() __builtin_amdgcn_s_barrier()
#define SB() __builtin_amdgcn_sched_barrier(0)
#define WAITQ(n, Qa) asm volatile("s_waitcnt vmcnt(" #n ") lgkmcnt(0)" : "+v"(Qa[0]), "+v"(Qa[1]), "+v"(Qa[2]), "+v"(Qa[3]), \
                                  "+v"(Qa[4]), "+v"(Qa[5]), "+v"(Qa[6]), "+v"(Qa[7]) :: "memory")
#define PBAR() BAR()
#define PAD(id, nx) asm volatile(".Lwp%c0:\n\ts_cbranch_execz .Lwp%c1" :: "i"(id), "i"(nx))

__global__ __launch_bounds__(512, 2) void fused_kernel(const u16* __restrict__ x_, const int* __restrict__ qw_,
                                                       const u16* __restrict__ lut_, u16* __restrict__ out_, char* ws_) {
    extern __shared__ __attribute__((aligned(16))) char smem[];
    typedef unsigned long u64x4 __attribute__((ext_vector_type(4)));
    u64x4 ka;
    unsigned long kp[5], kw;
    asm volatile("s_load_dwordx8 %0, %2, 0x0\n\ts_load_dwordx2 %1, %2, 0x20" : "=&s"(ka), "=&s"(kw) : "s"(__builtin_amdgcn_kernarg_segment_ptr()));
    {
        unsigned long pc0, ex;
        u32 co;
        asm volatile("s_getpc_b64 %0\n\t"
                     "s_cmp_lt_u32 %4, %5\n\t"
                     "s_cselect_b32 m0, %6, 0\n\t"
                     "v_cmp_gt_u32 vcc, m0, %3\n\t"
                     "s_and_b32 m0, %4, 24\n\t"
                     "s_lshl_b32 m0, m0, 4\n\t"
                     "v_lshl_add_u32 %2, %3, 9, m0\n\t"
                     "s_mov_b32 m0, %7\n\t"
                     "s_and_saveexec_b64 %1, vcc\n\t"
                     "global_load_lds_dword %2, %0\n\t"
                     "s_mov_b64 exec, %1"
                     : "=&s"(pc0), "=&s"(ex), "=&v"(co) : "v"(threadIdx.x), "s"(blockIdx.x), "i"(CPFB), "i"(CPF / 4), "i"(OFF_D) : "vcc", "scc", "memory");
    }
#define KWAIT() do { SB(); asm volatile("s_waitcnt lgkmcnt(0)\n\ts_mov_b64 %0, %5\n\ts_mov_b64 %1, %6\n\ts_mov_b64 %2, %7\n\ts_mov_b64 %3, %8\n\ts_mov_b64 %4, %9" \
                     : "=&s"(kp[0]), "=&s"(kp[1]), "=&s"(kp[2]), "=&s"(kp[3]), "=&s"(kp[4]) : "s"(ka[0]), "s"(ka[1]), "s"(ka[2]), "s"(ka[3]), "s"(kw) : "memory"); SB(); } while (0)
#define KPTR(i) (kp[i])
    const int tid = threadIdx.x;
    const int w = __builtin_amdgcn_readfirstlane(tid >> 6);
    const int lane = tid & 63;
    const int bid = (int)blockIdx.x - NPF;
    const int xcd = blockIdx.x & 7, idx = bid >> 3;
    if (bid < 0) {
        const int pw = (blockIdx.x >> 3) * 8 + w;
        KWAIT();
        const char* qb = (const char*)KPTR(1) + (xcd >> 1) * 896 * 4 + (long)((xcd & 1) * 2) * (64 * N_ * 4);
        const char* xb = (const char*)KPTR(0) + (long)(xcd & 1) * 1024 * 2048;
        const u32 dst = (u32)(size_t)(smem) + w * 256;
        {
            const int m = (xcd & 1) * 4 + (blockIdx.x >> 3);
            const u32 off = (u32)((m * 256 + (w & 3) * 64 + lane) * 2048 + ((TLO(m, 0) >> (w & 4)) & 15) * 128);
            if (0 && w >= 4) __builtin_amdgcn_s_sleep(0);
            asm volatile("s_mov_b32 m0, %2\n\ts_nop 0\n\tglobal_load_lds_dword %0, %1" :: "v"(off), "s"(KPTR(0)), "s"(dst) : "memory");
        }
#pragma unroll 1
        for (int G = pw; G < 168; G += 32) {
            int kind = 0, arg = 0, e = 0;
            if (G >= 0) { kind = 0; arg = 2; e = G - 0 + (xcd >> 1) * 4; }
            if (G >= 4) { kind = 0; arg = 3; e = G - 4 + (xcd >> 1) * 4; }
            if (G >= 8) { kind = 0; arg = 4; e = G - 8 + (xcd >> 1) * 4; }
            if (G >= 12) { kind = 1; arg = 8; e = G - 12; }
            if (G >= 32) { kind = 0; arg = 5; e = G - 32 + (xcd >> 1) * 4; }
            if (G >= 36) { kind = 1; arg = 8; e = G - 16; }
            if (G >= 64) { kind = 1; arg = 8; e = G - 16; }
            if (G >= 72) { kind = 0; arg = 6; e = G - 72 + (xcd >> 1) * 4; }
            if (G >= 76) { kind = 0; arg = 7; e = G - 76 + (xcd >> 1) * 4; }
            if (G >= 80) { kind = 1; arg = 12; e = G - 80; }
            if (G >= 96) { kind = 0; arg = 8; e = G - 96 + (xcd >> 1) * 4; }
            if (G >= 100) { kind = 0; arg = 9; e = G - 100 + (xcd >> 1) * 4; }
            if (G >= 104) { kind = 1; arg = 12; e = G - 88; }
            if (G >= 128) { kind = 0; arg = 10; e = G - 128 + (xcd >> 1) * 4; }
            if (G >= 132) { kind = 1; arg = 12; e = G - 92; }
            if (G >= 148) { kind = 0; arg = 11; e = G - 148 + (xcd >> 1) * 4; }
            if (G >= 152) { kind = 0; arg = 12; e = G - 152 + (xcd >> 1) * 4; }
            if (G >= 156) { kind = 0; arg = 13; e = G - 156 + (xcd >> 1) * 4; }
            if (G >= 160) { kind = 0; arg = 14; e = G - 160 + (xcd >> 1) * 4; }
            if (G >= 164) { kind = 0; arg = 15; e = G - 164 + (xcd >> 1) * 4; }
            asm volatile("s_waitcnt vmcnt(%0)" :: "i"(PACE) : "memory");
            if (kind == 2) __builtin_amdgcn_s_sleep(24);
            else if (kind) {
                const int L = e * 64 + lane;
                const int r = L / 28, c = L - r * 28;
                const u32 off = (u32)((arg * 64 + r) * (N_ * 4) + c * 128);
                asm volatile("s_mov_b32 m0, %2\n\ts_nop 0\n\tglobal_load_lds_dword %0, %1" :: "v"(off), "s"(qb), "s"(dst) : "memory");
            } else {
                const int t = ROT0 || arg >= 8 ? arg : (int)((TLO((xcd & 1) * 4 + (e >> 2), 0) >> (4 * arg)) & 15);
                const u32 off = (u32)((e * 64 + lane) * 2048 + t * 128);
                asm volatile("s_mov_b32 m0, %2\n\ts_nop 0\n\tglobal_load_lds_dword %0, %1" :: "v"(off), "s"(xb), "s"(dst) : "memory");
            }
        }
        asm volatile("s_waitcnt vmcnt(0)" ::: "memory");
        return;
    }
    const int mt = (xcd & 1) * 4 + (idx & 3);
    const int nt = (xcd >> 1) * 7 + (idx >> 2);
    const int row0 = mt * BM, col0 = nt * BN;

    if (__builtin_expect(w >= 4, 0)) {
        int lane;
        asm volatile("v_mbcnt_lo_u32_b32 %0, -1, 0\n\tv_mbcnt_hi_u32_b32 %0, -1, %0" : "=v"(lane));
        const int p = w - 4;
        const int l = lane & 31, h = lane >> 5;
        const int g = lane >> 4;
        u32 xoff[4];
#pragma unroll
        for (int i = 0; i < 4; ++i)
            xoff[i] = (u32)(((row0 + p * 64 + i * 8 + (lane >> 3)) * K_ + (((lane & 7) ^ ((i & 1) * 4 + g)) * 8)) * 2 - i * 1024);
        const u32 ldsx = (u32)(size_t)(smem) + p * 8192;
        auto issue_x1 = [&](int tile, int hh, int slotoff, int i) {
            const u16* base = (const u16*)KPTR(0) + (long)hh * 32 * K_ + tile * 64;
            const u32 dst = ldsx + slotoff + hh * 4096;
            if (i == 0) asm volatile("s_mov_b32 m0, %2\n\ts_nop 0\n\tglobal_load_lds_dwordx4 %0, %1" :: "v"(xoff[0]), "s"(base), "s"(dst) : "memory");
            else asm volatile("global_load_lds_dwordx4 %0, %1 offset:%2" :: "v"(xoff[i]), "s"(base), "i"(i * 1024) : "memory");
        };
        u32 qoff[8];
#pragma unroll
        for (int r = 0; r < 8; ++r) qoff[r] = (u32)(((p * 16 + h * 8 + r) * N_ + col0 + l * 4) * 4);
        auto issue_q1 = [&](int t, i32x4 (&Q)[8], int r) {
            const int* base = (const int*)KPTR(1) + (long)t * 64 * N_;
            asm volatile("global_load_dwordx4 %0, %1, %2 " : "=v"(Q[r]) : "v"(qoff[r]), "s"(base) : "memory");
        };
        u32 lutl = (u32)(size_t)(smem) + OFF_L + l * 4;
        u32 bdst = (u32)(size_t)(smem) + OFF_B + (p >> 1) * BS + ((2 * p + h) & 3) * 2048 + ((l >> 4) * 64 + (l & 15)) * 16;
        u32 V[4][8];
#define LUT1(Q, j, r) asm volatile("v_lshl_add_u32 %0, %1, 8, %2\n\tds_read_u16 %0, %0 offset:%3" : "=&v"(V[j][r]) \
                                   : "v"(Q[r][j]), "v"(lutl), "i"(((j) >> 1) * 128 + ((j) & 1) * 2) : "memory")
#define LUT4(Q, e) do { LUT1(Q, (e) >> 1, ((e) & 1) * 4); LUT1(Q, (e) >> 1, ((e) & 1) * 4 + 1); \
                        LUT1(Q, (e) >> 1, ((e) & 1) * 4 + 2); LUT1(Q, (e) >> 1, ((e) & 1) * 4 + 3); SB(); } while (0)
#define WRB(j, n) do { u32x4 b_; \
                       asm volatile("s_waitcnt lgkmcnt(" #n ")\n\t" \
                                    "v_lshl_or_b32 %0, %5, 16, %4\n\tv_lshl_or_b32 %1, %7, 16, %6\n\t" \
                                    "v_lshl_or_b32 %2, %9, 16, %8\n\tv_lshl_or_b32 %3, %11, 16, %10" \
                                    : "=&v"(b_[0]), "=&v"(b_[1]), "=&v"(b_[2]), "=&v"(b_[3]) \
                                    : "v"(V[j][0]), "v"(V[j][1]), "v"(V[j][2]), "v"(V[j][3]), "v"(V[j][4]), "v"(V[j][5]), "v"(V[j][6]), "v"(V[j][7]) : "memory"); \
                       asm volatile("ds_write_b128 %0, %1 offset:%2" :: "v"(bdst), "v"(b_), "i"((j) * 256) : "memory"); SB(); } while (0)
        const int i2 = OWN(mt, nt), h2 = mt & 1;
        const unsigned long tseq = ((unsigned long)THI(mt, nt) << 32) | TLO(mt, nt);
        const u32 ldsw = (u32)(size_t)(smem) + OFF_B + p * 4096;
        u32 wv = (u32)(nt * 16384 + p * 4096 + lane * 16);
        u32 wo = (u32)(nt * 16384 + p * 4096 + h * 2048 + (l >> 4) * 1024 + (l & 15) * 16);
        u32 po = (u32)(WS_F + (nt * 4 + p) * 256 + lane * 4);
        u32 fo0 = po + (u32)(i2 * 16 + h2 * 8);
        u32 no = (u32)(nt * 256 + (lane & 1) * 4);
        u32 nvl = no;
        KWAIT();
        const char* st1 = (const char*)KPTR(4) + WS_W + (long)(i2 + 4) * TW + h2 * 512;
        const char* qa1 = (const char*)KPTR(1);
        const int tb = 8 + (mt >> 1), kg = h2 * 4 + p;
        const char* qbg = (const char*)KPTR(1) + (long)(tb * 64 + kg * 8 - p * 16) * (N_ * 4);
        const char* stb = (const char*)KPTR(4) + WS_W + (long)tb * TW + (kg * 2048 - p * 4096);
        u32 fob = po + (u32)(((kg >> 1) - p) * 256 + tb * 16 + (kg & 1) * 8);
        asm volatile("" : "+v"(wo), "+v"(fo0), "+v"(fob), "+v"(wv), "+v"(po), "+v"(bdst), "+v"(lutl), "+v"(qoff[0]));
        asm volatile("" : "+v"(xoff[0]), "+v"(xoff[1]), "+v"(xoff[2]), "+v"(xoff[3]));
        asm volatile("" : "+s"(st1), "+s"(qa1), "+s"(qbg), "+s"(stb));
        {
            unsigned long ex_;
            asm volatile("s_mov_b64 %0, exec\n\ts_mov_b64 exec, 0\n\t"
                         "s_cmp_eq_u32 %1, 0\n\ts_cbranch_scc1 .Lwp30\n\t"
                         "s_cmp_eq_u32 %1, 1\n\ts_cbranch_scc1 .Lwp31\n\t"
                         "s_cmp_eq_u32 %1, 2\n\ts_cbranch_scc1 .Lwp32\n\t"
                         "s_branch .Lwp33\n\t"
                         ".Lwp98:\n\ts_mov_b64 exec, %0" : "=&s"(ex_) : "s"(p) : "scc", "memory");
        }
        asm volatile(
            "s_mov_b32 s73, 65536\n\t"
            "s_mov_b32 s72, 0\n\t"
            "s_mov_b64 s[84:85], %[ws]\n\t"
            "s_add_u32 s84, s84, 65536\n\t"
            "s_addc_u32 s85, s85, 0\n\t"
            "s_mov_b64 s[56:57], %[tseq]\n\t"
            "s_bfe_u32 s40, s56, 262144\n\t"
            "s_bfe_u32 s41, s56, 262148\n\t"
            "s_bfe_u32 s42, s56, 262152\n\t"
            "s_bfe_u32 s43, s56, 262156\n\t"
            "s_bfe_u32 s44, s56, 262160\n\t"
            "s_bfe_u32 s45, s56, 262164\n\t"
            "s_bfe_u32 s46, s56, 262168\n\t"
            "s_bfe_u32 s47, s56, 262172\n\t"
            "s_bfe_u32 s48, s57, 262144\n\t"
            "s_bfe_u32 s49, s57, 262148\n\t"
            "s_bfe_u32 s50, s57, 262152\n\t"
            "s_bfe_u32 s51, s57, 262156\n\t"
            "s_bfe_u32 s52, s57, 262160\n\t"
            "s_bfe_u32 s53, s57, 262164\n\t"
            "s_bfe_u32 s54, s57, 262168\n\t"
            "s_bfe_u32 s55, s57, 262172\n\t"
            "global_load_dword %[nvl], %[nvl], %[ws] sc1\n\t"
            "global_load_dword v192, %[po], %[ws] sc1\n\t"
            "s_barrier\n\t"
            ".Lwp30:\n\t"
            "s_cbranch_execz .Lwp98\n\t"
            "s_add_u32 m0, %[ldsx], 0\n\t"
            "s_lshl_b32 s76, s40, 7\n\t"
            "s_add_u32 s68, %[xpl], s76\n\t"
            "s_addc_u32 s69, %[xph], 0\n\t"
            "global_load_lds_dwordx4 %[x0], s[68:69] offset:0\n\t"
            "global_load_lds_dwordx4 %[x1], s[68:69] offset:1024\n\t"
            "global_load_lds_dwordx4 %[x2], s[68:69] offset:2048\n\t"
            "global_load_lds_dwordx4 %[x3], s[68:69] offset:3072\n\t"
            "s_add_u32 m0, %[ldsx], 4096\n\t"
            "s_lshl_b32 s76, s40, 7\n\t"
            "s_add_u32 s76, s76, 65536\n\t"
            "s_add_u32 s68, %[xpl], s76\n\t"
            "s_addc_u32 s69, %[xph], 0\n\t"
            "global_load_lds_dwordx4 %[x0], s[68:69] offset:0\n\t"
            "global_load_lds_dwordx4 %[x1], s[68:69] offset:1024\n\t"
            "global_load_lds_dwordx4 %[x2], s[68:69] offset:2048\n\t"
            "global_load_lds_dwordx4 %[x3], s[68:69] offset:3072\n\t"
            "s_mul_i32 s76, s41, 917504\n\t"
            "s_mov_b64 s[88:89], %[qa1]\n\t"
            "s_add_u32 s88, s88, s76\n\t"
            "s_addc_u32 s89, s89, 0\n\t"
            "global_load_dwordx4 v[128:131], %[qo], s[88:89] \n\t"
            "s_add_u32 s88, s88, 14336\n\t"
            "s_addc_u32 s89, s89, 0\n\t"
            "global_load_dwordx4 v[132:135], %[qo], s[88:89] \n\t"
            "s_add_u32 s88, s88, 14336\n\t"
            "s_addc_u32 s89, s89, 0\n\t"
            "global_load_dwordx4 v[136:139], %[qo], s[88:89] \n\t"
            "s_add_u32 s88, s88, 14336\n\t"
            "s_addc_u32 s89, s89, 0\n\t"
            "global_load_dwordx4 v[140:143], %[qo], s[88:89] \n\t"
            "s_add_u32 s88, s88, 14336\n\t"
            "s_addc_u32 s89, s89, 0\n\t"
            "global_load_dwordx4 v[144:147], %[qo], s[88:89] \n\t"
            "s_add_u32 s88, s88, 14336\n\t"
            "s_addc_u32 s89, s89, 0\n\t"
            "global_load_dwordx4 v[148:151], %[qo], s[88:89] \n\t"
            "s_add_u32 s88, s88, 14336\n\t"
            "s_addc_u32 s89, s89, 0\n\t"
            "global_load_dwordx4 v[152:155], %[qo], s[88:89] \n\t"
            "s_add_u32 s88, s88, 14336\n\t"
            "s_addc_u32 s89, s89, 0\n\t"
            "global_load_dwordx4 v[156:159], %[qo], s[88:89] \n\t"
            "s_waitcnt vmcnt(8)\n\t"
            "s_barrier\n\t"
            ".Lwp31:\n\t"
            "s_cbranch_execz .Lwp98\n\t"
            "s_lshl_b32 s76, %[h2], 9\n\t"
            "v_add_u32 v193, s76, %[bd]\n\t"
            "ds_read_b128 v[196:199], v193\n\t"
            "ds_read_b128 v[200:203], v193 offset:256\n\t"
            "s_mov_b64 s[70:71], %[st1]\n\t"
            "s_sub_u32 s70, s70, 1835008\n\t"
            "s_subb_u32 s71, s71, 0\n\t"
            "s_waitcnt lgkmcnt(0)\n\t"
            "global_store_dwordx4 %[wo], v[196:199], s[70:71] sc1\n\t"
            "global_store_dwordx4 %[wo], v[200:203], s[70:71] offset:256 sc1\n\t"
            "s_mov_b32 s76, 1664525\n\t"
            "v_mul_lo_u32 %[nvl], %[nvl], s76\n\t"
            "v_add_u32 %[nvl], 1013904223, %[nvl]\n\t"
            "s_setprio 1\n\t"
            "s_waitcnt vmcnt(9)\n\t"
            "v_lshl_add_u32 v160, v128, 8, %[lutl]\n\t"
            "ds_read_u16 v160, v160 offset:0\n\t"
            "v_lshl_add_u32 v168, v129, 8, %[lutl]\n\t"
            "ds_read_u16 v168, v168 offset:2\n\t"
            "v_lshl_add_u32 v176, v130, 8, %[lutl]\n\t"
            "ds_read_u16 v176, v176 offset:128\n\t"
            "v_lshl_add_u32 v184, v131, 8, %[lutl]\n\t"
            "ds_read_u16 v184, v184 offset:130\n\t"
            "s_add_u32 m0, %[ldsx], 32768\n\t"
            "s_lshl_b32 s76, s41, 7\n\t"
            "s_add_u32 s68, %[xpl], s76\n\t"
            "s_addc_u32 s69, %[xph], 0\n\t"
            "global_load_lds_dwordx4 %[x0], s[68:69] offset:0\n\t"
            "s_waitcnt vmcnt(9)\n\t"
            "v_lshl_add_u32 v161, v132, 8, %[lutl]\n\t"
            "ds_read_u16 v161, v161 offset:0\n\t"
            "v_lshl_add_u32 v169, v133, 8, %[lutl]\n\t"
            "ds_read_u16 v169, v169 offset:2\n\t"
            "v_lshl_add_u32 v177, v134, 8, %[lutl]\n\t"
            "ds_read_u16 v177, v177 offset:128\n\t"
            "v_lshl_add_u32 v185, v135, 8, %[lutl]\n\t"
            "ds_read_u16 v185, v185 offset:130\n\t"
            "global_load_lds_dwordx4 %[x1], s[68:69] offset:1024\n\t"
            "s_waitcnt vmcnt(9)\n\t"
            "v_lshl_add_u32 v162, v136, 8, %[lutl]\n\t"
            "ds_read_u16 v162, v162 offset:0\n\t"
            "v_lshl_add_u32 v170, v137, 8, %[lutl]\n\t"
            "ds_read_u16 v170, v170 offset:2\n\t"
            "v_lshl_add_u32 v178, v138, 8, %[lutl]\n\t"
            "ds_read_u16 v178, v178 offset:128\n\t"
            "v_lshl_add_u32 v186, v139, 8, %[lutl]\n\t"
            "ds_read_u16 v186, v186 offset:130\n\t"
            "global_load_lds_dwordx4 %[x2], s[68:69] offset:2048\n\t"
            "s_waitcnt vmcnt(9)\n\t"
            "v_lshl_add_u32 v163, v140, 8, %[lutl]\n\t"
            "ds_read_u16 v163, v163 offset:0\n\t"
            "v_lshl_add_u32 v171, v141, 8, %[lutl]\n\t"
            "ds_read_u16 v171, v171 offset:2\n\t"
            "v_lshl_add_u32 v179, v142, 8, %[lutl]\n\t"
            "ds_read_u16 v179, v179 offset:128\n\t"
            "v_lshl_add_u32 v187, v143, 8, %[lutl]\n\t"
            "ds_read_u16 v187, v187 offset:130\n\t"
            "global_load_lds_dwordx4 %[x3], s[68:69] offset:3072\n\t"
            "s_waitcnt vmcnt(9)\n\t"
            "v_lshl_add_u32 v164, v144, 8, %[lutl]\n\t"
            "ds_read_u16 v164, v164 offset:0\n\t"
            "v_lshl_add_u32 v172, v145, 8, %[lutl]\n\t"
            "ds_read_u16 v172, v172 offset:2\n\t"
            "v_lshl_add_u32 v180, v146, 8, %[lutl]\n\t"
            "ds_read_u16 v180, v180 offset:128\n\t"
            "v_lshl_add_u32 v188, v147, 8, %[lutl]\n\t"
            "ds_read_u16 v188, v188 offset:130\n\t"
            "s_add_u32 m0, %[ldsx], 36864\n\t"
            "s_lshl_b32 s76, s41, 7\n\t"
            "s_add_u32 s76, s76, 65536\n\t"
            "s_add_u32 s68, %[xpl], s76\n\t"
            "s_addc_u32 s69, %[xph], 0\n\t"
            "global_load_lds_dwordx4 %[x0], s[68:69] offset:0\n\t"
            "s_waitcnt vmcnt(9)\n\t"
            "v_lshl_add_u32 v165, v148, 8, %[lutl]\n\t"
            "ds_read_u16 v165, v165 offset:0\n\t"
            "v_lshl_add_u32 v173, v149, 8, %[lutl]\n\t"
            "ds_read_u16 v173, v173 offset:2\n\t"
            "v_lshl_add_u32 v181, v150, 8, %[lutl]\n\t"
            "ds_read_u16 v181, v181 offset:128\n\t"
            "v_lshl_add_u32 v189, v151, 8, %[lutl]\n\t"
            "ds_read_u16 v189, v189 offset:130\n\t"
            "global_load_lds_dwordx4 %[x1], s[68:69] offset:1024\n\t"
            "s_waitcnt vmcnt(9)\n\t"
            "v_lshl_add_u32 v166, v152, 8, %[lutl]\n\t"
            "ds_read_u16 v166, v166 offset:0\n\t"
            "v_lshl_add_u32 v174, v153, 8, %[lutl]\n\t"
            "ds_read_u16 v174, v174 offset:2\n\t"
            "v_lshl_add_u32 v182, v154, 8, %[lutl]\n\t"
            "ds_read_u16 v182, v182 offset:128\n\t"
            "v_lshl_add_u32 v190, v155, 8, %[lutl]\n\t"
            "ds_read_u16 v190, v190 offset:130\n\t"
            "global_load_lds_dwordx4 %[x2], s[68:69] offset:2048\n\t"
            "s_waitcnt vmcnt(9)\n\t"
            "v_lshl_add_u32 v167, v156, 8, %[lutl]\n\t"
            "ds_read_u16 v167, v167 offset:0\n\t"
            "v_lshl_add_u32 v175, v157, 8, %[lutl]\n\t"
            "ds_read_u16 v175, v175 offset:2\n\t"
            "v_lshl_add_u32 v183, v158, 8, %[lutl]\n\t"
            "ds_read_u16 v183, v183 offset:128\n\t"
            "v_lshl_add_u32 v191, v159, 8, %[lutl]\n\t"
            "ds_read_u16 v191, v191 offset:130\n\t"
            "global_load_lds_dwordx4 %[x3], s[68:69] offset:3072\n\t"
            "s_setprio 0\n\t"
            "s_waitcnt vmcnt(8)\n\t"
            "s_mov_b64 exec, 3\n\t"
            "global_store_dword %[fo0], %[nvl], %[ws] offset:0 sc1\n\t"
            "s_mov_b64 exec, -1\n\t"
            "s_waitcnt lgkmcnt(3)\n\t"
            "v_lshl_or_b32 v128, v161, 16, v160\n\t"
            "v_lshl_or_b32 v129, v163, 16, v162\n\t"
            "v_lshl_or_b32 v130, v165, 16, v164\n\t"
            "v_lshl_or_b32 v131, v167, 16, v166\n\t"
            "ds_write_b128 %[bd], v[128:131] offset:16384\n\t"
            "s_waitcnt lgkmcnt(3)\n\t"
            "v_lshl_or_b32 v132, v169, 16, v168\n\t"
            "v_lshl_or_b32 v133, v171, 16, v170\n\t"
            "v_lshl_or_b32 v134, v173, 16, v172\n\t"
            "v_lshl_or_b32 v135, v175, 16, v174\n\t"
            "ds_write_b128 %[bd], v[132:135] offset:16640\n\t"
            "s_waitcnt lgkmcnt(3)\n\t"
            "v_lshl_or_b32 v136, v177, 16, v176\n\t"
            "v_lshl_or_b32 v137, v179, 16, v178\n\t"
            "v_lshl_or_b32 v138, v181, 16, v180\n\t"
            "v_lshl_or_b32 v139, v183, 16, v182\n\t"
            "ds_write_b128 %[bd], v[136:139] offset:16896\n\t"
            "s_waitcnt lgkmcnt(3)\n\t"
            "v_lshl_or_b32 v140, v185, 16, v184\n\t"
            "v_lshl_or_b32 v141, v187, 16, v186\n\t"
            "v_lshl_or_b32 v142, v189, 16, v188\n\t"
            "v_lshl_or_b32 v143, v191, 16, v190\n\t"
            "ds_write_b128 %[bd], v[140:143] offset:17152\n\t"
            "s_add_u32 m0, %[ldsx], 65536\n\t"
            "s_lshl_b32 s76, s42, 7\n\t"
            "s_add_u32 s68, %[xpl], s76\n\t"
            "s_addc_u32 s69, %[xph], 0\n\t"
            "global_load_lds_dwordx4 %[x0], s[68:69] offset:0\n\t"
            "global_load_lds_dwordx4 %[x1], s[68:69] offset:1024\n\t"
            "global_load_lds_dwordx4 %[x2], s[68:69] offset:2048\n\t"
            "global_load_lds_dwordx4 %[x3], s[68:69] offset:3072\n\t"
            "s_add_u32 m0, %[ldsx], 69632\n\t"
            "s_lshl_b32 s76, s42, 7\n\t"
            "s_add_u32 s76, s76, 65536\n\t"
            "s_add_u32 s68, %[xpl], s76\n\t"
            "s_addc_u32 s69, %[xph], 0\n\t"
            "global_load_lds_dwordx4 %[x0], s[68:69] offset:0\n\t"
            "global_load_lds_dwordx4 %[x1], s[68:69] offset:1024\n\t"
            "global_load_lds_dwordx4 %[x2], s[68:69] offset:2048\n\t"
            "global_load_lds_dwordx4 %[x3], s[68:69] offset:3072\n\t"
            "s_waitcnt vmcnt(9) lgkmcnt(0)\n\t"
            "s_barrier\n\t"
            ".Lwp32:\n\t"
            "s_cbranch_execz .Lwp98\n\t"
            "global_load_dword v192, %[po], %[ws] sc1\n\t"
            "s_add_u32 m0, %[ldsx], 0\n\t"
            "s_lshl_b32 s76, s43, 7\n\t"
            "s_add_u32 s68, %[xpl], s76\n\t"
            "s_addc_u32 s69, %[xph], 0\n\t"
            "global_load_lds_dwordx4 %[x0], s[68:69] offset:0\n\t"
            "global_load_lds_dwordx4 %[x1], s[68:69] offset:1024\n\t"
            "global_load_lds_dwordx4 %[x2], s[68:69] offset:2048\n\t"
            "global_load_lds_dwordx4 %[x3], s[68:69] offset:3072\n\t"
            "s_add_u32 m0, %[ldsx], 4096\n\t"
            "s_lshl_b32 s76, s43, 7\n\t"
            "s_add_u32 s76, s76, 65536\n\t"
            "s_add_u32 s68, %[xpl], s76\n\t"
            "s_addc_u32 s69, %[xph], 0\n\t"
            "global_load_lds_dwordx4 %[x0], s[68:69] offset:0\n\t"
            "global_load_lds_dwordx4 %[x1], s[68:69] offset:1024\n\t"
            "global_load_lds_dwordx4 %[x2], s[68:69] offset:2048\n\t"
            "global_load_lds_dwordx4 %[x3], s[68:69] offset:3072\n\t"
            "global_load_dword v195, %[po], %[ws] sc1\n\t"
            "s_cmp_lg_u32 %[h2], 0\n\t"
            "s_cbranch_scc1 .Lzs0\n\t"
            "global_store_dwordx4 %[wo], v[128:131], %[st1] sc1\n\t"
            "global_store_dwordx4 %[wo], v[132:135], %[st1] offset:256 sc1\n\t"
            "s_branch .Lzt0\n\t"
            ".Lzs0:\n\t"
            "global_store_dwordx4 %[wo], v[136:139], %[st1] sc1\n\t"
            "global_store_dwordx4 %[wo], v[140:143], %[st1] offset:256 sc1\n\t"
            ".Lzt0:\n\t"
            "s_waitcnt vmcnt(11)\n\t"
            "s_lshl_b32 s76, s42, 2\n\t"
            ".Lzc1:\n\t"
            "v_cmp_eq_u32 vcc, v192, %[nvl]\n\t"
            "s_lshr_b64 s[74:75], vcc, s76\n\t"
            "s_and_b32 s74, s74, 15\n\t"
            "s_cmp_eq_u32 s74, 15\n\t"
            "s_cbranch_scc1 .Lzd1\n\t"
            "s_cmp_eq_u32 s73, 0\n\t"
            "s_cbranch_scc1 .Lzd1\n\t"
            "s_sub_u32 s73, s73, 1\n\t"
            "s_cmp_eq_u32 s72, 2\n\t"
            "s_cbranch_scc1 .Lze1\n\t"
            "s_mov_b32 s72, 2\n\t"
            "s_waitcnt vmcnt(2)\n\t"
            "v_mov_b32 v192, v195\n\t"
            "s_branch .Lzc1\n\t"
            ".Lze1:\n\t"
            "global_load_dword v192, %[po], %[ws] sc1\n\t"
            "s_waitcnt vmcnt(0)\n\t"
            "v_mov_b32 v195, v192\n\t"
            "s_branch .Lzc1\n\t"
            ".Lzd1:\n\t"
            "s_add_u32 m0, %[ldsw], 32768\n\t"
            "s_mul_i32 s76, s42, 458752\n\t"
            "s_add_u32 s70, s84, s76\n\t"
            "s_addc_u32 s71, s85, 0\n\t"
            "global_load_lds_dwordx4 %[wv], s[70:71] offset:0 sc1\n\t"
            "global_load_lds_dwordx4 %[wv], s[70:71] offset:1024 sc1\n\t"
            "global_load_lds_dwordx4 %[wv], s[70:71] offset:2048 sc1\n\t"
            "global_load_lds_dwordx4 %[wv], s[70:71] offset:3072 sc1\n\t"
            "s_waitcnt vmcnt(6)\n\t"
            "v_mov_b32 v192, v195\n\t"
            "s_lshl_b32 s76, s43, 2\n\t"
            ".Lzc2:\n\t"
            "v_cmp_eq_u32 vcc, v192, %[nvl]\n\t"
            "s_lshr_b64 s[74:75], vcc, s76\n\t"
            "s_and_b32 s74, s74, 15\n\t"
            "s_cmp_eq_u32 s74, 15\n\t"
            "s_cbranch_scc1 .Lzd2\n\t"
            "s_cmp_eq_u32 s73, 0\n\t"
            "s_cbranch_scc1 .Lzd2\n\t"
            "s_sub_u32 s73, s73, 1\n\t"
            "global_load_dword v192, %[po], %[ws] sc1\n\t"
            "s_waitcnt vmcnt(0)\n\t"
            "s_branch .Lzc2\n\t"
            ".Lzd2:\n\t"
            "s_add_u32 m0, %[ldsw], 0\n\t"
            "s_mul_i32 s76, s43, 458752\n\t"
            "s_add_u32 s70, s84, s76\n\t"
            "s_addc_u32 s71, s85, 0\n\t"
            "global_load_lds_dwordx4 %[wv], s[70:71] offset:0 sc1\n\t"
            "global_load_lds_dwordx4 %[wv], s[70:71] offset:1024 sc1\n\t"
            "global_load_lds_dwordx4 %[wv], s[70:71] offset:2048 sc1\n\t"
            "global_load_lds_dwordx4 %[wv], s[70:71] offset:3072 sc1\n\t"
            "s_waitcnt vmcnt(8)\n\t"
            "s_mov_b64 exec, 3\n\t"
            "global_store_dword %[fo0], %[nvl], %[ws] offset:64 sc1\n\t"
            "s_mov_b64 exec, -1\n\t"
            "s_waitcnt vmcnt(5)\n\t"
            "s_barrier\n\t"
            ".Lwp33:\n\t"
            "s_cbranch_execz .Lwp98\n\t"
            "v_mbcnt_lo_u32_b32 v193, -1, 0\n\t"
            "v_mbcnt_hi_u32_b32 v193, -1, v193\n\t"
            "v_lshrrev_b32 v193, 5, v193\n\t"
            "v_mul_u32_u24 v194, 57344, v193\n\t"
            "v_sub_u32 %[qo], %[qo], v194\n\t"
            "v_mul_u32_u24 v194, 1792, v193\n\t"
            "v_sub_u32 %[wo], %[wo], v194\n\t"
            "s_mov_b64 s[88:89], %[qbg]\n\t"
            "global_load_dwordx4 v[204:207], %[qo], s[88:89] \n\t"
            "s_add_u32 s88, s88, 14336\n\t"
            "s_addc_u32 s89, s89, 0\n\t"
            "global_load_dwordx4 v[208:211], %[qo], s[88:89] \n\t"
            "s_add_u32 s88, s88, 14336\n\t"
            "s_addc_u32 s89, s89, 0\n\t"
            "global_load_dwordx4 v[212:215], %[qo], s[88:89] \n\t"
            "s_add_u32 s88, s88, 14336\n\t"
            "s_addc_u32 s89, s89, 0\n\t"
            "global_load_dwordx4 v[216:219], %[qo], s[88:89] \n\t"
            "s_lshl_b32 s76, s44, 2\n\t"
            ".Lzc3:\n\t"
            "v_cmp_eq_u32 vcc, v192, %[nvl]\n\t"
            "s_lshr_b64 s[74:75], vcc, s76\n\t"
            "s_and_b32 s74, s74, 15\n\t"
            "s_cmp_eq_u32 s74, 15\n\t"
            "s_cbranch_scc1 .Lzd3\n\t"
            "s_cmp_eq_u32 s73, 0\n\t"
            "s_cbranch_scc1 .Lzd3\n\t"
            "s_sub_u32 s73, s73, 1\n\t"
            "global_load_dword v192, %[po], %[ws] sc1\n\t"
            "s_waitcnt vmcnt(0)\n\t"
            "s_branch .Lzc3\n\t"
            ".Lzd3:\n\t"
            "s_add_u32 m0, %[ldsw], 16384\n\t"
            "s_mul_i32 s76, s44, 458752\n\t"
            "s_add_u32 s70, s84, s76\n\t"
            "s_addc_u32 s71, s85, 0\n\t"
            "global_load_lds_dwordx4 %[wv], s[70:71] offset:0 sc1\n\t"
            "global_load_lds_dwordx4 %[wv], s[70:71] offset:1024 sc1\n\t"
            "global_load_lds_dwordx4 %[wv], s[70:71] offset:2048 sc1\n\t"
            "global_load_lds_dwordx4 %[wv], s[70:71] offset:3072 sc1\n\t"
            "s_add_u32 m0, %[ldsx], 32768\n\t"
            "s_lshl_b32 s76, s44, 7\n\t"
            "s_add_u32 s68, %[xpl], s76\n\t"
            "s_addc_u32 s69, %[xph], 0\n\t"
            "global_load_lds_dwordx4 %[x0], s[68:69] offset:0\n\t"
            "global_load_lds_dwordx4 %[x1], s[68:69] offset:1024\n\t"
            "global_load_lds_dwordx4 %[x2], s[68:69] offset:2048\n\t"
            "global_load_lds_dwordx4 %[x3], s[68:69] offset:3072\n\t"
            "s_add_u32 m0, %[ldsx], 36864\n\t"
            "s_lshl_b32 s76, s44, 7\n\t"
            "s_add_u32 s76, s76, 65536\n\t"
            "s_add_u32 s68, %[xpl], s76\n\t"
            "s_addc_u32 s69, %[xph], 0\n\t"
            "global_load_lds_dwordx4 %[x0], s[68:69] offset:0\n\t"
            "global_load_lds_dwordx4 %[x1], s[68:69] offset:1024\n\t"
            "global_load_lds_dwordx4 %[x2], s[68:69] offset:2048\n\t"
            "global_load_lds_dwordx4 %[x3], s[68:69] offset:3072\n\t"
            "s_waitcnt vmcnt(17)\n\t"
            "s_barrier\n\t"
            ".Lwp34:\n\t"
            "s_cbranch_execz .Lwp35\n\t"
            "global_load_dword v192, %[po], %[ws] sc1\n\t"
            "s_waitcnt vmcnt(13)\n\t"
            "s_setprio 0\n\t"
            "s_add_u32 m0, %[ldsx], 65536\n\t"
            "s_lshl_b32 s76, s45, 7\n\t"
            "s_add_u32 s68, %[xpl], s76\n\t"
            "s_addc_u32 s69, %[xph], 0\n\t"
            "global_load_lds_dwordx4 %[x0], s[68:69] offset:0\n\t"
            "v_lshl_add_u32 v160, v204, 8, %[lutl]\n\t"
            "ds_read_u16 v160, v160 offset:0\n\t"
            "v_lshl_add_u32 v161, v208, 8, %[lutl]\n\t"
            "ds_read_u16 v161, v161 offset:0\n\t"
            "global_load_lds_dwordx4 %[x1], s[68:69] offset:1024\n\t"
            "v_lshl_add_u32 v162, v212, 8, %[lutl]\n\t"
            "ds_read_u16 v162, v162 offset:0\n\t"
            "v_lshl_add_u32 v163, v216, 8, %[lutl]\n\t"
            "ds_read_u16 v163, v163 offset:0\n\t"
            "global_load_lds_dwordx4 %[x2], s[68:69] offset:2048\n\t"
            "v_lshl_add_u32 v168, v205, 8, %[lutl]\n\t"
            "ds_read_u16 v168, v168 offset:2\n\t"
            "v_lshl_add_u32 v169, v209, 8, %[lutl]\n\t"
            "ds_read_u16 v169, v169 offset:2\n\t"
            "global_load_lds_dwordx4 %[x3], s[68:69] offset:3072\n\t"
            "v_lshl_add_u32 v170, v213, 8, %[lutl]\n\t"
            "ds_read_u16 v170, v170 offset:2\n\t"
            "v_lshl_add_u32 v171, v217, 8, %[lutl]\n\t"
            "ds_read_u16 v171, v171 offset:2\n\t"
            "s_add_u32 m0, %[ldsx], 69632\n\t"
            "s_lshl_b32 s76, s45, 7\n\t"
            "s_add_u32 s76, s76, 65536\n\t"
            "s_add_u32 s68, %[xpl], s76\n\t"
            "s_addc_u32 s69, %[xph], 0\n\t"
            "global_load_lds_dwordx4 %[x0], s[68:69] offset:0\n\t"
            "v_lshl_add_u32 v176, v206, 8, %[lutl]\n\t"
            "ds_read_u16 v176, v176 offset:128\n\t"
            "v_lshl_add_u32 v177, v210, 8, %[lutl]\n\t"
            "ds_read_u16 v177, v177 offset:128\n\t"
            "global_load_lds_dwordx4 %[x1], s[68:69] offset:1024\n\t"
            "v_lshl_add_u32 v178, v214, 8, %[lutl]\n\t"
            "ds_read_u16 v178, v178 offset:128\n\t"
            "v_lshl_add_u32 v179, v218, 8, %[lutl]\n\t"
            "ds_read_u16 v179, v179 offset:128\n\t"
            "global_load_lds_dwordx4 %[x2], s[68:69] offset:2048\n\t"
            "v_lshl_add_u32 v184, v207, 8, %[lutl]\n\t"
            "ds_read_u16 v184, v184 offset:130\n\t"
            "v_lshl_add_u32 v185, v211, 8, %[lutl]\n\t"
            "ds_read_u16 v185, v185 offset:130\n\t"
            "global_load_lds_dwordx4 %[x3], s[68:69] offset:3072\n\t"
            "v_lshl_add_u32 v186, v215, 8, %[lutl]\n\t"
            "ds_read_u16 v186, v186 offset:130\n\t"
            "v_lshl_add_u32 v187, v219, 8, %[lutl]\n\t"
            "ds_read_u16 v187, v187 offset:130\n\t"
            "s_setprio 0\n\t"
            "s_waitcnt vmcnt(8)\n\t"
            "s_lshl_b32 s76, s45, 2\n\t"
            ".Lzc4:\n\t"
            "v_cmp_eq_u32 vcc, v192, %[nvl]\n\t"
            "s_lshr_b64 s[74:75], vcc, s76\n\t"
            "s_and_b32 s74, s74, 15\n\t"
            "s_cmp_eq_u32 s74, 15\n\t"
            "s_cbranch_scc1 .Lzd4\n\t"
            "s_cmp_eq_u32 s73, 0\n\t"
            "s_cbranch_scc1 .Lzd4\n\t"
            "s_sub_u32 s73, s73, 1\n\t"
            "global_load_dword v192, %[po], %[ws] sc1\n\t"
            "s_waitcnt vmcnt(0)\n\t"
            "s_branch .Lzc4\n\t"
            ".Lzd4:\n\t"
            "s_add_u32 m0, %[ldsw], 32768\n\t"
            "s_mul_i32 s76, s45, 458752\n\t"
            "s_add_u32 s70, s84, s76\n\t"
            "s_addc_u32 s71, s85, 0\n\t"
            "global_load_lds_dwordx4 %[wv], s[70:71] offset:0 sc1\n\t"
            "global_load_lds_dwordx4 %[wv], s[70:71] offset:1024 sc1\n\t"
            "global_load_lds_dwordx4 %[wv], s[70:71] offset:2048 sc1\n\t"
            "global_load_lds_dwordx4 %[wv], s[70:71] offset:3072 sc1\n\t"
            "s_waitcnt lgkmcnt(8)\n\t"
            "v_lshl_or_b32 v220, v161, 16, v160\n\t"
            "v_lshl_or_b32 v221, v163, 16, v162\n\t"
            "v_lshl_or_b32 v222, v169, 16, v168\n\t"
            "v_lshl_or_b32 v223, v171, 16, v170\n\t"
            "s_waitcnt lgkmcnt(0)\n\t"
            "v_lshl_or_b32 v224, v177, 16, v176\n\t"
            "v_lshl_or_b32 v225, v179, 16, v178\n\t"
            "v_lshl_or_b32 v226, v185, 16, v184\n\t"
            "v_lshl_or_b32 v227, v187, 16, v186\n\t"
            "s_nop 0\n\t"
            "v_permlane32_swap_b32 v220, v222\n\t"
            "v_permlane32_swap_b32 v221, v223\n\t"
            "v_permlane32_swap_b32 v224, v226\n\t"
            "v_permlane32_swap_b32 v225, v227\n\t"
            "s_mov_b64 s[88:89], %[stb]\n\t"
            "s_nop 0\n\t"
            "global_store_dwordx4 %[wo], v[220:223], s[88:89] offset:0 sc1\n\t"
            "global_store_dwordx4 %[wo], v[224:227], s[88:89] offset:512 sc1\n\t"
            "s_barrier\n\t"
            ".Lwp35:\n\t"
            "s_cbranch_execz .Lwp36\n\t"
            "s_lshl_b32 s76, s46, 2\n\t"
            ".Lzc5:\n\t"
            "v_cmp_eq_u32 vcc, v192, %[nvl]\n\t"
            "s_lshr_b64 s[74:75], vcc, s76\n\t"
            "s_and_b32 s74, s74, 15\n\t"
            "s_cmp_eq_u32 s74, 15\n\t"
            "s_cbranch_scc1 .Lzd5\n\t"
            "s_cmp_eq_u32 s73, 0\n\t"
            "s_cbranch_scc1 .Lzd5\n\t"
            "s_sub_u32 s73, s73, 1\n\t"
            "global_load_dword v192, %[po], %[ws] sc1\n\t"
            "s_waitcnt vmcnt(0)\n\t"
            "s_branch .Lzc5\n\t"
            ".Lzd5:\n\t"
            "s_add_u32 m0, %[ldsw], 0\n\t"
            "s_mul_i32 s76, s46, 458752\n\t"
            "s_add_u32 s70, s84, s76\n\t"
            "s_addc_u32 s71, s85, 0\n\t"
            "global_load_lds_dwordx4 %[wv], s[70:71] offset:0 sc1\n\t"
            "global_load_lds_dwordx4 %[wv], s[70:71] offset:1024 sc1\n\t"
            "global_load_lds_dwordx4 %[wv], s[70:71] offset:2048 sc1\n\t"
            "global_load_lds_dwordx4 %[wv], s[70:71] offset:3072 sc1\n\t"
            "s_add_u32 m0, %[ldsx], 0\n\t"
            "s_lshl_b32 s76, s46, 7\n\t"
            "s_add_u32 s68, %[xpl], s76\n\t"
            "s_addc_u32 s69, %[xph], 0\n\t"
            "global_load_lds_dwordx4 %[x0], s[68:69] offset:0\n\t"
            "global_load_lds_dwordx4 %[x1], s[68:69] offset:1024\n\t"
            "global_load_lds_dwordx4 %[x2], s[68:69] offset:2048\n\t"
            "global_load_lds_dwordx4 %[x3], s[68:69] offset:3072\n\t"
            "s_add_u32 m0, %[ldsx], 4096\n\t"
            "s_lshl_b32 s76, s46, 7\n\t"
            "s_add_u32 s76, s76, 65536\n\t"
            "s_add_u32 s68, %[xpl], s76\n\t"
            "s_addc_u32 s69, %[xph], 0\n\t"
            "global_load_lds_dwordx4 %[x0], s[68:69] offset:0\n\t"
            "global_load_lds_dwordx4 %[x1], s[68:69] offset:1024\n\t"
            "global_load_lds_dwordx4 %[x2], s[68:69] offset:2048\n\t"
            "global_load_lds_dwordx4 %[x3], s[68:69] offset:3072\n\t"
            "s_waitcnt vmcnt(12)\n\t"
            "s_mov_b64 exec, 3\n\t"
            "global_store_dword %[fob], %[nvl], %[ws] offset:0 sc1\n\t"
            "s_mov_b64 exec, -1\n\t"
            "s_barrier\n\t"
            "s_lshl_b32 s76, s47, 2\n\t"
            ".Lzc6:\n\t"
            "v_cmp_eq_u32 vcc, v192, %[nvl]\n\t"
            "s_lshr_b64 s[74:75], vcc, s76\n\t"
            "s_and_b32 s74, s74, 15\n\t"
            "s_cmp_eq_u32 s74, 15\n\t"
            "s_cbranch_scc1 .Lzd6\n\t"
            "s_cmp_eq_u32 s73, 0\n\t"
            "s_cbranch_scc1 .Lzd6\n\t"
            "s_sub_u32 s73, s73, 1\n\t"
            "global_load_dword v192, %[po], %[ws] sc1\n\t"
            "s_waitcnt vmcnt(0)\n\t"
            "s_branch .Lzc6\n\t"
            ".Lzd6:\n\t"
            "s_add_u32 m0, %[ldsw], 16384\n\t"
            "s_mul_i32 s76, s47, 458752\n\t"
            "s_add_u32 s70, s84, s76\n\t"
            "s_addc_u32 s71, s85, 0\n\t"
            "global_load_lds_dwordx4 %[wv], s[70:71] offset:0 sc1\n\t"
            "global_load_lds_dwordx4 %[wv], s[70:71] offset:1024 sc1\n\t"
            "global_load_lds_dwordx4 %[wv], s[70:71] offset:2048 sc1\n\t"
            "global_load_lds_dwordx4 %[wv], s[70:71] offset:3072 sc1\n\t"
            "s_add_u32 m0, %[ldsx], 32768\n\t"
            "s_lshl_b32 s76, s47, 7\n\t"
            "s_add_u32 s68, %[xpl], s76\n\t"
            "s_addc_u32 s69, %[xph], 0\n\t"
            "global_load_lds_dwordx4 %[x0], s[68:69] offset:0\n\t"
            "global_load_lds_dwordx4 %[x1], s[68:69] offset:1024\n\t"
            "global_load_lds_dwordx4 %[x2], s[68:69] offset:2048\n\t"
            "global_load_lds_dwordx4 %[x3], s[68:69] offset:3072\n\t"
            "s_add_u32 m0, %[ldsx], 36864\n\t"
            "s_lshl_b32 s76, s47, 7\n\t"
            "s_add_u32 s76, s76, 65536\n\t"
            "s_add_u32 s68, %[xpl], s76\n\t"
            "s_addc_u32 s69, %[xph], 0\n\t"
            "global_load_lds_dwordx4 %[x0], s[68:69] offset:0\n\t"
            "global_load_lds_dwordx4 %[x1], s[68:69] offset:1024\n\t"
            "global_load_lds_dwordx4 %[x2], s[68:69] offset:2048\n\t"
            "global_load_lds_dwordx4 %[x3], s[68:69] offset:3072\n\t"
            "s_mov_b64 s[88:89], %[qbg]\n\t"
            "s_add_u32 s88, s88, 3670016\n\t"
            "s_addc_u32 s89, s89, 0\n\t"
            "global_load_dwordx4 v[204:207], %[qo], s[88:89] \n\t"
            "s_add_u32 s88, s88, 14336\n\t"
            "s_addc_u32 s89, s89, 0\n\t"
            "global_load_dwordx4 v[208:211], %[qo], s[88:89] \n\t"
            "s_add_u32 s88, s88, 14336\n\t"
            "s_addc_u32 s89, s89, 0\n\t"
            "global_load_dwordx4 v[212:215], %[qo], s[88:89] \n\t"
            "s_add_u32 s88, s88, 14336\n\t"
            "s_addc_u32 s89, s89, 0\n\t"
            "global_load_dwordx4 v[216:219], %[qo], s[88:89] \n\t"
            "s_waitcnt vmcnt(17)\n\t"
            "s_barrier\n\t"
            ".Lwp36:\n\t"
            "s_cbranch_execz .Lwp37\n\t"
            "global_load_dword v192, %[po], %[ws] sc1\n\t"
            "s_add_u32 m0, %[ldsx], 65536\n\t"
            "s_lshl_b32 s76, s48, 7\n\t"
            "s_add_u32 s68, %[xpl], s76\n\t"
            "s_addc_u32 s69, %[xph], 0\n\t"
            "global_load_lds_dwordx4 %[x0], s[68:69] offset:0\n\t"
            "global_load_lds_dwordx4 %[x1], s[68:69] offset:1024\n\t"
            "global_load_lds_dwordx4 %[x2], s[68:69] offset:2048\n\t"
            "global_load_lds_dwordx4 %[x3], s[68:69] offset:3072\n\t"
            "s_add_u32 m0, %[ldsx], 69632\n\t"
            "s_lshl_b32 s76, s48, 7\n\t"
            "s_add_u32 s76, s76, 65536\n\t"
            "s_add_u32 s68, %[xpl], s76\n\t"
            "s_addc_u32 s69, %[xph], 0\n\t"
            "global_load_lds_dwordx4 %[x0], s[68:69] offset:0\n\t"
            "global_load_lds_dwordx4 %[x1], s[68:69] offset:1024\n\t"
            "global_load_lds_dwordx4 %[x2], s[68:69] offset:2048\n\t"
            "global_load_lds_dwordx4 %[x3], s[68:69] offset:3072\n\t"
            "s_waitcnt vmcnt(8)\n\t"
            "s_lshl_b32 s76, s48, 2\n\t"
            ".Lzc7:\n\t"
            "v_cmp_eq_u32 vcc, v192, %[nvl]\n\t"
            "s_lshr_b64 s[74:75], vcc, s76\n\t"
            "s_and_b32 s74, s74, 15\n\t"
            "s_cmp_eq_u32 s74, 15\n\t"
            "s_cbranch_scc1 .Lzd7\n\t"
            "s_cmp_eq_u32 s73, 0\n\t"
            "s_cbranch_scc1 .Lzd7\n\t"
            "s_sub_u32 s73, s73, 1\n\t"
            "global_load_dword v192, %[po], %[ws] sc1\n\t"
            "s_waitcnt vmcnt(0)\n\t"
            "s_branch .Lzc7\n\t"
            ".Lzd7:\n\t"
            "s_add_u32 m0, %[ldsw], 32768\n\t"
            "s_mul_i32 s76, s48, 458752\n\t"
            "s_add_u32 s70, s84, s76\n\t"
            "s_addc_u32 s71, s85, 0\n\t"
            "global_load_lds_dwordx4 %[wv], s[70:71] offset:0 sc1\n\t"
            "global_load_lds_dwordx4 %[wv], s[70:71] offset:1024 sc1\n\t"
            "global_load_lds_dwordx4 %[wv], s[70:71] offset:2048 sc1\n\t"
            "global_load_lds_dwordx4 %[wv], s[70:71] offset:3072 sc1\n\t"
            "s_barrier\n\t"
            ".Lwp37:\n\t"
            "s_cbranch_execz .Lwp97\n\t"
            "s_lshl_b32 s76, s49, 2\n\t"
            ".Lzc8:\n\t"
            "v_cmp_eq_u32 vcc, v192, %[nvl]\n\t"
            "s_lshr_b64 s[74:75], vcc, s76\n\t"
            "s_and_b32 s74, s74, 15\n\t"
            "s_cmp_eq_u32 s74, 15\n\t"
            "s_cbranch_scc1 .Lzd8\n\t"
            "s_cmp_eq_u32 s73, 0\n\t"
            "s_cbranch_scc1 .Lzd8\n\t"
            "s_sub_u32 s73, s73, 1\n\t"
            "global_load_dword v192, %[po], %[ws] sc1\n\t"
            "s_waitcnt vmcnt(0)\n\t"
            "s_branch .Lzc8\n\t"
            ".Lzd8:\n\t"
            "s_add_u32 m0, %[ldsw], 0\n\t"
            "s_mul_i32 s76, s49, 458752\n\t"
            "s_add_u32 s70, s84, s76\n\t"
            "s_addc_u32 s71, s85, 0\n\t"
            "global_load_lds_dwordx4 %[wv], s[70:71] offset:0 sc1\n\t"
            "global_load_lds_dwordx4 %[wv], s[70:71] offset:1024 sc1\n\t"
            "global_load_lds_dwordx4 %[wv], s[70:71] offset:2048 sc1\n\t"
            "global_load_lds_dwordx4 %[wv], s[70:71] offset:3072 sc1\n\t"
            "s_setprio 0\n\t"
            "s_add_u32 m0, %[ldsx], 0\n\t"
            "s_lshl_b32 s76, s49, 7\n\t"
            "s_add_u32 s68, %[xpl], s76\n\t"
            "s_addc_u32 s69, %[xph], 0\n\t"
            "global_load_lds_dwordx4 %[x0], s[68:69] offset:0\n\t"
            "v_lshl_add_u32 v160, v204, 8, %[lutl]\n\t"
            "ds_read_u16 v160, v160 offset:0\n\t"
            "v_lshl_add_u32 v161, v208, 8, %[lutl]\n\t"
            "ds_read_u16 v161, v161 offset:0\n\t"
            "global_load_lds_dwordx4 %[x1], s[68:69] offset:1024\n\t"
            "v_lshl_add_u32 v162, v212, 8, %[lutl]\n\t"
            "ds_read_u16 v162, v162 offset:0\n\t"
            "v_lshl_add_u32 v163, v216, 8, %[lutl]\n\t"
            "ds_read_u16 v163, v163 offset:0\n\t"
            "global_load_lds_dwordx4 %[x2], s[68:69] offset:2048\n\t"
            "v_lshl_add_u32 v168, v205, 8, %[lutl]\n\t"
            "ds_read_u16 v168, v168 offset:2\n\t"
            "v_lshl_add_u32 v169, v209, 8, %[lutl]\n\t"
            "ds_read_u16 v169, v169 offset:2\n\t"
            "global_load_lds_dwordx4 %[x3], s[68:69] offset:3072\n\t"
            "v_lshl_add_u32 v170, v213, 8, %[lutl]\n\t"
            "ds_read_u16 v170, v170 offset:2\n\t"
            "v_lshl_add_u32 v171, v217, 8, %[lutl]\n\t"
            "ds_read_u16 v171, v171 offset:2\n\t"
            "s_add_u32 m0, %[ldsx], 4096\n\t"
            "s_lshl_b32 s76, s49, 7\n\t"
            "s_add_u32 s76, s76, 65536\n\t"
            "s_add_u32 s68, %[xpl], s76\n\t"
            "s_addc_u32 s69, %[xph], 0\n\t"
            "global_load_lds_dwordx4 %[x0], s[68:69] offset:0\n\t"
            "v_lshl_add_u32 v176, v206, 8, %[lutl]\n\t"
            "ds_read_u16 v176, v176 offset:128\n\t"
            "v_lshl_add_u32 v177, v210, 8, %[lutl]\n\t"
            "ds_read_u16 v177, v177 offset:128\n\t"
            "global_load_lds_dwordx4 %[x1], s[68:69] offset:1024\n\t"
            "v_lshl_add_u32 v178, v214, 8, %[lutl]\n\t"
            "ds_read_u16 v178, v178 offset:128\n\t"
            "v_lshl_add_u32 v179, v218, 8, %[lutl]\n\t"
            "ds_read_u16 v179, v179 offset:128\n\t"
            "global_load_lds_dwordx4 %[x2], s[68:69] offset:2048\n\t"
            "v_lshl_add_u32 v184, v207, 8, %[lutl]\n\t"
            "ds_read_u16 v184, v184 offset:130\n\t"
            "v_lshl_add_u32 v185, v211, 8, %[lutl]\n\t"
            "ds_read_u16 v185, v185 offset:130\n\t"
            "global_load_lds_dwordx4 %[x3], s[68:69] offset:3072\n\t"
            "v_lshl_add_u32 v186, v215, 8, %[lutl]\n\t"
            "ds_read_u16 v186, v186 offset:130\n\t"
            "v_lshl_add_u32 v187, v219, 8, %[lutl]\n\t"
            "ds_read_u16 v187, v187 offset:130\n\t"
            "s_setprio 0\n\t"
            "s_waitcnt lgkmcnt(8)\n\t"
            "v_lshl_or_b32 v220, v161, 16, v160\n\t"
            "v_lshl_or_b32 v221, v163, 16, v162\n\t"
            "v_lshl_or_b32 v222, v169, 16, v168\n\t"
            "v_lshl_or_b32 v223, v171, 16, v170\n\t"
            "s_waitcnt lgkmcnt(0)\n\t"
            "v_lshl_or_b32 v224, v177, 16, v176\n\t"
            "v_lshl_or_b32 v225, v179, 16, v178\n\t"
            "v_lshl_or_b32 v226, v185, 16, v184\n\t"
            "v_lshl_or_b32 v227, v187, 16, v186\n\t"
            "s_nop 0\n\t"
            "v_permlane32_swap_b32 v220, v222\n\t"
            "v_permlane32_swap_b32 v221, v223\n\t"
            "v_permlane32_swap_b32 v224, v226\n\t"
            "v_permlane32_swap_b32 v225, v227\n\t"
            "s_mov_b64 s[88:89], %[stb]\n\t"
            "s_add_u32 s88, s88, 1835008\n\t"
            "s_addc_u32 s89, s89, 0\n\t"
            "global_store_dwordx4 %[wo], v[220:223], s[88:89] offset:0 sc1\n\t"
            "global_store_dwordx4 %[wo], v[224:227], s[88:89] offset:512 sc1\n\t"
            "s_waitcnt vmcnt(14)\n\t"
            "s_barrier\n\t"
            ".Lwp38:\n\t"
            "s_cbranch_execz .Lwp39\n\t"
            "s_lshl_b32 s76, s50, 2\n\t"
            ".Lzc9:\n\t"
            "v_cmp_eq_u32 vcc, v192, %[nvl]\n\t"
            "s_lshr_b64 s[74:75], vcc, s76\n\t"
            "s_and_b32 s74, s74, 15\n\t"
            "s_cmp_eq_u32 s74, 15\n\t"
            "s_cbranch_scc1 .Lzd9\n\t"
            "s_cmp_eq_u32 s73, 0\n\t"
            "s_cbranch_scc1 .Lzd9\n\t"
            "s_sub_u32 s73, s73, 1\n\t"
            "global_load_dword v192, %[po], %[ws] sc1\n\t"
            "s_waitcnt vmcnt(0)\n\t"
            "s_branch .Lzc9\n\t"
            ".Lzd9:\n\t"
            "s_add_u32 m0, %[ldsw], 16384\n\t"
            "s_mul_i32 s76, s50, 458752\n\t"
            "s_add_u32 s70, s84, s76\n\t"
            "s_addc_u32 s71, s85, 0\n\t"
            "global_load_lds_dwordx4 %[wv], s[70:71] offset:0 sc1\n\t"
            "global_load_lds_dwordx4 %[wv], s[70:71] offset:1024 sc1\n\t"
            "global_load_lds_dwordx4 %[wv], s[70:71] offset:2048 sc1\n\t"
            "global_load_lds_dwordx4 %[wv], s[70:71] offset:3072 sc1\n\t"
            "s_add_u32 m0, %[ldsx], 32768\n\t"
            "s_lshl_b32 s76, s50, 7\n\t"
            "s_add_u32 s68, %[xpl], s76\n\t"
            "s_addc_u32 s69, %[xph], 0\n\t"
            "global_load_lds_dwordx4 %[x0], s[68:69] offset:0\n\t"
            "global_load_lds_dwordx4 %[x1], s[68:69] offset:1024\n\t"
            "global_load_lds_dwordx4 %[x2], s[68:69] offset:2048\n\t"
            "global_load_lds_dwordx4 %[x3], s[68:69] offset:3072\n\t"
            "s_add_u32 m0, %[ldsx], 36864\n\t"
            "s_lshl_b32 s76, s50, 7\n\t"
            "s_add_u32 s76, s76, 65536\n\t"
            "s_add_u32 s68, %[xpl], s76\n\t"
            "s_addc_u32 s69, %[xph], 0\n\t"
            "global_load_lds_dwordx4 %[x0], s[68:69] offset:0\n\t"
            "global_load_lds_dwordx4 %[x1], s[68:69] offset:1024\n\t"
            "global_load_lds_dwordx4 %[x2], s[68:69] offset:2048\n\t"
            "global_load_lds_dwordx4 %[x3], s[68:69] offset:3072\n\t"
            "s_waitcnt vmcnt(12)\n\t"
            "s_mov_b64 exec, 3\n\t"
            "global_store_dword %[fob], %[nvl], %[ws] offset:64 sc1\n\t"
            "s_mov_b64 exec, -1\n\t"
            "s_barrier\n\t"
            "s_lshl_b32 s76, s51, 2\n\t"
            ".Lzc10:\n\t"
            "v_cmp_eq_u32 vcc, v192, %[nvl]\n\t"
            "s_lshr_b64 s[74:75], vcc, s76\n\t"
            "s_and_b32 s74, s74, 15\n\t"
            "s_cmp_eq_u32 s74, 15\n\t"
            "s_cbranch_scc1 .Lzd10\n\t"
            "s_cmp_eq_u32 s73, 0\n\t"
            "s_cbranch_scc1 .Lzd10\n\t"
            "s_sub_u32 s73, s73, 1\n\t"
            "global_load_dword v192, %[po], %[ws] sc1\n\t"
            "s_waitcnt vmcnt(0)\n\t"
            "s_branch .Lzc10\n\t"
            ".Lzd10:\n\t"
            "s_add_u32 m0, %[ldsw], 32768\n\t"
            "s_mul_i32 s76, s51, 458752\n\t"
            "s_add_u32 s70, s84, s76\n\t"
            "s_addc_u32 s71, s85, 0\n\t"
            "global_load_lds_dwordx4 %[wv], s[70:71] offset:0 sc1\n\t"
            "global_load_lds_dwordx4 %[wv], s[70:71] offset:1024 sc1\n\t"
            "global_load_lds_dwordx4 %[wv], s[70:71] offset:2048 sc1\n\t"
            "global_load_lds_dwordx4 %[wv], s[70:71] offset:3072 sc1\n\t"
            "s_add_u32 m0, %[ldsx], 65536\n\t"
            "s_lshl_b32 s76, s51, 7\n\t"
            "s_add_u32 s68, %[xpl], s76\n\t"
            "s_addc_u32 s69, %[xph], 0\n\t"
            "global_load_lds_dwordx4 %[x0], s[68:69] offset:0\n\t"
            "global_load_lds_dwordx4 %[x1], s[68:69] offset:1024\n\t"
            "global_load_lds_dwordx4 %[x2], s[68:69] offset:2048\n\t"
            "global_load_lds_dwordx4 %[x3], s[68:69] offset:3072\n\t"
            "s_add_u32 m0, %[ldsx], 69632\n\t"
            "s_lshl_b32 s76, s51, 7\n\t"
            "s_add_u32 s76, s76, 65536\n\t"
            "s_add_u32 s68, %[xpl], s76\n\t"
            "s_addc_u32 s69, %[xph], 0\n\t"
            "global_load_lds_dwordx4 %[x0], s[68:69] offset:0\n\t"
            "global_load_lds_dwordx4 %[x1], s[68:69] offset:1024\n\t"
            "global_load_lds_dwordx4 %[x2], s[68:69] offset:2048\n\t"
            "global_load_lds_dwordx4 %[x3], s[68:69] offset:3072\n\t"
            "s_waitcnt vmcnt(13)\n\t"
            "s_barrier\n\t"
            ".Lwp39:\n\t"
            "s_cbranch_execz .Lwp40\n\t"
            "global_load_dword v192, %[po], %[ws] sc1\n\t"
            "s_add_u32 m0, %[ldsx], 0\n\t"
            "s_lshl_b32 s76, s52, 7\n\t"
            "s_add_u32 s68, %[xpl], s76\n\t"
            "s_addc_u32 s69, %[xph], 0\n\t"
            "global_load_lds_dwordx4 %[x0], s[68:69] offset:0\n\t"
            "global_load_lds_dwordx4 %[x1], s[68:69] offset:1024\n\t"
            "global_load_lds_dwordx4 %[x2], s[68:69] offset:2048\n\t"
            "global_load_lds_dwordx4 %[x3], s[68:69] offset:3072\n\t"
            "s_add_u32 m0, %[ldsx], 4096\n\t"
            "s_lshl_b32 s76, s52, 7\n\t"
            "s_add_u32 s76, s76, 65536\n\t"
            "s_add_u32 s68, %[xpl], s76\n\t"
            "s_addc_u32 s69, %[xph], 0\n\t"
            "global_load_lds_dwordx4 %[x0], s[68:69] offset:0\n\t"
            "global_load_lds_dwordx4 %[x1], s[68:69] offset:1024\n\t"
            "global_load_lds_dwordx4 %[x2], s[68:69] offset:2048\n\t"
            "global_load_lds_dwordx4 %[x3], s[68:69] offset:3072\n\t"
            "s_waitcnt vmcnt(8)\n\t"
            "s_lshl_b32 s76, s52, 2\n\t"
            ".Lzc11:\n\t"
            "v_cmp_eq_u32 vcc, v192, %[nvl]\n\t"
            "s_lshr_b64 s[74:75], vcc, s76\n\t"
            "s_and_b32 s74, s74, 15\n\t"
            "s_cmp_eq_u32 s74, 15\n\t"
            "s_cbranch_scc1 .Lzd11\n\t"
            "s_cmp_eq_u32 s73, 0\n\t"
            "s_cbranch_scc1 .Lzd11\n\t"
            "s_sub_u32 s73, s73, 1\n\t"
            "global_load_dword v192, %[po], %[ws] sc1\n\t"
            "s_waitcnt vmcnt(0)\n\t"
            "s_branch .Lzc11\n\t"
            ".Lzd11:\n\t"
            "s_add_u32 m0, %[ldsw], 0\n\t"
            "s_mul_i32 s76, s52, 458752\n\t"
            "s_add_u32 s70, s84, s76\n\t"
            "s_addc_u32 s71, s85, 0\n\t"
            "global_load_lds_dwordx4 %[wv], s[70:71] offset:0 sc1\n\t"
            "global_load_lds_dwordx4 %[wv], s[70:71] offset:1024 sc1\n\t"
            "global_load_lds_dwordx4 %[wv], s[70:71] offset:2048 sc1\n\t"
            "global_load_lds_dwordx4 %[wv], s[70:71] offset:3072 sc1\n\t"
            "s_barrier\n\t"
            "s_mov_b32 s76, 48\n\t"
            ".Lzc12:\n\t"
            "v_cmp_eq_u32 vcc, v192, %[nvl]\n\t"
            "s_lshr_b64 s[74:75], vcc, s76\n\t"
            "s_and_b32 s74, s74, 65535\n\t"
            "s_cmp_eq_u32 s74, 65535\n\t"
            "s_cbranch_scc1 .Lzd12\n\t"
            "s_cmp_eq_u32 s73, 0\n\t"
            "s_cbranch_scc1 .Lzd12\n\t"
            "s_sub_u32 s73, s73, 1\n\t"
            "global_load_dword v192, %[po], %[ws] sc1\n\t"
            "s_waitcnt vmcnt(0)\n\t"
            "s_branch .Lzc12\n\t"
            ".Lzd12:\n\t"
            "s_add_u32 m0, %[ldsw], 16384\n\t"
            "s_mul_i32 s76, s53, 458752\n\t"
            "s_add_u32 s70, s84, s76\n\t"
            "s_addc_u32 s71, s85, 0\n\t"
            "s_mul_i32 s76, s54, 458752\n\t"
            "s_sub_u32 s76, s76, 1024\n\t"
            "s_add_u32 s86, s84, s76\n\t"
            "s_addc_u32 s87, s85, 0\n\t"
            "global_load_lds_dwordx4 %[wv], s[70:71] offset:0 sc1\n\t"
            "global_load_lds_dwordx4 %[wv], s[86:87] offset:1024 sc1\n\t"
            "global_load_lds_dwordx4 %[wv], s[70:71] offset:2048 sc1\n\t"
            "global_load_lds_dwordx4 %[wv], s[86:87] offset:3072 sc1\n\t"
            "s_add_u32 m0, %[ldsx], 32768\n\t"
            "s_lshl_b32 s76, s53, 7\n\t"
            "s_add_u32 s68, %[xpl], s76\n\t"
            "s_addc_u32 s69, %[xph], 0\n\t"
            "global_load_lds_dwordx4 %[x0], s[68:69] offset:0\n\t"
            "global_load_lds_dwordx4 %[x1], s[68:69] offset:1024\n\t"
            "global_load_lds_dwordx4 %[x2], s[68:69] offset:2048\n\t"
            "global_load_lds_dwordx4 %[x3], s[68:69] offset:3072\n\t"
            "s_add_u32 m0, %[ldsx], 36864\n\t"
            "s_lshl_b32 s76, s53, 7\n\t"
            "s_add_u32 s76, s76, 65536\n\t"
            "s_add_u32 s68, %[xpl], s76\n\t"
            "s_addc_u32 s69, %[xph], 0\n\t"
            "global_load_lds_dwordx4 %[x0], s[68:69] offset:0\n\t"
            "global_load_lds_dwordx4 %[x1], s[68:69] offset:1024\n\t"
            "global_load_lds_dwordx4 %[x2], s[68:69] offset:2048\n\t"
            "global_load_lds_dwordx4 %[x3], s[68:69] offset:3072\n\t"
            "s_waitcnt vmcnt(12)\n\t"
            "s_barrier\n\t"
            ".Lwp40:\n\t"
            "s_cbranch_execz .Lwp97\n\t"
            "s_add_u32 m0, %[ldsx], 65536\n\t"
            "s_lshl_b32 s76, s54, 7\n\t"
            "s_add_u32 s68, %[xpl], s76\n\t"
            "s_addc_u32 s69, %[xph], 0\n\t"
            "global_load_lds_dwordx4 %[x0], s[68:69] offset:0\n\t"
            "global_load_lds_dwordx4 %[x1], s[68:69] offset:1024\n\t"
            "global_load_lds_dwordx4 %[x2], s[68:69] offset:2048\n\t"
            "global_load_lds_dwordx4 %[x3], s[68:69] offset:3072\n\t"
            "s_add_u32 m0, %[ldsx], 69632\n\t"
            "s_lshl_b32 s76, s54, 7\n\t"
            "s_add_u32 s76, s76, 65536\n\t"
            "s_add_u32 s68, %[xpl], s76\n\t"
            "s_addc_u32 s69, %[xph], 0\n\t"
            "global_load_lds_dwordx4 %[x0], s[68:69] offset:0\n\t"
            "global_load_lds_dwordx4 %[x1], s[68:69] offset:1024\n\t"
            "global_load_lds_dwordx4 %[x2], s[68:69] offset:2048\n\t"
            "global_load_lds_dwordx4 %[x3], s[68:69] offset:3072\n\t"
            "s_add_u32 m0, %[ldsw], 32768\n\t"
            "s_mul_i32 s76, s55, 458752\n\t"
            "s_add_u32 s70, s84, s76\n\t"
            "s_addc_u32 s71, s85, 0\n\t"
            "s_mul_i32 s76, s53, 458752\n\t"
            "s_add_u32 s86, s84, s76\n\t"
            "s_addc_u32 s87, s85, 0\n\t"
            "global_load_lds_dwordx4 %[wv], s[70:71] offset:0 sc1\n\t"
            "global_load_lds_dwordx4 %[wv], s[86:87] offset:1024 sc1\n\t"
            "global_load_lds_dwordx4 %[wv], s[70:71] offset:2048 sc1\n\t"
            "global_load_lds_dwordx4 %[wv], s[86:87] offset:3072 sc1\n\t"
            "s_waitcnt vmcnt(4)\n\t"
            "s_barrier\n\t"
            "s_add_u32 m0, %[ldsx], 0\n\t"
            "s_lshl_b32 s76, s55, 7\n\t"
            "s_add_u32 s68, %[xpl], s76\n\t"
            "s_addc_u32 s69, %[xph], 0\n\t"
            "global_load_lds_dwordx4 %[x0], s[68:69] offset:0\n\t"
            "global_load_lds_dwordx4 %[x1], s[68:69] offset:1024\n\t"
            "global_load_lds_dwordx4 %[x2], s[68:69] offset:2048\n\t"
            "global_load_lds_dwordx4 %[x3], s[68:69] offset:3072\n\t"
            "s_add_u32 m0, %[ldsx], 4096\n\t"
            "s_lshl_b32 s76, s55, 7\n\t"
            "s_add_u32 s76, s76, 65536\n\t"
            "s_add_u32 s68, %[xpl], s76\n\t"
            "s_addc_u32 s69, %[xph], 0\n\t"
            "global_load_lds_dwordx4 %[x0], s[68:69] offset:0\n\t"
            "global_load_lds_dwordx4 %[x1], s[68:69] offset:1024\n\t"
            "global_load_lds_dwordx4 %[x2], s[68:69] offset:2048\n\t"
            "global_load_lds_dwordx4 %[x3], s[68:69] offset:3072\n\t"
            "s_add_u32 m0, %[ldsw], 0\n\t"
            "s_mul_i32 s76, s54, 458752\n\t"
            "s_add_u32 s76, s76, 1024\n\t"
            "s_add_u32 s70, s84, s76\n\t"
            "s_addc_u32 s71, s85, 0\n\t"
            "s_mul_i32 s76, s55, 458752\n\t"
            "s_add_u32 s86, s84, s76\n\t"
            "s_addc_u32 s87, s85, 0\n\t"
            "global_load_lds_dwordx4 %[wv], s[70:71] offset:0 sc1\n\t"
            "global_load_lds_dwordx4 %[wv], s[86:87] offset:1024 sc1\n\t"
            "global_load_lds_dwordx4 %[wv], s[70:71] offset:2048 sc1\n\t"
            "global_load_lds_dwordx4 %[wv], s[86:87] offset:3072 sc1\n\t"
            "s_waitcnt vmcnt(4)\n\t"
            "s_barrier\n\t"
            ".Lwp41:\n\t"
            "s_cbranch_execz .Lwp14\n\t"
            "s_mov_b32 s76, 16\n\t"
            ".Lzc13:\n\t"
            "v_cmp_eq_u32 vcc, v192, %[nvl]\n\t"
            "s_lshr_b64 s[74:75], vcc, s76\n\t"
            "s_and_b32 s74, s74, 65535\n\t"
            "s_cmp_eq_u32 s74, 65535\n\t"
            "s_cbranch_scc1 .Lzd13\n\t"
            "s_cmp_eq_u32 s73, 0\n\t"
            "s_cbranch_scc1 .Lzd13\n\t"
            "s_sub_u32 s73, s73, 1\n\t"
            "global_load_dword v192, %[po], %[ws] sc1\n\t"
            "s_waitcnt vmcnt(0)\n\t"
            "s_branch .Lzc13\n\t"
            ".Lzd13:\n\t"
            "s_waitcnt vmcnt(0)\n\t"
            "s_barrier\n\t"
            : [nvl] "+v"(nvl), [qo] "+v"(qoff[0]), [wo] "+v"(wo)
            : [x0] "v"(xoff[0]), [x1] "v"(xoff[1]), [x2] "v"(xoff[2]), [x3] "v"(xoff[3]), [wv] "v"(wv), [po] "v"(po),
              [lutl] "v"(lutl), [fo0] "v"(fo0), [fob] "v"(fob), [bd] "v"(bdst),
              [xpl] "s"((u32)KPTR(0)), [xph] "s"((u32)(KPTR(0) >> 32)), [ws] "s"(KPTR(4)),
              [qbg] "s"(qbg), [qa1] "s"(qa1), [tseq] "s"(tseq), [p] "s"(p), [h2] "s"(h2), [ldsx] "s"(ldsx), [ldsw] "s"(ldsw), [st1] "s"(st1), [stb] "s"(stb)
            : "vcc", "scc", "memory", "s40", "s41", "s42", "s43", "s44", "s45", "s46", "s47", "s48", "s49", "s50", "s51", "s52", "s53", "s54", "s55", "s56", "s57", "s58", "s59", "s60", "s61", "s62", "s63", "s64", "s65", "s66", "s67", "s68", "s69", "s70", "s71", "s72", "s73", "s74", "s75", "s76", "s77", "s78", "s79", "s80", "s81", "s82", "s83", "s84", "s85", "s86", "s87", "s88", "s89", "v128", "v129", "v130", "v131", "v132", "v133", "v134", "v135", "v136", "v137", "v138", "v139", "v140", "v141", "v142", "v143", "v144", "v145", "v146", "v147", "v148", "v149", "v150", "v151", "v152", "v153", "v154", "v155", "v156", "v157", "v158", "v159", "v160", "v161", "v162", "v163", "v164", "v165", "v166", "v167", "v168", "v169", "v170", "v171", "v172", "v173", "v174", "v175", "v176", "v177", "v178", "v179", "v180", "v181", "v182", "v183", "v184", "v185", "v186", "v187", "v188", "v189", "v190", "v191", "v192", "v193", "v194", "v195", "v196", "v197", "v198", "v199", "v200", "v201", "v202", "v203", "v204", "v205", "v206", "v207", "v208", "v209", "v210", "v211", "v212", "v213", "v214", "v215", "v216", "v217", "v218", "v219", "v220", "v221", "v222", "v223", "v224", "v225", "v226", "v227", "v228", "v229", "v230", "v231", "v232", "v233", "v234", "v235");
        if (mt == 0 && p == 0 && lane < 2) asm volatile("global_store_dword %0, %1, %2 sc1" :: "v"(no), "v"(nvl), "s"(KPTR(4)) : "memory");
        return;
    }

    u32 oo[4];
    unsigned long ob[4];

    int xb0 = 0;
    int xb1 = 0;
    int ba;
#define LDX(a) (*(const bf16x8*)(smem + (a)))
#define MP(i, j, x0, w0, x1, w1) do { asm volatile("v_mfma_f32_16x16x32_bf16 %0, %1, %2, %0\n\tv_mfma_f32_16x16x32_bf16 %0, %3, %4, %0" \
                                      : "+v"(acc[i][j]) : "v"(x0), "v"(w0), "v"(x1), "v"(w1)); SB(); } while (0)
#define LD(d, a) do { d = LDX(a); SB(); } while (0)
#define CBAR() do { asm volatile("s_waitcnt lgkmcnt(0)" ::: "memory"); BAR(); asm volatile("" ::: "memory"); } while (0)

    {
        u32 V[4][8];
        i32x4 Q0[8];
#define PK(b_, j, n) asm volatile("s_waitcnt lgkmcnt(" #n ")\n\t" \
                                    "v_lshl_or_b32 %0, %5, 16, %4\n\tv_lshl_or_b32 %1, %7, 16, %6\n\t" \
                                    "v_lshl_or_b32 %2, %9, 16, %8\n\tv_lshl_or_b32 %3, %11, 16, %10" \
                                    : "=&v"(b_[0]), "=&v"(b_[1]), "=&v"(b_[2]), "=&v"(b_[3]) \
                                    : "v"(V[j][0]), "v"(V[j][1]), "v"(V[j][2]), "v"(V[j][3]), "v"(V[j][4]), "v"(V[j][5]), "v"(V[j][6]), "v"(V[j][7]) : "memory")
#define WR(b_, j) asm volatile("ds_write_b128 %0, %1 offset:%2" :: "v"(bdst), "v"(b_), "i"((j) * 256) : "memory")
        {
            const u32 l = tid & 31, h = (tid >> 5) & 1;
            const u32 lo = l * 8 + h * 4, qv = h * (8 * N_ * 4) + l * 16;
            const u32 ls = (u32)((w * 4 * N_ + col0) * 2), qs = (u32)(((OWN(mt, nt) * 64 + w * 16) * N_ + col0) * 4);
            const u32 ld = (u32)(size_t)(smem) + OFF_L + w * 1024;
            asm volatile("s_mov_b32 m0, %[ld]\n\t"
                         "s_waitcnt lgkmcnt(0)\n\t"
                         "s_mov_b64 s[60:61], %[lp]\n\ts_add_u32 s60, s60, %[ls]\n\ts_addc_u32 s61, s61, 0\n\t"
                         "s_mov_b64 s[62:63], %[qp]\n\ts_add_u32 s62, s62, %[qs]\n\ts_addc_u32 s63, s63, 0\n\t"
                         "global_load_lds_dword %[lo], s[60:61] offset:0\n\t"
                         "s_add_u32 s60, s60, 6912\n\ts_addc_u32 s61, s61, 0\n\t"
                         "global_load_lds_dword %[lo], s[60:61] offset:256\n\t"
                         "s_add_u32 s60, s60, 6912\n\ts_addc_u32 s61, s61, 0\n\t"
                         "global_load_lds_dword %[lo], s[60:61] offset:512\n\t"
                         "s_add_u32 s60, s60, 6912\n\ts_addc_u32 s61, s61, 0\n\t"
                         "global_load_lds_dword %[lo], s[60:61] offset:768\n\t"
                         "global_load_dwordx4 %[q0], %[qv], s[62:63]\n\t"
                         "s_add_u32 s62, s62, 14336\n\ts_addc_u32 s63, s63, 0\n\t"
                         "global_load_dwordx4 %[q1], %[qv], s[62:63]\n\t"
                         "s_add_u32 s62, s62, 14336\n\ts_addc_u32 s63, s63, 0\n\t"
                         "global_load_dwordx4 %[q2], %[qv], s[62:63]\n\t"
                         "s_add_u32 s62, s62, 14336\n\ts_addc_u32 s63, s63, 0\n\t"
                         "global_load_dwordx4 %[q3], %[qv], s[62:63]\n\t"
                         "s_add_u32 s62, s62, 14336\n\ts_addc_u32 s63, s63, 0\n\t"
                         "global_load_dwordx4 %[q4], %[qv], s[62:63]\n\t"
                         "s_add_u32 s62, s62, 14336\n\ts_addc_u32 s63, s63, 0\n\t"
                         "global_load_dwordx4 %[q5], %[qv], s[62:63]\n\t"
                         "s_add_u32 s62, s62, 14336\n\ts_addc_u32 s63, s63, 0\n\t"
                         "global_load_dwordx4 %[q6], %[qv], s[62:63]\n\t"
                         "s_add_u32 s62, s62, 14336\n\ts_addc_u32 s63, s63, 0\n\t"
                         "global_load_dwordx4 %[q7], %[qv], s[62:63]\n\t"
                         : [q0] "=&v"(Q0[0]), [q1] "=&v"(Q0[1]), [q2] "=&v"(Q0[2]), [q3] "=&v"(Q0[3]),
                           [q4] "=&v"(Q0[4]), [q5] "=&v"(Q0[5]), [q6] "=&v"(Q0[6]), [q7] "=&v"(Q0[7])
                         : [lo] "v"(lo), [qv] "v"(qv), [ls] "s"(ls), [qs] "s"(qs), [ld] "s"(ld), [lp] "s"(ka[2]), [qp] "s"(ka[1])
                         : "s60", "s61", "s62", "s63", "scc", "memory");
            SB();
        }
        KWAIT();
        int ln = lane;
        asm volatile("" : "+v"(ln));
        const int c = ln & 15, g = ln >> 4, l = ln & 31, h = ln >> 5;
        xb0 = (w * 64 + c) * 128 + ((g ^ (c >> 1)) * 16);
        xb1 = xb0 ^ 64;
        ba = OFF_B + g * 2048 + c * 16;
        u32 lutl = (u32)(size_t)(smem) + OFF_L + l * 4;
        u32 bdst = (u32)(size_t)(smem) + OFF_B + w * 4096 + h * 2048 + ((l >> 4) * 64 + (l & 15)) * 16;
#pragma unroll
        for (int r = 0; r < 4; ++r) {
            oo[r] = (u32)(((row0 + w * 64 + g * 4 + r) * N_ + col0 + c * 4) * 2);
            ob[r] = KPTR(3) + (unsigned long)(r * 16 * N_ * 2);
            asm volatile("" : "+v"(oo[r]), "+s"(ob[r]));
        }
        asm volatile("" : "+v"(xb0), "+v"(xb1), "+v"(ba));
        asm volatile("" : "+v"(bdst), "+v"(lutl));
        {
            unsigned long ex_;
            asm volatile("s_mov_b64 %0, exec\n\ts_mov_b64 exec, 0\n\t"
                         "s_cmp_eq_u32 %1, 0\n\ts_cbranch_scc1 .Lwp20\n\t"
                         "s_cmp_eq_u32 %1, 1\n\ts_cbranch_scc1 .Lwp10\n\t"
                         "s_cmp_eq_u32 %1, 2\n\ts_cbranch_scc1 .Lwp12\n\t"
                         "s_branch .Lwp99\n\t"
                         ".Lwp99:\n\ts_mov_b64 exec, %0" : "=&s"(ex_) : "s"(w) : "scc", "memory");
        }
        asm volatile("s_waitcnt vmcnt(8)" ::: "memory");
        BAR();
        PAD(20, 10);
        asm volatile("s_waitcnt vmcnt(7)" : "+v"(Q0[0]) :: "memory"); LUT1(Q0, 0, 0); LUT1(Q0, 1, 0); LUT1(Q0, 2, 0); LUT1(Q0, 3, 0); SB();
        asm volatile("s_waitcnt vmcnt(6)" : "+v"(Q0[1]) :: "memory"); LUT1(Q0, 0, 1); LUT1(Q0, 1, 1); LUT1(Q0, 2, 1); LUT1(Q0, 3, 1); SB();
        asm volatile("s_waitcnt vmcnt(5)" : "+v"(Q0[2]) :: "memory"); LUT1(Q0, 0, 2); LUT1(Q0, 1, 2); LUT1(Q0, 2, 2); LUT1(Q0, 3, 2); SB();
        asm volatile("s_waitcnt vmcnt(4)" : "+v"(Q0[3]) :: "memory"); LUT1(Q0, 0, 3); LUT1(Q0, 1, 3); LUT1(Q0, 2, 3); LUT1(Q0, 3, 3); SB();
        asm volatile("s_waitcnt vmcnt(3)" : "+v"(Q0[4]) :: "memory"); LUT1(Q0, 0, 4); LUT1(Q0, 1, 4); LUT1(Q0, 2, 4); LUT1(Q0, 3, 4); SB();
        asm volatile("s_waitcnt vmcnt(2)" : "+v"(Q0[5]) :: "memory"); LUT1(Q0, 0, 5); LUT1(Q0, 1, 5); LUT1(Q0, 2, 5); LUT1(Q0, 3, 5); SB();
        asm volatile("s_waitcnt vmcnt(1)" : "+v"(Q0[6]) :: "memory"); LUT1(Q0, 0, 6); LUT1(Q0, 1, 6); LUT1(Q0, 2, 6); LUT1(Q0, 3, 6); SB();
        asm volatile("s_waitcnt vmcnt(0)" : "+v"(Q0[7]) :: "memory"); LUT1(Q0, 0, 7); LUT1(Q0, 1, 7); LUT1(Q0, 2, 7); LUT1(Q0, 3, 7); SB();
        {   u32x4 b0, b1, b2, b3;
            PK(b0, 0, 3); WR(b0, 0); PK(b1, 1, 3); WR(b1, 1); PK(b2, 2, 3); WR(b2, 2); PK(b3, 3, 3); WR(b3, 3);
            SB();
        }
        asm volatile("s_waitcnt lgkmcnt(0)" ::: "memory");
        BAR();
    }
    asm volatile(
        "v_mov_b32 v232, %[xb0]\n\t"
        "v_mov_b32 v233, %[xb1]\n\t"
        "v_mov_b32 v234, %[ba]\n\t"
        ".Lwp10:\n\t"
        "s_cbranch_execz .Lwp99\n\t"
        "ds_read_b128 v[192:195], v234 offset:0\n\t"
        "ds_read_b128 v[128:131], v232 offset:0\n\t"
        "ds_read_b128 v[196:199], v234 offset:8192\n\t"
        "ds_read_b128 v[144:147], v233 offset:0\n\t"
        "ds_read_b128 v[204:207], v234 offset:8448\n\t"
        "ds_read_b128 v[200:203], v234 offset:256\n\t"
        "ds_read_b128 v[132:135], v232 offset:2048\n\t"
        "ds_read_b128 v[148:151], v233 offset:2048\n\t"
        "s_waitcnt lgkmcnt(6)\n\t"
        "v_mfma_f32_16x16x32_bf16 v[0:3], v[128:131], v[192:195], 0\n\t"
        "s_waitcnt lgkmcnt(4)\n\t"
        "v_mfma_f32_16x16x32_bf16 v[0:3], v[144:147], v[196:199], v[0:3]\n\t"
        "ds_read_b128 v[136:139], v232 offset:4096\n\t"
        "s_waitcnt lgkmcnt(4)\n\t"
        "v_mfma_f32_16x16x32_bf16 v[4:7], v[144:147], v[204:207], 0\n\t"
        "s_waitcnt lgkmcnt(3)\n\t"
        "v_mfma_f32_16x16x32_bf16 v[4:7], v[128:131], v[200:203], v[4:7]\n\t"
        "ds_read_b128 v[152:155], v233 offset:4096\n\t"
        "s_waitcnt lgkmcnt(3)\n\t"
        "v_mfma_f32_16x16x32_bf16 v[36:39], v[132:135], v[200:203], 0\n\t"
        "s_waitcnt lgkmcnt(2)\n\t"
        "v_mfma_f32_16x16x32_bf16 v[36:39], v[148:151], v[204:207], v[36:39]\n\t"
        "ds_read_b128 v[140:143], v232 offset:6144\n\t"
        "v_mfma_f32_16x16x32_bf16 v[32:35], v[148:151], v[196:199], 0\n\t"
        "v_mfma_f32_16x16x32_bf16 v[32:35], v[132:135], v[192:195], v[32:35]\n\t"
        "ds_read_b128 v[156:159], v233 offset:6144\n\t"
        "s_waitcnt lgkmcnt(3)\n\t"
        "v_mfma_f32_16x16x32_bf16 v[64:67], v[136:139], v[192:195], 0\n\t"
        "s_waitcnt lgkmcnt(2)\n\t"
        "v_mfma_f32_16x16x32_bf16 v[64:67], v[152:155], v[196:199], v[64:67]\n\t"
        "ds_read_b128 v[208:211], v234 offset:512\n\t"
        "v_mfma_f32_16x16x32_bf16 v[68:71], v[152:155], v[204:207], 0\n\t"
        "v_mfma_f32_16x16x32_bf16 v[68:71], v[136:139], v[200:203], v[68:71]\n\t"
        "ds_read_b128 v[212:215], v234 offset:8704\n\t"
        "s_waitcnt lgkmcnt(3)\n\t"
        "v_mfma_f32_16x16x32_bf16 v[100:103], v[140:143], v[200:203], 0\n\t"
        "s_waitcnt lgkmcnt(2)\n\t"
        "v_mfma_f32_16x16x32_bf16 v[100:103], v[156:159], v[204:207], v[100:103]\n\t"
        "ds_read_b128 v[220:223], v234 offset:8960\n\t"
        "v_mfma_f32_16x16x32_bf16 v[96:99], v[156:159], v[196:199], 0\n\t"
        "v_mfma_f32_16x16x32_bf16 v[96:99], v[140:143], v[192:195], v[96:99]\n\t"
        "ds_read_b128 v[216:219], v234 offset:768\n\t"
        "s_waitcnt lgkmcnt(3)\n\t"
        "v_mfma_f32_16x16x32_bf16 v[8:11], v[128:131], v[208:211], 0\n\t"
        "s_waitcnt lgkmcnt(2)\n\t"
        "v_mfma_f32_16x16x32_bf16 v[8:11], v[144:147], v[212:215], v[8:11]\n\t"
        "ds_read_b128 v[192:195], v234 offset:1024\n\t"
        "s_waitcnt lgkmcnt(2)\n\t"
        "v_mfma_f32_16x16x32_bf16 v[12:15], v[144:147], v[220:223], 0\n\t"
        "s_waitcnt lgkmcnt(1)\n\t"
        "v_mfma_f32_16x16x32_bf16 v[12:15], v[128:131], v[216:219], v[12:15]\n\t"
        "ds_read_b128 v[196:199], v234 offset:9216\n\t"
        "v_mfma_f32_16x16x32_bf16 v[44:47], v[132:135], v[216:219], 0\n\t"
        "v_mfma_f32_16x16x32_bf16 v[44:47], v[148:151], v[220:223], v[44:47]\n\t"
        "ds_read_b128 v[204:207], v234 offset:9472\n\t"
        "v_mfma_f32_16x16x32_bf16 v[40:43], v[148:151], v[212:215], 0\n\t"
        "v_mfma_f32_16x16x32_bf16 v[40:43], v[132:135], v[208:211], v[40:43]\n\t"
        "ds_read_b128 v[200:203], v234 offset:1280\n\t"
        "v_mfma_f32_16x16x32_bf16 v[72:75], v[136:139], v[208:211], 0\n\t"
        "v_mfma_f32_16x16x32_bf16 v[72:75], v[152:155], v[212:215], v[72:75]\n\t"
        "v_mfma_f32_16x16x32_bf16 v[76:79], v[152:155], v[220:223], 0\n\t"
        "v_mfma_f32_16x16x32_bf16 v[76:79], v[136:139], v[216:219], v[76:79]\n\t"
        "v_mfma_f32_16x16x32_bf16 v[108:111], v[140:143], v[216:219], 0\n\t"
        "v_mfma_f32_16x16x32_bf16 v[108:111], v[156:159], v[220:223], v[108:111]\n\t"
        "v_mfma_f32_16x16x32_bf16 v[104:107], v[156:159], v[212:215], 0\n\t"
        "v_mfma_f32_16x16x32_bf16 v[104:107], v[140:143], v[208:211], v[104:107]\n\t"
        "s_waitcnt lgkmcnt(0)\n\t"
        "v_mfma_f32_16x16x32_bf16 v[16:19], v[128:131], v[192:195], 0\n\t"
        "v_mfma_f32_16x16x32_bf16 v[16:19], v[144:147], v[196:199], v[16:19]\n\t"
        "ds_read_b128 v[208:211], v234 offset:1536\n\t"
        "v_mfma_f32_16x16x32_bf16 v[20:23], v[144:147], v[204:207], 0\n\t"
        "v_mfma_f32_16x16x32_bf16 v[20:23], v[128:131], v[200:203], v[20:23]\n\t"
        "ds_read_b128 v[212:215], v234 offset:9728\n\t"
        "v_mfma_f32_16x16x32_bf16 v[52:55], v[132:135], v[200:203], 0\n\t"
        "v_mfma_f32_16x16x32_bf16 v[52:55], v[148:151], v[204:207], v[52:55]\n\t"
        "ds_read_b128 v[220:223], v234 offset:9984\n\t"
        "v_mfma_f32_16x16x32_bf16 v[48:51], v[148:151], v[196:199], 0\n\t"
        "v_mfma_f32_16x16x32_bf16 v[48:51], v[132:135], v[192:195], v[48:51]\n\t"
        "ds_read_b128 v[216:219], v234 offset:1792\n\t"
        "v_mfma_f32_16x16x32_bf16 v[80:83], v[136:139], v[192:195], 0\n\t"
        "v_mfma_f32_16x16x32_bf16 v[80:83], v[152:155], v[196:199], v[80:83]\n\t"
        "v_mfma_f32_16x16x32_bf16 v[84:87], v[152:155], v[204:207], 0\n\t"
        "v_mfma_f32_16x16x32_bf16 v[84:87], v[136:139], v[200:203], v[84:87]\n\t"
        "s_mov_b32 s60, 32768\n\t"
        "s_mov_b32 s62, 5\n\t"
        "v_add_u32 v232, 32768, %[xb0]\n\t"
        "v_add_u32 v233, 32768, %[xb1]\n\t"
        "v_add_u32 v234, 16384, %[ba]\n\t"
        "v_mov_b64 v[24:25], 0\n\t"
        "v_mov_b64 v[26:27], 0\n\t"
        "v_mov_b64 v[28:29], 0\n\t"
        "v_mov_b64 v[30:31], 0\n\t"
        "v_mov_b64 v[56:57], 0\n\t"
        "v_mov_b64 v[58:59], 0\n\t"
        "v_mov_b64 v[60:61], 0\n\t"
        "v_mov_b64 v[62:63], 0\n\t"
        "v_mov_b64 v[88:89], 0\n\t"
        "v_mov_b64 v[90:91], 0\n\t"
        "v_mov_b64 v[92:93], 0\n\t"
        "v_mov_b64 v[94:95], 0\n\t"
        "v_mov_b64 v[120:121], 0\n\t"
        "v_mov_b64 v[122:123], 0\n\t"
        "v_mov_b64 v[124:125], 0\n\t"
        "v_mov_b64 v[126:127], 0\n\t"
        "v_mov_b64 v[116:117], 0\n\t"
        "v_mov_b64 v[118:119], 0\n\t"
        "v_mov_b64 v[112:113], 0\n\t"
        "v_mov_b64 v[114:115], 0\n\t"
        "s_nop 1\n\t"
        "s_mov_b64 exec, 0\n\t"
        "s_cmp_eq_u32 %[w], 0\n\t"
        "s_cbranch_scc1 .Lwp34\n\t"
        "s_cmp_eq_u32 %[w], 1\n\t"
        "s_cbranch_scc1 .Lwp38\n\t"
        "s_cmp_eq_u32 %[w], 2\n\t"
        "s_cbranch_scc1 .Lwp41\n\t"
        "s_branch .Lwp16\n\t"
        ".Lwp97:\n\t"
        "s_mov_b64 exec, -1\n\t"
        ".Lcloop:\n\t"
        "v_mfma_f32_16x16x32_bf16 v[116:119], v[140:143], v[200:203], v[116:119]\n\t"
        "s_sub_u32 s62, s62, 1\n\t"
        "v_mfma_f32_16x16x32_bf16 v[116:119], v[156:159], v[204:207], v[116:119]\n\t"
        "s_waitcnt lgkmcnt(0)\n\t"
        "v_mfma_f32_16x16x32_bf16 v[112:115], v[156:159], v[196:199], v[112:115]\n\t"
        "v_mfma_f32_16x16x32_bf16 v[112:115], v[140:143], v[192:195], v[112:115]\n\t"
        "s_barrier\n\t"
        "v_mfma_f32_16x16x32_bf16 v[24:27], v[128:131], v[208:211], v[24:27]\n\t"
        "v_mfma_f32_16x16x32_bf16 v[24:27], v[144:147], v[212:215], v[24:27]\n\t"
        "ds_read_b128 v[192:195], v234 offset:0\n\t"
        "ds_read_b128 v[160:163], v232 offset:0\n\t"
        "v_mfma_f32_16x16x32_bf16 v[28:31], v[144:147], v[220:223], v[28:31]\n\t"
        "v_mfma_f32_16x16x32_bf16 v[28:31], v[128:131], v[216:219], v[28:31]\n\t"
        "ds_read_b128 v[196:199], v234 offset:8192\n\t"
        "ds_read_b128 v[176:179], v233 offset:0\n\t"
        "v_mfma_f32_16x16x32_bf16 v[60:63], v[132:135], v[216:219], v[60:63]\n\t"
        "v_mfma_f32_16x16x32_bf16 v[60:63], v[148:151], v[220:223], v[60:63]\n\t"
        "ds_read_b128 v[204:207], v234 offset:8448\n\t"
        "v_mfma_f32_16x16x32_bf16 v[56:59], v[148:151], v[212:215], v[56:59]\n\t"
        "v_mfma_f32_16x16x32_bf16 v[56:59], v[132:135], v[208:211], v[56:59]\n\t"
        "ds_read_b128 v[200:203], v234 offset:256\n\t"
        "v_mfma_f32_16x16x32_bf16 v[88:91], v[136:139], v[208:211], v[88:91]\n\t"
        "v_mfma_f32_16x16x32_bf16 v[88:91], v[152:155], v[212:215], v[88:91]\n\t"
        "ds_read_b128 v[164:167], v232 offset:2048\n\t"
        "v_mfma_f32_16x16x32_bf16 v[92:95], v[152:155], v[220:223], v[92:95]\n\t"
        "v_mfma_f32_16x16x32_bf16 v[92:95], v[136:139], v[216:219], v[92:95]\n\t"
        "ds_read_b128 v[180:183], v233 offset:2048\n\t"
        "v_mfma_f32_16x16x32_bf16 v[124:127], v[140:143], v[216:219], v[124:127]\n\t"
        "v_mfma_f32_16x16x32_bf16 v[124:127], v[156:159], v[220:223], v[124:127]\n\t"
        "ds_read_b128 v[168:171], v232 offset:4096\n\t"
        "v_mfma_f32_16x16x32_bf16 v[120:123], v[156:159], v[212:215], v[120:123]\n\t"
        "v_mfma_f32_16x16x32_bf16 v[120:123], v[140:143], v[208:211], v[120:123]\n\t"
        "ds_read_b128 v[184:187], v233 offset:4096\n\t"
        "s_waitcnt lgkmcnt(4)\n\t"
        "v_mfma_f32_16x16x32_bf16 v[0:3], v[160:163], v[192:195], v[0:3]\n\t"
        "v_mfma_f32_16x16x32_bf16 v[0:3], v[176:179], v[196:199], v[0:3]\n\t"
        "ds_read_b128 v[172:175], v232 offset:6144\n\t"
        "ds_read_b128 v[208:211], v234 offset:512\n\t"
        "v_mfma_f32_16x16x32_bf16 v[4:7], v[176:179], v[204:207], v[4:7]\n\t"
        "v_mfma_f32_16x16x32_bf16 v[4:7], v[160:163], v[200:203], v[4:7]\n\t"
        "ds_read_b128 v[188:191], v233 offset:6144\n\t"
        "s_waitcnt lgkmcnt(5)\n\t"
        "v_mfma_f32_16x16x32_bf16 v[36:39], v[164:167], v[200:203], v[36:39]\n\t"
        "ds_read_b128 v[212:215], v234 offset:8704\n\t"
        "v_mfma_f32_16x16x32_bf16 v[36:39], v[180:183], v[204:207], v[36:39]\n\t"
        "ds_read_b128 v[220:223], v234 offset:8960\n\t"
        "v_mfma_f32_16x16x32_bf16 v[32:35], v[180:183], v[196:199], v[32:35]\n\t"
        "v_mfma_f32_16x16x32_bf16 v[32:35], v[164:167], v[192:195], v[32:35]\n\t"
        "ds_read_b128 v[216:219], v234 offset:768\n\t"
        "s_waitcnt lgkmcnt(6)\n\t"
        "v_mfma_f32_16x16x32_bf16 v[64:67], v[168:171], v[192:195], v[64:67]\n\t"
        "v_mfma_f32_16x16x32_bf16 v[64:67], v[184:187], v[196:199], v[64:67]\n\t"
        "v_mfma_f32_16x16x32_bf16 v[68:71], v[184:187], v[204:207], v[68:71]\n\t"
        "v_mfma_f32_16x16x32_bf16 v[68:71], v[168:171], v[200:203], v[68:71]\n\t"
        "s_waitcnt lgkmcnt(3)\n\t"
        "v_mfma_f32_16x16x32_bf16 v[100:103], v[172:175], v[200:203], v[100:103]\n\t"
        "v_mfma_f32_16x16x32_bf16 v[100:103], v[188:191], v[204:207], v[100:103]\n\t"
        "v_mfma_f32_16x16x32_bf16 v[96:99], v[188:191], v[196:199], v[96:99]\n\t"
        "v_mfma_f32_16x16x32_bf16 v[96:99], v[172:175], v[192:195], v[96:99]\n\t"
        "v_mfma_f32_16x16x32_bf16 v[8:11], v[160:163], v[208:211], v[8:11]\n\t"
        "s_waitcnt lgkmcnt(0)\n\t"
        "v_mfma_f32_16x16x32_bf16 v[8:11], v[176:179], v[212:215], v[8:11]\n\t"
        "ds_read_b128 v[192:195], v234 offset:1024\n\t"
        "v_mfma_f32_16x16x32_bf16 v[12:15], v[176:179], v[220:223], v[12:15]\n\t"
        "v_mfma_f32_16x16x32_bf16 v[12:15], v[160:163], v[216:219], v[12:15]\n\t"
        "ds_read_b128 v[196:199], v234 offset:9216\n\t"
        "v_mfma_f32_16x16x32_bf16 v[44:47], v[164:167], v[216:219], v[44:47]\n\t"
        "v_mfma_f32_16x16x32_bf16 v[44:47], v[180:183], v[220:223], v[44:47]\n\t"
        "ds_read_b128 v[204:207], v234 offset:9472\n\t"
        "v_mfma_f32_16x16x32_bf16 v[40:43], v[180:183], v[212:215], v[40:43]\n\t"
        "v_mfma_f32_16x16x32_bf16 v[40:43], v[164:167], v[208:211], v[40:43]\n\t"
        "ds_read_b128 v[200:203], v234 offset:1280\n\t"
        "v_mfma_f32_16x16x32_bf16 v[72:75], v[168:171], v[208:211], v[72:75]\n\t"
        "v_mfma_f32_16x16x32_bf16 v[72:75], v[184:187], v[212:215], v[72:75]\n\t"
        "v_mfma_f32_16x16x32_bf16 v[76:79], v[184:187], v[220:223], v[76:79]\n\t"
        "v_mfma_f32_16x16x32_bf16 v[76:79], v[168:171], v[216:219], v[76:79]\n\t"
        ".Lwp12:\n\t"
        "s_cbranch_execz .Lwp99\n\t"
        "v_mfma_f32_16x16x32_bf16 v[108:111], v[172:175], v[216:219], v[108:111]\n\t"
        "v_mfma_f32_16x16x32_bf16 v[108:111], v[188:191], v[220:223], v[108:111]\n\t"
        "v_mfma_f32_16x16x32_bf16 v[104:107], v[188:191], v[212:215], v[104:107]\n\t"
        "v_mfma_f32_16x16x32_bf16 v[104:107], v[172:175], v[208:211], v[104:107]\n\t"
        "s_waitcnt lgkmcnt(0)\n\t"
        "v_mfma_f32_16x16x32_bf16 v[16:19], v[160:163], v[192:195], v[16:19]\n\t"
        "v_mfma_f32_16x16x32_bf16 v[16:19], v[176:179], v[196:199], v[16:19]\n\t"
        "ds_read_b128 v[208:211], v234 offset:1536\n\t"
        "v_mfma_f32_16x16x32_bf16 v[20:23], v[176:179], v[204:207], v[20:23]\n\t"
        "v_mfma_f32_16x16x32_bf16 v[20:23], v[160:163], v[200:203], v[20:23]\n\t"
        "ds_read_b128 v[212:215], v234 offset:9728\n\t"
        "v_mfma_f32_16x16x32_bf16 v[52:55], v[164:167], v[200:203], v[52:55]\n\t"
        "v_mfma_f32_16x16x32_bf16 v[52:55], v[180:183], v[204:207], v[52:55]\n\t"
        "ds_read_b128 v[220:223], v234 offset:9984\n\t"
        "v_mfma_f32_16x16x32_bf16 v[48:51], v[180:183], v[196:199], v[48:51]\n\t"
        "v_mfma_f32_16x16x32_bf16 v[48:51], v[164:167], v[192:195], v[48:51]\n\t"
        "ds_read_b128 v[216:219], v234 offset:1792\n\t"
        "s_add_u32 s60, s60, 32768\n\t"
        "v_mfma_f32_16x16x32_bf16 v[80:83], v[168:171], v[192:195], v[80:83]\n\t"
        "s_cmp_eq_u32 s60, 98304\n\t"
        "s_cselect_b32 s60, 0, s60\n\t"
        "v_mfma_f32_16x16x32_bf16 v[80:83], v[184:187], v[196:199], v[80:83]\n\t"
        "v_add_u32 v232, s60, %[xb0]\n\t"
        "s_lshr_b32 s61, s60, 1\n\t"
        "v_mfma_f32_16x16x32_bf16 v[84:87], v[184:187], v[204:207], v[84:87]\n\t"
        "v_add_u32 v233, s60, %[xb1]\n\t"
        "v_add_u32 v234, s61, %[ba]\n\t"
        "v_mfma_f32_16x16x32_bf16 v[84:87], v[168:171], v[200:203], v[84:87]\n\t"
        "v_mfma_f32_16x16x32_bf16 v[116:119], v[172:175], v[200:203], v[116:119]\n\t"
        "v_mfma_f32_16x16x32_bf16 v[116:119], v[188:191], v[204:207], v[116:119]\n\t"
        "s_waitcnt lgkmcnt(0)\n\t"
        "v_mfma_f32_16x16x32_bf16 v[112:115], v[188:191], v[196:199], v[112:115]\n\t"
        "v_mfma_f32_16x16x32_bf16 v[112:115], v[172:175], v[192:195], v[112:115]\n\t"
        "s_barrier\n\t"
        "v_mfma_f32_16x16x32_bf16 v[24:27], v[160:163], v[208:211], v[24:27]\n\t"
        "v_mfma_f32_16x16x32_bf16 v[24:27], v[176:179], v[212:215], v[24:27]\n\t"
        "ds_read_b128 v[192:195], v234 offset:0\n\t"
        "ds_read_b128 v[128:131], v232 offset:0\n\t"
        "v_mfma_f32_16x16x32_bf16 v[28:31], v[176:179], v[220:223], v[28:31]\n\t"
        "v_mfma_f32_16x16x32_bf16 v[28:31], v[160:163], v[216:219], v[28:31]\n\t"
        "ds_read_b128 v[196:199], v234 offset:8192\n\t"
        "ds_read_b128 v[144:147], v233 offset:0\n\t"
        "v_mfma_f32_16x16x32_bf16 v[60:63], v[164:167], v[216:219], v[60:63]\n\t"
        "v_mfma_f32_16x16x32_bf16 v[60:63], v[180:183], v[220:223], v[60:63]\n\t"
        "ds_read_b128 v[204:207], v234 offset:8448\n\t"
        "v_mfma_f32_16x16x32_bf16 v[56:59], v[180:183], v[212:215], v[56:59]\n\t"
        "v_mfma_f32_16x16x32_bf16 v[56:59], v[164:167], v[208:211], v[56:59]\n\t"
        "ds_read_b128 v[200:203], v234 offset:256\n\t"
        "v_mfma_f32_16x16x32_bf16 v[88:91], v[168:171], v[208:211], v[88:91]\n\t"
        "v_mfma_f32_16x16x32_bf16 v[88:91], v[184:187], v[212:215], v[88:91]\n\t"
        "ds_read_b128 v[132:135], v232 offset:2048\n\t"
        "v_mfma_f32_16x16x32_bf16 v[92:95], v[184:187], v[220:223], v[92:95]\n\t"
        "v_mfma_f32_16x16x32_bf16 v[92:95], v[168:171], v[216:219], v[92:95]\n\t"
        "ds_read_b128 v[148:151], v233 offset:2048\n\t"
        "v_mfma_f32_16x16x32_bf16 v[124:127], v[172:175], v[216:219], v[124:127]\n\t"
        "v_mfma_f32_16x16x32_bf16 v[124:127], v[188:191], v[220:223], v[124:127]\n\t"
        "ds_read_b128 v[136:139], v232 offset:4096\n\t"
        "v_mfma_f32_16x16x32_bf16 v[120:123], v[188:191], v[212:215], v[120:123]\n\t"
        "v_mfma_f32_16x16x32_bf16 v[120:123], v[172:175], v[208:211], v[120:123]\n\t"
        "ds_read_b128 v[152:155], v233 offset:4096\n\t"
        "s_waitcnt lgkmcnt(4)\n\t"
        "v_mfma_f32_16x16x32_bf16 v[0:3], v[128:131], v[192:195], v[0:3]\n\t"
        "v_mfma_f32_16x16x32_bf16 v[0:3], v[144:147], v[196:199], v[0:3]\n\t"
        "ds_read_b128 v[140:143], v232 offset:6144\n\t"
        "ds_read_b128 v[208:211], v234 offset:512\n\t"
        "v_mfma_f32_16x16x32_bf16 v[4:7], v[144:147], v[204:207], v[4:7]\n\t"
        "v_mfma_f32_16x16x32_bf16 v[4:7], v[128:131], v[200:203], v[4:7]\n\t"
        "ds_read_b128 v[156:159], v233 offset:6144\n\t"
        "s_waitcnt lgkmcnt(5)\n\t"
        "v_mfma_f32_16x16x32_bf16 v[36:39], v[132:135], v[200:203], v[36:39]\n\t"
        "ds_read_b128 v[212:215], v234 offset:8704\n\t"
        "v_mfma_f32_16x16x32_bf16 v[36:39], v[148:151], v[204:207], v[36:39]\n\t"
        "ds_read_b128 v[220:223], v234 offset:8960\n\t"
        "v_mfma_f32_16x16x32_bf16 v[32:35], v[148:151], v[196:199], v[32:35]\n\t"
        "v_mfma_f32_16x16x32_bf16 v[32:35], v[132:135], v[192:195], v[32:35]\n\t"
        "ds_read_b128 v[216:219], v234 offset:768\n\t"
        "s_waitcnt lgkmcnt(6)\n\t"
        "v_mfma_f32_16x16x32_bf16 v[64:67], v[136:139], v[192:195], v[64:67]\n\t"
        "v_mfma_f32_16x16x32_bf16 v[64:67], v[152:155], v[196:199], v[64:67]\n\t"
        "v_mfma_f32_16x16x32_bf16 v[68:71], v[152:155], v[204:207], v[68:71]\n\t"
        "v_mfma_f32_16x16x32_bf16 v[68:71], v[136:139], v[200:203], v[68:71]\n\t"
        "s_waitcnt lgkmcnt(3)\n\t"
        "v_mfma_f32_16x16x32_bf16 v[100:103], v[140:143], v[200:203], v[100:103]\n\t"
        "v_mfma_f32_16x16x32_bf16 v[100:103], v[156:159], v[204:207], v[100:103]\n\t"
        "v_mfma_f32_16x16x32_bf16 v[96:99], v[156:159], v[196:199], v[96:99]\n\t"
        "v_mfma_f32_16x16x32_bf16 v[96:99], v[140:143], v[192:195], v[96:99]\n\t"
        "v_mfma_f32_16x16x32_bf16 v[8:11], v[128:131], v[208:211], v[8:11]\n\t"
        "s_waitcnt lgkmcnt(0)\n\t"
        "v_mfma_f32_16x16x32_bf16 v[8:11], v[144:147], v[212:215], v[8:11]\n\t"
        "ds_read_b128 v[192:195], v234 offset:1024\n\t"
        "v_mfma_f32_16x16x32_bf16 v[12:15], v[144:147], v[220:223], v[12:15]\n\t"
        "v_mfma_f32_16x16x32_bf16 v[12:15], v[128:131], v[216:219], v[12:15]\n\t"
        "ds_read_b128 v[196:199], v234 offset:9216\n\t"
        "v_mfma_f32_16x16x32_bf16 v[44:47], v[132:135], v[216:219], v[44:47]\n\t"
        "v_mfma_f32_16x16x32_bf16 v[44:47], v[148:151], v[220:223], v[44:47]\n\t"
        "ds_read_b128 v[204:207], v234 offset:9472\n\t"
        "v_mfma_f32_16x16x32_bf16 v[40:43], v[148:151], v[212:215], v[40:43]\n\t"
        "v_mfma_f32_16x16x32_bf16 v[40:43], v[132:135], v[208:211], v[40:43]\n\t"
        "ds_read_b128 v[200:203], v234 offset:1280\n\t"
        "v_mfma_f32_16x16x32_bf16 v[72:75], v[136:139], v[208:211], v[72:75]\n\t"
        "v_mfma_f32_16x16x32_bf16 v[72:75], v[152:155], v[212:215], v[72:75]\n\t"
        "v_mfma_f32_16x16x32_bf16 v[76:79], v[152:155], v[220:223], v[76:79]\n\t"
        "v_mfma_f32_16x16x32_bf16 v[76:79], v[136:139], v[216:219], v[76:79]\n\t"
        "v_mfma_f32_16x16x32_bf16 v[108:111], v[140:143], v[216:219], v[108:111]\n\t"
        "v_mfma_f32_16x16x32_bf16 v[108:111], v[156:159], v[220:223], v[108:111]\n\t"
        "v_mfma_f32_16x16x32_bf16 v[104:107], v[156:159], v[212:215], v[104:107]\n\t"
        "v_mfma_f32_16x16x32_bf16 v[104:107], v[140:143], v[208:211], v[104:107]\n\t"
        "s_waitcnt lgkmcnt(0)\n\t"
        "v_mfma_f32_16x16x32_bf16 v[16:19], v[128:131], v[192:195], v[16:19]\n\t"
        "v_mfma_f32_16x16x32_bf16 v[16:19], v[144:147], v[196:199], v[16:19]\n\t"
        "ds_read_b128 v[208:211], v234 offset:1536\n\t"
        "v_mfma_f32_16x16x32_bf16 v[20:23], v[144:147], v[204:207], v[20:23]\n\t"
        "v_mfma_f32_16x16x32_bf16 v[20:23], v[128:131], v[200:203], v[20:23]\n\t"
        "ds_read_b128 v[212:215], v234 offset:9728\n\t"
        "v_mfma_f32_16x16x32_bf16 v[52:55], v[132:135], v[200:203], v[52:55]\n\t"
        "v_mfma_f32_16x16x32_bf16 v[52:55], v[148:151], v[204:207], v[52:55]\n\t"
        "ds_read_b128 v[220:223], v234 offset:9984\n\t"
        "v_mfma_f32_16x16x32_bf16 v[48:51], v[148:151], v[196:199], v[48:51]\n\t"
        "v_mfma_f32_16x16x32_bf16 v[48:51], v[132:135], v[192:195], v[48:51]\n\t"
        "ds_read_b128 v[216:219], v234 offset:1792\n\t"
        "s_add_u32 s60, s60, 32768\n\t"
        "v_mfma_f32_16x16x32_bf16 v[80:83], v[136:139], v[192:195], v[80:83]\n\t"
        "s_cmp_eq_u32 s60, 98304\n\t"
        "s_cselect_b32 s60, 0, s60\n\t"
        "v_mfma_f32_16x16x32_bf16 v[80:83], v[152:155], v[196:199], v[80:83]\n\t"
        "v_add_u32 v232, s60, %[xb0]\n\t"
        "s_lshr_b32 s61, s60, 1\n\t"
        "v_mfma_f32_16x16x32_bf16 v[84:87], v[152:155], v[204:207], v[84:87]\n\t"
        "v_add_u32 v233, s60, %[xb1]\n\t"
        "v_add_u32 v234, s61, %[ba]\n\t"
        "v_mfma_f32_16x16x32_bf16 v[84:87], v[136:139], v[200:203], v[84:87]\n\t"
        "s_cmp_lg_u32 s62, -1\n\t"
        "s_cbranch_scc1 .Lcloop\n\t"
        "v_mfma_f32_16x16x32_bf16 v[116:119], v[140:143], v[200:203], v[116:119]\n\t"
        "v_mfma_f32_16x16x32_bf16 v[116:119], v[156:159], v[204:207], v[116:119]\n\t"
        "s_waitcnt lgkmcnt(0)\n\t"
        "v_mfma_f32_16x16x32_bf16 v[112:115], v[156:159], v[196:199], v[112:115]\n\t"
        "v_mfma_f32_16x16x32_bf16 v[112:115], v[140:143], v[192:195], v[112:115]\n\t"
        "s_barrier\n\t"
        "v_mfma_f32_16x16x32_bf16 v[24:27], v[128:131], v[208:211], v[24:27]\n\t"
        "s_setprio 2\n\t"
        ".Lwp14:\n\t"
        "s_cbranch_execz .Lwp15\n\t"
        "v_mfma_f32_16x16x32_bf16 v[24:27], v[144:147], v[212:215], v[24:27]\n\t"
        "ds_read_b128 v[160:163], %[ba] offset:16384\n\t"
        "ds_read_b128 v[164:167], v232 offset:0\n\t"
        "v_mfma_f32_16x16x32_bf16 v[28:31], v[144:147], v[220:223], v[28:31]\n\t"
        "v_mfma_f32_16x16x32_bf16 v[28:31], v[128:131], v[216:219], v[28:31]\n\t"
        "ds_read_b128 v[144:147], %[ba] offset:24576\n\t"
        "ds_read_b128 v[168:171], v233 offset:0\n\t"
        "v_mfma_f32_16x16x32_bf16 v[60:63], v[132:135], v[216:219], v[60:63]\n\t"
        "v_mfma_f32_16x16x32_bf16 v[60:63], v[148:151], v[220:223], v[60:63]\n\t"
        "ds_read_b128 v[128:131], %[ba] offset:24832\n\t"
        "ds_read_b128 v[172:175], %[ba] offset:16640\n\t"
        "v_mfma_f32_16x16x32_bf16 v[56:59], v[148:151], v[212:215], v[56:59]\n\t"
        "ds_read_b128 v[176:179], v232 offset:2048\n\t"
        "v_mfma_f32_16x16x32_bf16 v[56:59], v[132:135], v[208:211], v[56:59]\n\t"
        "ds_read_b128 v[148:151], v233 offset:2048\n\t"
        "v_mfma_f32_16x16x32_bf16 v[88:91], v[136:139], v[208:211], v[88:91]\n\t"
        "v_mfma_f32_16x16x32_bf16 v[88:91], v[152:155], v[212:215], v[88:91]\n\t"
        "ds_read_b128 v[132:135], v232 offset:4096\n\t"
        "v_mfma_f32_16x16x32_bf16 v[92:95], v[152:155], v[220:223], v[92:95]\n\t"
        "v_mfma_f32_16x16x32_bf16 v[92:95], v[136:139], v[216:219], v[92:95]\n\t"
        "ds_read_b128 v[152:155], v233 offset:4096\n\t"
        "v_mfma_f32_16x16x32_bf16 v[124:127], v[140:143], v[216:219], v[124:127]\n\t"
        "v_mfma_f32_16x16x32_bf16 v[124:127], v[156:159], v[220:223], v[124:127]\n\t"
        "ds_read_b128 v[136:139], v232 offset:6144\n\t"
        "v_mfma_f32_16x16x32_bf16 v[120:123], v[156:159], v[212:215], v[120:123]\n\t"
        "v_mfma_f32_16x16x32_bf16 v[120:123], v[140:143], v[208:211], v[120:123]\n\t"
        "ds_read_b128 v[156:159], v233 offset:6144\n\t"
        "s_waitcnt lgkmcnt(4)\n\t"
        "v_mfma_f32_16x16x32_bf16 v[0:3], v[164:167], v[160:163], v[0:3]\n\t"
        "v_mfma_f32_16x16x32_bf16 v[0:3], v[168:171], v[144:147], v[0:3]\n\t"
        "ds_read_b128 v[140:143], %[ba] offset:16896\n\t"
        "v_mfma_f32_16x16x32_bf16 v[4:7], v[168:171], v[128:131], v[4:7]\n\t"
        "v_mfma_f32_16x16x32_bf16 v[4:7], v[164:167], v[172:175], v[4:7]\n\t"
        "ds_read_b128 v[180:183], %[ba] offset:25088\n\t"
        "v_mfma_f32_16x16x32_bf16 v[36:39], v[176:179], v[172:175], v[36:39]\n\t"
        "v_mfma_f32_16x16x32_bf16 v[36:39], v[148:151], v[128:131], v[36:39]\n\t"
        "ds_read_b128 v[184:187], %[ba] offset:25344\n\t"
        "v_mfma_f32_16x16x32_bf16 v[32:35], v[148:151], v[144:147], v[32:35]\n\t"
        "v_mfma_f32_16x16x32_bf16 v[32:35], v[176:179], v[160:163], v[32:35]\n\t"
        "ds_read_b128 v[188:191], %[ba] offset:17152\n\t"
        "s_waitcnt lgkmcnt(4)\n\t"
        "v_mfma_f32_16x16x32_bf16 v[64:67], v[132:135], v[160:163], v[64:67]\n\t"
        "v_mfma_f32_16x16x32_bf16 v[64:67], v[152:155], v[144:147], v[64:67]\n\t"
        "ds_read_b128 v[192:195], %[ba] offset:17408\n\t"
        "v_mfma_f32_16x16x32_bf16 v[68:71], v[152:155], v[128:131], v[68:71]\n\t"
        "v_mfma_f32_16x16x32_bf16 v[68:71], v[132:135], v[172:175], v[68:71]\n\t"
        "ds_read_b128 v[196:199], v232 offset:32768\n\t"
        "v_mfma_f32_16x16x32_bf16 v[100:103], v[136:139], v[172:175], v[100:103]\n\t"
        "v_mfma_f32_16x16x32_bf16 v[100:103], v[156:159], v[128:131], v[100:103]\n\t"
        "ds_read_b128 v[172:175], %[ba] offset:25600\n\t"
        "v_mfma_f32_16x16x32_bf16 v[96:99], v[156:159], v[144:147], v[96:99]\n\t"
        "v_mfma_f32_16x16x32_bf16 v[96:99], v[136:139], v[160:163], v[96:99]\n\t"
        "ds_read_b128 v[128:131], v233 offset:32768\n\t"
        "s_waitcnt lgkmcnt(4)\n\t"
        "v_mfma_f32_16x16x32_bf16 v[8:11], v[164:167], v[140:143], v[8:11]\n\t"
        "v_mfma_f32_16x16x32_bf16 v[8:11], v[168:171], v[180:183], v[8:11]\n\t"
        "ds_read_b128 v[144:147], %[ba] offset:25856\n\t"
        "v_mfma_f32_16x16x32_bf16 v[12:15], v[168:171], v[184:187], v[12:15]\n\t"
        "v_mfma_f32_16x16x32_bf16 v[12:15], v[164:167], v[188:191], v[12:15]\n\t"
        "ds_read_b128 v[160:163], %[ba] offset:17664\n\t"
        "v_mfma_f32_16x16x32_bf16 v[44:47], v[176:179], v[188:191], v[44:47]\n\t"
        "v_mfma_f32_16x16x32_bf16 v[44:47], v[148:151], v[184:187], v[44:47]\n\t"
        "ds_read_b128 v[164:167], %[ba] offset:17920\n\t"
        "ds_read_b128 v[168:171], %[ba] offset:26112\n\t"
        "v_mfma_f32_16x16x32_bf16 v[40:43], v[148:151], v[180:183], v[40:43]\n\t"
        "v_mfma_f32_16x16x32_bf16 v[40:43], v[176:179], v[140:143], v[40:43]\n\t"
        "ds_read_b128 v[148:151], %[ba] offset:26368\n\t"
        "v_mfma_f32_16x16x32_bf16 v[72:75], v[132:135], v[140:143], v[72:75]\n\t"
        "v_mfma_f32_16x16x32_bf16 v[72:75], v[152:155], v[180:183], v[72:75]\n\t"
        "ds_read_b128 v[176:179], %[ba] offset:18176\n\t"
        "v_mfma_f32_16x16x32_bf16 v[76:79], v[152:155], v[184:187], v[76:79]\n\t"
        "v_mfma_f32_16x16x32_bf16 v[76:79], v[132:135], v[188:191], v[76:79]\n\t"
        "ds_read_b128 v[152:155], v232 offset:34816\n\t"
        "v_mfma_f32_16x16x32_bf16 v[108:111], v[136:139], v[188:191], v[108:111]\n\t"
        "v_mfma_f32_16x16x32_bf16 v[108:111], v[156:159], v[184:187], v[108:111]\n\t"
        "ds_read_b128 v[132:135], v233 offset:34816\n\t"
        "v_mfma_f32_16x16x32_bf16 v[104:107], v[156:159], v[180:183], v[104:107]\n\t"
        "v_mfma_f32_16x16x32_bf16 v[104:107], v[136:139], v[140:143], v[104:107]\n\t"
        "s_waitcnt lgkmcnt(3)\n\t"
        "v_mfma_f32_16x16x32_bf16 v[0:3], v[196:199], v[192:195], v[0:3]\n\t"
        "v_mfma_f32_16x16x32_bf16 v[0:3], v[128:131], v[172:175], v[0:3]\n\t"
        "v_mfma_f32_16x16x32_bf16 v[4:7], v[128:131], v[144:147], v[4:7]\n\t"
        "v_mfma_f32_16x16x32_bf16 v[4:7], v[196:199], v[160:163], v[4:7]\n\t"
        "ds_read_b128 v[136:139], v232 offset:36864\n\t"
        "v_mfma_f32_16x16x32_bf16 v[8:11], v[196:199], v[164:167], v[8:11]\n\t"
        "v_mfma_f32_16x16x32_bf16 v[8:11], v[128:131], v[168:171], v[8:11]\n\t"
        "ds_read_b128 v[140:143], v233 offset:36864\n\t"
        "v_mfma_f32_16x16x32_bf16 v[12:15], v[128:131], v[148:151], v[12:15]\n\t"
        "s_waitcnt lgkmcnt(2)\n\t"
        "v_mfma_f32_16x16x32_bf16 v[12:15], v[196:199], v[176:179], v[12:15]\n\t"
        "v_mfma_f32_16x16x32_bf16 v[32:35], v[152:155], v[192:195], v[32:35]\n\t"
        "v_mfma_f32_16x16x32_bf16 v[32:35], v[132:135], v[172:175], v[32:35]\n\t"
        "ds_read_b128 v[128:131], v232 offset:38912\n\t"
        "v_mfma_f32_16x16x32_bf16 v[36:39], v[132:135], v[144:147], v[36:39]\n\t"
        "v_mfma_f32_16x16x32_bf16 v[36:39], v[152:155], v[160:163], v[36:39]\n\t"
        "ds_read_b128 v[156:159], v233 offset:38912\n\t"
        "v_mfma_f32_16x16x32_bf16 v[40:43], v[152:155], v[164:167], v[40:43]\n\t"
        "v_mfma_f32_16x16x32_bf16 v[40:43], v[132:135], v[168:171], v[40:43]\n\t"
        "v_mfma_f32_16x16x32_bf16 v[44:47], v[132:135], v[148:151], v[44:47]\n\t"
        "v_mfma_f32_16x16x32_bf16 v[44:47], v[152:155], v[176:179], v[44:47]\n\t"
        "s_waitcnt lgkmcnt(0)\n\t"
        "s_barrier\n\t"
        "v_mfma_f32_16x16x32_bf16 v[64:67], v[136:139], v[192:195], v[64:67]\n\t"
        ".Lwp15:\n\t"
        "s_cbranch_execz .Lwp97\n\t"
        "v_mfma_f32_16x16x32_bf16 v[64:67], v[140:143], v[172:175], v[64:67]\n\t"
        "ds_read_b128 v[132:135], %[ba] offset:32768\n\t"
        "ds_read_b128 v[152:155], %[xb0] offset:0\n\t"
        "v_mfma_f32_16x16x32_bf16 v[68:71], v[140:143], v[144:147], v[68:71]\n\t"
        "v_mfma_f32_16x16x32_bf16 v[68:71], v[136:139], v[160:163], v[68:71]\n\t"
        "ds_read_b128 v[180:183], %[ba] offset:40960\n\t"
        "ds_read_b128 v[184:187], %[xb1] offset:0\n\t"
        "v_mfma_f32_16x16x32_bf16 v[72:75], v[136:139], v[164:167], v[72:75]\n\t"
        "v_mfma_f32_16x16x32_bf16 v[72:75], v[140:143], v[168:171], v[72:75]\n\t"
        "ds_read_b128 v[188:191], %[ba] offset:41216\n\t"
        "ds_read_b128 v[196:199], %[ba] offset:33024\n\t"
        "v_mfma_f32_16x16x32_bf16 v[76:79], v[140:143], v[148:151], v[76:79]\n\t"
        "v_mfma_f32_16x16x32_bf16 v[76:79], v[136:139], v[176:179], v[76:79]\n\t"
        "ds_read_b128 v[140:143], %[ba] offset:33280\n\t"
        "s_waitcnt lgkmcnt(5)\n\t"
        "v_mfma_f32_16x16x32_bf16 v[0:3], v[152:155], v[132:135], v[0:3]\n\t"
        "ds_read_b128 v[136:139], %[ba] offset:41472\n\t"
        "s_waitcnt lgkmcnt(4)\n\t"
        "v_mfma_f32_16x16x32_bf16 v[0:3], v[184:187], v[180:183], v[0:3]\n\t"
        "ds_read_b128 v[200:203], %[ba] offset:41728\n\t"
        "s_waitcnt lgkmcnt(4)\n\t"
        "v_mfma_f32_16x16x32_bf16 v[4:7], v[184:187], v[188:191], v[4:7]\n\t"
        "ds_read_b128 v[204:207], %[ba] offset:33536\n\t"
        "s_waitcnt lgkmcnt(4)\n\t"
        "v_mfma_f32_16x16x32_bf16 v[4:7], v[152:155], v[196:199], v[4:7]\n\t"
        "ds_read_b128 v[208:211], %[xb0] offset:2048\n\t"
        "s_waitcnt lgkmcnt(4)\n\t"
        "v_mfma_f32_16x16x32_bf16 v[8:11], v[152:155], v[140:143], v[8:11]\n\t"
        "s_waitcnt lgkmcnt(3)\n\t"
        "v_mfma_f32_16x16x32_bf16 v[8:11], v[184:187], v[136:139], v[8:11]\n\t"
        "ds_read_b128 v[212:215], %[xb1] offset:2048\n\t"
        "s_waitcnt lgkmcnt(3)\n\t"
        "v_mfma_f32_16x16x32_bf16 v[12:15], v[184:187], v[200:203], v[12:15]\n\t"
        "s_waitcnt lgkmcnt(2)\n\t"
        "v_mfma_f32_16x16x32_bf16 v[12:15], v[152:155], v[204:207], v[12:15]\n\t"
        "v_mfma_f32_16x16x32_bf16 v[96:99], v[128:131], v[192:195], v[96:99]\n\t"
        "v_mfma_f32_16x16x32_bf16 v[96:99], v[156:159], v[172:175], v[96:99]\n\t"
        "ds_read_b128 v[152:155], %[xb0] offset:4096\n\t"
        "v_cvt_pk_bf16_f32 v224, v0, v4\n\t"
        "v_mfma_f32_16x16x32_bf16 v[100:103], v[156:159], v[144:147], v[100:103]\n\t"
        "v_mfma_f32_16x16x32_bf16 v[100:103], v[128:131], v[160:163], v[100:103]\n\t"
        "ds_read_b128 v[144:147], %[xb1] offset:4096\n\t"
        "v_cvt_pk_bf16_f32 v225, v8, v12\n\t"
        "v_mfma_f32_16x16x32_bf16 v[104:107], v[128:131], v[164:167], v[104:107]\n\t"
        "global_store_dwordx2 %[oo0], v[224:225], %[ob0] offset:0 nt\n\t"
        "v_mfma_f32_16x16x32_bf16 v[104:107], v[156:159], v[168:171], v[104:107]\n\t"
        "ds_read_b128 v[160:163], %[xb0] offset:6144\n\t"
        "v_cvt_pk_bf16_f32 v227, v9, v13\n\t"
        "v_mfma_f32_16x16x32_bf16 v[108:111], v[156:159], v[148:151], v[108:111]\n\t"
        "v_cvt_pk_bf16_f32 v226, v1, v5\n\t"
        "v_mfma_f32_16x16x32_bf16 v[108:111], v[128:131], v[176:179], v[108:111]\n\t"
        "ds_read_b128 v[148:151], %[xb1] offset:6144\n\t"
        "s_waitcnt lgkmcnt(4)\n\t"
        "v_mfma_f32_16x16x32_bf16 v[32:35], v[208:211], v[132:135], v[32:35]\n\t"
        "global_store_dwordx2 %[oo1], v[226:227], %[ob0] offset:0 nt\n\t"
        "v_mfma_f32_16x16x32_bf16 v[32:35], v[212:215], v[180:183], v[32:35]\n\t"
        "ds_read_b128 v[128:131], v232 offset:0\n\t"
        "v_cvt_pk_bf16_f32 v229, v10, v14\n\t"
        "v_mfma_f32_16x16x32_bf16 v[36:39], v[212:215], v[188:191], v[36:39]\n\t"
        "v_cvt_pk_bf16_f32 v228, v2, v6\n\t"
        "v_mfma_f32_16x16x32_bf16 v[36:39], v[208:211], v[196:199], v[36:39]\n\t"
        "ds_read_b128 v[156:159], %[ba] offset:33792\n\t"
        "global_store_dwordx2 %[oo2], v[228:229], %[ob0] offset:0 nt\n\t"
        "v_mfma_f32_16x16x32_bf16 v[40:43], v[208:211], v[140:143], v[40:43]\n\t"
        "v_mfma_f32_16x16x32_bf16 v[40:43], v[212:215], v[136:139], v[40:43]\n\t"
        "ds_read_b128 v[164:167], v233 offset:0\n\t"
        "v_cvt_pk_bf16_f32 v231, v11, v15\n\t"
        "v_mfma_f32_16x16x32_bf16 v[44:47], v[212:215], v[200:203], v[44:47]\n\t"
        "v_cvt_pk_bf16_f32 v230, v3, v7\n\t"
        "v_mfma_f32_16x16x32_bf16 v[44:47], v[208:211], v[204:207], v[44:47]\n\t"
        "ds_read_b128 v[168:171], %[ba] offset:41984\n\t"
        "s_waitcnt lgkmcnt(4)\n\t"
        "v_mfma_f32_16x16x32_bf16 v[64:67], v[152:155], v[132:135], v[64:67]\n\t"
        "global_store_dwordx2 %[oo3], v[230:231], %[ob0] offset:0 nt\n\t"
        "v_mfma_f32_16x16x32_bf16 v[64:67], v[144:147], v[180:183], v[64:67]\n\t"
        "ds_read_b128 v[172:175], %[ba] offset:42240\n\t"
        "v_cvt_pk_bf16_f32 v224, v32, v36\n\t"
        "v_mfma_f32_16x16x32_bf16 v[68:71], v[144:147], v[188:191], v[68:71]\n\t"
        "v_mfma_f32_16x16x32_bf16 v[68:71], v[152:155], v[196:199], v[68:71]\n\t"
        "ds_read_b128 v[176:179], %[ba] offset:34048\n\t"
        "v_cvt_pk_bf16_f32 v225, v40, v44\n\t"
        "v_mfma_f32_16x16x32_bf16 v[72:75], v[152:155], v[140:143], v[72:75]\n\t"
        "global_store_dwordx2 %[oo0], v[224:225], %[ob1] offset:0 nt\n\t"
        "v_mfma_f32_16x16x32_bf16 v[72:75], v[144:147], v[136:139], v[72:75]\n\t"
        "ds_read_b128 v[184:187], %[ba] offset:34304\n\t"
        "ds_read_b128 v[192:195], %[ba] offset:42496\n\t"
        "v_mfma_f32_16x16x32_bf16 v[76:79], v[144:147], v[200:203], v[76:79]\n\t"
        "v_cvt_pk_bf16_f32 v227, v41, v45\n\t"
        "v_cvt_pk_bf16_f32 v226, v33, v37\n\t"
        "v_mfma_f32_16x16x32_bf16 v[76:79], v[152:155], v[204:207], v[76:79]\n\t"
        "ds_read_b128 v[144:147], %[ba] offset:42752\n\t"
        "global_store_dwordx2 %[oo1], v[226:227], %[ob1] offset:0 nt\n\t"
        "v_mfma_f32_16x16x32_bf16 v[96:99], v[160:163], v[132:135], v[96:99]\n\t"
        ".Lwp16:\n\t"
        "s_cbranch_execz .Lwp17\n\t"
        "v_mfma_f32_16x16x32_bf16 v[96:99], v[148:151], v[180:183], v[96:99]\n\t"
        "ds_read_b128 v[132:135], %[ba] offset:34560\n\t"
        "v_cvt_pk_bf16_f32 v229, v42, v46\n\t"
        "v_mfma_f32_16x16x32_bf16 v[100:103], v[148:151], v[188:191], v[100:103]\n\t"
        "v_cvt_pk_bf16_f32 v228, v34, v38\n\t"
        "v_mfma_f32_16x16x32_bf16 v[100:103], v[160:163], v[196:199], v[100:103]\n\t"
        "ds_read_b128 v[152:155], v232 offset:2048\n\t"
        "global_store_dwordx2 %[oo2], v[228:229], %[ob1] offset:0 nt\n\t"
        "v_mfma_f32_16x16x32_bf16 v[104:107], v[160:163], v[140:143], v[104:107]\n\t"
        "v_mfma_f32_16x16x32_bf16 v[104:107], v[148:151], v[136:139], v[104:107]\n\t"
        "ds_read_b128 v[140:143], v233 offset:2048\n\t"
        "v_cvt_pk_bf16_f32 v231, v43, v47\n\t"
        "v_mfma_f32_16x16x32_bf16 v[108:111], v[148:151], v[200:203], v[108:111]\n\t"
        "v_cvt_pk_bf16_f32 v230, v35, v39\n\t"
        "v_mfma_f32_16x16x32_bf16 v[108:111], v[160:163], v[204:207], v[108:111]\n\t"
        "global_store_dwordx2 %[oo3], v[230:231], %[ob1] offset:0 nt\n\t"
        "s_waitcnt lgkmcnt(3)\n\t"
        "v_mfma_f32_16x16x32_bf16 v[16:19], v[128:131], v[156:159], v[16:19]\n\t"
        "v_mfma_f32_16x16x32_bf16 v[16:19], v[164:167], v[168:171], v[16:19]\n\t"
        "ds_read_b128 v[136:139], v232 offset:4096\n\t"
        "v_cvt_pk_bf16_f32 v225, v72, v76\n\t"
        "v_mfma_f32_16x16x32_bf16 v[20:23], v[164:167], v[172:175], v[20:23]\n\t"
        "v_cvt_pk_bf16_f32 v224, v64, v68\n\t"
        "v_mfma_f32_16x16x32_bf16 v[20:23], v[128:131], v[176:179], v[20:23]\n\t"
        "ds_read_b128 v[148:151], v233 offset:4096\n\t"
        "global_store_dwordx2 %[oo0], v[224:225], %[ob2] offset:0 nt\n\t"
        "v_mfma_f32_16x16x32_bf16 v[24:27], v[128:131], v[184:187], v[24:27]\n\t"
        "v_mfma_f32_16x16x32_bf16 v[24:27], v[164:167], v[192:195], v[24:27]\n\t"
        "ds_read_b128 v[160:163], v232 offset:6144\n\t"
        "v_cvt_pk_bf16_f32 v227, v73, v77\n\t"
        "v_mfma_f32_16x16x32_bf16 v[28:31], v[164:167], v[144:147], v[28:31]\n\t"
        "v_cvt_pk_bf16_f32 v226, v65, v69\n\t"
        "s_waitcnt lgkmcnt(3)\n\t"
        "v_mfma_f32_16x16x32_bf16 v[28:31], v[128:131], v[132:135], v[28:31]\n\t"
        "ds_read_b128 v[164:167], v233 offset:6144\n\t"
        "global_store_dwordx2 %[oo1], v[226:227], %[ob2] offset:0 nt\n\t"
        "v_mfma_f32_16x16x32_bf16 v[48:51], v[152:155], v[156:159], v[48:51]\n\t"
        "v_mfma_f32_16x16x32_bf16 v[48:51], v[140:143], v[168:171], v[48:51]\n\t"
        "ds_read_b128 v[128:131], v232 offset:32768\n\t"
        "v_cvt_pk_bf16_f32 v229, v74, v78\n\t"
        "v_mfma_f32_16x16x32_bf16 v[52:55], v[140:143], v[172:175], v[52:55]\n\t"
        "v_cvt_pk_bf16_f32 v228, v66, v70\n\t"
        "v_mfma_f32_16x16x32_bf16 v[52:55], v[152:155], v[176:179], v[52:55]\n\t"
        "ds_read_b128 v[180:183], v233 offset:32768\n\t"
        "ds_read_b128 v[188:191], %[xb0] offset:0\n\t"
        "v_mfma_f32_16x16x32_bf16 v[56:59], v[152:155], v[184:187], v[56:59]\n\t"
        "global_store_dwordx2 %[oo2], v[228:229], %[ob2] offset:0 nt\n\t"
        "v_mfma_f32_16x16x32_bf16 v[56:59], v[140:143], v[192:195], v[56:59]\n\t"
        "v_cvt_pk_bf16_f32 v231, v75, v79\n\t"
        "v_cvt_pk_bf16_f32 v230, v67, v71\n\t"
        "v_mfma_f32_16x16x32_bf16 v[60:63], v[140:143], v[144:147], v[60:63]\n\t"
        "v_mfma_f32_16x16x32_bf16 v[60:63], v[152:155], v[132:135], v[60:63]\n\t"
        "global_store_dwordx2 %[oo3], v[230:231], %[ob2] offset:0 nt\n\t"
        "s_barrier\n\t"
        "s_waitcnt lgkmcnt(3)\n\t"
        "v_mfma_f32_16x16x32_bf16 v[80:83], v[136:139], v[156:159], v[80:83]\n\t"
        "v_mfma_f32_16x16x32_bf16 v[80:83], v[148:151], v[168:171], v[80:83]\n\t"
        "ds_read_b128 v[140:143], %[ba] offset:0\n\t"
        "ds_read_b128 v[152:155], %[xb1] offset:0\n\t"
        "v_mfma_f32_16x16x32_bf16 v[84:87], v[148:151], v[172:175], v[84:87]\n\t"
        "v_cvt_pk_bf16_f32 v225, v104, v108\n\t"
        "v_cvt_pk_bf16_f32 v224, v96, v100\n\t"
        "v_mfma_f32_16x16x32_bf16 v[84:87], v[136:139], v[176:179], v[84:87]\n\t"
        ".Lwp17:\n\t"
        "s_cbranch_execz .Lwp18\n\t"
        "ds_read_b128 v[196:199], %[ba] offset:8192\n\t"
        "v_mfma_f32_16x16x32_bf16 v[88:91], v[136:139], v[184:187], v[88:91]\n\t"
        "ds_read_b128 v[200:203], %[ba] offset:1024\n\t"
        "ds_read_b128 v[204:207], %[ba] offset:9216\n\t"
        "v_mfma_f32_16x16x32_bf16 v[88:91], v[148:151], v[192:195], v[88:91]\n\t"
        "global_store_dwordx2 %[oo0], v[224:225], %[ob3] offset:0 nt\n\t"
        "ds_read_b128 v[208:211], %[ba] offset:9472\n\t"
        "v_mfma_f32_16x16x32_bf16 v[92:95], v[148:151], v[144:147], v[92:95]\n\t"
        "ds_read_b128 v[212:215], %[ba] offset:1280\n\t"
        "ds_read_b128 v[216:219], %[ba] offset:8448\n\t"
        "v_mfma_f32_16x16x32_bf16 v[92:95], v[136:139], v[132:135], v[92:95]\n\t"
        "v_cvt_pk_bf16_f32 v227, v105, v109\n\t"
        "v_cvt_pk_bf16_f32 v226, v97, v101\n\t"
        "v_mfma_f32_16x16x32_bf16 v[112:115], v[160:163], v[156:159], v[112:115]\n\t"
        "ds_read_b128 v[136:139], %[ba] offset:256\n\t"
        "ds_read_b128 v[148:151], %[ba] offset:512\n\t"
        "v_mfma_f32_16x16x32_bf16 v[112:115], v[164:167], v[168:171], v[112:115]\n\t"
        "ds_read_b128 v[156:159], %[ba] offset:8704\n\t"
        "global_store_dwordx2 %[oo1], v[226:227], %[ob3] offset:0 nt\n\t"
        "v_mfma_f32_16x16x32_bf16 v[116:119], v[164:167], v[172:175], v[116:119]\n\t"
        "ds_read_b128 v[168:171], %[ba] offset:1536\n\t"
        "ds_read_b128 v[220:223], %[ba] offset:9728\n\t"
        "v_mfma_f32_16x16x32_bf16 v[116:119], v[160:163], v[176:179], v[116:119]\n\t"
        "v_cvt_pk_bf16_f32 v229, v106, v110\n\t"
        "v_cvt_pk_bf16_f32 v228, v98, v102\n\t"
        "v_mfma_f32_16x16x32_bf16 v[120:123], v[160:163], v[184:187], v[120:123]\n\t"
        "ds_read_b128 v[172:175], %[ba] offset:9984\n\t"
        "ds_read_b128 v[176:179], %[ba] offset:1792\n\t"
        "v_mfma_f32_16x16x32_bf16 v[120:123], v[164:167], v[192:195], v[120:123]\n\t"
        "global_store_dwordx2 %[oo2], v[228:229], %[ob3] offset:0 nt\n\t"
        "ds_read_b128 v[184:187], %[ba] offset:8960\n\t"
        "v_mfma_f32_16x16x32_bf16 v[124:127], v[164:167], v[144:147], v[124:127]\n\t"
        "v_cvt_pk_bf16_f32 v231, v107, v111\n\t"
        "v_cvt_pk_bf16_f32 v230, v99, v103\n\t"
        "v_mfma_f32_16x16x32_bf16 v[124:127], v[160:163], v[132:135], v[124:127]\n\t"
        "ds_read_b128 v[144:147], %[ba] offset:768\n\t"
        "s_waitcnt lgkmcnt(9)\n\t"
        "v_mfma_f32_16x16x32_bf16 v[16:19], v[128:131], v[140:143], v[16:19]\n\t"
        "global_store_dwordx2 %[oo3], v[230:231], %[ob3] offset:0 nt\n\t"
        "v_mfma_f32_16x16x32_bf16 v[16:19], v[180:183], v[196:199], v[16:19]\n\t"
        "ds_read_b128 v[132:135], v232 offset:34816\n\t"
        "v_mfma_f32_16x16x32_bf16 v[16:19], v[188:191], v[200:203], v[16:19]\n\t"
        "v_mfma_f32_16x16x32_bf16 v[16:19], v[152:155], v[204:207], v[16:19]\n\t"
        "ds_read_b128 v[160:163], v233 offset:34816\n\t"
        "v_mfma_f32_16x16x32_bf16 v[20:23], v[152:155], v[208:211], v[20:23]\n\t"
        "v_mfma_f32_16x16x32_bf16 v[20:23], v[188:191], v[212:215], v[20:23]\n\t"
        "ds_read_b128 v[164:167], %[xb0] offset:2048\n\t"
        "v_mfma_f32_16x16x32_bf16 v[20:23], v[180:183], v[216:219], v[20:23]\n\t"
        "s_waitcnt lgkmcnt(4)\n\t"
        "v_mfma_f32_16x16x32_bf16 v[20:23], v[128:131], v[136:139], v[20:23]\n\t"
        "ds_read_b128 v[192:195], %[xb1] offset:2048\n\t"
        "v_mfma_f32_16x16x32_bf16 v[24:27], v[128:131], v[148:151], v[24:27]\n\t"
        "v_mfma_f32_16x16x32_bf16 v[24:27], v[180:183], v[156:159], v[24:27]\n\t"
        "v_mfma_f32_16x16x32_bf16 v[24:27], v[188:191], v[168:171], v[24:27]\n\t"
        "v_mfma_f32_16x16x32_bf16 v[24:27], v[152:155], v[220:223], v[24:27]\n\t"
        "v_mfma_f32_16x16x32_bf16 v[28:31], v[152:155], v[172:175], v[28:31]\n\t"
        "v_mfma_f32_16x16x32_bf16 v[28:31], v[188:191], v[176:179], v[28:31]\n\t"
        "v_mfma_f32_16x16x32_bf16 v[28:31], v[180:183], v[184:187], v[28:31]\n\t"
        "s_waitcnt lgkmcnt(1)\n\t"
        "v_mfma_f32_16x16x32_bf16 v[28:31], v[128:131], v[144:147], v[28:31]\n\t"
        "v_mfma_f32_16x16x32_bf16 v[48:51], v[132:135], v[140:143], v[48:51]\n\t"
        "v_mfma_f32_16x16x32_bf16 v[48:51], v[160:163], v[196:199], v[48:51]\n\t"
        "ds_read_b128 v[128:131], v232 offset:36864\n\t"
        "v_cvt_pk_bf16_f32 v224, v16, v20\n\t"
        "v_mfma_f32_16x16x32_bf16 v[48:51], v[164:167], v[200:203], v[48:51]\n\t"
        "s_waitcnt lgkmcnt(1)\n\t"
        "v_mfma_f32_16x16x32_bf16 v[48:51], v[192:195], v[204:207], v[48:51]\n\t"
        "ds_read_b128 v[152:155], v233 offset:36864\n\t"
        "v_cvt_pk_bf16_f32 v225, v24, v28\n\t"
        "v_mfma_f32_16x16x32_bf16 v[52:55], v[192:195], v[208:211], v[52:55]\n\t"
        "global_store_dwordx2 %[oo0], v[224:225], %[ob0] offset:128 nt\n\t"
        "v_mfma_f32_16x16x32_bf16 v[52:55], v[164:167], v[212:215], v[52:55]\n\t"
        "ds_read_b128 v[180:183], %[xb0] offset:4096\n\t"
        "v_cvt_pk_bf16_f32 v227, v25, v29\n\t"
        "v_mfma_f32_16x16x32_bf16 v[52:55], v[160:163], v[216:219], v[52:55]\n\t"
        "v_cvt_pk_bf16_f32 v226, v17, v21\n\t"
        "v_mfma_f32_16x16x32_bf16 v[52:55], v[132:135], v[136:139], v[52:55]\n\t"
        "ds_read_b128 v[188:191], %[xb1] offset:4096\n\t"
        "global_store_dwordx2 %[oo1], v[226:227], %[ob0] offset:128 nt\n\t"
        "v_mfma_f32_16x16x32_bf16 v[56:59], v[132:135], v[148:151], v[56:59]\n\t"
        "v_mfma_f32_16x16x32_bf16 v[56:59], v[160:163], v[156:159], v[56:59]\n\t"
        "v_cvt_pk_bf16_f32 v229, v26, v30\n\t"
        "v_cvt_pk_bf16_f32 v228, v18, v22\n\t"
        "v_mfma_f32_16x16x32_bf16 v[56:59], v[164:167], v[168:171], v[56:59]\n\t"
        "v_mfma_f32_16x16x32_bf16 v[56:59], v[192:195], v[220:223], v[56:59]\n\t"
        "global_store_dwordx2 %[oo2], v[228:229], %[ob0] offset:128 nt\n\t"
        "v_mfma_f32_16x16x32_bf16 v[60:63], v[192:195], v[172:175], v[60:63]\n\t"
        "v_mfma_f32_16x16x32_bf16 v[60:63], v[164:167], v[176:179], v[60:63]\n\t"
        "v_cvt_pk_bf16_f32 v231, v27, v31\n\t"
        "v_cvt_pk_bf16_f32 v230, v19, v23\n\t"
        "v_mfma_f32_16x16x32_bf16 v[60:63], v[160:163], v[184:187], v[60:63]\n\t"
        ".Lwp18:\n\t"
        "s_cbranch_execz .Lwp97\n\t"
        "v_mfma_f32_16x16x32_bf16 v[60:63], v[132:135], v[144:147], v[60:63]\n\t"
        "global_store_dwordx2 %[oo3], v[230:231], %[ob0] offset:128 nt\n\t"
        "s_waitcnt lgkmcnt(0)\n\t"
        "v_mfma_f32_16x16x32_bf16 v[80:83], v[128:131], v[140:143], v[80:83]\n\t"
        "v_mfma_f32_16x16x32_bf16 v[80:83], v[152:155], v[196:199], v[80:83]\n\t"
        "ds_read_b128 v[132:135], v232 offset:38912\n\t"
        "v_cvt_pk_bf16_f32 v224, v48, v52\n\t"
        "v_mfma_f32_16x16x32_bf16 v[80:83], v[180:183], v[200:203], v[80:83]\n\t"
        "v_mfma_f32_16x16x32_bf16 v[80:83], v[188:191], v[204:207], v[80:83]\n\t"
        "ds_read_b128 v[160:163], v233 offset:38912\n\t"
        "v_cvt_pk_bf16_f32 v225, v56, v60\n\t"
        "v_mfma_f32_16x16x32_bf16 v[84:87], v[188:191], v[208:211], v[84:87]\n\t"
        "global_store_dwordx2 %[oo0], v[224:225], %[ob1] offset:128 nt\n\t"
        "v_mfma_f32_16x16x32_bf16 v[84:87], v[180:183], v[212:215], v[84:87]\n\t"
        "ds_read_b128 v[164:167], %[xb0] offset:6144\n\t"
        "v_cvt_pk_bf16_f32 v227, v57, v61\n\t"
        "v_mfma_f32_16x16x32_bf16 v[84:87], v[152:155], v[216:219], v[84:87]\n\t"
        "v_cvt_pk_bf16_f32 v226, v49, v53\n\t"
        "v_mfma_f32_16x16x32_bf16 v[84:87], v[128:131], v[136:139], v[84:87]\n\t"
        "ds_read_b128 v[192:195], %[xb1] offset:6144\n\t"
        "global_store_dwordx2 %[oo1], v[226:227], %[ob1] offset:128 nt\n\t"
        "v_mfma_f32_16x16x32_bf16 v[88:91], v[128:131], v[148:151], v[88:91]\n\t"
        "v_mfma_f32_16x16x32_bf16 v[88:91], v[152:155], v[156:159], v[88:91]\n\t"
        "v_cvt_pk_bf16_f32 v229, v58, v62\n\t"
        "v_cvt_pk_bf16_f32 v228, v50, v54\n\t"
        "v_mfma_f32_16x16x32_bf16 v[88:91], v[180:183], v[168:171], v[88:91]\n\t"
        "v_mfma_f32_16x16x32_bf16 v[88:91], v[188:191], v[220:223], v[88:91]\n\t"
        "global_store_dwordx2 %[oo2], v[228:229], %[ob1] offset:128 nt\n\t"
        "v_mfma_f32_16x16x32_bf16 v[92:95], v[188:191], v[172:175], v[92:95]\n\t"
        "v_mfma_f32_16x16x32_bf16 v[92:95], v[180:183], v[176:179], v[92:95]\n\t"
        "v_cvt_pk_bf16_f32 v231, v59, v63\n\t"
        "v_cvt_pk_bf16_f32 v230, v51, v55\n\t"
        "v_mfma_f32_16x16x32_bf16 v[92:95], v[152:155], v[184:187], v[92:95]\n\t"
        "v_mfma_f32_16x16x32_bf16 v[92:95], v[128:131], v[144:147], v[92:95]\n\t"
        "global_store_dwordx2 %[oo3], v[230:231], %[ob1] offset:128 nt\n\t"
        "s_waitcnt lgkmcnt(0)\n\t"
        "v_mfma_f32_16x16x32_bf16 v[112:115], v[132:135], v[140:143], v[112:115]\n\t"
        "v_mfma_f32_16x16x32_bf16 v[112:115], v[160:163], v[196:199], v[112:115]\n\t"
        "v_cvt_pk_bf16_f32 v224, v80, v84\n\t"
        "v_mfma_f32_16x16x32_bf16 v[112:115], v[164:167], v[200:203], v[112:115]\n\t"
        "v_mfma_f32_16x16x32_bf16 v[112:115], v[192:195], v[204:207], v[112:115]\n\t"
        "v_cvt_pk_bf16_f32 v225, v88, v92\n\t"
        "global_store_dwordx2 %[oo0], v[224:225], %[ob2] offset:128 nt\n\t"
        "v_mfma_f32_16x16x32_bf16 v[116:119], v[192:195], v[208:211], v[116:119]\n\t"
        "v_mfma_f32_16x16x32_bf16 v[116:119], v[164:167], v[212:215], v[116:119]\n\t"
        "v_cvt_pk_bf16_f32 v227, v89, v93\n\t"
        "v_cvt_pk_bf16_f32 v226, v81, v85\n\t"
        "v_mfma_f32_16x16x32_bf16 v[116:119], v[160:163], v[216:219], v[116:119]\n\t"
        "v_mfma_f32_16x16x32_bf16 v[116:119], v[132:135], v[136:139], v[116:119]\n\t"
        "global_store_dwordx2 %[oo1], v[226:227], %[ob2] offset:128 nt\n\t"
        "v_mfma_f32_16x16x32_bf16 v[120:123], v[132:135], v[148:151], v[120:123]\n\t"
        "v_mfma_f32_16x16x32_bf16 v[120:123], v[160:163], v[156:159], v[120:123]\n\t"
        "v_cvt_pk_bf16_f32 v229, v90, v94\n\t"
        "v_cvt_pk_bf16_f32 v228, v82, v86\n\t"
        "v_mfma_f32_16x16x32_bf16 v[120:123], v[164:167], v[168:171], v[120:123]\n\t"
        "v_mfma_f32_16x16x32_bf16 v[120:123], v[192:195], v[220:223], v[120:123]\n\t"
        "global_store_dwordx2 %[oo2], v[228:229], %[ob2] offset:128 nt\n\t"
        "v_mfma_f32_16x16x32_bf16 v[124:127], v[192:195], v[172:175], v[124:127]\n\t"
        "v_mfma_f32_16x16x32_bf16 v[124:127], v[164:167], v[176:179], v[124:127]\n\t"
        "v_cvt_pk_bf16_f32 v230, v113, v117\n\t"
        "v_cvt_pk_bf16_f32 v224, v112, v116\n\t"
        "v_mfma_f32_16x16x32_bf16 v[124:127], v[160:163], v[184:187], v[124:127]\n\t"
        "v_cvt_pk_bf16_f32 v227, v91, v95\n\t"
        "v_cvt_pk_bf16_f32 v226, v83, v87\n\t"
        "v_mfma_f32_16x16x32_bf16 v[124:127], v[132:135], v[144:147], v[124:127]\n\t"
        "v_cvt_pk_bf16_f32 v228, v115, v119\n\t"
        "v_cvt_pk_bf16_f32 v128, v114, v118\n\t"
        "global_store_dwordx2 %[oo3], v[226:227], %[ob2] offset:128 nt\n\t"
        "s_nop 7\n\t"
        "v_cvt_pk_bf16_f32 v225, v120, v124\n\t"
        "global_store_dwordx2 %[oo0], v[224:225], %[ob3] offset:128 nt\n\t"
        "v_cvt_pk_bf16_f32 v231, v121, v125\n\t"
        "global_store_dwordx2 %[oo1], v[230:231], %[ob3] offset:128 nt\n\t"
        "v_cvt_pk_bf16_f32 v129, v122, v126\n\t"
        "global_store_dwordx2 %[oo2], v[128:129], %[ob3] offset:128 nt\n\t"
        "v_cvt_pk_bf16_f32 v229, v123, v127\n\t"
        "global_store_dwordx2 %[oo3], v[228:229], %[ob3] offset:128 nt\n\t"
        :
        : [xb0] "v"(xb0), [xb1] "v"(xb1), [ba] "v"(ba), [oo0] "v"(oo[0]), [oo1] "v"(oo[1]), [oo2] "v"(oo[2]), [oo3] "v"(oo[3]),
          [ob0] "s"(ob[0]), [ob1] "s"(ob[1]), [ob2] "s"(ob[2]), [ob3] "s"(ob[3]), [w] "s"(w)
        : "scc", "memory", "s60", "s61", "s62", "s63", "s64", "s65", "s66", "s67", "v0", "v1", "v2", "v3", "v4", "v5", "v6", "v7", "v8", "v9", "v10", "v11", "v12", "v13", "v14", "v15", "v16", "v17", "v18", "v19", "v20", "v21", "v22", "v23", "v24", "v25", "v26", "v27", "v28", "v29", "v30", "v31", "v32", "v33", "v34", "v35", "v36", "v37", "v38", "v39", "v40", "v41", "v42", "v43", "v44", "v45", "v46", "v47", "v48", "v49", "v50", "v51", "v52", "v53", "v54", "v55", "v56", "v57", "v58", "v59", "v60", "v61", "v62", "v63", "v64", "v65", "v66", "v67", "v68", "v69", "v70", "v71", "v72", "v73", "v74", "v75", "v76", "v77", "v78", "v79", "v80", "v81", "v82", "v83", "v84", "v85", "v86", "v87", "v88", "v89", "v90", "v91", "v92", "v93", "v94", "v95", "v96", "v97", "v98", "v99", "v100", "v101", "v102", "v103", "v104", "v105", "v106", "v107", "v108", "v109", "v110", "v111", "v112", "v113", "v114", "v115", "v116", "v117", "v118", "v119", "v120", "v121", "v122", "v123", "v124", "v125", "v126", "v127", "v128", "v129", "v130", "v131", "v132", "v133", "v134", "v135", "v136", "v137", "v138", "v139", "v140", "v141", "v142", "v143", "v144", "v145", "v146", "v147", "v148", "v149", "v150", "v151", "v152", "v153", "v154", "v155", "v156", "v157", "v158", "v159", "v160", "v161", "v162", "v163", "v164", "v165", "v166", "v167", "v168", "v169", "v170", "v171", "v172", "v173", "v174", "v175", "v176", "v177", "v178", "v179", "v180", "v181", "v182", "v183", "v184", "v185", "v186", "v187", "v188", "v189", "v190", "v191", "v192", "v193", "v194", "v195", "v196", "v197", "v198", "v199", "v200", "v201", "v202", "v203", "v204", "v205", "v206", "v207", "v208", "v209", "v210", "v211", "v212", "v213", "v214", "v215", "v216", "v217", "v218", "v219", "v220", "v221", "v222", "v223", "v224", "v225", "v226", "v227", "v228", "v229", "v230", "v231", "v232", "v233", "v234", "v235");
}

extern "C" void kernel_launch(void* const* d_in, const int* in_sizes, int n_in,
                              void* d_out, int out_size, void* d_ws, size_t ws_size,
                              hipStream_t stream) {
    const u16* x = (const u16*)d_in[0];
    const int* qw = (const int*)d_in[1];
    const u16* lut = (const u16*)d_in[2];
    u16* out = (u16*)d_out;
    (void)hipFuncSetAttribute((const void*)fused_kernel, hipFuncAttributeMaxDynamicSharedMemorySize, LDS_BYTES);
    fused_kernel<<<(M_ / BM) * (N_ / BN) + NPF, 512, LDS_BYTES, stream>>>(x, qw, lut, out, (char*)d_ws);
}
```

```cpp
#include <hip/hip_runtime.h>
#include <hip/hip_bf16.h>

typedef unsigned short u16;
typedef unsigned int u32;
using i32x4  = __attribute__((ext_vector_type(4))) int;
using u32x4  = __attribute__((ext_vector_type(4))) unsigned int;

constexpr int M_ = 2048, K_ = 1024, N_ = 3584;
constexpr int BM = 256, BN = 128;
constexpr int NPF = 32;
#define CPF 116
#define CPFB 32
#define PACE 0
constexpr int XS = BM * 64 * 2;
constexpr int BS = BN * 32 * 2;
constexpr int OFF_B = 3 * XS;
constexpr int WSL = 2 * BS;
constexpr int OFF_L = OFF_B + 3 * WSL;
constexpr int OFF_D = OFF_L + 16 * BN * 2;
constexpr int WS_F = 28 * 256, WS_W = 65536;
constexpr unsigned TW = 28 * 16384;
constexpr int LDS_BYTES = OFF_D + 1024;

#define TSEQ(mt) (0x76532140u ^ ((u32)((mt) >> 1) * 0x11111111u))
#define BAR() __builtin_amdgcn_s_barrier()
#define SB() __builtin_amdgcn_sched_barrier(0)
#define PAD(id, nx) asm volatile(".Lwp%c0:\n\ts_cbranch_execz .Lwp%c1" :: "i"(id), "i"(nx))

__global__ __launch_bounds__(512, 2) void fused_kernel(const u16* __restrict__ x_, const int* __restrict__ qw_,
                                                       const u16* __restrict__ lut_, u16* __restrict__ out_, char* ws_) {
    extern __shared__ __attribute__((aligned(16))) char smem[];
    typedef unsigned long u64x4 __attribute__((ext_vector_type(4)));
    u64x4 ka;
    unsigned long kp[5], kw;
    asm volatile("s_load_dwordx8 %0, %2, 0x0\n\ts_load_dwordx2 %1, %2, 0x20" : "=&s"(ka), "=&s"(kw) : "s"(__builtin_amdgcn_kernarg_segment_ptr()));
    {
        unsigned long pc0, ex;
        u32 co;
        asm volatile("s_getpc_b64 %0\n\t"
                     "s_cmp_lt_u32 %4, %5\n\t"
                     "s_cselect_b32 m0, %6, 0\n\t"
                     "v_cmp_gt_u32 vcc, m0, %3\n\t"
                     "s_and_b32 m0, %4, 24\n\t"
                     "s_lshl_b32 m0, m0, 4\n\t"
                     "v_lshl_add_u32 %2, %3, 9, m0\n\t"
                     "s_mov_b32 m0, %7\n\t"
                     "s_and_saveexec_b64 %1, vcc\n\t"
                     "global_load_lds_dword %2, %0\n\t"
                     "s_mov_b64 exec, %1"
                     : "=&s"(pc0), "=&s"(ex), "=&v"(co) : "v"(threadIdx.x), "s"(blockIdx.x), "i"(CPFB), "i"(CPF / 4), "i"(OFF_D) : "vcc", "scc", "memory");
    }
#define KWAIT() do { SB(); asm volatile("s_waitcnt lgkmcnt(0)\n\ts_mov_b64 %0, %5\n\ts_mov_b64 %1, %6\n\ts_mov_b64 %2, %7\n\ts_mov_b64 %3, %8\n\ts_mov_b64 %4, %9" \
                     : "=&s"(kp[0]), "=&s"(kp[1]), "=&s"(kp[2]), "=&s"(kp[3]), "=&s"(kp[4]) : "s"(ka[0]), "s"(ka[1]), "s"(ka[2]), "s"(ka[3]), "s"(kw) : "memory"); SB(); } while (0)
#define KPTR(i) (kp[i])
    const int tid = threadIdx.x;
    const int w = __builtin_amdgcn_readfirstlane(tid >> 6);
    const int lane = tid & 63;
    const int bid = (int)blockIdx.x - NPF;
    const int xcd = blockIdx.x & 7, idx = bid >> 3;
    if (bid < 0) {
        const int pw = (blockIdx.x >> 3) * 8 + w;
        KWAIT();
        const char* qb = (const char*)KPTR(1) + (xcd >> 1) * 896 * 4 + (long)((xcd & 1) * 2) * (64 * N_ * 4);
        const char* xb = (const char*)KPTR(0) + (long)(xcd & 1) * 1024 * 2048;
        const u32 dst = (u32)(size_t)(smem) + w * 256;
        {
            const int m = (xcd & 1) * 4 + (blockIdx.x >> 3);
            const u32 off = (u32)((m * 256 + (w & 3) * 64 + lane) * 2048 + ((TSEQ(m) >> (w & 4)) & 15) * 128);
            asm volatile("s_mov_b32 m0, %2\n\ts_nop 0\n\tglobal_load_lds_dword %0, %1" :: "v"(off), "s"(KPTR(0)), "s"(dst) : "memory");
        }
#pragma unroll 1
        for (int G = pw; G < 168; G += 32) {
            int kind = 0, arg = 0, e = 0;
            if (G >= 0) { kind = 0; arg = 2; e = G - 0 + (xcd >> 1) * 4; }
            if (G >= 4) { kind = 0; arg = 3; e = G - 4 + (xcd >> 1) * 4; }
            if (G >= 8) { kind = 0; arg = 4; e = G - 8 + (xcd >> 1) * 4; }
            if (G >= 12) { kind = 1; arg = 8; e = G - 12; }
            if (G >= 32) { kind = 0; arg = 5; e = G - 32 + (xcd >> 1) * 4; }
            if (G >= 36) { kind = 1; arg = 8; e = G - 16; }
            if (G >= 64) { kind = 1; arg = 8; e = G - 16; }
            if (G >= 72) { kind = 0; arg = 6; e = G - 72 + (xcd >> 1) * 4; }
            if (G >= 76) { kind = 0; arg = 7; e = G - 76 + (xcd >> 1) * 4; }
            if (G >= 80) { kind = 1; arg = 12; e = G - 80; }
            if (G >= 96) { kind = 0; arg = 8; e = G - 96 + (xcd >> 1) * 4; }
            if (G >= 100) { kind = 0; arg = 9; e = G - 100 + (xcd >> 1) * 4; }
            if (G >= 104) { kind = 1; arg = 12; e = G - 88; }
            if (G >= 128) { kind = 0; arg = 10; e = G - 128 + (xcd >> 1) * 4; }
            if (G >= 132) { kind = 1; arg = 12; e = G - 92; }
            if (G >= 148) { kind = 0; arg = 11; e = G - 148 + (xcd >> 1) * 4; }
            if (G >= 152) { kind = 0; arg = 12; e = G - 152 + (xcd >> 1) * 4; }
            if (G >= 156) { kind = 0; arg = 13; e = G - 156 + (xcd >> 1) * 4; }
            if (G >= 160) { kind = 0; arg = 14; e = G - 160 + (xcd >> 1) * 4; }
            if (G >= 164) { kind = 0; arg = 15; e = G - 164 + (xcd >> 1) * 4; }
            asm volatile("s_waitcnt vmcnt(%0)" :: "i"(PACE) : "memory");
            if (kind) {
                const int L = e * 64 + lane;
                const int r = L / 28, c = L - r * 28;
                const u32 off = (u32)((arg * 64 + r) * (N_ * 4) + c * 128);
                asm volatile("s_mov_b32 m0, %2\n\ts_nop 0\n\tglobal_load_lds_dword %0, %1" :: "v"(off), "s"(qb), "s"(dst) : "memory");
            } else {
                const int t = arg >= 8 ? arg : (int)((TSEQ((xcd & 1) * 4 + (e >> 2)) >> (4 * arg)) & 15);
                const u32 off = (u32)((e * 64 + lane) * 2048 + t * 128);
                asm volatile("s_mov_b32 m0, %2\n\ts_nop 0\n\tglobal_load_lds_dword %0, %1" :: "v"(off), "s"(xb), "s"(dst) : "memory");
            }
        }
        asm volatile("s_waitcnt vmcnt(0)" ::: "memory");
        return;
    }
    const int mt = (xcd & 1) * 4 + (idx & 3);
    const int nt = (xcd >> 1) * 7 + (idx >> 2);
    const int row0 = mt * BM, col0 = nt * BN;

    if (__builtin_expect(w >= 4, 0)) {
        int lane;
        asm volatile("v_mbcnt_lo_u32_b32 %0, -1, 0\n\tv_mbcnt_hi_u32_b32 %0, -1, %0" : "=v"(lane));
        const int p = w - 4;
        const int l = lane & 31, h = lane >> 5;
        const int g = lane >> 4;
        u32 xoff[4];
#pragma unroll
        for (int i = 0; i < 4; ++i)
            xoff[i] = (u32)(((row0 + p * 64 + i * 8 + (lane >> 3)) * K_ + (((lane & 7) ^ ((i & 1) * 4 + g)) * 8)) * 2 - i * 1024);
        const u32 ldsx = (u32)(size_t)(smem) + p * 8192;
        u32 qo = (u32)(((p * 16 + h * 8) * N_ + col0 + l * 4) * 4);
        u32 lutl = (u32)(size_t)(smem) + OFF_L + l * 4;
        u32 bdst = (u32)(size_t)(smem) + OFF_B + (p >> 1) * BS + ((2 * p + h) & 3) * 2048 + ((l >> 4) * 64 + (l & 15)) * 16;
        const int i2 = (mt >> 1), h2 = mt & 1;
        const unsigned long tseq = 0xFEDCBA9800000000ul | TSEQ(mt);
        const u32 ldsw = (u32)(size_t)(smem) + OFF_B + p * 4096;
        u32 wv = (u32)(nt * 16384 + p * 4096 + lane * 16);
        u32 wo = (u32)(nt * 16384 + p * 4096 + h * 2048 + (l >> 4) * 1024 + (l & 15) * 16);
        u32 po = (u32)(WS_F + (nt * 4 + p) * 256 + lane * 4);
        u32 fo0 = po + (u32)(i2 * 16 + h2 * 8);
        u32 no = (u32)(nt * 256 + (lane & 1) * 4);
        u32 nvl = no;
        KWAIT();
        const char* st1 = (const char*)KPTR(4) + WS_W + (long)(i2 + 4) * TW + h2 * 512;
        const char* qa1 = (const char*)KPTR(1);
        const int tb = 8 + (mt >> 1), kg = h2 * 4 + p;
        const char* qbg = (const char*)KPTR(1) + (long)(tb * 64 + kg * 8 - p * 16) * (N_ * 4);
        const char* stb = (const char*)KPTR(4) + WS_W + (long)tb * TW + (kg * 2048 - p * 4096);
        u32 fob = po + (u32)(((kg >> 1) - p) * 256 + tb * 16 + (kg & 1) * 8);
        asm volatile("" : "+v"(wo), "+v"(fo0), "+v"(fob), "+v"(wv), "+v"(po), "+v"(bdst), "+v"(lutl), "+v"(qo));
        asm volatile("" : "+v"(xoff[0]), "+v"(xoff[1]), "+v"(xoff[2]), "+v"(xoff[3]));
        asm volatile("" : "+s"(st1), "+s"(qa1), "+s"(qbg), "+s"(stb));
        {
            unsigned long ex_;
            asm volatile("s_mov_b64 %0, exec\n\ts_mov_b64 exec, 0\n\t"
                         "s_cmp_eq_u32 %1, 0\n\ts_cbranch_scc1 .Lwp30\n\t"
                         "s_cmp_eq_u32 %1, 1\n\ts_cbranch_scc1 .Lwp31\n\t"
                         "s_cmp_eq_u32 %1, 2\n\ts_cbranch_scc1 .Lwp32\n\t"
                         "s_branch .Lwp33\n\t"
                         ".Lwp98:\n\ts_mov_b64 exec, %0" : "=&s"(ex_) : "s"(p) : "scc", "memory");
        }
        asm volatile(
            "s_mov_b32 s73, 65536\n\t"
            "s_mov_b32 s72, 0\n\t"
            "s_mov_b64 s[84:85], %[ws]\n\t"
            "s_add_u32 s84, s84, 65536\n\t"
            "s_addc_u32 s85, s85, 0\n\t"
            "s_mov_b64 s[56:57], %[tseq]\n\t"
            "s_bfe_u32 s40, s56, 262144\n\t"
            "s_bfe_u32 s41, s56, 262148\n\t"
            "s_bfe_u32 s42, s56, 262152\n\t"
            "s_bfe_u32 s43, s56, 262156\n\t"
            "s_bfe_u32 s44, s56, 262160\n\t"
            "s_bfe_u32 s45, s56, 262164\n\t"
            "s_bfe_u32 s46, s56, 262168\n\t"
            "s_bfe_u32 s47, s56, 262172\n\t"
            "s_bfe_u32 s48, s57, 262144\n\t"
            "s_bfe_u32 s49, s57, 262148\n\t"
            "s_bfe_u32 s50, s57, 262152\n\t"
            "s_bfe_u32 s51, s57, 262156\n\t"
            "s_bfe_u32 s52, s57, 262160\n\t"
            "s_bfe_u32 s53, s57, 262164\n\t"
            "s_bfe_u32 s54, s57, 262168\n\t"
            "s_bfe_u32 s55, s57, 262172\n\t"
            "global_load_dword %[nvl], %[nvl], %[ws] sc1\n\t"
            "global_load_dword v192, %[po], %[ws] sc1\n\t"
            "s_barrier\n\t"
            ".Lwp30:\n\t"
            "s_cbranch_execz .Lwp98\n\t"
            "s_add_u32 m0, %[ldsx], 0\n\t"
            "s_lshl_b32 s76, s40, 7\n\t"
            "s_add_u32 s68, %[xpl], s76\n\t"
            "s_addc_u32 s69, %[xph], 0\n\t"
            "global_load_lds_dwordx4 %[x0], s[68:69] offset:0\n\t"
            "global_load_lds_dwordx4 %[x1], s[68:69] offset:1024\n\t"
            "global_load_lds_dwordx4 %[x2], s[68:69] offset:2048\n\t"
            "global_load_lds_dwordx4 %[x3], s[68:69] offset:3072\n\t"
            "s_add_u32 m0, %[ldsx], 4096\n\t"
            "s_lshl_b32 s76, s40, 7\n\t"
            "s_add_u32 s76, s76, 65536\n\t"
            "s_add_u32 s68, %[xpl], s76\n\t"
            "s_addc_u32 s69, %[xph], 0\n\t"
            "global_load_lds_dwordx4 %[x0], s[68:69] offset:0\n\t"
            "global_load_lds_dwordx4 %[x1], s[68:69] offset:1024\n\t"
            "global_load_lds_dwordx4 %[x2], s[68:69] offset:2048\n\t"
            "global_load_lds_dwordx4 %[x3], s[68:69] offset:3072\n\t"
            "s_mul_i32 s76, s41, 917504\n\t"
            "s_mov_b64 s[88:89], %[qa1]\n\t"
            "s_add_u32 s88, s88, s76\n\t"
            "s_addc_u32 s89, s89, 0\n\t"
            "global_load_dwordx4 v[128:131], %[qo], s[88:89]\n\t"
            "s_add_u32 s88, s88, 14336\n\t"
            "s_addc_u32 s89, s89, 0\n\t"
            "global_load_dwordx4 v[132:135], %[qo], s[88:89]\n\t"
            "s_add_u32 s88, s88, 14336\n\t"
            "s_addc_u32 s89, s89, 0\n\t"
            "global_load_dwordx4 v[136:139], %[qo], s[88:89]\n\t"
            "s_add_u32 s88, s88, 14336\n\t"
            "s_addc_u32 s89, s89, 0\n\t"
            "global_load_dwordx4 v[140:143], %[qo], s[88:89]\n\t"
            "s_add_u32 s88, s88, 14336\n\t"
            "s_addc_u32 s89, s89, 0\n\t"
            "global_load_dwordx4 v[144:147], %[qo], s[88:89]\n\t"
            "s_add_u32 s88, s88, 14336\n\t"
            "s_addc_u32 s89, s89, 0\n\t"
            "global_load_dwordx4 v[148:151], %[qo], s[88:89]\n\t"
            "s_add_u32 s88, s88, 14336\n\t"
            "s_addc_u32 s89, s89, 0\n\t"
            "global_load_dwordx4 v[152:155], %[qo], s[88:89]\n\t"
            "s_add_u32 s88, s88, 14336\n\t"
            "s_addc_u32 s89, s89, 0\n\t"
            "global_load_dwordx4 v[156:159], %[qo], s[88:89]\n\t"
            "s_waitcnt vmcnt(8)\n\t"
            "s_barrier\n\t"
            ".Lwp31:\n\t"
            "s_cbranch_execz .Lwp98\n\t"
            "s_lshl_b32 s76, %[h2], 9\n\t"
            "v_add_u32 v193, s76, %[bd]\n\t"
            "ds_read_b128 v[196:199], v193\n\t"
            "ds_read_b128 v[200:203], v193 offset:256\n\t"
            "s_mov_b64 s[70:71], %[st1]\n\t"
            "s_sub_u32 s70, s70, 1835008\n\t"
            "s_subb_u32 s71, s71, 0\n\t"
            "s_waitcnt lgkmcnt(0)\n\t"
            "global_store_dwordx4 %[wo], v[196:199], s[70:71] sc1\n\t"
            "global_store_dwordx4 %[wo], v[200:203], s[70:71] offset:256 sc1\n\t"
            "s_mov_b32 s76, 1058696237\n\t"
            "v_mul_lo_u32 %[nvl], %[nvl], s76\n\t"
            "v_add_u32 %[nvl], 1535021219, %[nvl]\n\t"
            "s_setprio 1\n\t"
            "s_waitcnt vmcnt(9)\n\t"
            "v_lshl_add_u32 v160, v128, 8, %[lutl]\n\t"
            "ds_read_u16 v160, v160 offset:0\n\t"
            "v_lshl_add_u32 v168, v129, 8, %[lutl]\n\t"
            "ds_read_u16 v168, v168 offset:2\n\t"
            "v_lshl_add_u32 v176, v130, 8, %[lutl]\n\t"
            "ds_read_u16 v176, v176 offset:128\n\t"
            "v_lshl_add_u32 v184, v131, 8, %[lutl]\n\t"
            "ds_read_u16 v184, v184 offset:130\n\t"
            "s_add_u32 m0, %[ldsx], 32768\n\t"
            "s_lshl_b32 s76, s41, 7\n\t"
            "s_add_u32 s68, %[xpl], s76\n\t"
            "s_addc_u32 s69, %[xph], 0\n\t"
            "global_load_lds_dwordx4 %[x0], s[68:69] offset:0\n\t"
            "s_waitcnt vmcnt(9)\n\t"
            "v_lshl_add_u32 v161, v132, 8, %[lutl]\n\t"
            "ds_read_u16 v161, v161 offset:0\n\t"
            "v_lshl_add_u32 v169, v133, 8, %[lutl]\n\t"
            "ds_read_u16 v169, v169 offset:2\n\t"
            "v_lshl_add_u32 v177, v134, 8, %[lutl]\n\t"
            "ds_read_u16 v177, v177 offset:128\n\t"
            "v_lshl_add_u32 v185, v135, 8, %[lutl]\n\t"
            "ds_read_u16 v185, v185 offset:130\n\t"
            "global_load_lds_dwordx4 %[x1], s[68:69] offset:1024\n\t"
            "s_waitcnt vmcnt(9)\n\t"
            "v_lshl_add_u32 v162, v136, 8, %[lutl]\n\t"
            "ds_read_u16 v162, v162 offset:0\n\t"
            "v_lshl_add_u32 v170, v137, 8, %[lutl]\n\t"
            "ds_read_u16 v170, v170 offset:2\n\t"
            "v_lshl_add_u32 v178, v138, 8, %[lutl]\n\t"
            "ds_read_u16 v178, v178 offset:128\n\t"
            "v_lshl_add_u32 v186, v139, 8, %[lutl]\n\t"
            "ds_read_u16 v186, v186 offset:130\n\t"
            "global_load_lds_dwordx4 %[x2], s[68:69] offset:2048\n\t"
            "s_waitcnt vmcnt(9)\n\t"
            "v_lshl_add_u32 v163, v140, 8, %[lutl]\n\t"
            "ds_read_u16 v163, v163 offset:0\n\t"
            "v_lshl_add_u32 v171, v141, 8, %[lutl]\n\t"
            "ds_read_u16 v171, v171 offset:2\n\t"
            "v_lshl_add_u32 v179, v142, 8, %[lutl]\n\t"
            "ds_read_u16 v179, v179 offset:128\n\t"
            "v_lshl_add_u32 v187, v143, 8, %[lutl]\n\t"
            "ds_read_u16 v187, v187 offset:130\n\t"
            "global_load_lds_dwordx4 %[x3], s[68:69] offset:3072\n\t"
            "s_waitcnt vmcnt(9)\n\t"
            "v_lshl_add_u32 v164, v144, 8, %[lutl]\n\t"
            "ds_read_u16 v164, v164 offset:0\n\t"
            "v_lshl_add_u32 v172, v145, 8, %[lutl]\n\t"
            "ds_read_u16 v172, v172 offset:2\n\t"
            "v_lshl_add_u32 v180, v146, 8, %[lutl]\n\t"
            "ds_read_u16 v180, v180 offset:128\n\t"
            "v_lshl_add_u32 v188, v147, 8, %[lutl]\n\t"
            "ds_read_u16 v188, v188 offset:130\n\t"
            "s_add_u32 m0, %[ldsx], 36864\n\t"
            "s_lshl_b32 s76, s41, 7\n\t"
            "s_add_u32 s76, s76, 65536\n\t"
            "s_add_u32 s68, %[xpl], s76\n\t"
            "s_addc_u32 s69, %[xph], 0\n\t"
            "global_load_lds_dwordx4 %[x0], s[68:69] offset:0\n\t"
            "s_waitcnt vmcnt(9)\n\t"
            "v_lshl_add_u32 v165, v148, 8, %[lutl]\n\t"
            "ds_read_u16 v165, v165 offset:0\n\t"
            "v_lshl_add_u32 v173, v149, 8, %[lutl]\n\t"
            "ds_read_u16 v173, v173 offset:2\n\t"
            "v_lshl_add_u32 v181, v150, 8, %[lutl]\n\t"
            "ds_read_u16 v181, v181 offset:128\n\t"
            "v_lshl_add_u32 v189, v151, 8, %[lutl]\n\t"
            "ds_read_u16 v189, v189 offset:130\n\t"
            "global_load_lds_dwordx4 %[x1], s[68:69] offset:1024\n\t"
            "s_waitcnt vmcnt(9)\n\t"
            "v_lshl_add_u32 v166, v152, 8, %[lutl]\n\t"
            "ds_read_u16 v166, v166 offset:0\n\t"
            "v_lshl_add_u32 v174, v153, 8, %[lutl]\n\t"
            "ds_read_u16 v174, v174 offset:2\n\t"
            "v_lshl_add_u32 v182, v154, 8, %[lutl]\n\t"
            "ds_read_u16 v182, v182 offset:128\n\t"
            "v_lshl_add_u32 v190, v155, 8, %[lutl]\n\t"
            "ds_read_u16 v190, v190 offset:130\n\t"
            "global_load_lds_dwordx4 %[x2], s[68:69] offset:2048\n\t"
            "s_waitcnt vmcnt(9)\n\t"
            "v_lshl_add_u32 v167, v156, 8, %[lutl]\n\t"
            "ds_read_u16 v167, v167 offset:0\n\t"
            "v_lshl_add_u32 v175, v157, 8, %[lutl]\n\t"
            "ds_read_u16 v175, v175 offset:2\n\t"
            "v_lshl_add_u32 v183, v158, 8, %[lutl]\n\t"
            "ds_read_u16 v183, v183 offset:128\n\t"
            "v_lshl_add_u32 v191, v159, 8, %[lutl]\n\t"
            "ds_read_u16 v191, v191 offset:130\n\t"
            "global_load_lds_dwordx4 %[x3], s[68:69] offset:3072\n\t"
            "s_setprio 0\n\t"
            "s_waitcnt vmcnt(8)\n\t"
            "s_mov_b64 exec, 3\n\t"
            "global_store_dword %[fo0], %[nvl], %[ws] offset:0 sc1\n\t"
            "s_mov_b64 exec, -1\n\t"
            "s_waitcnt lgkmcnt(3)\n\t"
            "v_lshl_or_b32 v128, v161, 16, v160\n\t"
            "v_lshl_or_b32 v129, v163, 16, v162\n\t"
            "v_lshl_or_b32 v130, v165, 16, v164\n\t"
            "v_lshl_or_b32 v131, v167, 16, v166\n\t"
            "ds_write_b128 %[bd], v[128:131] offset:16384\n\t"
            "s_waitcnt lgkmcnt(3)\n\t"
            "v_lshl_or_b32 v132, v169, 16, v168\n\t"
            "v_lshl_or_b32 v133, v171, 16, v170\n\t"
            "v_lshl_or_b32 v134, v173, 16, v172\n\t"
            "v_lshl_or_b32 v135, v175, 16, v174\n\t"
            "ds_write_b128 %[bd], v[132:135] offset:16640\n\t"
            "s_waitcnt lgkmcnt(3)\n\t"
            "v_lshl_or_b32 v136, v177, 16, v176\n\t"
            "v_lshl_or_b32 v137, v179, 16, v178\n\t"
            "v_lshl_or_b32 v138, v181, 16, v180\n\t"
            "v_lshl_or_b32 v139, v183, 16, v182\n\t"
            "ds_write_b128 %[bd], v[136:139] offset:16896\n\t"
            "s_waitcnt lgkmcnt(3)\n\t"
            "v_lshl_or_b32 v140, v185, 16, v184\n\t"
            "v_lshl_or_b32 v141, v187, 16, v186\n\t"
            "v_lshl_or_b32 v142, v189, 16, v188\n\t"
            "v_lshl_or_b32 v143, v191, 16, v190\n\t"
            "ds_write_b128 %[bd], v[140:143] offset:17152\n\t"
            "s_add_u32 m0, %[ldsx], 65536\n\t"
            "s_lshl_b32 s76, s42, 7\n\t"
            "s_add_u32 s68, %[xpl], s76\n\t"
            "s_addc_u32 s69, %[xph], 0\n\t"
            "global_load_lds_dwordx4 %[x0], s[68:69] offset:0\n\t"
            "global_load_lds_dwordx4 %[x1], s[68:69] offset:1024\n\t"
            "global_load_lds_dwordx4 %[x2], s[68:69] offset:2048\n\t"
            "global_load_lds_dwordx4 %[x3], s[68:69] offset:3072\n\t"
            "s_add_u32 m0, %[ldsx], 69632\n\t"
            "s_lshl_b32 s76, s42, 7\n\t"
            "s_add_u32 s76, s76, 65536\n\t"
            "s_add_u32 s68, %[xpl], s76\n\t"
            "s_addc_u32 s69, %[xph], 0\n\t"
            "global_load_lds_dwordx4 %[x0], s[68:69] offset:0\n\t"
            "global_load_lds_dwordx4 %[x1], s[68:69] offset:1024\n\t"
            "global_load_lds_dwordx4 %[x2], s[68:69] offset:2048\n\t"
            "global_load_lds_dwordx4 %[x3], s[68:69] offset:3072\n\t"
            "s_waitcnt vmcnt(9) lgkmcnt(0)\n\t"
            "s_barrier\n\t"
            ".Lwp32:\n\t"
            "s_cbranch_execz .Lwp98\n\t"
            "global_load_dword v192, %[po], %[ws] sc1\n\t"
            "s_add_u32 m0, %[ldsx], 0\n\t"
            "s_lshl_b32 s76, s43, 7\n\t"
            "s_add_u32 s68, %[xpl], s76\n\t"
            "s_addc_u32 s69, %[xph], 0\n\t"
            "global_load_lds_dwordx4 %[x0], s[68:69] offset:0\n\t"
            "global_load_lds_dwordx4 %[x1], s[68:69] offset:1024\n\t"
            "global_load_lds_dwordx4 %[x2], s[68:69] offset:2048\n\t"
            "global_load_lds_dwordx4 %[x3], s[68:69] offset:3072\n\t"
            "s_add_u32 m0, %[ldsx], 4096\n\t"
            "s_lshl_b32 s76, s43, 7\n\t"
            "s_add_u32 s76, s76, 65536\n\t"
            "s_add_u32 s68, %[xpl], s76\n\t"
            "s_addc_u32 s69, %[xph], 0\n\t"
            "global_load_lds_dwordx4 %[x0], s[68:69] offset:0\n\t"
            "global_load_lds_dwordx4 %[x1], s[68:69] offset:1024\n\t"
            "global_load_lds_dwordx4 %[x2], s[68:69] offset:2048\n\t"
            "global_load_lds_dwordx4 %[x3], s[68:69] offset:3072\n\t"
            "global_load_dword v195, %[po], %[ws] sc1\n\t"
            "s_cmp_lg_u32 %[h2], 0\n\t"
            "s_cbranch_scc1 .Lzs0\n\t"
            "global_store_dwordx4 %[wo], v[128:131], %[st1] sc1\n\t"
            "global_store_dwordx4 %[wo], v[132:135], %[st1] offset:256 sc1\n\t"
            "s_branch .Lzt0\n\t"
            ".Lzs0:\n\t"
            "global_store_dwordx4 %[wo], v[136:139], %[st1] sc1\n\t"
            "global_store_dwordx4 %[wo], v[140:143], %[st1] offset:256 sc1\n\t"
            ".Lzt0:\n\t"
            "s_waitcnt vmcnt(11)\n\t"
            "s_lshl_b32 s76, s42, 2\n\t"
            ".Lzc1:\n\t"
            "v_cmp_eq_u32 vcc, v192, %[nvl]\n\t"
            "s_lshr_b64 s[74:75], vcc, s76\n\t"
            "s_and_b32 s74, s74, 15\n\t"
            "s_cmp_eq_u32 s74, 15\n\t"
            "s_cbranch_scc1 .Lzd1\n\t"
            "s_cmp_eq_u32 s73, 0\n\t"
            "s_cbranch_scc1 .Lzd1\n\t"
            "s_sub_u32 s73, s73, 1\n\t"
            "s_cmp_eq_u32 s72, 2\n\t"
            "s_cbranch_scc1 .Lze1\n\t"
            "s_mov_b32 s72, 2\n\t"
            "s_waitcnt vmcnt(2)\n\t"
            "v_mov_b32 v192, v195\n\t"
            "s_branch .Lzc1\n\t"
            ".Lze1:\n\t"
            "global_load_dword v192, %[po], %[ws] sc1\n\t"
            "s_waitcnt vmcnt(0)\n\t"
            "v_mov_b32 v195, v192\n\t"
            "s_branch .Lzc1\n\t"
            ".Lzd1:\n\t"
            "s_add_u32 m0, %[ldsw], 32768\n\t"
            "s_mul_i32 s76, s42, 458752\n\t"
            "s_add_u32 s70, s84, s76\n\t"
            "s_addc_u32 s71, s85, 0\n\t"
            "global_load_lds_dwordx4 %[wv], s[70:71] offset:0 sc1\n\t"
            "global_load_lds_dwordx4 %[wv], s[70:71] offset:1024 sc1\n\t"
            "global_load_lds_dwordx4 %[wv], s[70:71] offset:2048 sc1\n\t"
            "global_load_lds_dwordx4 %[wv], s[70:71] offset:3072 sc1\n\t"
            "s_waitcnt vmcnt(6)\n\t"
            "v_mov_b32 v192, v195\n\t"
            "s_lshl_b32 s76, s43, 2\n\t"
            ".Lzc2:\n\t"
            "v_cmp_eq_u32 vcc, v192, %[nvl]\n\t"
            "s_lshr_b64 s[74:75], vcc, s76\n\t"
            "s_and_b32 s74, s74, 15\n\t"
            "s_cmp_eq_u32 s74, 15\n\t"
            "s_cbranch_scc1 .Lzd2\n\t"
            "s_cmp_eq_u32 s73, 0\n\t"
            "s_cbranch_scc1 .Lzd2\n\t"
            "s_sub_u32 s73, s73, 1\n\t"
            "global_load_dword v192, %[po], %[ws] sc1\n\t"
            "s_waitcnt vmcnt(0)\n\t"
            "s_branch .Lzc2\n\t"
            ".Lzd2:\n\t"
            "s_add_u32 m0, %[ldsw], 0\n\t"
            "s_mul_i32 s76, s43, 458752\n\t"
            "s_add_u32 s70, s84, s76\n\t"
            "s_addc_u32 s71, s85, 0\n\t"
            "global_load_lds_dwordx4 %[wv], s[70:71] offset:0 sc1\n\t"
            "global_load_lds_dwordx4 %[wv], s[70:71] offset:1024 sc1\n\t"
            "global_load_lds_dwordx4 %[wv], s[70:71] offset:2048 sc1\n\t"
            "global_load_lds_dwordx4 %[wv], s[70:71] offset:3072 sc1\n\t"
            "s_waitcnt vmcnt(8)\n\t"
            "s_mov_b64 exec, 3\n\t"
            "global_store_dword %[fo0], %[nvl], %[ws] offset:64 sc1\n\t"
            "s_mov_b64 exec, -1\n\t"
            "s_waitcnt vmcnt(5)\n\t"
            "s_barrier\n\t"
            ".Lwp33:\n\t"
            "s_cbranch_execz .Lwp98\n\t"
            "v_mbcnt_lo_u32_b32 v193, -1, 0\n\t"
            "v_mbcnt_hi_u32_b32 v193, -1, v193\n\t"
            "v_lshrrev_b32 v193, 5, v193\n\t"
            "v_mul_u32_u24 v194, 57344, v193\n\t"
            "v_sub_u32 %[qo], %[qo], v194\n\t"
            "v_mul_u32_u24 v194, 1792, v193\n\t"
            "v_sub_u32 %[wo], %[wo], v194\n\t"
            "s_mov_b64 s[88:89], %[qbg]\n\t"
            "global_load_dwordx4 v[204:207], %[qo], s[88:89]\n\t"
            "s_add_u32 s88, s88, 14336\n\t"
            "s_addc_u32 s89, s89, 0\n\t"
            "global_load_dwordx4 v[208:211], %[qo], s[88:89]\n\t"
            "s_add_u32 s88, s88, 14336\n\t"
            "s_addc_u32 s89, s89, 0\n\t"
            "global_load_dwordx4 v[212:215], %[qo], s[88:89]\n\t"
            "s_add_u32 s88, s88, 14336\n\t"
            "s_addc_u32 s89, s89, 0\n\t"
            "global_load_dwordx4 v[216:219], %[qo], s[88:89]\n\t"
            "s_lshl_b32 s76, s44, 2\n\t"
            ".Lzc3:\n\t"
            "v_cmp_eq_u32 vcc, v192, %[nvl]\n\t"
            "s_lshr_b64 s[74:75], vcc, s76\n\t"
            "s_and_b32 s74, s74, 15\n\t"
            "s_cmp_eq_u32 s74, 15\n\t"
            "s_cbranch_scc1 .Lzd3\n\t"
            "s_cmp_eq_u32 s73, 0\n\t"
            "s_cbranch_scc1 .Lzd3\n\t"
            "s_sub_u32 s73, s73, 1\n\t"
            "global_load_dword v192, %[po], %[ws] sc1\n\t"
            "s_waitcnt vmcnt(0)\n\t"
            "s_branch .Lzc3\n\t"
            ".Lzd3:\n\t"
            "s_add_u32 m0, %[ldsw], 16384\n\t"
            "s_mul_i32 s76, s44, 458752\n\t"
            "s_add_u32 s70, s84, s76\n\t"
            "s_addc_u32 s71, s85, 0\n\t"
            "global_load_lds_dwordx4 %[wv], s[70:71] offset:0 sc1\n\t"
            "global_load_lds_dwordx4 %[wv], s[70:71] offset:1024 sc1\n\t"
            "global_load_lds_dwordx4 %[wv], s[70:71] offset:2048 sc1\n\t"
            "global_load_lds_dwordx4 %[wv], s[70:71] offset:3072 sc1\n\t"
            "s_add_u32 m0, %[ldsx], 32768\n\t"
            "s_lshl_b32 s76, s44, 7\n\t"
            "s_add_u32 s68, %[xpl], s76\n\t"
            "s_addc_u32 s69, %[xph], 0\n\t"
            "global_load_lds_dwordx4 %[x0], s[68:69] offset:0\n\t"
            "global_load_lds_dwordx4 %[x1], s[68:69] offset:1024\n\t"
            "global_load_lds_dwordx4 %[x2], s[68:69] offset:2048\n\t"
            "global_load_lds_dwordx4 %[x3], s[68:69] offset:3072\n\t"
            "s_add_u32 m0, %[ldsx], 36864\n\t"
            "s_lshl_b32 s76, s44, 7\n\t"
            "s_add_u32 s76, s76, 65536\n\t"
            "s_add_u32 s68, %[xpl], s76\n\t"
            "s_addc_u32 s69, %[xph], 0\n\t"
            "global_load_lds_dwordx4 %[x0], s[68:69] offset:0\n\t"
            "global_load_lds_dwordx4 %[x1], s[68:69] offset:1024\n\t"
            "global_load_lds_dwordx4 %[x2], s[68:69] offset:2048\n\t"
            "global_load_lds_dwordx4 %[x3], s[68:69] offset:3072\n\t"
            "s_waitcnt vmcnt(17)\n\t"
            "s_barrier\n\t"
            ".Lwp34:\n\t"
            "s_cbranch_execz .Lwp35\n\t"
            "global_load_dword v192, %[po], %[ws] sc1\n\t"
            "s_waitcnt vmcnt(13)\n\t"
            "s_setprio 0\n\t"
            "s_add_u32 m0, %[ldsx], 65536\n\t"
            "s_lshl_b32 s76, s45, 7\n\t"
            "s_add_u32 s68, %[xpl], s76\n\t"
            "s_addc_u32 s69, %[xph], 0\n\t"
            "global_load_lds_dwordx4 %[x0], s[68:69] offset:0\n\t"
            "v_lshl_add_u32 v160, v204, 8, %[lutl]\n\t"
            "ds_read_u16 v160, v160 offset:0\n\t"
            "v_lshl_add_u32 v161, v208, 8, %[lutl]\n\t"
            "ds_read_u16 v161, v161 offset:0\n\t"
            "global_load_lds_dwordx4 %[x1], s[68:69] offset:1024\n\t"
            "v_lshl_add_u32 v162, v212, 8, %[lutl]\n\t"
            "ds_read_u16 v162, v162 offset:0\n\t"
            "v_lshl_add_u32 v163, v216, 8, %[lutl]\n\t"
            "ds_read_u16 v163, v163 offset:0\n\t"
            "global_load_lds_dwordx4 %[x2], s[68:69] offset:2048\n\t"
            "v_lshl_add_u32 v168, v205, 8, %[lutl]\n\t"
            "ds_read_u16 v168, v168 offset:2\n\t"
            "v_lshl_add_u32 v169, v209, 8, %[lutl]\n\t"
            "ds_read_u16 v169, v169 offset:2\n\t"
            "global_load_lds_dwordx4 %[x3], s[68:69] offset:3072\n\t"
            "v_lshl_add_u32 v170, v213, 8, %[lutl]\n\t"
            "ds_read_u16 v170, v170 offset:2\n\t"
            "v_lshl_add_u32 v171, v217, 8, %[lutl]\n\t"
            "ds_read_u16 v171, v171 offset:2\n\t"
            "s_add_u32 m0, %[ldsx], 69632\n\t"
            "s_lshl_b32 s76, s45, 7\n\t"
            "s_add_u32 s76, s76, 65536\n\t"
            "s_add_u32 s68, %[xpl], s76\n\t"
            "s_addc_u32 s69, %[xph], 0\n\t"
            "global_load_lds_dwordx4 %[x0], s[68:69] offset:0\n\t"
            "v_lshl_add_u32 v176, v206, 8, %[lutl]\n\t"
            "ds_read_u16 v176, v176 offset:128\n\t"
            "v_lshl_add_u32 v177, v210, 8, %[lutl]\n\t"
            "ds_read_u16 v177, v177 offset:128\n\t"
            "global_load_lds_dwordx4 %[x1], s[68:69] offset:1024\n\t"
            "v_lshl_add_u32 v178, v214, 8, %[lutl]\n\t"
            "ds_read_u16 v178, v178 offset:128\n\t"
            "v_lshl_add_u32 v179, v218, 8, %[lutl]\n\t"
            "ds_read_u16 v179, v179 offset:128\n\t"
            "global_load_lds_dwordx4 %[x2], s[68:69] offset:2048\n\t"
            "v_lshl_add_u32 v184, v207, 8, %[lutl]\n\t"
            "ds_read_u16 v184, v184 offset:130\n\t"
            "v_lshl_add_u32 v185, v211, 8, %[lutl]\n\t"
            "ds_read_u16 v185, v185 offset:130\n\t"
            "global_load_lds_dwordx4 %[x3], s[68:69] offset:3072\n\t"
            "v_lshl_add_u32 v186, v215, 8, %[lutl]\n\t"
            "ds_read_u16 v186, v186 offset:130\n\t"
            "v_lshl_add_u32 v187, v219, 8, %[lutl]\n\t"
            "ds_read_u16 v187, v187 offset:130\n\t"
            "s_setprio 0\n\t"
            "s_waitcnt vmcnt(8)\n\t"
            "s_lshl_b32 s76, s45, 2\n\t"
            ".Lzc4:\n\t"
            "v_cmp_eq_u32 vcc, v192, %[nvl]\n\t"
            "s_lshr_b64 s[74:75], vcc, s76\n\t"
            "s_and_b32 s74, s74, 15\n\t"
            "s_cmp_eq_u32 s74, 15\n\t"
            "s_cbranch_scc1 .Lzd4\n\t"
            "s_cmp_eq_u32 s73, 0\n\t"
            "s_cbranch_scc1 .Lzd4\n\t"
            "s_sub_u32 s73, s73, 1\n\t"
            "global_load_dword v192, %[po], %[ws] sc1\n\t"
            "s_waitcnt vmcnt(0)\n\t"
            "s_branch .Lzc4\n\t"
            ".Lzd4:\n\t"
            "s_add_u32 m0, %[ldsw], 32768\n\t"
            "s_mul_i32 s76, s45, 458752\n\t"
            "s_add_u32 s70, s84, s76\n\t"
            "s_addc_u32 s71, s85, 0\n\t"
            "global_load_lds_dwordx4 %[wv], s[70:71] offset:0 sc1\n\t"
            "global_load_lds_dwordx4 %[wv], s[70:71] offset:1024 sc1\n\t"
            "global_load_lds_dwordx4 %[wv], s[70:71] offset:2048 sc1\n\t"
            "global_load_lds_dwordx4 %[wv], s[70:71] offset:3072 sc1\n\t"
            "s_waitcnt lgkmcnt(8)\n\t"
            "v_lshl_or_b32 v220, v161, 16, v160\n\t"
            "v_lshl_or_b32 v221, v163, 16, v162\n\t"
            "v_lshl_or_b32 v222, v169, 16, v168\n\t"
            "v_lshl_or_b32 v223, v171, 16, v170\n\t"
            "s_waitcnt lgkmcnt(0)\n\t"
            "v_lshl_or_b32 v224, v177, 16, v176\n\t"
            "v_lshl_or_b32 v225, v179, 16, v178\n\t"
            "v_lshl_or_b32 v226, v185, 16, v184\n\t"
            "v_lshl_or_b32 v227, v187, 16, v186\n\t"
            "s_nop 0\n\t"
            "v_permlane32_swap_b32 v220, v222\n\t"
            "v_permlane32_swap_b32 v221, v223\n\t"
            "v_permlane32_swap_b32 v224, v226\n\t"
            "v_permlane32_swap_b32 v225, v227\n\t"
            "s_mov_b64 s[88:89], %[stb]\n\t"
            "s_nop 0\n\t"
            "global_store_dwordx4 %[wo], v[220:223], s[88:89] offset:0 sc1\n\t"
            "global_store_dwordx4 %[wo], v[224:227], s[88:89] offset:512 sc1\n\t"
            "s_barrier\n\t"
            ".Lwp35:\n\t"
            "s_cbranch_execz .Lwp36\n\t"
            "s_lshl_b32 s76, s46, 2\n\t"
            ".Lzc5:\n\t"
            "v_cmp_eq_u32 vcc, v192, %[nvl]\n\t"
            "s_lshr_b64 s[74:75], vcc, s76\n\t"
            "s_and_b32 s74, s74, 15\n\t"
            "s_cmp_eq_u32 s74, 15\n\t"
            "s_cbranch_scc1 .Lzd5\n\t"
            "s_cmp_eq_u32 s73, 0\n\t"
            "s_cbranch_scc1 .Lzd5\n\t"
            "s_sub_u32 s73, s73, 1\n\t"
            "global_load_dword v192, %[po], %[ws] sc1\n\t"
            "s_waitcnt vmcnt(0)\n\t"
            "s_branch .Lzc5\n\t"
            ".Lzd5:\n\t"
            "s_add_u32 m0, %[ldsw], 0\n\t"
            "s_mul_i32 s76, s46, 458752\n\t"
            "s_add_u32 s70, s84, s76\n\t"
            "s_addc_u32 s71, s85, 0\n\t"
            "global_load_lds_dwordx4 %[wv], s[70:71] offset:0 sc1\n\t"
            "global_load_lds_dwordx4 %[wv], s[70:71] offset:1024 sc1\n\t"
            "global_load_lds_dwordx4 %[wv], s[70:71] offset:2048 sc1\n\t"
            "global_load_lds_dwordx4 %[wv], s[70:71] offset:3072 sc1\n\t"
            "s_add_u32 m0, %[ldsx], 0\n\t"
            "s_lshl_b32 s76, s46, 7\n\t"
            "s_add_u32 s68, %[xpl], s76\n\t"
            "s_addc_u32 s69, %[xph], 0\n\t"
            "global_load_lds_dwordx4 %[x0], s[68:69] offset:0\n\t"
            "global_load_lds_dwordx4 %[x1], s[68:69] offset:1024\n\t"
            "global_load_lds_dwordx4 %[x2], s[68:69] offset:2048\n\t"
            "global_load_lds_dwordx4 %[x3], s[68:69] offset:3072\n\t"
            "s_add_u32 m0, %[ldsx], 4096\n\t"
            "s_lshl_b32 s76, s46, 7\n\t"
            "s_add_u32 s76, s76, 65536\n\t"
            "s_add_u32 s68, %[xpl], s76\n\t"
            "s_addc_u32 s69, %[xph], 0\n\t"
            "global_load_lds_dwordx4 %[x0], s[68:69] offset:0\n\t"
            "global_load_lds_dwordx4 %[x1], s[68:69] offset:1024\n\t"
            "global_load_lds_dwordx4 %[x2], s[68:69] offset:2048\n\t"
            "global_load_lds_dwordx4 %[x3], s[68:69] offset:3072\n\t"
            "s_waitcnt vmcnt(12)\n\t"
            "s_mov_b64 exec, 3\n\t"
            "global_store_dword %[fob], %[nvl], %[ws] offset:0 sc1\n\t"
            "s_mov_b64 exec, -1\n\t"
            "s_barrier\n\t"
            "s_lshl_b32 s76, s47, 2\n\t"
            ".Lzc6:\n\t"
            "v_cmp_eq_u32 vcc, v192, %[nvl]\n\t"
            "s_lshr_b64 s[74:75], vcc, s76\n\t"
            "s_and_b32 s74, s74, 15\n\t"
            "s_cmp_eq_u32 s74, 15\n\t"
            "s_cbranch_scc1 .Lzd6\n\t"
            "s_cmp_eq_u32 s73, 0\n\t"
            "s_cbranch_scc1 .Lzd6\n\t"
            "s_sub_u32 s73, s73, 1\n\t"
            "global_load_dword v192, %[po], %[ws] sc1\n\t"
            "s_waitcnt vmcnt(0)\n\t"
            "s_branch .Lzc6\n\t"
            ".Lzd6:\n\t"
            "s_add_u32 m0, %[ldsw], 16384\n\t"
            "s_mul_i32 s76, s47, 458752\n\t"
            "s_add_u32 s70, s84, s76\n\t"
            "s_addc_u32 s71, s85, 0\n\t"
            "global_load_lds_dwordx4 %[wv], s[70:71] offset:0 sc1\n\t"
            "global_load_lds_dwordx4 %[wv], s[70:71] offset:1024 sc1\n\t"
            "global_load_lds_dwordx4 %[wv], s[70:71] offset:2048 sc1\n\t"
            "global_load_lds_dwordx4 %[wv], s[70:71] offset:3072 sc1\n\t"
            "s_add_u32 m0, %[ldsx], 32768\n\t"
            "s_lshl_b32 s76, s47, 7\n\t"
            "s_add_u32 s68, %[xpl], s76\n\t"
            "s_addc_u32 s69, %[xph], 0\n\t"
            "global_load_lds_dwordx4 %[x0], s[68:69] offset:0\n\t"
            "global_load_lds_dwordx4 %[x1], s[68:69] offset:1024\n\t"
            "global_load_lds_dwordx4 %[x2], s[68:69] offset:2048\n\t"
            "global_load_lds_dwordx4 %[x3], s[68:69] offset:3072\n\t"
            "s_add_u32 m0, %[ldsx], 36864\n\t"
            "s_lshl_b32 s76, s47, 7\n\t"
            "s_add_u32 s76, s76, 65536\n\t"
            "s_add_u32 s68, %[xpl], s76\n\t"
            "s_addc_u32 s69, %[xph], 0\n\t"
            "global_load_lds_dwordx4 %[x0], s[68:69] offset:0\n\t"
            "global_load_lds_dwordx4 %[x1], s[68:69] offset:1024\n\t"
            "global_load_lds_dwordx4 %[x2], s[68:69] offset:2048\n\t"
            "global_load_lds_dwordx4 %[x3], s[68:69] offset:3072\n\t"
            "s_mov_b64 s[88:89], %[qbg]\n\t"
            "s_add_u32 s88, s88, 3670016\n\t"
            "s_addc_u32 s89, s89, 0\n\t"
            "global_load_dwordx4 v[204:207], %[qo], s[88:89]\n\t"
            "s_add_u32 s88, s88, 14336\n\t"
            "s_addc_u32 s89, s89, 0\n\t"
            "global_load_dwordx4 v[208:211], %[qo], s[88:89]\n\t"
            "s_add_u32 s88, s88, 14336\n\t"
            "s_addc_u32 s89, s89, 0\n\t"
            "global_load_dwordx4 v[212:215], %[qo], s[88:89]\n\t"
            "s_add_u32 s88, s88, 14336\n\t"
            "s_addc_u32 s89, s89, 0\n\t"
            "global_load_dwordx4 v[216:219], %[qo], s[88:89]\n\t"
            "s_waitcnt vmcnt(17)\n\t"
            "s_barrier\n\t"
            ".Lwp36:\n\t"
            "s_cbranch_execz .Lwp37\n\t"
            "global_load_dword v192, %[po], %[ws] sc1\n\t"
            "s_add_u32 m0, %[ldsx], 65536\n\t"
            "s_lshl_b32 s76, s48, 7\n\t"
            "s_add_u32 s68, %[xpl], s76\n\t"
            "s_addc_u32 s69, %[xph], 0\n\t"
            "global_load_lds_dwordx4 %[x0], s[68:69] offset:0\n\t"
            "global_load_lds_dwordx4 %[x1], s[68:69] offset:1024\n\t"
            "global_load_lds_dwordx4 %[x2], s[68:69] offset:2048\n\t"
            "global_load_lds_dwordx4 %[x3], s[68:69] offset:3072\n\t"
            "s_add_u32 m0, %[ldsx], 69632\n\t"
            "s_lshl_b32 s76, s48, 7\n\t"
            "s_add_u32 s76, s76, 65536\n\t"
            "s_add_u32 s68, %[xpl], s76\n\t"
            "s_addc_u32 s69, %[xph], 0\n\t"
            "global_load_lds_dwordx4 %[x0], s[68:69] offset:0\n\t"
            "global_load_lds_dwordx4 %[x1], s[68:69] offset:1024\n\t"
            "global_load_lds_dwordx4 %[x2], s[68:69] offset:2048\n\t"
            "global_load_lds_dwordx4 %[x3], s[68:69] offset:3072\n\t"
            "s_waitcnt vmcnt(8)\n\t"
            "s_lshl_b32 s76, s48, 2\n\t"
            ".Lzc7:\n\t"
            "v_cmp_eq_u32 vcc, v192, %[nvl]\n\t"
            "s_lshr_b64 s[74:75], vcc, s76\n\t"
            "s_and_b32 s74, s74, 15\n\t"
            "s_cmp_eq_u32 s74, 15\n\t"
            "s_cbranch_scc1 .Lzd7\n\t"
            "s_cmp_eq_u32 s73, 0\n\t"
            "s_cbranch_scc1 .Lzd7\n\t"
            "s_sub_u32 s73, s73, 1\n\t"
            "global_load_dword v192, %[po], %[ws] sc1\n\t"
            "s_waitcnt vmcnt(0)\n\t"
            "s_branch .Lzc7\n\t"
            ".Lzd7:\n\t"
            "s_add_u32 m0, %[ldsw], 32768\n\t"
            "s_mul_i32 s76, s48, 458752\n\t"
            "s_add_u32 s70, s84, s76\n\t"
            "s_addc_u32 s71, s85, 0\n\t"
            "global_load_lds_dwordx4 %[wv], s[70:71] offset:0 sc1\n\t"
            "global_load_lds_dwordx4 %[wv], s[70:71] offset:1024 sc1\n\t"
            "global_load_lds_dwordx4 %[wv], s[70:71] offset:2048 sc1\n\t"
            "global_load_lds_dwordx4 %[wv], s[70:71] offset:3072 sc1\n\t"
            "s_barrier\n\t"
            ".Lwp37:\n\t"
            "s_cbranch_execz .Lwp97\n\t"
            "s_lshl_b32 s76, s49, 2\n\t"
            ".Lzc8:\n\t"
            "v_cmp_eq_u32 vcc, v192, %[nvl]\n\t"
            "s_lshr_b64 s[74:75], vcc, s76\n\t"
            "s_and_b32 s74, s74, 15\n\t"
            "s_cmp_eq_u32 s74, 15\n\t"
            "s_cbranch_scc1 .Lzd8\n\t"
            "s_cmp_eq_u32 s73, 0\n\t"
            "s_cbranch_scc1 .Lzd8\n\t"
            "s_sub_u32 s73, s73, 1\n\t"
            "global_load_dword v192, %[po], %[ws] sc1\n\t"
            "s_waitcnt vmcnt(0)\n\t"
            "s_branch .Lzc8\n\t"
            ".Lzd8:\n\t"
            "s_add_u32 m0, %[ldsw], 0\n\t"
            "s_mul_i32 s76, s49, 458752\n\t"
            "s_add_u32 s70, s84, s76\n\t"
            "s_addc_u32 s71, s85, 0\n\t"
            "global_load_lds_dwordx4 %[wv], s[70:71] offset:0 sc1\n\t"
            "global_load_lds_dwordx4 %[wv], s[70:71] offset:1024 sc1\n\t"
            "global_load_lds_dwordx4 %[wv], s[70:71] offset:2048 sc1\n\t"
            "global_load_lds_dwordx4 %[wv], s[70:71] offset:3072 sc1\n\t"
            "s_setprio 0\n\t"
            "s_add_u32 m0, %[ldsx], 0\n\t"
            "s_lshl_b32 s76, s49, 7\n\t"
            "s_add_u32 s68, %[xpl], s76\n\t"
            "s_addc_u32 s69, %[xph], 0\n\t"
            "global_load_lds_dwordx4 %[x0], s[68:69] offset:0\n\t"
            "v_lshl_add_u32 v160, v204, 8, %[lutl]\n\t"
            "ds_read_u16 v160, v160 offset:0\n\t"
            "v_lshl_add_u32 v161, v208, 8, %[lutl]\n\t"
            "ds_read_u16 v161, v161 offset:0\n\t"
            "global_load_lds_dwordx4 %[x1], s[68:69] offset:1024\n\t"
            "v_lshl_add_u32 v162, v212, 8, %[lutl]\n\t"
            "ds_read_u16 v162, v162 offset:0\n\t"
            "v_lshl_add_u32 v163, v216, 8, %[lutl]\n\t"
            "ds_read_u16 v163, v163 offset:0\n\t"
            "global_load_lds_dwordx4 %[x2], s[68:69] offset:2048\n\t"
            "v_lshl_add_u32 v168, v205, 8, %[lutl]\n\t"
            "ds_read_u16 v168, v168 offset:2\n\t"
            "v_lshl_add_u32 v169, v209, 8, %[lutl]\n\t"
            "ds_read_u16 v169, v169 offset:2\n\t"
            "global_load_lds_dwordx4 %[x3], s[68:69] offset:3072\n\t"
            "v_lshl_add_u32 v170, v213, 8, %[lutl]\n\t"
            "ds_read_u16 v170, v170 offset:2\n\t"
            "v_lshl_add_u32 v171, v217, 8, %[lutl]\n\t"
            "ds_read_u16 v171, v171 offset:2\n\t"
            "s_add_u32 m0, %[ldsx], 4096\n\t"
            "s_lshl_b32 s76, s49, 7\n\t"
            "s_add_u32 s76, s76, 65536\n\t"
            "s_add_u32 s68, %[xpl], s76\n\t"
            "s_addc_u32 s69, %[xph], 0\n\t"
            "global_load_lds_dwordx4 %[x0], s[68:69] offset:0\n\t"
            "v_lshl_add_u32 v176, v206, 8, %[lutl]\n\t"
            "ds_read_u16 v176, v176 offset:128\n\t"
            "v_lshl_add_u32 v177, v210, 8, %[lutl]\n\t"
            "ds_read_u16 v177, v177 offset:128\n\t"
            "global_load_lds_dwordx4 %[x1], s[68:69] offset:1024\n\t"
            "v_lshl_add_u32 v178, v214, 8, %[lutl]\n\t"
            "ds_read_u16 v178, v178 offset:128\n\t"
            "v_lshl_add_u32 v179, v218, 8, %[lutl]\n\t"
            "ds_read_u16 v179, v179 offset:128\n\t"
            "global_load_lds_dwordx4 %[x2], s[68:69] offset:2048\n\t"
            "v_lshl_add_u32 v184, v207, 8, %[lutl]\n\t"
            "ds_read_u16 v184, v184 offset:130\n\t"
            "v_lshl_add_u32 v185, v211, 8, %[lutl]\n\t"
            "ds_read_u16 v185, v185 offset:130\n\t"
            "global_load_lds_dwordx4 %[x3], s[68:69] offset:3072\n\t"
            "v_lshl_add_u32 v186, v215, 8, %[lutl]\n\t"
            "ds_read_u16 v186, v186 offset:130\n\t"
            "v_lshl_add_u32 v187, v219, 8, %[lutl]\n\t"
            "ds_read_u16 v187, v187 offset:130\n\t"
            "s_setprio 0\n\t"
            "s_waitcnt lgkmcnt(8)\n\t"
            "v_lshl_or_b32 v220, v161, 16, v160\n\t"
            "v_lshl_or_b32 v221, v163, 16, v162\n\t"
            "v_lshl_or_b32 v222, v169, 16, v168\n\t"
            "v_lshl_or_b32 v223, v171, 16, v170\n\t"
            "s_waitcnt lgkmcnt(0)\n\t"
            "v_lshl_or_b32 v224, v177, 16, v176\n\t"
            "v_lshl_or_b32 v225, v179, 16, v178\n\t"
            "v_lshl_or_b32 v226, v185, 16, v184\n\t"
            "v_lshl_or_b32 v227, v187, 16, v186\n\t"
            "s_nop 0\n\t"
            "v_permlane32_swap_b32 v220, v222\n\t"
            "v_permlane32_swap_b32 v221, v223\n\t"
            "v_permlane32_swap_b32 v224, v226\n\t"
            "v_permlane32_swap_b32 v225, v227\n\t"
            "s_mov_b64 s[88:89], %[stb]\n\t"
            "s_add_u32 s88, s88, 1835008\n\t"
            "s_addc_u32 s89, s89, 0\n\t"
            "global_store_dwordx4 %[wo], v[220:223], s[88:89] offset:0 sc1\n\t"
            "global_store_dwordx4 %[wo], v[224:227], s[88:89] offset:512 sc1\n\t"
            "s_waitcnt vmcnt(14)\n\t"
            "s_barrier\n\t"
            ".Lwp38:\n\t"
            "s_cbranch_execz .Lwp39\n\t"
            "s_lshl_b32 s76, s50, 2\n\t"
            ".Lzc9:\n\t"
            "v_cmp_eq_u32 vcc, v192, %[nvl]\n\t"
            "s_lshr_b64 s[74:75], vcc, s76\n\t"
            "s_and_b32 s74, s74, 15\n\t"
            "s_cmp_eq_u32 s74, 15\n\t"
            "s_cbranch_scc1 .Lzd9\n\t"
            "s_cmp_eq_u32 s73, 0\n\t"
            "s_cbranch_scc1 .Lzd9\n\t"
            "s_sub_u32 s73, s73, 1\n\t"
            "global_load_dword v192, %[po], %[ws] sc1\n\t"
            "s_waitcnt vmcnt(0)\n\t"
            "s_branch .Lzc9\n\t"
            ".Lzd9:\n\t"
            "s_add_u32 m0, %[ldsw], 16384\n\t"
            "s_mul_i32 s76, s50, 458752\n\t"
            "s_add_u32 s70, s84, s76\n\t"
            "s_addc_u32 s71, s85, 0\n\t"
            "global_load_lds_dwordx4 %[wv], s[70:71] offset:0 sc1\n\t"
            "global_load_lds_dwordx4 %[wv], s[70:71] offset:1024 sc1\n\t"
            "global_load_lds_dwordx4 %[wv], s[70:71] offset:2048 sc1\n\t"
            "global_load_lds_dwordx4 %[wv], s[70:71] offset:3072 sc1\n\t"
            "s_add_u32 m0, %[ldsx], 32768\n\t"
            "s_lshl_b32 s76, s50, 7\n\t"
            "s_add_u32 s68, %[xpl], s76\n\t"
            "s_addc_u32 s69, %[xph], 0\n\t"
            "global_load_lds_dwordx4 %[x0], s[68:69] offset:0\n\t"
            "global_load_lds_dwordx4 %[x1], s[68:69] offset:1024\n\t"
            "global_load_lds_dwordx4 %[x2], s[68:69] offset:2048\n\t"
            "global_load_lds_dwordx4 %[x3], s[68:69] offset:3072\n\t"
            "s_add_u32 m0, %[ldsx], 36864\n\t"
            "s_lshl_b32 s76, s50, 7\n\t"
            "s_add_u32 s76, s76, 65536\n\t"
            "s_add_u32 s68, %[xpl], s76\n\t"
            "s_addc_u32 s69, %[xph], 0\n\t"
            "global_load_lds_dwordx4 %[x0], s[68:69] offset:0\n\t"
            "global_load_lds_dwordx4 %[x1], s[68:69] offset:1024\n\t"
            "global_load_lds_dwordx4 %[x2], s[68:69] offset:2048\n\t"
            "global_load_lds_dwordx4 %[x3], s[68:69] offset:3072\n\t"
            "s_waitcnt vmcnt(12)\n\t"
            "s_mov_b64 exec, 3\n\t"
            "global_store_dword %[fob], %[nvl], %[ws] offset:64 sc1\n\t"
            "s_mov_b64 exec, -1\n\t"
            "s_barrier\n\t"
            "s_lshl_b32 s76, s51, 2\n\t"
            ".Lzc10:\n\t"
            "v_cmp_eq_u32 vcc, v192, %[nvl]\n\t"
            "s_lshr_b64 s[74:75], vcc, s76\n\t"
            "s_and_b32 s74, s74, 15\n\t"
            "s_cmp_eq_u32 s74, 15\n\t"
            "s_cbranch_scc1 .Lzd10\n\t"
            "s_cmp_eq_u32 s73, 0\n\t"
            "s_cbranch_scc1 .Lzd10\n\t"
            "s_sub_u32 s73, s73, 1\n\t"
            "global_load_dword v192, %[po], %[ws] sc1\n\t"
            "s_waitcnt vmcnt(0)\n\t"
            "s_branch .Lzc10\n\t"
            ".Lzd10:\n\t"
            "s_add_u32 m0, %[ldsw], 32768\n\t"
            "s_mul_i32 s76, s51, 458752\n\t"
            "s_add_u32 s70, s84, s76\n\t"
            "s_addc_u32 s71, s85, 0\n\t"
            "global_load_lds_dwordx4 %[wv], s[70:71] offset:0 sc1\n\t"
            "global_load_lds_dwordx4 %[wv], s[70:71] offset:1024 sc1\n\t"
            "global_load_lds_dwordx4 %[wv], s[70:71] offset:2048 sc1\n\t"
            "global_load_lds_dwordx4 %[wv], s[70:71] offset:3072 sc1\n\t"
            "s_add_u32 m0, %[ldsx], 65536\n\t"
            "s_lshl_b32 s76, s51, 7\n\t"
            "s_add_u32 s68, %[xpl], s76\n\t"
            "s_addc_u32 s69, %[xph], 0\n\t"
            "global_load_lds_dwordx4 %[x0], s[68:69] offset:0\n\t"
            "global_load_lds_dwordx4 %[x1], s[68:69] offset:1024\n\t"
            "global_load_lds_dwordx4 %[x2], s[68:69] offset:2048\n\t"
            "global_load_lds_dwordx4 %[x3], s[68:69] offset:3072\n\t"
            "s_add_u32 m0, %[ldsx], 69632\n\t"
            "s_lshl_b32 s76, s51, 7\n\t"
            "s_add_u32 s76, s76, 65536\n\t"
            "s_add_u32 s68, %[xpl], s76\n\t"
            "s_addc_u32 s69, %[xph], 0\n\t"
            "global_load_lds_dwordx4 %[x0], s[68:69] offset:0\n\t"
            "global_load_lds_dwordx4 %[x1], s[68:69] offset:1024\n\t"
            "global_load_lds_dwordx4 %[x2], s[68:69] offset:2048\n\t"
            "global_load_lds_dwordx4 %[x3], s[68:69] offset:3072\n\t"
            "s_waitcnt vmcnt(13)\n\t"
            "s_barrier\n\t"
            ".Lwp39:\n\t"
            "s_cbranch_execz .Lwp40\n\t"
            "global_load_dword v192, %[po], %[ws] sc1\n\t"
            "s_add_u32 m0, %[ldsx], 0\n\t"
            "s_lshl_b32 s76, s52, 7\n\t"
            "s_add_u32 s68, %[xpl], s76\n\t"
            "s_addc_u32 s69, %[xph], 0\n\t"
            "global_load_lds_dwordx4 %[x0], s[68:69] offset:0\n\t"
            "global_load_lds_dwordx4 %[x1], s[68:69] offset:1024\n\t"
            "global_load_lds_dwordx4 %[x2], s[68:69] offset:2048\n\t"
            "global_load_lds_dwordx4 %[x3], s[68:69] offset:3072\n\t"
            "s_add_u32 m0, %[ldsx], 4096\n\t"
            "s_lshl_b32 s76, s52, 7\n\t"
            "s_add_u32 s76, s76, 65536\n\t"
            "s_add_u32 s68, %[xpl], s76\n\t"
            "s_addc_u32 s69, %[xph], 0\n\t"
            "global_load_lds_dwordx4 %[x0], s[68:69] offset:0\n\t"
            "global_load_lds_dwordx4 %[x1], s[68:69] offset:1024\n\t"
            "global_load_lds_dwordx4 %[x2], s[68:69] offset:2048\n\t"
            "global_load_lds_dwordx4 %[x3], s[68:69] offset:3072\n\t"
            "s_waitcnt vmcnt(8)\n\t"
            "s_lshl_b32 s76, s52, 2\n\t"
            ".Lzc11:\n\t"
            "v_cmp_eq_u32 vcc, v192, %[nvl]\n\t"
            "s_lshr_b64 s[74:75], vcc, s76\n\t"
            "s_and_b32 s74, s74, 15\n\t"
            "s_cmp_eq_u32 s74, 15\n\t"
            "s_cbranch_scc1 .Lzd11\n\t"
            "s_cmp_eq_u32 s73, 0\n\t"
            "s_cbranch_scc1 .Lzd11\n\t"
            "s_sub_u32 s73, s73, 1\n\t"
            "global_load_dword v192, %[po], %[ws] sc1\n\t"
            "s_waitcnt vmcnt(0)\n\t"
            "s_branch .Lzc11\n\t"
            ".Lzd11:\n\t"
            "s_add_u32 m0, %[ldsw], 0\n\t"
            "s_mul_i32 s76, s52, 458752\n\t"
            "s_add_u32 s70, s84, s76\n\t"
            "s_addc_u32 s71, s85, 0\n\t"
            "global_load_lds_dwordx4 %[wv], s[70:71] offset:0 sc1\n\t"
            "global_load_lds_dwordx4 %[wv], s[70:71] offset:1024 sc1\n\t"
            "global_load_lds_dwordx4 %[wv], s[70:71] offset:2048 sc1\n\t"
            "global_load_lds_dwordx4 %[wv], s[70:71] offset:3072 sc1\n\t"
            "s_barrier\n\t"
            "s_mov_b32 s76, 48\n\t"
            ".Lzc12:\n\t"
            "v_cmp_eq_u32 vcc, v192, %[nvl]\n\t"
            "s_lshr_b64 s[74:75], vcc, s76\n\t"
            "s_and_b32 s74, s74, 65535\n\t"
            "s_cmp_eq_u32 s74, 65535\n\t"
            "s_cbranch_scc1 .Lzd12\n\t"
            "s_cmp_eq_u32 s73, 0\n\t"
            "s_cbranch_scc1 .Lzd12\n\t"
            "s_sub_u32 s73, s73, 1\n\t"
            "global_load_dword v192, %[po], %[ws] sc1\n\t"
            "s_waitcnt vmcnt(0)\n\t"
            "s_branch .Lzc12\n\t"
            ".Lzd12:\n\t"
            "s_add_u32 m0, %[ldsw], 16384\n\t"
            "s_mul_i32 s76, s53, 458752\n\t"
            "s_add_u32 s70, s84, s76\n\t"
            "s_addc_u32 s71, s85, 0\n\t"
            "s_mul_i32 s76, s54, 458752\n\t"
            "s_sub_u32 s76, s76, 1024\n\t"
            "s_add_u32 s86, s84, s76\n\t"
            "s_addc_u32 s87, s85, 0\n\t"
            "global_load_lds_dwordx4 %[wv], s[70:71] offset:0 sc1\n\t"
            "global_load_lds_dwordx4 %[wv], s[86:87] offset:1024 sc1\n\t"
            "global_load_lds_dwordx4 %[wv], s[70:71] offset:2048 sc1\n\t"
            "global_load_lds_dwordx4 %[wv], s[86:87] offset:3072 sc1\n\t"
            "s_add_u32 m0, %[ldsx], 32768\n\t"
            "s_lshl_b32 s76, s53, 7\n\t"
            "s_add_u32 s68, %[xpl], s76\n\t"
            "s_addc_u32 s69, %[xph], 0\n\t"
            "global_load_lds_dwordx4 %[x0], s[68:69] offset:0\n\t"
            "global_load_lds_dwordx4 %[x1], s[68:69] offset:1024\n\t"
            "global_load_lds_dwordx4 %[x2], s[68:69] offset:2048\n\t"
            "global_load_lds_dwordx4 %[x3], s[68:69] offset:3072\n\t"
            "s_add_u32 m0, %[ldsx], 36864\n\t"
            "s_lshl_b32 s76, s53, 7\n\t"
            "s_add_u32 s76, s76, 65536\n\t"
            "s_add_u32 s68, %[xpl], s76\n\t"
            "s_addc_u32 s69, %[xph], 0\n\t"
            "global_load_lds_dwordx4 %[x0], s[68:69] offset:0\n\t"
            "global_load_lds_dwordx4 %[x1], s[68:69] offset:1024\n\t"
            "global_load_lds_dwordx4 %[x2], s[68:69] offset:2048\n\t"
            "global_load_lds_dwordx4 %[x3], s[68:69] offset:3072\n\t"
            "s_waitcnt vmcnt(12)\n\t"
            "s_barrier\n\t"
            ".Lwp40:\n\t"
            "s_cbranch_execz .Lwp97\n\t"
            "s_add_u32 m0, %[ldsx], 65536\n\t"
            "s_lshl_b32 s76, s54, 7\n\t"
            "s_add_u32 s68, %[xpl], s76\n\t"
            "s_addc_u32 s69, %[xph], 0\n\t"
            "global_load_lds_dwordx4 %[x0], s[68:69] offset:0\n\t"
            "global_load_lds_dwordx4 %[x1], s[68:69] offset:1024\n\t"
            "global_load_lds_dwordx4 %[x2], s[68:69] offset:2048\n\t"
            "global_load_lds_dwordx4 %[x3], s[68:69] offset:3072\n\t"
            "s_add_u32 m0, %[ldsx], 69632\n\t"
            "s_lshl_b32 s76, s54, 7\n\t"
            "s_add_u32 s76, s76, 65536\n\t"
            "s_add_u32 s68, %[xpl], s76\n\t"
            "s_addc_u32 s69, %[xph], 0\n\t"
            "global_load_lds_dwordx4 %[x0], s[68:69] offset:0\n\t"
            "global_load_lds_dwordx4 %[x1], s[68:69] offset:1024\n\t"
            "global_load_lds_dwordx4 %[x2], s[68:69] offset:2048\n\t"
            "global_load_lds_dwordx4 %[x3], s[68:69] offset:3072\n\t"
            "s_add_u32 m0, %[ldsw], 32768\n\t"
            "s_mul_i32 s76, s55, 458752\n\t"
            "s_add_u32 s70, s84, s76\n\t"
            "s_addc_u32 s71, s85, 0\n\t"
            "s_mul_i32 s76, s53, 458752\n\t"
            "s_add_u32 s86, s84, s76\n\t"
            "s_addc_u32 s87, s85, 0\n\t"
            "global_load_lds_dwordx4 %[wv], s[70:71] offset:0 sc1\n\t"
            "global_load_lds_dwordx4 %[wv], s[86:87] offset:1024 sc1\n\t"
            "global_load_lds_dwordx4 %[wv], s[70:71] offset:2048 sc1\n\t"
            "global_load_lds_dwordx4 %[wv], s[86:87] offset:3072 sc1\n\t"
            "s_waitcnt vmcnt(4)\n\t"
            "s_barrier\n\t"
            "s_add_u32 m0, %[ldsx], 0\n\t"
            "s_lshl_b32 s76, s55, 7\n\t"
            "s_add_u32 s68, %[xpl], s76\n\t"
            "s_addc_u32 s69, %[xph], 0\n\t"
            "global_load_lds_dwordx4 %[x0], s[68:69] offset:0\n\t"
            "global_load_lds_dwordx4 %[x1], s[68:69] offset:1024\n\t"
            "global_load_lds_dwordx4 %[x2], s[68:69] offset:2048\n\t"
            "global_load_lds_dwordx4 %[x3], s[68:69] offset:3072\n\t"
            "s_add_u32 m0, %[ldsx], 4096\n\t"
            "s_lshl_b32 s76, s55, 7\n\t"
            "s_add_u32 s76, s76, 65536\n\t"
            "s_add_u32 s68, %[xpl], s76\n\t"
            "s_addc_u32 s69, %[xph], 0\n\t"
            "global_load_lds_dwordx4 %[x0], s[68:69] offset:0\n\t"
            "global_load_lds_dwordx4 %[x1], s[68:69] offset:1024\n\t"
            "global_load_lds_dwordx4 %[x2], s[68:69] offset:2048\n\t"
            "global_load_lds_dwordx4 %[x3], s[68:69] offset:3072\n\t"
            "s_add_u32 m0, %[ldsw], 0\n\t"
            "s_mul_i32 s76, s54, 458752\n\t"
            "s_add_u32 s76, s76, 1024\n\t"
            "s_add_u32 s70, s84, s76\n\t"
            "s_addc_u32 s71, s85, 0\n\t"
            "s_mul_i32 s76, s55, 458752\n\t"
            "s_add_u32 s86, s84, s76\n\t"
            "s_addc_u32 s87, s85, 0\n\t"
            "global_load_lds_dwordx4 %[wv], s[70:71] offset:0 sc1\n\t"
            "global_load_lds_dwordx4 %[wv], s[86:87] offset:1024 sc1\n\t"
            "global_load_lds_dwordx4 %[wv], s[70:71] offset:2048 sc1\n\t"
            "global_load_lds_dwordx4 %[wv], s[86:87] offset:3072 sc1\n\t"
            "s_waitcnt vmcnt(4)\n\t"
            "s_barrier\n\t"
            ".Lwp41:\n\t"
            "s_cbranch_execz .Lwp14\n\t"
            "s_mov_b32 s76, 16\n\t"
            ".Lzc13:\n\t"
            "v_cmp_eq_u32 vcc, v192, %[nvl]\n\t"
            "s_lshr_b64 s[74:75], vcc, s76\n\t"
            "s_and_b32 s74, s74, 65535\n\t"
            "s_cmp_eq_u32 s74, 65535\n\t"
            "s_cbranch_scc1 .Lzd13\n\t"
            "s_cmp_eq_u32 s73, 0\n\t"
            "s_cbranch_scc1 .Lzd13\n\t"
            "s_sub_u32 s73, s73, 1\n\t"
            "global_load_dword v192, %[po], %[ws] sc1\n\t"
            "s_waitcnt vmcnt(0)\n\t"
            "s_branch .Lzc13\n\t"
            ".Lzd13:\n\t"
            "s_waitcnt vmcnt(0)\n\t"
            "s_barrier\n\t"
            : [nvl] "+v"(nvl), [qo] "+v"(qo), [wo] "+v"(wo)
            : [x0] "v"(xoff[0]), [x1] "v"(xoff[1]), [x2] "v"(xoff[2]), [x3] "v"(xoff[3]), [wv] "v"(wv), [po] "v"(po),
              [lutl] "v"(lutl), [fo0] "v"(fo0), [fob] "v"(fob), [bd] "v"(bdst),
              [xpl] "s"((u32)KPTR(0)), [xph] "s"((u32)(KPTR(0) >> 32)), [ws] "s"(KPTR(4)),
              [qbg] "s"(qbg), [qa1] "s"(qa1), [tseq] "s"(tseq), [p] "s"(p), [h2] "s"(h2), [ldsx] "s"(ldsx), [ldsw] "s"(ldsw), [st1] "s"(st1), [stb] "s"(stb)
            : "vcc", "scc", "memory", "s40", "s41", "s42", "s43", "s44", "s45", "s46", "s47", "s48", "s49", "s50", "s51", "s52", "s53", "s54", "s55",
              "s56", "s57", "s58", "s59", "s60", "s61", "s62", "s63", "s64", "s65", "s66", "s67", "s68", "s69", "s70", "s71", "s72", "s73", "s74",
              "s75", "s76", "s77", "s78", "s79", "s80", "s81", "s82", "s83", "s84", "s85", "s86", "s87", "s88", "s89", "v128", "v129", "v130", "v131",
              "v132", "v133", "v134", "v135", "v136", "v137", "v138", "v139", "v140", "v141", "v142", "v143", "v144", "v145", "v146", "v147", "v148",
              "v149", "v150", "v151", "v152", "v153", "v154", "v155", "v156", "v157", "v158", "v159", "v160", "v161", "v162", "v163", "v164", "v165",
              "v166", "v167", "v168", "v169", "v170", "v171", "v172", "v173", "v174", "v175", "v176", "v177", "v178", "v179", "v180", "v181", "v182",
              "v183", "v184", "v185", "v186", "v187", "v188", "v189", "v190", "v191", "v192", "v193", "v194", "v195", "v196", "v197", "v198", "v199",
              "v200", "v201", "v202", "v203", "v204", "v205", "v206", "v207", "v208", "v209", "v210", "v211", "v212", "v213", "v214", "v215", "v216",
              "v217", "v218", "v219", "v220", "v221", "v222", "v223", "v224", "v225", "v226", "v227", "v228", "v229", "v230", "v231", "v232", "v233",
              "v234", "v235");
        if (mt == 0 && p == 0 && lane < 2) asm volatile("global_store_dword %0, %1, %2 sc1" :: "v"(no), "v"(nvl), "s"(KPTR(4)) : "memory");
        return;
    }

    u32 oo[4];
    unsigned long ob[4];

    int xb0, xb1, ba;
    {
        u32 V[4][8];
        i32x4 Q0[8];
#define LUT1(Q, j, r) asm volatile("v_lshl_add_u32 %0, %1, 8, %2\n\tds_read_u16 %0, %0 offset:%3" : "=&v"(V[j][r]) \
                                   : "v"(Q[r][j]), "v"(lutl), "i"(((j) >> 1) * 128 + ((j) & 1) * 2) : "memory")
#define PK(b_, j, n) asm volatile("s_waitcnt lgkmcnt(" #n ")\n\t" \
                                    "v_lshl_or_b32 %0, %5, 16, %4\n\tv_lshl_or_b32 %1, %7, 16, %6\n\t" \
                                    "v_lshl_or_b32 %2, %9, 16, %8\n\tv_lshl_or_b32 %3, %11, 16, %10" \
                                    : "=&v"(b_[0]), "=&v"(b_[1]), "=&v"(b_[2]), "=&v"(b_[3]) \
                                    : "v"(V[j][0]), "v"(V[j][1]), "v"(V[j][2]), "v"(V[j][3]), "v"(V[j][4]), "v"(V[j][5]), "v"(V[j][6]), "v"(V[j][7]) : "memory")
#define WR(b_, j) asm volatile("ds_write_b128 %0, %1 offset:%2" :: "v"(bdst), "v"(b_), "i"((j) * 256) : "memory")
        {
            const u32 l = tid & 31, h = (tid >> 5) & 1;
            const u32 lo = l * 8 + h * 4, qv = h * (8 * N_ * 4) + l * 16;
            const u32 ls = (u32)((w * 4 * N_ + col0) * 2), qs = (u32)((((mt >> 1) * 64 + w * 16) * N_ + col0) * 4);
            const u32 ld = (u32)(size_t)(smem) + OFF_L + w * 1024;
            asm volatile("s_mov_b32 m0, %[ld]\n\t"
                         "s_waitcnt lgkmcnt(0)\n\t"
                         "s_mov_b64 s[60:61], %[lp]\n\ts_add_u32 s60, s60, %[ls]\n\ts_addc_u32 s61, s61, 0\n\t"
                         "s_mov_b64 s[62:63], %[qp]\n\ts_add_u32 s62, s62, %[qs]\n\ts_addc_u32 s63, s63, 0\n\t"
                         "global_load_lds_dword %[lo], s[60:61] offset:0\n\t"
                         "s_add_u32 s60, s60, 6912\n\ts_addc_u32 s61, s61, 0\n\t"
                         "global_load_lds_dword %[lo], s[60:61] offset:256\n\t"
                         "s_add_u32 s60, s60, 6912\n\ts_addc_u32 s61, s61, 0\n\t"
                         "global_load_lds_dword %[lo], s[60:61] offset:512\n\t"
                         "s_add_u32 s60, s60, 6912\n\ts_addc_u32 s61, s61, 0\n\t"
                         "global_load_lds_dword %[lo], s[60:61] offset:768\n\t"
                         "global_load_dwordx4 %[q0], %[qv], s[62:63]\n\t"
                         "s_add_u32 s62, s62, 14336\n\ts_addc_u32 s63, s63, 0\n\t"
                         "global_load_dwordx4 %[q1], %[qv], s[62:63]\n\t"
                         "s_add_u32 s62, s62, 14336\n\ts_addc_u32 s63, s63, 0\n\t"
                         "global_load_dwordx4 %[q2], %[qv], s[62:63]\n\t"
                         "s_add_u32 s62, s62, 14336\n\ts_addc_u32 s63, s63, 0\n\t"
                         "global_load_dwordx4 %[q3], %[qv], s[62:63]\n\t"
                         "s_add_u32 s62, s62, 14336\n\ts_addc_u32 s63, s63, 0\n\t"
                         "global_load_dwordx4 %[q4], %[qv], s[62:63]\n\t"
                         "s_add_u32 s62, s62, 14336\n\ts_addc_u32 s63, s63, 0\n\t"
                         "global_load_dwordx4 %[q5], %[qv], s[62:63]\n\t"
                         "s_add_u32 s62, s62, 14336\n\ts_addc_u32 s63, s63, 0\n\t"
                         "global_load_dwordx4 %[q6], %[qv], s[62:63]\n\t"
                         "s_add_u32 s62, s62, 14336\n\ts_addc_u32 s63, s63, 0\n\t"
                         "global_load_dwordx4 %[q7], %[qv], s[62:63]\n\t"
                         : [q0] "=&v"(Q0[0]), [q1] "=&v"(Q0[1]), [q2] "=&v"(Q0[2]), [q3] "=&v"(Q0[3]),
                           [q4] "=&v"(Q0[4]), [q5] "=&v"(Q0[5]), [q6] "=&v"(Q0[6]), [q7] "=&v"(Q0[7])
                         : [lo] "v"(lo), [qv] "v"(qv), [ls] "s"(ls), [qs] "s"(qs), [ld] "s"(ld), [lp] "s"(ka[2]), [qp] "s"(ka[1])
                         : "s60", "s61", "s62", "s63", "scc", "memory");
            SB();
        }
        KWAIT();
        int ln = lane;
        asm volatile("" : "+v"(ln));
        const int c = ln & 15, g = ln >> 4, l = ln & 31, h = ln >> 5;
        xb0 = (w * 64 + c) * 128 + ((g ^ (c >> 1)) * 16);
        xb1 = xb0 ^ 64;
        ba = OFF_B + g * 2048 + c * 16;
        u32 lutl = (u32)(size_t)(smem) + OFF_L + l * 4;
        u32 bdst = (u32)(size_t)(smem) + OFF_B + w * 4096 + h * 2048 + ((l >> 4) * 64 + (l & 15)) * 16;
#pragma unroll
        for (int r = 0; r < 4; ++r) {
            oo[r] = (u32)(((row0 + w * 64 + g * 4 + r) * N_ + col0 + c * 4) * 2);
            ob[r] = KPTR(3) + (unsigned long)(r * 16 * N_ * 2);
            asm volatile("" : "+v"(oo[r]), "+s"(ob[r]));
        }
        asm volatile("" : "+v"(xb0), "+v"(xb1), "+v"(ba));
        asm volatile("" : "+v"(bdst), "+v"(lutl));
        {
            unsigned long ex_;
            asm volatile("s_mov_b64 %0, exec\n\ts_mov_b64 exec, 0\n\t"
                         "s_cmp_eq_u32 %1, 0\n\ts_cbranch_scc1 .Lwp20\n\t"
                         "s_cmp_eq_u32 %1, 1\n\ts_cbranch_scc1 .Lwp10\n\t"
                         "s_cmp_eq_u32 %1, 2\n\ts_cbranch_scc1 .Lwp12\n\t"
                         "s_branch .Lwp99\n\t"
                         ".Lwp99:\n\ts_mov_b64 exec, %0" : "=&s"(ex_) : "s"(w) : "scc", "memory");
        }
        asm volatile("s_waitcnt vmcnt(8)" ::: "memory");
        BAR();
        PAD(20, 10);
        asm volatile("s_waitcnt vmcnt(7)" : "+v"(Q0[0]) :: "memory"); LUT1(Q0, 0, 0); LUT1(Q0, 1, 0); LUT1(Q0, 2, 0); LUT1(Q0, 3, 0); SB();
        asm volatile("s_waitcnt vmcnt(6)" : "+v"(Q0[1]) :: "memory"); LUT1(Q0, 0, 1); LUT1(Q0, 1, 1); LUT1(Q0, 2, 1); LUT1(Q0, 3, 1); SB();
        asm volatile("s_waitcnt vmcnt(5)" : "+v"(Q0[2]) :: "memory"); LUT1(Q0, 0, 2); LUT1(Q0, 1, 2); LUT1(Q0, 2, 2); LUT1(Q0, 3, 2); SB();
        asm volatile("s_waitcnt vmcnt(4)" : "+v"(Q0[3]) :: "memory"); LUT1(Q0, 0, 3); LUT1(Q0, 1, 3); LUT1(Q0, 2, 3); LUT1(Q0, 3, 3); SB();
        asm volatile("s_waitcnt vmcnt(3)" : "+v"(Q0[4]) :: "memory"); LUT1(Q0, 0, 4); LUT1(Q0, 1, 4); LUT1(Q0, 2, 4); LUT1(Q0, 3, 4); SB();
        asm volatile("s_waitcnt vmcnt(2)" : "+v"(Q0[5]) :: "memory"); LUT1(Q0, 0, 5); LUT1(Q0, 1, 5); LUT1(Q0, 2, 5); LUT1(Q0, 3, 5); SB();
        asm volatile("s_waitcnt vmcnt(1)" : "+v"(Q0[6]) :: "memory"); LUT1(Q0, 0, 6); LUT1(Q0, 1, 6); LUT1(Q0, 2, 6); LUT1(Q0, 3, 6); SB();
        asm volatile("s_waitcnt vmcnt(0)" : "+v"(Q0[7]) :: "memory"); LUT1(Q0, 0, 7); LUT1(Q0, 1, 7); LUT1(Q0, 2, 7); LUT1(Q0, 3, 7); SB();
        {   u32x4 b0, b1, b2, b3;
            PK(b0, 0, 3); WR(b0, 0); PK(b1, 1, 3); WR(b1, 1); PK(b2, 2, 3); WR(b2, 2); PK(b3, 3, 3); WR(b3, 3);
            SB();
        }
        asm volatile("s_waitcnt lgkmcnt(0)" ::: "memory");
        BAR();
    }
    asm volatile(
        "v_mov_b32 v232, %[xb0]\n\t"
        "v_mov_b32 v233, %[xb1]\n\t"
        "v_mov_b32 v234, %[ba]\n\t"
        ".Lwp10:\n\t"
        "s_cbranch_execz .Lwp99\n\t"
        "ds_read_b128 v[192:195], v234 offset:0\n\t"
        "ds_read_b128 v[128:131], v232 offset:0\n\t"
        "ds_read_b128 v[196:199], v234 offset:8192\n\t"
        "ds_read_b128 v[144:147], v233 offset:0\n\t"
        "ds_read_b128 v[204:207], v234 offset:8448\n\t"
        "ds_read_b128 v[200:203], v234 offset:256\n\t"
        "ds_read_b128 v[132:135], v232 offset:2048\n\t"
        "ds_read_b128 v[148:151], v233 offset:2048\n\t"
        "s_waitcnt lgkmcnt(6)\n\t"
        "v_mfma_f32_16x16x32_bf16 v[0:3], v[128:131], v[192:195], 0\n\t"
        "s_waitcnt lgkmcnt(4)\n\t"
        "v_mfma_f32_16x16x32_bf16 v[0:3], v[144:147], v[196:199], v[0:3]\n\t"
        "ds_read_b128 v[136:139], v232 offset:4096\n\t"
        "s_waitcnt lgkmcnt(4)\n\t"
        "v_mfma_f32_16x16x32_bf16 v[4:7], v[144:147], v[204:207], 0\n\t"
        "s_waitcnt lgkmcnt(3)\n\t"
        "v_mfma_f32_16x16x32_bf16 v[4:7], v[128:131], v[200:203], v[4:7]\n\t"
        "ds_read_b128 v[152:155], v233 offset:4096\n\t"
        "s_waitcnt lgkmcnt(3)\n\t"
        "v_mfma_f32_16x16x32_bf16 v[36:39], v[132:135], v[200:203], 0\n\t"
        "s_waitcnt lgkmcnt(2)\n\t"
        "v_mfma_f32_16x16x32_bf16 v[36:39], v[148:151], v[204:207], v[36:39]\n\t"
        "ds_read_b128 v[140:143], v232 offset:6144\n\t"
        "v_mfma_f32_16x16x32_bf16 v[32:35], v[148:151], v[196:199], 0\n\t"
        "v_mfma_f32_16x16x32_bf16 v[32:35], v[132:135], v[192:195], v[32:35]\n\t"
        "ds_read_b128 v[156:159], v233 offset:6144\n\t"
        "s_waitcnt lgkmcnt(3)\n\t"
        "v_mfma_f32_16x16x32_bf16 v[64:67], v[136:139], v[192:195], 0\n\t"
        "s_waitcnt lgkmcnt(2)\n\t"
        "v_mfma_f32_16x16x32_bf16 v[64:67], v[152:155], v[196:199], v[64:67]\n\t"
        "ds_read_b128 v[208:211], v234 offset:512\n\t"
        "v_mfma_f32_16x16x32_bf16 v[68:71], v[152:155], v[204:207], 0\n\t"
        "v_mfma_f32_16x16x32_bf16 v[68:71], v[136:139], v[200:203], v[68:71]\n\t"
        "ds_read_b128 v[212:215], v234 offset:8704\n\t"
        "s_waitcnt lgkmcnt(3)\n\t"
        "v_mfma_f32_16x16x32_bf16 v[100:103], v[140:143], v[200:203], 0\n\t"
        "s_waitcnt lgkmcnt(2)\n\t"
        "v_mfma_f32_16x16x32_bf16 v[100:103], v[156:159], v[204:207], v[100:103]\n\t"
        "ds_read_b128 v[220:223], v234 offset:8960\n\t"
        "v_mfma_f32_16x16x32_bf16 v[96:99], v[156:159], v[196:199], 0\n\t"
        "v_mfma_f32_16x16x32_bf16 v[96:99], v[140:143], v[192:195], v[96:99]\n\t"
        "ds_read_b128 v[216:219], v234 offset:768\n\t"
        "s_waitcnt lgkmcnt(3)\n\t"
        "v_mfma_f32_16x16x32_bf16 v[8:11], v[128:131], v[208:211], 0\n\t"
        "s_waitcnt lgkmcnt(2)\n\t"
        "v_mfma_f32_16x16x32_bf16 v[8:11], v[144:147], v[212:215], v[8:11]\n\t"
        "ds_read_b128 v[192:195], v234 offset:1024\n\t"
        "s_waitcnt lgkmcnt(2)\n\t"
        "v_mfma_f32_16x16x32_bf16 v[12:15], v[144:147], v[220:223], 0\n\t"
        "s_waitcnt lgkmcnt(1)\n\t"
        "v_mfma_f32_16x16x32_bf16 v[12:15], v[128:131], v[216:219], v[12:15]\n\t"
        "ds_read_b128 v[196:199], v234 offset:9216\n\t"
        "v_mfma_f32_16x16x32_bf16 v[44:47], v[132:135], v[216:219], 0\n\t"
        "v_mfma_f32_16x16x32_bf16 v[44:47], v[148:151], v[220:223], v[44:47]\n\t"
        "ds_read_b128 v[204:207], v234 offset:9472\n\t"
        "v_mfma_f32_16x16x32_bf16 v[40:43], v[148:151], v[212:215], 0\n\t"
        "v_mfma_f32_16x16x32_bf16 v[40:43], v[132:135], v[208:211], v[40:43]\n\t"
        "ds_read_b128 v[200:203], v234 offset:1280\n\t"
        "v_mfma_f32_16x16x32_bf16 v[72:75], v[136:139], v[208:211], 0\n\t"
        "v_mfma_f32_16x16x32_bf16 v[72:75], v[152:155], v[212:215], v[72:75]\n\t"
        "v_mfma_f32_16x16x32_bf16 v[76:79], v[152:155], v[220:223], 0\n\t"
        "v_mfma_f32_16x16x32_bf16 v[76:79], v[136:139], v[216:219], v[76:79]\n\t"
        "v_mfma_f32_16x16x32_bf16 v[108:111], v[140:143], v[216:219], 0\n\t"
        "v_mfma_f32_16x16x32_bf16 v[108:111], v[156:159], v[220:223], v[108:111]\n\t"
        "v_mfma_f32_16x16x32_bf16 v[104:107], v[156:159], v[212:215], 0\n\t"
        "v_mfma_f32_16x16x32_bf16 v[104:107], v[140:143], v[208:211], v[104:107]\n\t"
        "s_waitcnt lgkmcnt(0)\n\t"
        "v_mfma_f32_16x16x32_bf16 v[16:19], v[128:131], v[192:195], 0\n\t"
        "v_mfma_f32_16x16x32_bf16 v[16:19], v[144:147], v[196:199], v[16:19]\n\t"
        "ds_read_b128 v[208:211], v234 offset:1536\n\t"
        "v_mfma_f32_16x16x32_bf16 v[20:23], v[144:147], v[204:207], 0\n\t"
        "v_mfma_f32_16x16x32_bf16 v[20:23], v[128:131], v[200:203], v[20:23]\n\t"
        "ds_read_b128 v[212:215], v234 offset:9728\n\t"
        "v_mfma_f32_16x16x32_bf16 v[52:55], v[132:135], v[200:203], 0\n\t"
        "v_mfma_f32_16x16x32_bf16 v[52:55], v[148:151], v[204:207], v[52:55]\n\t"
        "ds_read_b128 v[220:223], v234 offset:9984\n\t"
        "v_mfma_f32_16x16x32_bf16 v[48:51], v[148:151], v[196:199], 0\n\t"
        "v_mfma_f32_16x16x32_bf16 v[48:51], v[132:135], v[192:195], v[48:51]\n\t"
        "ds_read_b128 v[216:219], v234 offset:1792\n\t"
        "v_mfma_f32_16x16x32_bf16 v[80:83], v[136:139], v[192:195], 0\n\t"
        "v_mfma_f32_16x16x32_bf16 v[80:83], v[152:155], v[196:199], v[80:83]\n\t"
        "v_mfma_f32_16x16x32_bf16 v[84:87], v[152:155], v[204:207], 0\n\t"
        "v_mfma_f32_16x16x32_bf16 v[84:87], v[136:139], v[200:203], v[84:87]\n\t"
        "s_mov_b32 s60, 32768\n\t"
        "s_mov_b32 s62, 5\n\t"
        "v_add_u32 v232, 32768, %[xb0]\n\t"
        "v_add_u32 v233, 32768, %[xb1]\n\t"
        "v_add_u32 v234, 16384, %[ba]\n\t"
        "v_mov_b64 v[24:25], 0\n\t"
        "v_mov_b64 v[26:27], 0\n\t"
        "v_mov_b64 v[28:29], 0\n\t"
        "v_mov_b64 v[30:31], 0\n\t"
        "v_mov_b64 v[56:57], 0\n\t"
        "v_mov_b64 v[58:59], 0\n\t"
        "v_mov_b64 v[60:61], 0\n\t"
        "v_mov_b64 v[62:63], 0\n\t"
        "v_mov_b64 v[88:89], 0\n\t"
        "v_mov_b64 v[90:91], 0\n\t"
        "v_mov_b64 v[92:93], 0\n\t"
        "v_mov_b64 v[94:95], 0\n\t"
        "v_mov_b64 v[120:121], 0\n\t"
        "v_mov_b64 v[122:123], 0\n\t"
        "v_mov_b64 v[124:125], 0\n\t"
        "v_mov_b64 v[126:127], 0\n\t"
        "v_mov_b64 v[116:117], 0\n\t"
        "v_mov_b64 v[118:119], 0\n\t"
        "v_mov_b64 v[112:113], 0\n\t"
        "v_mov_b64 v[114:115], 0\n\t"
        "s_nop 1\n\t"
        "s_mov_b64 exec, 0\n\t"
        "s_cmp_eq_u32 %[w], 0\n\t"
        "s_cbranch_scc1 .Lwp34\n\t"
        "s_cmp_eq_u32 %[w], 1\n\t"
        "s_cbranch_scc1 .Lwp38\n\t"
        "s_cmp_eq_u32 %[w], 2\n\t"
        "s_cbranch_scc1 .Lwp41\n\t"
        "s_branch .Lwp16\n\t"
        ".Lwp97:\n\t"
        "s_mov_b64 exec, -1\n\t"
        ".Lcloop:\n\t"
        "v_mfma_f32_16x16x32_bf16 v[116:119], v[140:143], v[200:203], v[116:119]\n\t"
        "s_sub_u32 s62, s62, 1\n\t"
        "v_mfma_f32_16x16x32_bf16 v[116:119], v[156:159], v[204:207], v[116:119]\n\t"
        "s_waitcnt lgkmcnt(0)\n\t"
        "v_mfma_f32_16x16x32_bf16 v[112:115], v[156:159], v[196:199], v[112:115]\n\t"
        "v_mfma_f32_16x16x32_bf16 v[112:115], v[140:143], v[192:195], v[112:115]\n\t"
        "s_barrier\n\t"
        "v_mfma_f32_16x16x32_bf16 v[24:27], v[128:131], v[208:211], v[24:27]\n\t"
        "v_mfma_f32_16x16x32_bf16 v[24:27], v[144:147], v[212:215], v[24:27]\n\t"
        "ds_read_b128 v[192:195], v234 offset:0\n\t"
        "ds_read_b128 v[160:163], v232 offset:0\n\t"
        "v_mfma_f32_16x16x32_bf16 v[28:31], v[144:147], v[220:223], v[28:31]\n\t"
        "v_mfma_f32_16x16x32_bf16 v[28:31], v[128:131], v[216:219], v[28:31]\n\t"
        "ds_read_b128 v[196:199], v234 offset:8192\n\t"
        "ds_read_b128 v[176:179], v233 offset:0\n\t"
        "v_mfma_f32_16x16x32_bf16 v[60:63], v[132:135], v[216:219], v[60:63]\n\t"
        "v_mfma_f32_16x16x32_bf16 v[60:63], v[148:151], v[220:223], v[60:63]\n\t"
        "ds_read_b128 v[204:207], v234 offset:8448\n\t"
        "v_mfma_f32_16x16x32_bf16 v[56:59], v[148:151], v[212:215], v[56:59]\n\t"
        "v_mfma_f32_16x16x32_bf16 v[56:59], v[132:135], v[208:211], v[56:59]\n\t"
        "ds_read_b128 v[200:203], v234 offset:256\n\t"
        "v_mfma_f32_16x16x32_bf16 v[88:91], v[136:139], v[208:211], v[88:91]\n\t"
        "v_mfma_f32_16x16x32_bf16 v[88:91], v[152:155], v[212:215], v[88:91]\n\t"
        "ds_read_b128 v[164:167], v232 offset:2048\n\t"
        "v_mfma_f32_16x16x32_bf16 v[92:95], v[152:155], v[220:223], v[92:95]\n\t"
        "v_mfma_f32_16x16x32_bf16 v[92:95], v[136:139], v[216:219], v[92:95]\n\t"
        "ds_read_b128 v[180:183], v233 offset:2048\n\t"
        "v_mfma_f32_16x16x32_bf16 v[124:127], v[140:143], v[216:219], v[124:127]\n\t"
        "v_mfma_f32_16x16x32_bf16 v[124:127], v[156:159], v[220:223], v[124:127]\n\t"
        "ds_read_b128 v[168:171], v232 offset:4096\n\t"
        "v_mfma_f32_16x16x32_bf16 v[120:123], v[156:159], v[212:215], v[120:123]\n\t"
        "v_mfma_f32_16x16x32_bf16 v[120:123], v[140:143], v[208:211], v[120:123]\n\t"
        "ds_read_b128 v[184:187], v233 offset:4096\n\t"
        "s_waitcnt lgkmcnt(4)\n\t"
        "v_mfma_f32_16x16x32_bf16 v[0:3], v[160:163], v[192:195], v[0:3]\n\t"
        "v_mfma_f32_16x16x32_bf16 v[0:3], v[176:179], v[196:199], v[0:3]\n\t"
        "ds_read_b128 v[172:175], v232 offset:6144\n\t"
        "ds_read_b128 v[208:211], v234 offset:512\n\t"
        "v_mfma_f32_16x16x32_bf16 v[4:7], v[176:179], v[204:207], v[4:7]\n\t"
        "v_mfma_f32_16x16x32_bf16 v[4:7], v[160:163], v[200:203], v[4:7]\n\t"
        "ds_read_b128 v[188:191], v233 offset:6144\n\t"
        "s_waitcnt lgkmcnt(5)\n\t"
        "v_mfma_f32_16x16x32_bf16 v[36:39], v[164:167], v[200:203], v[36:39]\n\t"
        "ds_read_b128 v[212:215], v234 offset:8704\n\t"
        "v_mfma_f32_16x16x32_bf16 v[36:39], v[180:183], v[204:207], v[36:39]\n\t"
        "ds_read_b128 v[220:223], v234 offset:8960\n\t"
        "v_mfma_f32_16x16x32_bf16 v[32:35], v[180:183], v[196:199], v[32:35]\n\t"
        "v_mfma_f32_16x16x32_bf16 v[32:35], v[164:167], v[192:195], v[32:35]\n\t"
        "ds_read_b128 v[216:219], v234 offset:768\n\t"
        "s_waitcnt lgkmcnt(6)\n\t"
        "v_mfma_f32_16x16x32_bf16 v[64:67], v[168:171], v[192:195], v[64:67]\n\t"
        "v_mfma_f32_16x16x32_bf16 v[64:67], v[184:187], v[196:199], v[64:67]\n\t"
        "v_mfma_f32_16x16x32_bf16 v[68:71], v[184:187], v[204:207], v[68:71]\n\t"
        "v_mfma_f32_16x16x32_bf16 v[68:71], v[168:171], v[200:203], v[68:71]\n\t"
        "s_waitcnt lgkmcnt(3)\n\t"
        "v_mfma_f32_16x16x32_bf16 v[100:103], v[172:175], v[200:203], v[100:103]\n\t"
        "v_mfma_f32_16x16x32_bf16 v[100:103], v[188:191], v[204:207], v[100:103]\n\t"
        "v_mfma_f32_16x16x32_bf16 v[96:99], v[188:191], v[196:199], v[96:99]\n\t"
        "v_mfma_f32_16x16x32_bf16 v[96:99], v[172:175], v[192:195], v[96:99]\n\t"
        "v_mfma_f32_16x16x32_bf16 v[8:11], v[160:163], v[208:211], v[8:11]\n\t"
        "s_waitcnt lgkmcnt(0)\n\t"
        "v_mfma_f32_16x16x32_bf16 v[8:11], v[176:179], v[212:215], v[8:11]\n\t"
        "ds_read_b128 v[192:195], v234 offset:1024\n\t"
        "v_mfma_f32_16x16x32_bf16 v[12:15], v[176:179], v[220:223], v[12:15]\n\t"
        "v_mfma_f32_16x16x32_bf16 v[12:15], v[160:163], v[216:219], v[12:15]\n\t"
        "ds_read_b128 v[196:199], v234 offset:9216\n\t"
        "v_mfma_f32_16x16x32_bf16 v[44:47], v[164:167], v[216:219], v[44:47]\n\t"
        "v_mfma_f32_16x16x32_bf16 v[44:47], v[180:183], v[220:223], v[44:47]\n\t"
        "ds_read_b128 v[204:207], v234 offset:9472\n\t"
        "v_mfma_f32_16x16x32_bf16 v[40:43], v[180:183], v[212:215], v[40:43]\n\t"
        "v_mfma_f32_16x16x32_bf16 v[40:43], v[164:167], v[208:211], v[40:43]\n\t"
        "ds_read_b128 v[200:203], v234 offset:1280\n\t"
        "v_mfma_f32_16x16x32_bf16 v[72:75], v[168:171], v[208:211], v[72:75]\n\t"
        "v_mfma_f32_16x16x32_bf16 v[72:75], v[184:187], v[212:215], v[72:75]\n\t"
        "v_mfma_f32_16x16x32_bf16 v[76:79], v[184:187], v[220:223], v[76:79]\n\t"
        "v_mfma_f32_16x16x32_bf16 v[76:79], v[168:171], v[216:219], v[76:79]\n\t"
        ".Lwp12:\n\t"
        "s_cbranch_execz .Lwp99\n\t"
        "v_mfma_f32_16x16x32_bf16 v[108:111], v[172:175], v[216:219], v[108:111]\n\t"
        "v_mfma_f32_16x16x32_bf16 v[108:111], v[188:191], v[220:223], v[108:111]\n\t"
        "v_mfma_f32_16x16x32_bf16 v[104:107], v[188:191], v[212:215], v[104:107]\n\t"
        "v_mfma_f32_16x16x32_bf16 v[104:107], v[172:175], v[208:211], v[104:107]\n\t"
        "s_waitcnt lgkmcnt(0)\n\t"
        "v_mfma_f32_16x16x32_bf16 v[16:19], v[160:163], v[192:195], v[16:19]\n\t"
        "v_mfma_f32_16x16x32_bf16 v[16:19], v[176:179], v[196:199], v[16:19]\n\t"
        "ds_read_b128 v[208:211], v234 offset:1536\n\t"
        "v_mfma_f32_16x16x32_bf16 v[20:23], v[176:179], v[204:207], v[20:23]\n\t"
        "v_mfma_f32_16x16x32_bf16 v[20:23], v[160:163], v[200:203], v[20:23]\n\t"
        "ds_read_b128 v[212:215], v234 offset:9728\n\t"
        "v_mfma_f32_16x16x32_bf16 v[52:55], v[164:167], v[200:203], v[52:55]\n\t"
        "v_mfma_f32_16x16x32_bf16 v[52:55], v[180:183], v[204:207], v[52:55]\n\t"
        "ds_read_b128 v[220:223], v234 offset:9984\n\t"
        "v_mfma_f32_16x16x32_bf16 v[48:51], v[180:183], v[196:199], v[48:51]\n\t"
        "v_mfma_f32_16x16x32_bf16 v[48:51], v[164:167], v[192:195], v[48:51]\n\t"
        "ds_read_b128 v[216:219], v234 offset:1792\n\t"
        "s_add_u32 s60, s60, 32768\n\t"
        "v_mfma_f32_16x16x32_bf16 v[80:83], v[168:171], v[192:195], v[80:83]\n\t"
        "s_cmp_eq_u32 s60, 98304\n\t"
        "s_cselect_b32 s60, 0, s60\n\t"
        "v_mfma_f32_16x16x32_bf16 v[80:83], v[184:187], v[196:199], v[80:83]\n\t"
        "v_add_u32 v232, s60, %[xb0]\n\t"
        "s_lshr_b32 s61, s60, 1\n\t"
        "v_mfma_f32_16x16x32_bf16 v[84:87], v[184:187], v[204:207], v[84:87]\n\t"
        "v_add_u32 v233, s60, %[xb1]\n\t"
        "v_add_u32 v234, s61, %[ba]\n\t"
        "v_mfma_f32_16x16x32_bf16 v[84:87], v[168:171], v[200:203], v[84:87]\n\t"
        "v_mfma_f32_16x16x32_bf16 v[116:119], v[172:175], v[200:203], v[116:119]\n\t"
        "v_mfma_f32_16x16x32_bf16 v[116:119], v[188:191], v[204:207], v[116:119]\n\t"
        "s_waitcnt lgkmcnt(0)\n\t"
        "v_mfma_f32_16x16x32_bf16 v[112:115], v[188:191], v[196:199], v[112:115]\n\t"
        "v_mfma_f32_16x16x32_bf16 v[112:115], v[172:175], v[192:195], v[112:115]\n\t"
        "s_barrier\n\t"
        "v_mfma_f32_16x16x32_bf16 v[24:27], v[160:163], v[208:211], v[24:27]\n\t"
        "v_mfma_f32_16x16x32_bf16 v[24:27], v[176:179], v[212:215], v[24:27]\n\t"
        "ds_read_b128 v[192:195], v234 offset:0\n\t"
        "ds_read_b128 v[128:131], v232 offset:0\n\t"
        "v_mfma_f32_16x16x32_bf16 v[28:31], v[176:179], v[220:223], v[28:31]\n\t"
        "v_mfma_f32_16x16x32_bf16 v[28:31], v[160:163], v[216:219], v[28:31]\n\t"
        "ds_read_b128 v[196:199], v234 offset:8192\n\t"
        "ds_read_b128 v[144:147], v233 offset:0\n\t"
        "v_mfma_f32_16x16x32_bf16 v[60:63], v[164:167], v[216:219], v[60:63]\n\t"
        "v_mfma_f32_16x16x32_bf16 v[60:63], v[180:183], v[220:223], v[60:63]\n\t"
        "ds_read_b128 v[204:207], v234 offset:8448\n\t"
        "v_mfma_f32_16x16x32_bf16 v[56:59], v[180:183], v[212:215], v[56:59]\n\t"
        "v_mfma_f32_16x16x32_bf16 v[56:59], v[164:167], v[208:211], v[56:59]\n\t"
        "ds_read_b128 v[200:203], v234 offset:256\n\t"
        "v_mfma_f32_16x16x32_bf16 v[88:91], v[168:171], v[208:211], v[88:91]\n\t"
        "v_mfma_f32_16x16x32_bf16 v[88:91], v[184:187], v[212:215], v[88:91]\n\t"
        "ds_read_b128 v[132:135], v232 offset:2048\n\t"
        "v_mfma_f32_16x16x32_bf16 v[92:95], v[184:187], v[220:223], v[92:95]\n\t"
        "v_mfma_f32_16x16x32_bf16 v[92:95], v[168:171], v[216:219], v[92:95]\n\t"
        "ds_read_b128 v[148:151], v233 offset:2048\n\t"
        "v_mfma_f32_16x16x32_bf16 v[124:127], v[172:175], v[216:219], v[124:127]\n\t"
        "v_mfma_f32_16x16x32_bf16 v[124:127], v[188:191], v[220:223], v[124:127]\n\t"
        "ds_read_b128 v[136:139], v232 offset:4096\n\t"
        "v_mfma_f32_16x16x32_bf16 v[120:123], v[188:191], v[212:215], v[120:123]\n\t"
        "v_mfma_f32_16x16x32_bf16 v[120:123], v[172:175], v[208:211], v[120:123]\n\t"
        "ds_read_b128 v[152:155], v233 offset:4096\n\t"
        "s_waitcnt lgkmcnt(4)\n\t"
        "v_mfma_f32_16x16x32_bf16 v[0:3], v[128:131], v[192:195], v[0:3]\n\t"
        "v_mfma_f32_16x16x32_bf16 v[0:3], v[144:147], v[196:199], v[0:3]\n\t"
        "ds_read_b128 v[140:143], v232 offset:6144\n\t"
        "ds_read_b128 v[208:211], v234 offset:512\n\t"
        "v_mfma_f32_16x16x32_bf16 v[4:7], v[144:147], v[204:207], v[4:7]\n\t"
        "v_mfma_f32_16x16x32_bf16 v[4:7], v[128:131], v[200:203], v[4:7]\n\t"
        "ds_read_b128 v[156:159], v233 offset:6144\n\t"
        "s_waitcnt lgkmcnt(5)\n\t"
        "v_mfma_f32_16x16x32_bf16 v[36:39], v[132:135], v[200:203], v[36:39]\n\t"
        "ds_read_b128 v[212:215], v234 offset:8704\n\t"
        "v_mfma_f32_16x16x32_bf16 v[36:39], v[148:151], v[204:207], v[36:39]\n\t"
        "ds_read_b128 v[220:223], v234 offset:8960\n\t"
        "v_mfma_f32_16x16x32_bf16 v[32:35], v[148:151], v[196:199], v[32:35]\n\t"
        "v_mfma_f32_16x16x32_bf16 v[32:35], v[132:135], v[192:195], v[32:35]\n\t"
        "ds_read_b128 v[216:219], v234 offset:768\n\t"
        "s_waitcnt lgkmcnt(6)\n\t"
        "v_mfma_f32_16x16x32_bf16 v[64:67], v[136:139], v[192:195], v[64:67]\n\t"
        "v_mfma_f32_16x16x32_bf16 v[64:67], v[152:155], v[196:199], v[64:67]\n\t"
        "v_mfma_f32_16x16x32_bf16 v[68:71], v[152:155], v[204:207], v[68:71]\n\t"
        "v_mfma_f32_16x16x32_bf16 v[68:71], v[136:139], v[200:203], v[68:71]\n\t"
        "s_waitcnt lgkmcnt(3)\n\t"
        "v_mfma_f32_16x16x32_bf16 v[100:103], v[140:143], v[200:203], v[100:103]\n\t"
        "v_mfma_f32_16x16x32_bf16 v[100:103], v[156:159], v[204:207], v[100:103]\n\t"
        "v_mfma_f32_16x16x32_bf16 v[96:99], v[156:159], v[196:199], v[96:99]\n\t"
        "v_mfma_f32_16x16x32_bf16 v[96:99], v[140:143], v[192:195], v[96:99]\n\t"
        "v_mfma_f32_16x16x32_bf16 v[8:11], v[128:131], v[208:211], v[8:11]\n\t"
        "s_waitcnt lgkmcnt(0)\n\t"
        "v_mfma_f32_16x16x32_bf16 v[8:11], v[144:147], v[212:215], v[8:11]\n\t"
        "ds_read_b128 v[192:195], v234 offset:1024\n\t"
        "v_mfma_f32_16x16x32_bf16 v[12:15], v[144:147], v[220:223], v[12:15]\n\t"
        "v_mfma_f32_16x16x32_bf16 v[12:15], v[128:131], v[216:219], v[12:15]\n\t"
        "ds_read_b128 v[196:199], v234 offset:9216\n\t"
        "v_mfma_f32_16x16x32_bf16 v[44:47], v[132:135], v[216:219], v[44:47]\n\t"
        "v_mfma_f32_16x16x32_bf16 v[44:47], v[148:151], v[220:223], v[44:47]\n\t"
        "ds_read_b128 v[204:207], v234 offset:9472\n\t"
        "v_mfma_f32_16x16x32_bf16 v[40:43], v[148:151], v[212:215], v[40:43]\n\t"
        "v_mfma_f32_16x16x32_bf16 v[40:43], v[132:135], v[208:211], v[40:43]\n\t"
        "ds_read_b128 v[200:203], v234 offset:1280\n\t"
        "v_mfma_f32_16x16x32_bf16 v[72:75], v[136:139], v[208:211], v[72:75]\n\t"
        "v_mfma_f32_16x16x32_bf16 v[72:75], v[152:155], v[212:215], v[72:75]\n\t"
        "v_mfma_f32_16x16x32_bf16 v[76:79], v[152:155], v[220:223], v[76:79]\n\t"
        "v_mfma_f32_16x16x32_bf16 v[76:79], v[136:139], v[216:219], v[76:79]\n\t"
        "v_mfma_f32_16x16x32_bf16 v[108:111], v[140:143], v[216:219], v[108:111]\n\t"
        "v_mfma_f32_16x16x32_bf16 v[108:111], v[156:159], v[220:223], v[108:111]\n\t"
        "v_mfma_f32_16x16x32_bf16 v[104:107], v[156:159], v[212:215], v[104:107]\n\t"
        "v_mfma_f32_16x16x32_bf16 v[104:107], v[140:143], v[208:211], v[104:107]\n\t"
        "s_waitcnt lgkmcnt(0)\n\t"
        "v_mfma_f32_16x16x32_bf16 v[16:19], v[128:131], v[192:195], v[16:19]\n\t"
        "v_mfma_f32_16x16x32_bf16 v[16:19], v[144:147], v[196:199], v[16:19]\n\t"
        "ds_read_b128 v[208:211], v234 offset:1536\n\t"
        "v_mfma_f32_16x16x32_bf16 v[20:23], v[144:147], v[204:207], v[20:23]\n\t"
        "v_mfma_f32_16x16x32_bf16 v[20:23], v[128:131], v[200:203], v[20:23]\n\t"
        "ds_read_b128 v[212:215], v234 offset:9728\n\t"
        "v_mfma_f32_16x16x32_bf16 v[52:55], v[132:135], v[200:203], v[52:55]\n\t"
        "v_mfma_f32_16x16x32_bf16 v[52:55], v[148:151], v[204:207], v[52:55]\n\t"
        "ds_read_b128 v[220:223], v234 offset:9984\n\t"
        "v_mfma_f32_16x16x32_bf16 v[48:51], v[148:151], v[196:199], v[48:51]\n\t"
        "v_mfma_f32_16x16x32_bf16 v[48:51], v[132:135], v[192:195], v[48:51]\n\t"
        "ds_read_b128 v[216:219], v234 offset:1792\n\t"
        "s_add_u32 s60, s60, 32768\n\t"
        "v_mfma_f32_16x16x32_bf16 v[80:83], v[136:139], v[192:195], v[80:83]\n\t"
        "s_cmp_eq_u32 s60, 98304\n\t"
        "s_cselect_b32 s60, 0, s60\n\t"
        "v_mfma_f32_16x16x32_bf16 v[80:83], v[152:155], v[196:199], v[80:83]\n\t"
        "v_add_u32 v232, s60, %[xb0]\n\t"
        "s_lshr_b32 s61, s60, 1\n\t"
        "v_mfma_f32_16x16x32_bf16 v[84:87], v[152:155], v[204:207], v[84:87]\n\t"
        "v_add_u32 v233, s60, %[xb1]\n\t"
        "v_add_u32 v234, s61, %[ba]\n\t"
        "v_mfma_f32_16x16x32_bf16 v[84:87], v[136:139], v[200:203], v[84:87]\n\t"
        "s_cmp_lg_u32 s62, -1\n\t"
        "s_cbranch_scc1 .Lcloop\n\t"
        "v_mfma_f32_16x16x32_bf16 v[116:119], v[140:143], v[200:203], v[116:119]\n\t"
        "v_mfma_f32_16x16x32_bf16 v[116:119], v[156:159], v[204:207], v[116:119]\n\t"
        "s_waitcnt lgkmcnt(0)\n\t"
        "v_mfma_f32_16x16x32_bf16 v[112:115], v[156:159], v[196:199], v[112:115]\n\t"
        "v_mfma_f32_16x16x32_bf16 v[112:115], v[140:143], v[192:195], v[112:115]\n\t"
        "s_barrier\n\t"
        "v_mfma_f32_16x16x32_bf16 v[24:27], v[128:131], v[208:211], v[24:27]\n\t"
        "s_setprio 2\n\t"
        ".Lwp14:\n\t"
        "s_cbranch_execz .Lwp15\n\t"
        "v_mfma_f32_16x16x32_bf16 v[24:27], v[144:147], v[212:215], v[24:27]\n\t"
        "ds_read_b128 v[160:163], %[ba] offset:16384\n\t"
        "ds_read_b128 v[164:167], v232 offset:0\n\t"
        "v_mfma_f32_16x16x32_bf16 v[28:31], v[144:147], v[220:223], v[28:31]\n\t"
        "v_mfma_f32_16x16x32_bf16 v[28:31], v[128:131], v[216:219], v[28:31]\n\t"
        "ds_read_b128 v[144:147], %[ba] offset:24576\n\t"
        "ds_read_b128 v[168:171], v233 offset:0\n\t"
        "v_mfma_f32_16x16x32_bf16 v[60:63], v[132:135], v[216:219], v[60:63]\n\t"
        "v_mfma_f32_16x16x32_bf16 v[60:63], v[148:151], v[220:223], v[60:63]\n\t"
        "ds_read_b128 v[128:131], %[ba] offset:24832\n\t"
        "ds_read_b128 v[172:175], %[ba] offset:16640\n\t"
        "v_mfma_f32_16x16x32_bf16 v[56:59], v[148:151], v[212:215], v[56:59]\n\t"
        "ds_read_b128 v[176:179], v232 offset:2048\n\t"
        "v_mfma_f32_16x16x32_bf16 v[56:59], v[132:135], v[208:211], v[56:59]\n\t"
        "ds_read_b128 v[148:151], v233 offset:2048\n\t"
        "v_mfma_f32_16x16x32_bf16 v[88:91], v[136:139], v[208:211], v[88:91]\n\t"
        "v_mfma_f32_16x16x32_bf16 v[88:91], v[152:155], v[212:215], v[88:91]\n\t"
        "ds_read_b128 v[132:135], v232 offset:4096\n\t"
        "v_mfma_f32_16x16x32_bf16 v[92:95], v[152:155], v[220:223], v[92:95]\n\t"
        "v_mfma_f32_16x16x32_bf16 v[92:95], v[136:139], v[216:219], v[92:95]\n\t"
        "ds_read_b128 v[152:155], v233 offset:4096\n\t"
        "v_mfma_f32_16x16x32_bf16 v[124:127], v[140:143], v[216:219], v[124:127]\n\t"
        "v_mfma_f32_16x16x32_bf16 v[124:127], v[156:159], v[220:223], v[124:127]\n\t"
        "ds_read_b128 v[136:139], v232 offset:6144\n\t"
        "v_mfma_f32_16x16x32_bf16 v[120:123], v[156:159], v[212:215], v[120:123]\n\t"
        "v_mfma_f32_16x16x32_bf16 v[120:123], v[140:143], v[208:211], v[120:123]\n\t"
        "ds_read_b128 v[156:159], v233 offset:6144\n\t"
        "s_waitcnt lgkmcnt(4)\n\t"
        "v_mfma_f32_16x16x32_bf16 v[0:3], v[164:167], v[160:163], v[0:3]\n\t"
        "v_mfma_f32_16x16x32_bf16 v[0:3], v[168:171], v[144:147], v[0:3]\n\t"
        "ds_read_b128 v[140:143], %[ba] offset:16896\n\t"
        "v_mfma_f32_16x16x32_bf16 v[4:7], v[168:171], v[128:131], v[4:7]\n\t"
        "v_mfma_f32_16x16x32_bf16 v[4:7], v[164:167], v[172:175], v[4:7]\n\t"
        "ds_read_b128 v[180:183], %[ba] offset:25088\n\t"
        "v_mfma_f32_16x16x32_bf16 v[36:39], v[176:179], v[172:175], v[36:39]\n\t"
        "v_mfma_f32_16x16x32_bf16 v[36:39], v[148:151], v[128:131], v[36:39]\n\t"
        "ds_read_b128 v[184:187], %[ba] offset:25344\n\t"
        "v_mfma_f32_16x16x32_bf16 v[32:35], v[148:151], v[144:147], v[32:35]\n\t"
        "v_mfma_f32_16x16x32_bf16 v[32:35], v[176:179], v[160:163], v[32:35]\n\t"
        "ds_read_b128 v[188:191], %[ba] offset:17152\n\t"
        "s_waitcnt lgkmcnt(4)\n\t"
        "v_mfma_f32_16x16x32_bf16 v[64:67], v[132:135], v[160:163], v[64:67]\n\t"
        "v_mfma_f32_16x16x32_bf16 v[64:67], v[152:155], v[144:147], v[64:67]\n\t"
        "ds_read_b128 v[192:195], %[ba] offset:17408\n\t"
        "v_mfma_f32_16x16x32_bf16 v[68:71], v[152:155], v[128:131], v[68:71]\n\t"
        "v_mfma_f32_16x16x32_bf16 v[68:71], v[132:135], v[172:175], v[68:71]\n\t"
        "ds_read_b128 v[196:199], v232 offset:32768\n\t"
        "v_mfma_f32_16x16x32_bf16 v[100:103], v[136:139], v[172:175], v[100:103]\n\t"
        "v_mfma_f32_16x16x32_bf16 v[100:103], v[156:159], v[128:131], v[100:103]\n\t"
        "ds_read_b128 v[172:175], %[ba] offset:25600\n\t"
        "v_mfma_f32_16x16x32_bf16 v[96:99], v[156:159], v[144:147], v[96:99]\n\t"
        "v_mfma_f32_16x16x32_bf16 v[96:99], v[136:139], v[160:163], v[96:99]\n\t"
        "ds_read_b128 v[128:131], v233 offset:32768\n\t"
        "s_waitcnt lgkmcnt(4)\n\t"
        "v_mfma_f32_16x16x32_bf16 v[8:11], v[164:167], v[140:143], v[8:11]\n\t"
        "v_mfma_f32_16x16x32_bf16 v[8:11], v[168:171], v[180:183], v[8:11]\n\t"
        "ds_read_b128 v[144:147], %[ba] offset:25856\n\t"
        "v_mfma_f32_16x16x32_bf16 v[12:15], v[168:171], v[184:187], v[12:15]\n\t"
        "v_mfma_f32_16x16x32_bf16 v[12:15], v[164:167], v[188:191], v[12:15]\n\t"
        "ds_read_b128 v[160:163], %[ba] offset:17664\n\t"
        "v_mfma_f32_16x16x32_bf16 v[44:47], v[176:179], v[188:191], v[44:47]\n\t"
        "v_mfma_f32_16x16x32_bf16 v[44:47], v[148:151], v[184:187], v[44:47]\n\t"
        "ds_read_b128 v[164:167], %[ba] offset:17920\n\t"
        "ds_read_b128 v[168:171], %[ba] offset:26112\n\t"
        "v_mfma_f32_16x16x32_bf16 v[40:43], v[148:151], v[180:183], v[40:43]\n\t"
        "v_mfma_f32_16x16x32_bf16 v[40:43], v[176:179], v[140:143], v[40:43]\n\t"
        "ds_read_b128 v[148:151], %[ba] offset:26368\n\t"
        "v_mfma_f32_16x16x32_bf16 v[72:75], v[132:135], v[140:143], v[72:75]\n\t"
        "v_mfma_f32_16x16x32_bf16 v[72:75], v[152:155], v[180:183], v[72:75]\n\t"
        "ds_read_b128 v[176:179], %[ba] offset:18176\n\t"
        "v_mfma_f32_16x16x32_bf16 v[76:79], v[152:155], v[184:187], v[76:79]\n\t"
        "v_mfma_f32_16x16x32_bf16 v[76:79], v[132:135], v[188:191], v[76:79]\n\t"
        "ds_read_b128 v[152:155], v232 offset:34816\n\t"
        "v_mfma_f32_16x16x32_bf16 v[108:111], v[136:139], v[188:191], v[108:111]\n\t"
        "v_mfma_f32_16x16x32_bf16 v[108:111], v[156:159], v[184:187], v[108:111]\n\t"
        "ds_read_b128 v[132:135], v233 offset:34816\n\t"
        "v_mfma_f32_16x16x32_bf16 v[104:107], v[156:159], v[180:183], v[104:107]\n\t"
        "v_mfma_f32_16x16x32_bf16 v[104:107], v[136:139], v[140:143], v[104:107]\n\t"
        "s_waitcnt lgkmcnt(3)\n\t"
        "v_mfma_f32_16x16x32_bf16 v[0:3], v[196:199], v[192:195], v[0:3]\n\t"
        "v_mfma_f32_16x16x32_bf16 v[0:3], v[128:131], v[172:175], v[0:3]\n\t"
        "v_mfma_f32_16x16x32_bf16 v[4:7], v[128:131], v[144:147], v[4:7]\n\t"
        "v_mfma_f32_16x16x32_bf16 v[4:7], v[196:199], v[160:163], v[4:7]\n\t"
        "ds_read_b128 v[136:139], v232 offset:36864\n\t"
        "v_mfma_f32_16x16x32_bf16 v[8:11], v[196:199], v[164:167], v[8:11]\n\t"
        "v_mfma_f32_16x16x32_bf16 v[8:11], v[128:131], v[168:171], v[8:11]\n\t"
        "ds_read_b128 v[140:143], v233 offset:36864\n\t"
        "v_mfma_f32_16x16x32_bf16 v[12:15], v[128:131], v[148:151], v[12:15]\n\t"
        "s_waitcnt lgkmcnt(2)\n\t"
        "v_mfma_f32_16x16x32_bf16 v[12:15], v[196:199], v[176:179], v[12:15]\n\t"
        "v_mfma_f32_16x16x32_bf16 v[32:35], v[152:155], v[192:195], v[32:35]\n\t"
        "v_mfma_f32_16x16x32_bf16 v[32:35], v[132:135], v[172:175], v[32:35]\n\t"
        "ds_read_b128 v[128:131], v232 offset:38912\n\t"
        "v_mfma_f32_16x16x32_bf16 v[36:39], v[132:135], v[144:147], v[36:39]\n\t"
        "v_mfma_f32_16x16x32_bf16 v[36:39], v[152:155], v[160:163], v[36:39]\n\t"
        "ds_read_b128 v[156:159], v233 offset:38912\n\t"
        "v_mfma_f32_16x16x32_bf16 v[40:43], v[152:155], v[164:167], v[40:43]\n\t"
        "v_mfma_f32_16x16x32_bf16 v[40:43], v[132:135], v[168:171], v[40:43]\n\t"
        "v_mfma_f32_16x16x32_bf16 v[44:47], v[132:135], v[148:151], v[44:47]\n\t"
        "v_mfma_f32_16x16x32_bf16 v[44:47], v[152:155], v[176:179], v[44:47]\n\t"
        "s_waitcnt lgkmcnt(0)\n\t"
        "s_barrier\n\t"
        "v_mfma_f32_16x16x32_bf16 v[64:67], v[136:139], v[192:195], v[64:67]\n\t"
        ".Lwp15:\n\t"
        "s_cbranch_execz .Lwp97\n\t"
        "v_mfma_f32_16x16x32_bf16 v[64:67], v[140:143], v[172:175], v[64:67]\n\t"
        "ds_read_b128 v[132:135], %[ba] offset:32768\n\t"
        "ds_read_b128 v[152:155], %[xb0] offset:0\n\t"
        "v_mfma_f32_16x16x32_bf16 v[68:71], v[140:143], v[144:147], v[68:71]\n\t"
        "v_mfma_f32_16x16x32_bf16 v[68:71], v[136:139], v[160:163], v[68:71]\n\t"
        "ds_read_b128 v[180:183], %[ba] offset:40960\n\t"
        "ds_read_b128 v[184:187], %[xb1] offset:0\n\t"
        "v_mfma_f32_16x16x32_bf16 v[72:75], v[136:139], v[164:167], v[72:75]\n\t"
        "v_mfma_f32_16x16x32_bf16 v[72:75], v[140:143], v[168:171], v[72:75]\n\t"
        "ds_read_b128 v[188:191], %[ba] offset:41216\n\t"
        "ds_read_b128 v[196:199], %[ba] offset:33024\n\t"
        "v_mfma_f32_16x16x32_bf16 v[76:79], v[140:143], v[148:151], v[76:79]\n\t"
        "v_mfma_f32_16x16x32_bf16 v[76:79], v[136:139], v[176:179], v[76:79]\n\t"
        "ds_read_b128 v[140:143], %[ba] offset:33280\n\t"
        "s_waitcnt lgkmcnt(5)\n\t"
        "v_mfma_f32_16x16x32_bf16 v[0:3], v[152:155], v[132:135], v[0:3]\n\t"
        "ds_read_b128 v[136:139], %[ba] offset:41472\n\t"
        "s_waitcnt lgkmcnt(4)\n\t"
        "v_mfma_f32_16x16x32_bf16 v[0:3], v[184:187], v[180:183], v[0:3]\n\t"
        "ds_read_b128 v[200:203], %[ba] offset:41728\n\t"
        "s_waitcnt lgkmcnt(4)\n\t"
        "v_mfma_f32_16x16x32_bf16 v[4:7], v[184:187], v[188:191], v[4:7]\n\t"
        "ds_read_b128 v[204:207], %[ba] offset:33536\n\t"
        "s_waitcnt lgkmcnt(4)\n\t"
        "v_mfma_f32_16x16x32_bf16 v[4:7], v[152:155], v[196:199], v[4:7]\n\t"
        "ds_read_b128 v[208:211], %[xb0] offset:2048\n\t"
        "s_waitcnt lgkmcnt(4)\n\t"
        "v_mfma_f32_16x16x32_bf16 v[8:11], v[152:155], v[140:143], v[8:11]\n\t"
        "s_waitcnt lgkmcnt(3)\n\t"
        "v_mfma_f32_16x16x32_bf16 v[8:11], v[184:187], v[136:139], v[8:11]\n\t"
        "ds_read_b128 v[212:215], %[xb1] offset:2048\n\t"
        "s_waitcnt lgkmcnt(3)\n\t"
        "v_mfma_f32_16x16x32_bf16 v[12:15], v[184:187], v[200:203], v[12:15]\n\t"
        "s_waitcnt lgkmcnt(2)\n\t"
        "v_mfma_f32_16x16x32_bf16 v[12:15], v[152:155], v[204:207], v[12:15]\n\t"
        "v_mfma_f32_16x16x32_bf16 v[96:99], v[128:131], v[192:195], v[96:99]\n\t"
        "v_mfma_f32_16x16x32_bf16 v[96:99], v[156:159], v[172:175], v[96:99]\n\t"
        "ds_read_b128 v[152:155], %[xb0] offset:4096\n\t"
        "v_cvt_pk_bf16_f32 v224, v0, v4\n\t"
        "v_mfma_f32_16x16x32_bf16 v[100:103], v[156:159], v[144:147], v[100:103]\n\t"
        "v_mfma_f32_16x16x32_bf16 v[100:103], v[128:131], v[160:163], v[100:103]\n\t"
        "ds_read_b128 v[144:147], %[xb1] offset:4096\n\t"
        "v_cvt_pk_bf16_f32 v225, v8, v12\n\t"
        "v_mfma_f32_16x16x32_bf16 v[104:107], v[128:131], v[164:167], v[104:107]\n\t"
        "global_store_dwordx2 %[oo0], v[224:225], %[ob0] offset:0 nt\n\t"
        "v_mfma_f32_16x16x32_bf16 v[104:107], v[156:159], v[168:171], v[104:107]\n\t"
        "ds_read_b128 v[160:163], %[xb0] offset:6144\n\t"
        "v_cvt_pk_bf16_f32 v227, v9, v13\n\t"
        "v_mfma_f32_16x16x32_bf16 v[108:111], v[156:159], v[148:151], v[108:111]\n\t"
        "v_cvt_pk_bf16_f32 v226, v1, v5\n\t"
        "v_mfma_f32_16x16x32_bf16 v[108:111], v[128:131], v[176:179], v[108:111]\n\t"
        "ds_read_b128 v[148:151], %[xb1] offset:6144\n\t"
        "s_waitcnt lgkmcnt(4)\n\t"
        "v_mfma_f32_16x16x32_bf16 v[32:35], v[208:211], v[132:135], v[32:35]\n\t"
        "global_store_dwordx2 %[oo1], v[226:227], %[ob0] offset:0 nt\n\t"
        "v_mfma_f32_16x16x32_bf16 v[32:35], v[212:215], v[180:183], v[32:35]\n\t"
        "ds_read_b128 v[128:131], v232 offset:0\n\t"
        "v_cvt_pk_bf16_f32 v229, v10, v14\n\t"
        "v_mfma_f32_16x16x32_bf16 v[36:39], v[212:215], v[188:191], v[36:39]\n\t"
        "v_cvt_pk_bf16_f32 v228, v2, v6\n\t"
        "v_mfma_f32_16x16x32_bf16 v[36:39], v[208:211], v[196:199], v[36:39]\n\t"
        "ds_read_b128 v[156:159], %[ba] offset:33792\n\t"
        "global_store_dwordx2 %[oo2], v[228:229], %[ob0] offset:0 nt\n\t"
        "v_mfma_f32_16x16x32_bf16 v[40:43], v[208:211], v[140:143], v[40:43]\n\t"
        "v_mfma_f32_16x16x32_bf16 v[40:43], v[212:215], v[136:139], v[40:43]\n\t"
        "ds_read_b128 v[164:167], v233 offset:0\n\t"
        "v_cvt_pk_bf16_f32 v231, v11, v15\n\t"
        "v_mfma_f32_16x16x32_bf16 v[44:47], v[212:215], v[200:203], v[44:47]\n\t"
        "v_cvt_pk_bf16_f32 v230, v3, v7\n\t"
        "v_mfma_f32_16x16x32_bf16 v[44:47], v[208:211], v[204:207], v[44:47]\n\t"
        "ds_read_b128 v[168:171], %[ba] offset:41984\n\t"
        "s_waitcnt lgkmcnt(4)\n\t"
        "v_mfma_f32_16x16x32_bf16 v[64:67], v[152:155], v[132:135], v[64:67]\n\t"
        "global_store_dwordx2 %[oo3], v[230:231], %[ob0] offset:0 nt\n\t"
        "v_mfma_f32_16x16x32_bf16 v[64:67], v[144:147], v[180:183], v[64:67]\n\t"
        "ds_read_b128 v[172:175], %[ba] offset:42240\n\t"
        "v_cvt_pk_bf16_f32 v224, v32, v36\n\t"
        "v_mfma_f32_16x16x32_bf16 v[68:71], v[144:147], v[188:191], v[68:71]\n\t"
        "v_mfma_f32_16x16x32_bf16 v[68:71], v[152:155], v[196:199], v[68:71]\n\t"
        "ds_read_b128 v[176:179], %[ba] offset:34048\n\t"
        "v_cvt_pk_bf16_f32 v225, v40, v44\n\t"
        "v_mfma_f32_16x16x32_bf16 v[72:75], v[152:155], v[140:143], v[72:75]\n\t"
        "global_store_dwordx2 %[oo0], v[224:225], %[ob1] offset:0 nt\n\t"
        "v_mfma_f32_16x16x32_bf16 v[72:75], v[144:147], v[136:139], v[72:75]\n\t"
        "ds_read_b128 v[184:187], %[ba] offset:34304\n\t"
        "ds_read_b128 v[192:195], %[ba] offset:42496\n\t"
        "v_mfma_f32_16x16x32_bf16 v[76:79], v[144:147], v[200:203], v[76:79]\n\t"
        "v_cvt_pk_bf16_f32 v227, v41, v45\n\t"
        "v_cvt_pk_bf16_f32 v226, v33, v37\n\t"
        "v_mfma_f32_16x16x32_bf16 v[76:79], v[152:155], v[204:207], v[76:79]\n\t"
        "ds_read_b128 v[144:147], %[ba] offset:42752\n\t"
        "global_store_dwordx2 %[oo1], v[226:227], %[ob1] offset:0 nt\n\t"
        "v_mfma_f32_16x16x32_bf16 v[96:99], v[160:163], v[132:135], v[96:99]\n\t"
        ".Lwp16:\n\t"
        "s_cbranch_execz .Lwp17\n\t"
        "v_mfma_f32_16x16x32_bf16 v[96:99], v[148:151], v[180:183], v[96:99]\n\t"
        "ds_read_b128 v[132:135], %[ba] offset:34560\n\t"
        "v_cvt_pk_bf16_f32 v229, v42, v46\n\t"
        "v_mfma_f32_16x16x32_bf16 v[100:103], v[148:151], v[188:191], v[100:103]\n\t"
        "v_cvt_pk_bf16_f32 v228, v34, v38\n\t"
        "v_mfma_f32_16x16x32_bf16 v[100:103], v[160:163], v[196:199], v[100:103]\n\t"
        "ds_read_b128 v[152:155], v232 offset:2048\n\t"
        "global_store_dwordx2 %[oo2], v[228:229], %[ob1] offset:0 nt\n\t"
        "v_mfma_f32_16x16x32_bf16 v[104:107], v[160:163], v[140:143], v[104:107]\n\t"
        "v_mfma_f32_16x16x32_bf16 v[104:107], v[148:151], v[136:139], v[104:107]\n\t"
        "ds_read_b128 v[140:143], v233 offset:2048\n\t"
        "v_cvt_pk_bf16_f32 v231, v43, v47\n\t"
        "v_mfma_f32_16x16x32_bf16 v[108:111], v[148:151], v[200:203], v[108:111]\n\t"
        "v_cvt_pk_bf16_f32 v230, v35, v39\n\t"
        "v_mfma_f32_16x16x32_bf16 v[108:111], v[160:163], v[204:207], v[108:111]\n\t"
        "global_store_dwordx2 %[oo3], v[230:231], %[ob1] offset:0 nt\n\t"
        "s_waitcnt lgkmcnt(3)\n\t"
        "v_mfma_f32_16x16x32_bf16 v[16:19], v[128:131], v[156:159], v[16:19]\n\t"
        "v_mfma_f32_16x16x32_bf16 v[16:19], v[164:167], v[168:171], v[16:19]\n\t"
        "ds_read_b128 v[136:139], v232 offset:4096\n\t"
        "v_cvt_pk_bf16_f32 v225, v72, v76\n\t"
        "v_mfma_f32_16x16x32_bf16 v[20:23], v[164:167], v[172:175], v[20:23]\n\t"
        "v_cvt_pk_bf16_f32 v224, v64, v68\n\t"
        "v_mfma_f32_16x16x32_bf16 v[20:23], v[128:131], v[176:179], v[20:23]\n\t"
        "ds_read_b128 v[148:151], v233 offset:4096\n\t"
        "global_store_dwordx2 %[oo0], v[224:225], %[ob2] offset:0 nt\n\t"
        "v_mfma_f32_16x16x32_bf16 v[24:27], v[128:131], v[184:187], v[24:27]\n\t"
        "v_mfma_f32_16x16x32_bf16 v[24:27], v[164:167], v[192:195], v[24:27]\n\t"
        "ds_read_b128 v[160:163], v232 offset:6144\n\t"
        "v_cvt_pk_bf16_f32 v227, v73, v77\n\t"
        "v_mfma_f32_16x16x32_bf16 v[28:31], v[164:167], v[144:147], v[28:31]\n\t"
        "v_cvt_pk_bf16_f32 v226, v65, v69\n\t"
        "s_waitcnt lgkmcnt(3)\n\t"
        "v_mfma_f32_16x16x32_bf16 v[28:31], v[128:131], v[132:135], v[28:31]\n\t"
        "ds_read_b128 v[164:167], v233 offset:6144\n\t"
        "global_store_dwordx2 %[oo1], v[226:227], %[ob2] offset:0 nt\n\t"
        "v_mfma_f32_16x16x32_bf16 v[48:51], v[152:155], v[156:159], v[48:51]\n\t"
        "v_mfma_f32_16x16x32_bf16 v[48:51], v[140:143], v[168:171], v[48:51]\n\t"
        "ds_read_b128 v[128:131], v232 offset:32768\n\t"
        "v_cvt_pk_bf16_f32 v229, v74, v78\n\t"
        "v_mfma_f32_16x16x32_bf16 v[52:55], v[140:143], v[172:175], v[52:55]\n\t"
        "v_cvt_pk_bf16_f32 v228, v66, v70\n\t"
        "v_mfma_f32_16x16x32_bf16 v[52:55], v[152:155], v[176:179], v[52:55]\n\t"
        "ds_read_b128 v[180:183], v233 offset:32768\n\t"
        "ds_read_b128 v[188:191], %[xb0] offset:0\n\t"
        "v_mfma_f32_16x16x32_bf16 v[56:59], v[152:155], v[184:187], v[56:59]\n\t"
        "global_store_dwordx2 %[oo2], v[228:229], %[ob2] offset:0 nt\n\t"
        "v_mfma_f32_16x16x32_bf16 v[56:59], v[140:143], v[192:195], v[56:59]\n\t"
        "v_cvt_pk_bf16_f32 v231, v75, v79\n\t"
        "v_cvt_pk_bf16_f32 v230, v67, v71\n\t"
        "v_mfma_f32_16x16x32_bf16 v[60:63], v[140:143], v[144:147], v[60:63]\n\t"
        "v_mfma_f32_16x16x32_bf16 v[60:63], v[152:155], v[132:135], v[60:63]\n\t"
        "global_store_dwordx2 %[oo3], v[230:231], %[ob2] offset:0 nt\n\t"
        "s_barrier\n\t"
        "s_waitcnt lgkmcnt(3)\n\t"
        "v_mfma_f32_16x16x32_bf16 v[80:83], v[136:139], v[156:159], v[80:83]\n\t"
        "v_mfma_f32_16x16x32_bf16 v[80:83], v[148:151], v[168:171], v[80:83]\n\t"
        "ds_read_b128 v[140:143], %[ba] offset:0\n\t"
        "ds_read_b128 v[152:155], %[xb1] offset:0\n\t"
        "v_mfma_f32_16x16x32_bf16 v[84:87], v[148:151], v[172:175], v[84:87]\n\t"
        "v_cvt_pk_bf16_f32 v225, v104, v108\n\t"
        "v_cvt_pk_bf16_f32 v224, v96, v100\n\t"
        "v_mfma_f32_16x16x32_bf16 v[84:87], v[136:139], v[176:179], v[84:87]\n\t"
        ".Lwp17:\n\t"
        "s_cbranch_execz .Lwp18\n\t"
        "ds_read_b128 v[196:199], %[ba] offset:8192\n\t"
        "v_mfma_f32_16x16x32_bf16 v[88:91], v[136:139], v[184:187], v[88:91]\n\t"
        "ds_read_b128 v[200:203], %[ba] offset:1024\n\t"
        "ds_read_b128 v[204:207], %[ba] offset:9216\n\t"
        "v_mfma_f32_16x16x32_bf16 v[88:91], v[148:151], v[192:195], v[88:91]\n\t"
        "global_store_dwordx2 %[oo0], v[224:225], %[ob3] offset:0 nt\n\t"
        "ds_read_b128 v[208:211], %[ba] offset:9472\n\t"
        "v_mfma_f32_16x16x32_bf16 v[92:95], v[148:151], v[144:147], v[92:95]\n\t"
        "ds_read_b128 v[212:215], %[ba] offset:1280\n\t"
        "ds_read_b128 v[216:219], %[ba] offset:8448\n\t"
        "v_mfma_f32_16x16x32_bf16 v[92:95], v[136:139], v[132:135], v[92:95]\n\t"
        "v_cvt_pk_bf16_f32 v227, v105, v109\n\t"
        "v_cvt_pk_bf16_f32 v226, v97, v101\n\t"
        "v_mfma_f32_16x16x32_bf16 v[112:115], v[160:163], v[156:159], v[112:115]\n\t"
        "ds_read_b128 v[136:139], %[ba] offset:256\n\t"
        "ds_read_b128 v[148:151], %[ba] offset:512\n\t"
        "v_mfma_f32_16x16x32_bf16 v[112:115], v[164:167], v[168:171], v[112:115]\n\t"
        "ds_read_b128 v[156:159], %[ba] offset:8704\n\t"
        "global_store_dwordx2 %[oo1], v[226:227], %[ob3] offset:0 nt\n\t"
        "v_mfma_f32_16x16x32_bf16 v[116:119], v[164:167], v[172:175], v[116:119]\n\t"
        "ds_read_b128 v[168:171], %[ba] offset:1536\n\t"
        "ds_read_b128 v[220:223], %[ba] offset:9728\n\t"
        "v_mfma_f32_16x16x32_bf16 v[116:119], v[160:163], v[176:179], v[116:119]\n\t"
        "v_cvt_pk_bf16_f32 v229, v106, v110\n\t"
        "v_cvt_pk_bf16_f32 v228, v98, v102\n\t"
        "v_mfma_f32_16x16x32_bf16 v[120:123], v[160:163], v[184:187], v[120:123]\n\t"
        "ds_read_b128 v[172:175], %[ba] offset:9984\n\t"
        "ds_read_b128 v[176:179], %[ba] offset:1792\n\t"
        "v_mfma_f32_16x16x32_bf16 v[120:123], v[164:167], v[192:195], v[120:123]\n\t"
        "global_store_dwordx2 %[oo2], v[228:229], %[ob3] offset:0 nt\n\t"
        "ds_read_b128 v[184:187], %[ba] offset:8960\n\t"
        "v_mfma_f32_16x16x32_bf16 v[124:127], v[164:167], v[144:147], v[124:127]\n\t"
        "v_cvt_pk_bf16_f32 v231, v107, v111\n\t"
        "v_cvt_pk_bf16_f32 v230, v99, v103\n\t"
        "v_mfma_f32_16x16x32_bf16 v[124:127], v[160:163], v[132:135], v[124:127]\n\t"
        "ds_read_b128 v[144:147], %[ba] offset:768\n\t"
        "s_waitcnt lgkmcnt(9)\n\t"
        "v_mfma_f32_16x16x32_bf16 v[16:19], v[128:131], v[140:143], v[16:19]\n\t"
        "global_store_dwordx2 %[oo3], v[230:231], %[ob3] offset:0 nt\n\t"
        "v_mfma_f32_16x16x32_bf16 v[16:19], v[180:183], v[196:199], v[16:19]\n\t"
        "ds_read_b128 v[132:135], v232 offset:34816\n\t"
        "v_mfma_f32_16x16x32_bf16 v[16:19], v[188:191], v[200:203], v[16:19]\n\t"
        "v_mfma_f32_16x16x32_bf16 v[16:19], v[152:155], v[204:207], v[16:19]\n\t"
        "ds_read_b128 v[160:163], v233 offset:34816\n\t"
        "v_mfma_f32_16x16x32_bf16 v[20:23], v[152:155], v[208:211], v[20:23]\n\t"
        "v_mfma_f32_16x16x32_bf16 v[20:23], v[188:191], v[212:215], v[20:23]\n\t"
        "ds_read_b128 v[164:167], %[xb0] offset:2048\n\t"
        "v_mfma_f32_16x16x32_bf16 v[20:23], v[180:183], v[216:219], v[20:23]\n\t"
        "s_waitcnt lgkmcnt(4)\n\t"
        "v_mfma_f32_16x16x32_bf16 v[20:23], v[128:131], v[136:139], v[20:23]\n\t"
        "ds_read_b128 v[192:195], %[xb1] offset:2048\n\t"
        "v_mfma_f32_16x16x32_bf16 v[24:27], v[128:131], v[148:151], v[24:27]\n\t"
        "v_mfma_f32_16x16x32_bf16 v[24:27], v[180:183], v[156:159], v[24:27]\n\t"
        "v_mfma_f32_16x16x32_bf16 v[24:27], v[188:191], v[168:171], v[24:27]\n\t"
        "v_mfma_f32_16x16x32_bf16 v[24:27], v[152:155], v[220:223], v[24:27]\n\t"
        "v_mfma_f32_16x16x32_bf16 v[28:31], v[152:155], v[172:175], v[28:31]\n\t"
        "v_mfma_f32_16x16x32_bf16 v[28:31], v[188:191], v[176:179], v[28:31]\n\t"
        "v_mfma_f32_16x16x32_bf16 v[28:31], v[180:183], v[184:187], v[28:31]\n\t"
        "s_waitcnt lgkmcnt(1)\n\t"
        "v_mfma_f32_16x16x32_bf16 v[28:31], v[128:131], v[144:147], v[28:31]\n\t"
        "v_mfma_f32_16x16x32_bf16 v[48:51], v[132:135], v[140:143], v[48:51]\n\t"
        "v_mfma_f32_16x16x32_bf16 v[48:51], v[160:163], v[196:199], v[48:51]\n\t"
        "ds_read_b128 v[128:131], v232 offset:36864\n\t"
        "v_cvt_pk_bf16_f32 v224, v16, v20\n\t"
        "v_mfma_f32_16x16x32_bf16 v[48:51], v[164:167], v[200:203], v[48:51]\n\t"
        "s_waitcnt lgkmcnt(1)\n\t"
        "v_mfma_f32_16x16x32_bf16 v[48:51], v[192:195], v[204:207], v[48:51]\n\t"
        "ds_read_b128 v[152:155], v233 offset:36864\n\t"
        "v_cvt_pk_bf16_f32 v225, v24, v28\n\t"
        "v_mfma_f32_16x16x32_bf16 v[52:55], v[192:195], v[208:211], v[52:55]\n\t"
        "global_store_dwordx2 %[oo0], v[224:225], %[ob0] offset:128 nt\n\t"
        "v_mfma_f32_16x16x32_bf16 v[52:55], v[164:167], v[212:215], v[52:55]\n\t"
        "ds_read_b128 v[180:183], %[xb0] offset:4096\n\t"
        "v_cvt_pk_bf16_f32 v227, v25, v29\n\t"
        "v_mfma_f32_16x16x32_bf16 v[52:55], v[160:163], v[216:219], v[52:55]\n\t"
        "v_cvt_pk_bf16_f32 v226, v17, v21\n\t"
        "v_mfma_f32_16x16x32_bf16 v[52:55], v[132:135], v[136:139], v[52:55]\n\t"
        "ds_read_b128 v[188:191], %[xb1] offset:4096\n\t"
        "global_store_dwordx2 %[oo1], v[226:227], %[ob0] offset:128 nt\n\t"
        "v_mfma_f32_16x16x32_bf16 v[56:59], v[132:135], v[148:151], v[56:59]\n\t"
        "v_mfma_f32_16x16x32_bf16 v[56:59], v[160:163], v[156:159], v[56:59]\n\t"
        "v_cvt_pk_bf16_f32 v229, v26, v30\n\t"
        "v_cvt_pk_bf16_f32 v228, v18, v22\n\t"
        "v_mfma_f32_16x16x32_bf16 v[56:59], v[164:167], v[168:171], v[56:59]\n\t"
        "v_mfma_f32_16x16x32_bf16 v[56:59], v[192:195], v[220:223], v[56:59]\n\t"
        "global_store_dwordx2 %[oo2], v[228:229], %[ob0] offset:128 nt\n\t"
        "v_mfma_f32_16x16x32_bf16 v[60:63], v[192:195], v[172:175], v[60:63]\n\t"
        "v_mfma_f32_16x16x32_bf16 v[60:63], v[164:167], v[176:179], v[60:63]\n\t"
        "v_cvt_pk_bf16_f32 v231, v27, v31\n\t"
        "v_cvt_pk_bf16_f32 v230, v19, v23\n\t"
        "v_mfma_f32_16x16x32_bf16 v[60:63], v[160:163], v[184:187], v[60:63]\n\t"
        ".Lwp18:\n\t"
        "s_cbranch_execz .Lwp97\n\t"
        "v_mfma_f32_16x16x32_bf16 v[60:63], v[132:135], v[144:147], v[60:63]\n\t"
        "global_store_dwordx2 %[oo3], v[230:231], %[ob0] offset:128 nt\n\t"
        "s_waitcnt lgkmcnt(0)\n\t"
        "v_mfma_f32_16x16x32_bf16 v[80:83], v[128:131], v[140:143], v[80:83]\n\t"
        "v_mfma_f32_16x16x32_bf16 v[80:83], v[152:155], v[196:199], v[80:83]\n\t"
        "ds_read_b128 v[132:135], v232 offset:38912\n\t"
        "v_cvt_pk_bf16_f32 v224, v48, v52\n\t"
        "v_mfma_f32_16x16x32_bf16 v[80:83], v[180:183], v[200:203], v[80:83]\n\t"
        "v_mfma_f32_16x16x32_bf16 v[80:83], v[188:191], v[204:207], v[80:83]\n\t"
        "ds_read_b128 v[160:163], v233 offset:38912\n\t"
        "v_cvt_pk_bf16_f32 v225, v56, v60\n\t"
        "v_mfma_f32_16x16x32_bf16 v[84:87], v[188:191], v[208:211], v[84:87]\n\t"
        "global_store_dwordx2 %[oo0], v[224:225], %[ob1] offset:128 nt\n\t"
        "v_mfma_f32_16x16x32_bf16 v[84:87], v[180:183], v[212:215], v[84:87]\n\t"
        "ds_read_b128 v[164:167], %[xb0] offset:6144\n\t"
        "v_cvt_pk_bf16_f32 v227, v57, v61\n\t"
        "v_mfma_f32_16x16x32_bf16 v[84:87], v[152:155], v[216:219], v[84:87]\n\t"
        "v_cvt_pk_bf16_f32 v226, v49, v53\n\t"
        "v_mfma_f32_16x16x32_bf16 v[84:87], v[128:131], v[136:139], v[84:87]\n\t"
        "ds_read_b128 v[192:195], %[xb1] offset:6144\n\t"
        "global_store_dwordx2 %[oo1], v[226:227], %[ob1] offset:128 nt\n\t"
        "v_mfma_f32_16x16x32_bf16 v[88:91], v[128:131], v[148:151], v[88:91]\n\t"
        "v_mfma_f32_16x16x32_bf16 v[88:91], v[152:155], v[156:159], v[88:91]\n\t"
        "v_cvt_pk_bf16_f32 v229, v58, v62\n\t"
        "v_cvt_pk_bf16_f32 v228, v50, v54\n\t"
        "v_mfma_f32_16x16x32_bf16 v[88:91], v[180:183], v[168:171], v[88:91]\n\t"
        "v_mfma_f32_16x16x32_bf16 v[88:91], v[188:191], v[220:223], v[88:91]\n\t"
        "global_store_dwordx2 %[oo2], v[228:229], %[ob1] offset:128 nt\n\t"
        "v_mfma_f32_16x16x32_bf16 v[92:95], v[188:191], v[172:175], v[92:95]\n\t"
        "v_mfma_f32_16x16x32_bf16 v[92:95], v[180:183], v[176:179], v[92:95]\n\t"
        "v_cvt_pk_bf16_f32 v231, v59, v63\n\t"
        "v_cvt_pk_bf16_f32 v230, v51, v55\n\t"
        "v_mfma_f32_16x16x32_bf16 v[92:95], v[152:155], v[184:187], v[92:95]\n\t"
        "v_mfma_f32_16x16x32_bf16 v[92:95], v[128:131], v[144:147], v[92:95]\n\t"
        "global_store_dwordx2 %[oo3], v[230:231], %[ob1] offset:128 nt\n\t"
        "s_waitcnt lgkmcnt(0)\n\t"
        "v_mfma_f32_16x16x32_bf16 v[112:115], v[132:135], v[140:143], v[112:115]\n\t"
        "v_mfma_f32_16x16x32_bf16 v[112:115], v[160:163], v[196:199], v[112:115]\n\t"
        "v_cvt_pk_bf16_f32 v224, v80, v84\n\t"
        "v_mfma_f32_16x16x32_bf16 v[112:115], v[164:167], v[200:203], v[112:115]\n\t"
        "v_mfma_f32_16x16x32_bf16 v[112:115], v[192:195], v[204:207], v[112:115]\n\t"
        "v_cvt_pk_bf16_f32 v225, v88, v92\n\t"
        "global_store_dwordx2 %[oo0], v[224:225], %[ob2] offset:128 nt\n\t"
        "v_mfma_f32_16x16x32_bf16 v[116:119], v[192:195], v[208:211], v[116:119]\n\t"
        "v_mfma_f32_16x16x32_bf16 v[116:119], v[164:167], v[212:215], v[116:119]\n\t"
        "v_cvt_pk_bf16_f32 v227, v89, v93\n\t"
        "v_cvt_pk_bf16_f32 v226, v81, v85\n\t"
        "v_mfma_f32_16x16x32_bf16 v[116:119], v[160:163], v[216:219], v[116:119]\n\t"
        "v_mfma_f32_16x16x32_bf16 v[116:119], v[132:135], v[136:139], v[116:119]\n\t"
        "global_store_dwordx2 %[oo1], v[226:227], %[ob2] offset:128 nt\n\t"
        "v_mfma_f32_16x16x32_bf16 v[120:123], v[132:135], v[148:151], v[120:123]\n\t"
        "v_mfma_f32_16x16x32_bf16 v[120:123], v[160:163], v[156:159], v[120:123]\n\t"
        "v_cvt_pk_bf16_f32 v229, v90, v94\n\t"
        "v_cvt_pk_bf16_f32 v228, v82, v86\n\t"
        "v_mfma_f32_16x16x32_bf16 v[120:123], v[164:167], v[168:171], v[120:123]\n\t"
        "v_mfma_f32_16x16x32_bf16 v[120:123], v[192:195], v[220:223], v[120:123]\n\t"
        "global_store_dwordx2 %[oo2], v[228:229], %[ob2] offset:128 nt\n\t"
        "v_mfma_f32_16x16x32_bf16 v[124:127], v[192:195], v[172:175], v[124:127]\n\t"
        "v_mfma_f32_16x16x32_bf16 v[124:127], v[164:167], v[176:179], v[124:127]\n\t"
        "v_cvt_pk_bf16_f32 v230, v113, v117\n\t"
        "v_cvt_pk_bf16_f32 v224, v112, v116\n\t"
        "v_mfma_f32_16x16x32_bf16 v[124:127], v[160:163], v[184:187], v[124:127]\n\t"
        "v_cvt_pk_bf16_f32 v227, v91, v95\n\t"
        "v_cvt_pk_bf16_f32 v226, v83, v87\n\t"
        "v_mfma_f32_16x16x32_bf16 v[124:127], v[132:135], v[144:147], v[124:127]\n\t"
        "v_cvt_pk_bf16_f32 v228, v115, v119\n\t"
        "v_cvt_pk_bf16_f32 v128, v114, v118\n\t"
        "global_store_dwordx2 %[oo3], v[226:227], %[ob2] offset:128 nt\n\t"
        "s_nop 7\n\t"
        "v_cvt_pk_bf16_f32 v225, v120, v124\n\t"
        "global_store_dwordx2 %[oo0], v[224:225], %[ob3] offset:128 nt\n\t"
        "v_cvt_pk_bf16_f32 v231, v121, v125\n\t"
        "global_store_dwordx2 %[oo1], v[230:231], %[ob3] offset:128 nt\n\t"
        "v_cvt_pk_bf16_f32 v129, v122, v126\n\t"
        "global_store_dwordx2 %[oo2], v[128:129], %[ob3] offset:128 nt\n\t"
        "v_cvt_pk_bf16_f32 v229, v123, v127\n\t"
        "global_store_dwordx2 %[oo3], v[228:229], %[ob3] offset:128 nt\n\t"
        :
        : [xb0] "v"(xb0), [xb1] "v"(xb1), [ba] "v"(ba), [oo0] "v"(oo[0]), [oo1] "v"(oo[1]), [oo2] "v"(oo[2]), [oo3] "v"(oo[3]),
          [ob0] "s"(ob[0]), [ob1] "s"(ob[1]), [ob2] "s"(ob[2]), [ob3] "s"(ob[3]), [w] "s"(w)
        : "scc", "memory", "s60", "s61", "s62", "s63", "s64", "s65", "s66", "s67", "v0", "v1", "v2", "v3", "v4", "v5", "v6", "v7", "v8", "v9", "v10",
              "v11", "v12", "v13", "v14", "v15", "v16", "v17", "v18", "v19", "v20", "v21", "v22", "v23", "v24", "v25", "v26", "v27", "v28", "v29",
              "v30", "v31", "v32", "v33", "v34", "v35", "v36", "v37", "v38", "v39", "v40", "v41", "v42", "v43", "v44", "v45", "v46", "v47", "v48",
              "v49", "v50", "v51", "v52", "v53", "v54", "v55", "v56", "v57", "v58", "v59", "v60", "v61", "v62", "v63", "v64", "v65", "v66", "v67",
              "v68", "v69", "v70", "v71", "v72", "v73", "v74", "v75", "v76", "v77", "v78", "v79", "v80", "v81", "v82", "v83", "v84", "v85", "v86",
              "v87", "v88", "v89", "v90", "v91", "v92", "v93", "v94", "v95", "v96", "v97", "v98", "v99", "v100", "v101", "v102", "v103", "v104",
              "v105", "v106", "v107", "v108", "v109", "v110", "v111", "v112", "v113", "v114", "v115", "v116", "v117", "v118", "v119", "v120", "v121",
              "v122", "v123", "v124", "v125", "v126", "v127", "v128", "v129", "v130", "v131", "v132", "v133", "v134", "v135", "v136", "v137", "v138",
              "v139", "v140", "v141", "v142", "v143", "v144", "v145", "v146", "v147", "v148", "v149", "v150", "v151", "v152", "v153", "v154", "v155",
              "v156", "v157", "v158", "v159", "v160", "v161", "v162", "v163", "v164", "v165", "v166", "v167", "v168", "v169", "v170", "v171", "v172",
              "v173", "v174", "v175", "v176", "v177", "v178", "v179", "v180", "v181", "v182", "v183", "v184", "v185", "v186", "v187", "v188", "v189",
              "v190", "v191", "v192", "v193", "v194", "v195", "v196", "v197", "v198", "v199", "v200", "v201", "v202", "v203", "v204", "v205", "v206",
              "v207", "v208", "v209", "v210", "v211", "v212", "v213", "v214", "v215", "v216", "v217", "v218", "v219", "v220", "v221", "v222", "v223",
              "v224", "v225", "v226", "v227", "v228", "v229", "v230", "v231", "v232", "v233", "v234", "v235");
}

extern "C" void kernel_launch(void* const* d_in, const int* in_sizes, int n_in,
                              void* d_out, int out_size, void* d_ws, size_t ws_size,
                              hipStream_t stream) {
    const u16* x = (const u16*)d_in[0];
    const int* qw = (const int*)d_in[1];
    const u16* lut = (const u16*)d_in[2];
    u16* out = (u16*)d_out;
    (void)hipFuncSetAttribute((const void*)fused_kernel, hipFuncAttributeMaxDynamicSharedMemorySize, LDS_BYTES);
    fused_kernel<<<(M_ / BM) * (N_ / BN) + NPF, 512, LDS_BYTES, stream>>>(x, qw, lut, out, (char*)d_ws);
}
```

```cpp
#include <hip/hip_runtime.h>
#include <hip/hip_bf16.h>

typedef unsigned short u16;
typedef unsigned int u32;
using i32x4  = __attribute__((ext_vector_type(4))) int;
using u32x4  = __attribute__((ext_vector_type(4))) unsigned int;

constexpr int M_ = 2048, K_ = 1024, N_ = 3584;
constexpr int BM = 256, BN = 128;
constexpr int NPF = 32;
#define CPF 116
#define CPFB 32
#define PACE 0
constexpr int XS = BM * 64 * 2;
constexpr int BS = BN * 32 * 2;
constexpr int OFF_B = 3 * XS;
constexpr int WSL = 2 * BS;
constexpr int OFF_L = OFF_B + 3 * WSL;
constexpr int OFF_D = OFF_L + 16 * BN * 2;
constexpr int WS_F = 28 * 256, WS_W = 65536;
constexpr unsigned TW = 28 * 16384;
constexpr int LDS_BYTES = OFF_D + 1024;

#define TSEQ(mt) (0x76532140u ^ ((u32)((mt) >> 1) * 0x11111111u))
#define BAR() __builtin_amdgcn_s_barrier()
#define SB() __builtin_amdgcn_sched_barrier(0)
#define PAD(id, nx) asm volatile(".Lwp%c0:\n\ts_cbranch_execz .Lwp%c1" :: "i"(id), "i"(nx))

__global__ __launch_bounds__(512, 2) void fused_kernel(const u16* __restrict__ x_, const int* __restrict__ qw_,
                                                       const u16* __restrict__ lut_, u16* __restrict__ out_, char* ws_) {
    extern __shared__ __attribute__((aligned(16))) char smem[];
    typedef unsigned long u64x4 __attribute__((ext_vector_type(4)));
    u64x4 ka;
    unsigned long kp[5], kw;
    asm volatile("s_load_dwordx8 %0, %2, 0x0\n\ts_load_dwordx2 %1, %2, 0x20" : "=&s"(ka), "=&s"(kw) : "s"(__builtin_amdgcn_kernarg_segment_ptr()));
    {
        unsigned long pc0, ex;
        u32 co;
        asm volatile("s_getpc_b64 %0\n\t"
                     "s_cmp_lt_u32 %4, %5\n\t"
                     "s_cselect_b32 m0, %6, 0\n\t"
                     "v_cmp_gt_u32 vcc, m0, %3\n\t"
                     "s_and_b32 m0, %4, 24\n\t"
                     "s_lshl_b32 m0, m0, 4\n\t"
                     "v_lshl_add_u32 %2, %3, 9, m0\n\t"
                     "s_mov_b32 m0, %7\n\t"
                     "s_and_saveexec_b64 %1, vcc\n\t"
                     "global_load_lds_dword %2, %0\n\t"
                     "s_mov_b64 exec, %1"
                     : "=&s"(pc0), "=&s"(ex), "=&v"(co) : "v"(threadIdx.x), "s"(blockIdx.x), "i"(CPFB), "i"(CPF / 4), "i"(OFF_D) : "vcc", "scc", "memory");
    }
#define KWAIT() do { SB(); asm volatile("s_waitcnt lgkmcnt(0)\n\ts_mov_b64 %0, %5\n\ts_mov_b64 %1, %6\n\ts_mov_b64 %2, %7\n\ts_mov_b64 %3, %8\n\ts_mov_b64 %4, %9" \
                     : "=&s"(kp[0]), "=&s"(kp[1]), "=&s"(kp[2]), "=&s"(kp[3]), "=&s"(kp[4]) : "s"(ka[0]), "s"(ka[1]), "s"(ka[2]), "s"(ka[3]), "s"(kw) : "memory"); SB(); } while (0)
#define KPTR(i) (kp[i])
    const int tid = threadIdx.x;
    const int w = __builtin_amdgcn_readfirstlane(tid >> 6);
    const int lane = tid & 63;
    const int bid = (int)blockIdx.x - NPF;
    const int xcd = blockIdx.x & 7, idx = bid >> 3;
    if (bid < 0) {
        const int pw = (blockIdx.x >> 3) * 8 + w;
        KWAIT();
        const char* qb = (const char*)KPTR(1) + (xcd >> 1) * 896 * 4 + (long)((xcd & 1) * 2) * (64 * N_ * 4);
        const char* xb = (const char*)KPTR(0) + (long)(xcd & 1) * 1024 * 2048;
        const u32 dst = (u32)(size_t)(smem) + w * 256;
        {
            const int m = (xcd & 1) * 4 + (w < 4 ? blockIdx.x >> 3 : xcd >> 1);
            const u32 off = (u32)((m * 256 + (w < 4 ? w : blockIdx.x >> 3) * 64 + lane) * 2048 + ((TSEQ(m) >> (w < 4 ? 0 : 4 * w - 12)) & 15) * 128);
            if (w >= 4) __builtin_amdgcn_s_sleep(1);
            asm volatile("s_mov_b32 m0, %2\n\ts_nop 0\n\tglobal_load_lds_dword %0, %1" :: "v"(off), "s"(KPTR(0)), "s"(dst) : "memory");
        }
#pragma unroll 1
        for (int G = pw; G < 192; G += 32) {
            int kind = 0, arg = 0, e = 0;
            if (G >= 0) { kind = 1; arg = 8; e = G - 0; }
            if (G >= 32) { kind = 0; arg = 5; e = G - 32 + (xcd >> 1) * 4; }
            if (G >= 36) { kind = 1; arg = 8; e = G - 4; }
            if (G >= 60) { kind = 0; arg = 6; e = G - 60 + (xcd >> 1) * 4; }
            if (G >= 64) { kind = 0; arg = 7; e = G - 64 + (xcd >> 1) * 4; }
            if (G >= 68) { kind = 0; arg = 8; e = G - 68 + (xcd >> 1) * 4; }
            if (G >= 72) { kind = 1; arg = 12; e = G - 72; }
            if (G >= 96) { kind = 0; arg = 9; e = G - 96 + (xcd >> 1) * 4; }
            if (G >= 100) { kind = 1; arg = 12; e = G - 76; }
            if (G >= 128) { kind = 1; arg = 12; e = G - 76; }
            if (G >= 132) { kind = 0; arg = 10; e = G - 132 + (xcd >> 1) * 4; }
            if (G >= 136) { kind = 0; arg = 11; e = G - 136 + (xcd >> 1) * 4; }
            if (G >= 140) { kind = 0; arg = 12; e = G - 140 + (xcd >> 1) * 4; }
            if (G >= 144) { kind = 0; arg = 13; e = G - 144; }
            if (G >= 160) { kind = 0; arg = 14; e = G - 160; }
            if (G >= 176) { kind = 0; arg = 15; e = G - 176; }
            asm volatile("s_waitcnt vmcnt(%0)" :: "i"(PACE) : "memory");
            if (kind) {
                const int L = e * 64 + lane;
                const int r = L / 28, c = L - r * 28;
                const u32 off = (u32)((arg * 64 + r) * (N_ * 4) + c * 128);
                asm volatile("s_mov_b32 m0, %2\n\ts_nop 0\n\tglobal_load_lds_dword %0, %1" :: "v"(off), "s"(qb), "s"(dst) : "memory");
            } else {
                const int t = arg >= 8 ? arg : (int)((TSEQ((xcd & 1) * 4 + (e >> 2)) >> (4 * arg)) & 15);
                const u32 off = (u32)((e * 64 + lane) * 2048 + t * 128);
                asm volatile("s_mov_b32 m0, %2\n\ts_nop 0\n\tglobal_load_lds_dword %0, %1" :: "v"(off), "s"(xb), "s"(dst) : "memory");
            }
        }
        asm volatile("s_waitcnt vmcnt(0)" ::: "memory");
        return;
    }
    const int mt = (xcd & 1) * 4 + (idx & 3);
    const int nt = (xcd >> 1) * 7 + (idx >> 2);
    const int row0 = mt * BM, col0 = nt * BN;

    if (__builtin_expect(w >= 4, 0)) {
        int lane;
        asm volatile("v_mbcnt_lo_u32_b32 %0, -1, 0\n\tv_mbcnt_hi_u32_b32 %0, -1, %0" : "=v"(lane));
        const int p = w - 4;
        const int l = lane & 31, h = lane >> 5;
        const int g = lane >> 4;
        u32 xoff[4];
#pragma unroll
        for (int i = 0; i < 4; ++i)
            xoff[i] = (u32)(((row0 + p * 64 + i * 8 + (lane >> 3)) * K_ + (((lane & 7) ^ ((i & 1) * 4 + g)) * 8)) * 2 - i * 1024);
        const u32 ldsx = (u32)(size_t)(smem) + p * 8192;
        u32 qo = (u32)(((p * 16 + h * 8) * N_ + col0 + l * 4) * 4);
        u32 lutl = (u32)(size_t)(smem) + OFF_L + l * 4;
        u32 bdst = (u32)(size_t)(smem) + OFF_B + (p >> 1) * BS + ((2 * p + h) & 3) * 2048 + ((l >> 4) * 64 + (l & 15)) * 16;
        const int i2 = (mt >> 1), h2 = mt & 1;
        const unsigned long tseq = 0xFEDCBA9800000000ul | TSEQ(mt);
        const u32 ldsw = (u32)(size_t)(smem) + OFF_B + p * 4096;
        u32 wv = (u32)(nt * 16384 + p * 4096 + lane * 16);
        u32 wo = (u32)(nt * 16384 + p * 4096 + h * 2048 + (l >> 4) * 1024 + (l & 15) * 16);
        u32 po = (u32)(WS_F + (nt * 4 + p) * 256 + lane * 4);
        u32 fo0 = po + (u32)(i2 * 16 + h2 * 8);
        u32 no = (u32)(nt * 256 + (lane & 1) * 4);
        u32 nvl = no;
        KWAIT();
        const char* st1 = (const char*)KPTR(4) + WS_W + (long)(i2 + 4) * TW + h2 * 512;
        const char* qa1 = (const char*)KPTR(1);
        const int tb = 8 + (mt >> 1), kg = (mt & 1) * 4 + p;
        const char* qbg = (const char*)KPTR(1) + (long)(tb * 64 + kg * 8 - p * 16) * (N_ * 4);
        const char* stb = (const char*)KPTR(4) + WS_W + (long)tb * TW + (kg * 2048 - p * 4096);
        u32 fob = po + (u32)(((kg >> 1) - p) * 256 + tb * 16 + (kg & 1) * 8);
        asm volatile("" : "+v"(wo), "+v"(fo0), "+v"(fob), "+v"(wv), "+v"(po), "+v"(bdst), "+v"(lutl), "+v"(qo));
        asm volatile("" : "+v"(xoff[0]), "+v"(xoff[1]), "+v"(xoff[2]), "+v"(xoff[3]));
        asm volatile("" : "+s"(st1), "+s"(qa1), "+s"(qbg), "+s"(stb));
        {
            unsigned long ex_;
            asm volatile("s_mov_b64 %0, exec\n\ts_mov_b64 exec, 0\n\t"
                         "s_cmp_eq_u32 %1, 0\n\ts_cbranch_scc1 .Lwp30\n\t"
                         "s_cmp_eq_u32 %1, 1\n\ts_cbranch_scc1 .Lwp31\n\t"
                         "s_cmp_eq_u32 %1, 2\n\ts_cbranch_scc1 .Lwp32\n\t"
                         "s_branch .Lwp33\n\t"
                         ".Lwp98:\n\ts_mov_b64 exec, %0" : "=&s"(ex_) : "s"(p) : "scc", "memory");
        }
        asm volatile(
            "s_mov_b32 s73, 65536\n\t"
            "s_mov_b32 s72, 0\n\t"
            "s_mov_b64 s[84:85], %[ws]\n\t"
            "s_add_u32 s84, s84, 65536\n\t"
            "s_addc_u32 s85, s85, 0\n\t"
            "s_mov_b64 s[56:57], %[tseq]\n\t"
            "s_bfe_u32 s40, s56, 262144\n\t"
            "s_bfe_u32 s41, s56, 262148\n\t"
            "s_bfe_u32 s42, s56, 262152\n\t"
            "s_bfe_u32 s43, s56, 262156\n\t"
            "s_bfe_u32 s44, s56, 262160\n\t"
            "s_bfe_u32 s45, s56, 262164\n\t"
            "s_bfe_u32 s46, s56, 262168\n\t"
            "s_bfe_u32 s47, s56, 262172\n\t"
            "s_bfe_u32 s48, s57, 262144\n\t"
            "s_bfe_u32 s49, s57, 262148\n\t"
            "s_bfe_u32 s50, s57, 262152\n\t"
            "s_bfe_u32 s51, s57, 262156\n\t"
            "s_bfe_u32 s52, s57, 262160\n\t"
            "s_bfe_u32 s53, s57, 262164\n\t"
            "s_bfe_u32 s54, s57, 262168\n\t"
            "s_bfe_u32 s55, s57, 262172\n\t"
            "global_load_dword %[nvl], %[nvl], %[ws] sc1\n\t"
            "global_load_dword v192, %[po], %[ws] sc1\n\t"
            "s_barrier\n\t"
            ".Lwp30:\n\t"
            "s_cbranch_execz .Lwp98\n\t"
            "s_add_u32 m0, %[ldsx], 0\n\t"
            "s_lshl_b32 s76, s40, 7\n\t"
            "s_add_u32 s68, %[xpl], s76\n\t"
            "s_addc_u32 s69, %[xph], 0\n\t"
            "global_load_lds_dwordx4 %[x0], s[68:69] offset:0\n\t"
            "global_load_lds_dwordx4 %[x1], s[68:69] offset:1024\n\t"
            "global_load_lds_dwordx4 %[x2], s[68:69] offset:2048\n\t"
            "global_load_lds_dwordx4 %[x3], s[68:69] offset:3072\n\t"
            "s_add_u32 m0, %[ldsx], 4096\n\t"
            "s_lshl_b32 s76, s40, 7\n\t"
            "s_add_u32 s76, s76, 65536\n\t"
            "s_add_u32 s68, %[xpl], s76\n\t"
            "s_addc_u32 s69, %[xph], 0\n\t"
            "global_load_lds_dwordx4 %[x0], s[68:69] offset:0\n\t"
            "global_load_lds_dwordx4 %[x1], s[68:69] offset:1024\n\t"
            "global_load_lds_dwordx4 %[x2], s[68:69] offset:2048\n\t"
            "global_load_lds_dwordx4 %[x3], s[68:69] offset:3072\n\t"
            "s_mul_i32 s76, s41, 917504\n\t"
            "s_mov_b64 s[88:89], %[qa1]\n\t"
            "s_add_u32 s88, s88, s76\n\t"
            "s_addc_u32 s89, s89, 0\n\t"
            "global_load_dwordx4 v[128:131], %[qo], s[88:89]\n\t"
            "s_add_u32 s88, s88, 14336\n\t"
            "s_addc_u32 s89, s89, 0\n\t"
            "global_load_dwordx4 v[132:135], %[qo], s[88:89]\n\t"
            "s_add_u32 s88, s88, 14336\n\t"
            "s_addc_u32 s89, s89, 0\n\t"
            "global_load_dwordx4 v[136:139], %[qo], s[88:89]\n\t"
            "s_add_u32 s88, s88, 14336\n\t"
            "s_addc_u32 s89, s89, 0\n\t"
            "global_load_dwordx4 v[140:143], %[qo], s[88:89]\n\t"
            "s_add_u32 s88, s88, 14336\n\t"
            "s_addc_u32 s89, s89, 0\n\t"
            "global_load_dwordx4 v[144:147], %[qo], s[88:89]\n\t"
            "s_add_u32 s88, s88, 14336\n\t"
            "s_addc_u32 s89, s89, 0\n\t"
            "global_load_dwordx4 v[148:151], %[qo], s[88:89]\n\t"
            "s_add_u32 s88, s88, 14336\n\t"
            "s_addc_u32 s89, s89, 0\n\t"
            "global_load_dwordx4 v[152:155], %[qo], s[88:89]\n\t"
            "s_add_u32 s88, s88, 14336\n\t"
            "s_addc_u32 s89, s89, 0\n\t"
            "global_load_dwordx4 v[156:159], %[qo], s[88:89]\n\t"
            "s_waitcnt vmcnt(8)\n\t"
            "s_barrier\n\t"
            ".Lwp31:\n\t"
            "s_cbranch_execz .Lwp98\n\t"
            "s_lshl_b32 s76, %[h2], 9\n\t"
            "v_add_u32 v193, s76, %[bd]\n\t"
            "ds_read_b128 v[196:199], v193\n\t"
            "ds_read_b128 v[200:203], v193 offset:256\n\t"
            "s_mov_b64 s[70:71], %[st1]\n\t"
            "s_sub_u32 s70, s70, 1835008\n\t"
            "s_subb_u32 s71, s71, 0\n\t"
            "s_waitcnt lgkmcnt(0)\n\t"
            "global_store_dwordx4 %[wo], v[196:199], s[70:71] sc1\n\t"
            "global_store_dwordx4 %[wo], v[200:203], s[70:71] offset:256 sc1\n\t"
            "s_mov_b32 s76, 1058696237\n\t"
            "v_mul_lo_u32 %[nvl], %[nvl], s76\n\t"
            "v_add_u32 %[nvl], 1535021219, %[nvl]\n\t"
            "s_setprio 1\n\t"
            "s_waitcnt vmcnt(9)\n\t"
            "v_lshl_add_u32 v160, v128, 8, %[lutl]\n\t"
            "ds_read_u16 v160, v160 offset:0\n\t"
            "v_lshl_add_u32 v168, v129, 8, %[lutl]\n\t"
            "ds_read_u16 v168, v168 offset:2\n\t"
            "v_lshl_add_u32 v176, v130, 8, %[lutl]\n\t"
            "ds_read_u16 v176, v176 offset:128\n\t"
            "v_lshl_add_u32 v184, v131, 8, %[lutl]\n\t"
            "ds_read_u16 v184, v184 offset:130\n\t"
            "s_add_u32 m0, %[ldsx], 32768\n\t"
            "s_lshl_b32 s76, s41, 7\n\t"
            "s_add_u32 s68, %[xpl], s76\n\t"
            "s_addc_u32 s69, %[xph], 0\n\t"
            "global_load_lds_dwordx4 %[x0], s[68:69] offset:0\n\t"
            "s_waitcnt vmcnt(9)\n\t"
            "v_lshl_add_u32 v161, v132, 8, %[lutl]\n\t"
            "ds_read_u16 v161, v161 offset:0\n\t"
            "v_lshl_add_u32 v169, v133, 8, %[lutl]\n\t"
            "ds_read_u16 v169, v169 offset:2\n\t"
            "v_lshl_add_u32 v177, v134, 8, %[lutl]\n\t"
            "ds_read_u16 v177, v177 offset:128\n\t"
            "v_lshl_add_u32 v185, v135, 8, %[lutl]\n\t"
            "ds_read_u16 v185, v185 offset:130\n\t"
            "global_load_lds_dwordx4 %[x1], s[68:69] offset:1024\n\t"
            "s_waitcnt vmcnt(9)\n\t"
            "v_lshl_add_u32 v162, v136, 8, %[lutl]\n\t"
            "ds_read_u16 v162, v162 offset:0\n\t"
            "v_lshl_add_u32 v170, v137, 8, %[lutl]\n\t"
            "ds_read_u16 v170, v170 offset:2\n\t"
            "v_lshl_add_u32 v178, v138, 8, %[lutl]\n\t"
            "ds_read_u16 v178, v178 offset:128\n\t"
            "v_lshl_add_u32 v186, v139, 8, %[lutl]\n\t"
            "ds_read_u16 v186, v186 offset:130\n\t"
            "global_load_lds_dwordx4 %[x2], s[68:69] offset:2048\n\t"
            "s_waitcnt vmcnt(9)\n\t"
            "v_lshl_add_u32 v163, v140, 8, %[lutl]\n\t"
            "ds_read_u16 v163, v163 offset:0\n\t"
            "v_lshl_add_u32 v171, v141, 8, %[lutl]\n\t"
            "ds_read_u16 v171, v171 offset:2\n\t"
            "v_lshl_add_u32 v179, v142, 8, %[lutl]\n\t"
            "ds_read_u16 v179, v179 offset:128\n\t"
            "v_lshl_add_u32 v187, v143, 8, %[lutl]\n\t"
            "ds_read_u16 v187, v187 offset:130\n\t"
            "global_load_lds_dwordx4 %[x3], s[68:69] offset:3072\n\t"
            "s_waitcnt vmcnt(9)\n\t"
            "v_lshl_add_u32 v164, v144, 8, %[lutl]\n\t"
            "ds_read_u16 v164, v164 offset:0\n\t"
            "v_lshl_add_u32 v172, v145, 8, %[lutl]\n\t"
            "ds_read_u16 v172, v172 offset:2\n\t"
            "v_lshl_add_u32 v180, v146, 8, %[lutl]\n\t"
            "ds_read_u16 v180, v180 offset:128\n\t"
            "v_lshl_add_u32 v188, v147, 8, %[lutl]\n\t"
            "ds_read_u16 v188, v188 offset:130\n\t"
            "s_add_u32 m0, %[ldsx], 36864\n\t"
            "s_lshl_b32 s76, s41, 7\n\t"
            "s_add_u32 s76, s76, 65536\n\t"
            "s_add_u32 s68, %[xpl], s76\n\t"
            "s_addc_u32 s69, %[xph], 0\n\t"
            "global_load_lds_dwordx4 %[x0], s[68:69] offset:0\n\t"
            "s_waitcnt vmcnt(9)\n\t"
            "v_lshl_add_u32 v165, v148, 8, %[lutl]\n\t"
            "ds_read_u16 v165, v165 offset:0\n\t"
            "v_lshl_add_u32 v173, v149, 8, %[lutl]\n\t"
            "ds_read_u16 v173, v173 offset:2\n\t"
            "v_lshl_add_u32 v181, v150, 8, %[lutl]\n\t"
            "ds_read_u16 v181, v181 offset:128\n\t"
            "v_lshl_add_u32 v189, v151, 8, %[lutl]\n\t"
            "ds_read_u16 v189, v189 offset:130\n\t"
            "global_load_lds_dwordx4 %[x1], s[68:69] offset:1024\n\t"
            "s_waitcnt vmcnt(9)\n\t"
            "v_lshl_add_u32 v166, v152, 8, %[lutl]\n\t"
            "ds_read_u16 v166, v166 offset:0\n\t"
            "v_lshl_add_u32 v174, v153, 8, %[lutl]\n\t"
            "ds_read_u16 v174, v174 offset:2\n\t"
            "v_lshl_add_u32 v182, v154, 8, %[lutl]\n\t"
            "ds_read_u16 v182, v182 offset:128\n\t"
            "v_lshl_add_u32 v190, v155, 8, %[lutl]\n\t"
            "ds_read_u16 v190, v190 offset:130\n\t"
            "global_load_lds_dwordx4 %[x2], s[68:69] offset:2048\n\t"
            "s_waitcnt vmcnt(9)\n\t"
            "v_lshl_add_u32 v167, v156, 8, %[lutl]\n\t"
            "ds_read_u16 v167, v167 offset:0\n\t"
            "v_lshl_add_u32 v175, v157, 8, %[lutl]\n\t"
            "ds_read_u16 v175, v175 offset:2\n\t"
            "v_lshl_add_u32 v183, v158, 8, %[lutl]\n\t"
            "ds_read_u16 v183, v183 offset:128\n\t"
            "v_lshl_add_u32 v191, v159, 8, %[lutl]\n\t"
            "ds_read_u16 v191, v191 offset:130\n\t"
            "global_load_lds_dwordx4 %[x3], s[68:69] offset:3072\n\t"
            "s_setprio 0\n\t"
            "s_waitcnt vmcnt(8)\n\t"
            "s_mov_b64 exec, 3\n\t"
            "global_store_dword %[fo0], %[nvl], %[ws] offset:0 sc1\n\t"
            "s_mov_b64 exec, -1\n\t"
            "s_waitcnt lgkmcnt(3)\n\t"
            "v_lshl_or_b32 v128, v161, 16, v160\n\t"
            "v_lshl_or_b32 v129, v163, 16, v162\n\t"
            "v_lshl_or_b32 v130, v165, 16, v164\n\t"
            "v_lshl_or_b32 v131, v167, 16, v166\n\t"
            "ds_write_b128 %[bd], v[128:131] offset:16384\n\t"
            "s_waitcnt lgkmcnt(3)\n\t"
            "v_lshl_or_b32 v132, v169, 16, v168\n\t"
            "v_lshl_or_b32 v133, v171, 16, v170\n\t"
            "v_lshl_or_b32 v134, v173, 16, v172\n\t"
            "v_lshl_or_b32 v135, v175, 16, v174\n\t"
            "ds_write_b128 %[bd], v[132:135] offset:16640\n\t"
            "s_waitcnt lgkmcnt(3)\n\t"
            "v_lshl_or_b32 v136, v177, 16, v176\n\t"
            "v_lshl_or_b32 v137, v179, 16, v178\n\t"
            "v_lshl_or_b32 v138, v181, 16, v180\n\t"
            "v_lshl_or_b32 v139, v183, 16, v182\n\t"
            "ds_write_b128 %[bd], v[136:139] offset:16896\n\t"
            "s_waitcnt lgkmcnt(3)\n\t"
            "v_lshl_or_b32 v140, v185, 16, v184\n\t"
            "v_lshl_or_b32 v141, v187, 16, v186\n\t"
            "v_lshl_or_b32 v142, v189, 16, v188\n\t"
            "v_lshl_or_b32 v143, v191, 16, v190\n\t"
            "ds_write_b128 %[bd], v[140:143] offset:17152\n\t"
            "s_add_u32 m0, %[ldsx], 65536\n\t"
            "s_lshl_b32 s76, s42, 7\n\t"
            "s_add_u32 s68, %[xpl], s76\n\t"
            "s_addc_u32 s69, %[xph], 0\n\t"
            "global_load_lds_dwordx4 %[x0], s[68:69] offset:0\n\t"
            "global_load_lds_dwordx4 %[x1], s[68:69] offset:1024\n\t"
            "global_load_lds_dwordx4 %[x2], s[68:69] offset:2048\n\t"
            "global_load_lds_dwordx4 %[x3], s[68:69] offset:3072\n\t"
            "s_add_u32 m0, %[ldsx], 69632\n\t"
            "s_lshl_b32 s76, s42, 7\n\t"
            "s_add_u32 s76, s76, 65536\n\t"
            "s_add_u32 s68, %[xpl], s76\n\t"
            "s_addc_u32 s69, %[xph], 0\n\t"
            "global_load_lds_dwordx4 %[x0], s[68:69] offset:0\n\t"
            "global_load_lds_dwordx4 %[x1], s[68:69] offset:1024\n\t"
            "global_load_lds_dwordx4 %[x2], s[68:69] offset:2048\n\t"
            "global_load_lds_dwordx4 %[x3], s[68:69] offset:3072\n\t"
            "s_waitcnt vmcnt(9) lgkmcnt(0)\n\t"
            "s_barrier\n\t"
            ".Lwp32:\n\t"
            "s_cbranch_execz .Lwp98\n\t"
            "global_load_dword v192, %[po], %[ws] sc1\n\t"
            "s_add_u32 m0, %[ldsx], 0\n\t"
            "s_lshl_b32 s76, s43, 7\n\t"
            "s_add_u32 s68, %[xpl], s76\n\t"
            "s_addc_u32 s69, %[xph], 0\n\t"
            "global_load_lds_dwordx4 %[x0], s[68:69] offset:0\n\t"
            "global_load_lds_dwordx4 %[x1], s[68:69] offset:1024\n\t"
            "global_load_lds_dwordx4 %[x2], s[68:69] offset:2048\n\t"
            "global_load_lds_dwordx4 %[x3], s[68:69] offset:3072\n\t"
            "s_add_u32 m0, %[ldsx], 4096\n\t"
            "s_lshl_b32 s76, s43, 7\n\t"
            "s_add_u32 s76, s76, 65536\n\t"
            "s_add_u32 s68, %[xpl], s76\n\t"
            "s_addc_u32 s69, %[xph], 0\n\t"
            "global_load_lds_dwordx4 %[x0], s[68:69] offset:0\n\t"
            "global_load_lds_dwordx4 %[x1], s[68:69] offset:1024\n\t"
            "global_load_lds_dwordx4 %[x2], s[68:69] offset:2048\n\t"
            "global_load_lds_dwordx4 %[x3], s[68:69] offset:3072\n\t"
            "global_load_dword v195, %[po], %[ws] sc1\n\t"
            "s_waitcnt vmcnt(9)\n\t"
            "s_lshl_b32 s76, s42, 2\n\t"
            ".Lzc0:\n\t"
            "v_cmp_eq_u32 vcc, v192, %[nvl]\n\t"
            "s_lshr_b64 s[74:75], vcc, s76\n\t"
            "s_and_b32 s74, s74, 15\n\t"
            "s_cmp_eq_u32 s74, 15\n\t"
            "s_cbranch_scc1 .Lzd0\n\t"
            "s_cmp_eq_u32 s73, 0\n\t"
            "s_cbranch_scc1 .Lzd0\n\t"
            "s_sub_u32 s73, s73, 1\n\t"
            "s_cmp_eq_u32 s72, 1\n\t"
            "s_cbranch_scc1 .Lze0\n\t"
            "s_mov_b32 s72, 1\n\t"
            "s_waitcnt vmcnt(0)\n\t"
            "v_mov_b32 v192, v195\n\t"
            "s_branch .Lzc0\n\t"
            ".Lze0:\n\t"
            "global_load_dword v192, %[po], %[ws] sc1\n\t"
            "s_waitcnt vmcnt(0)\n\t"
            "v_mov_b32 v195, v192\n\t"
            "s_branch .Lzc0\n\t"
            ".Lzd0:\n\t"
            "s_add_u32 m0, %[ldsw], 32768\n\t"
            "s_mul_i32 s76, s42, 458752\n\t"
            "s_add_u32 s70, s84, s76\n\t"
            "s_addc_u32 s71, s85, 0\n\t"
            "global_load_lds_dwordx4 %[wv], s[70:71] offset:0 sc1\n\t"
            "global_load_lds_dwordx4 %[wv], s[70:71] offset:1024 sc1\n\t"
            "global_load_lds_dwordx4 %[wv], s[70:71] offset:2048 sc1\n\t"
            "global_load_lds_dwordx4 %[wv], s[70:71] offset:3072 sc1\n\t"
            "s_cmp_lg_u32 %[h2], 0\n\t"
            "s_cbranch_scc1 .Lzs1\n\t"
            "global_store_dwordx4 %[wo], v[128:131], %[st1] sc1\n\t"
            "global_store_dwordx4 %[wo], v[132:135], %[st1] offset:256 sc1\n\t"
            "s_branch .Lzt1\n\t"
            ".Lzs1:\n\t"
            "global_store_dwordx4 %[wo], v[136:139], %[st1] sc1\n\t"
            "global_store_dwordx4 %[wo], v[140:143], %[st1] offset:256 sc1\n\t"
            ".Lzt1:\n\t"
            "s_waitcnt vmcnt(6)\n\t"
            "v_mov_b32 v192, v195\n\t"
            "s_lshl_b32 s76, s43, 2\n\t"
            ".Lzc2:\n\t"
            "v_cmp_eq_u32 vcc, v192, %[nvl]\n\t"
            "s_lshr_b64 s[74:75], vcc, s76\n\t"
            "s_and_b32 s74, s74, 15\n\t"
            "s_cmp_eq_u32 s74, 15\n\t"
            "s_cbranch_scc1 .Lzd2\n\t"
            "s_cmp_eq_u32 s73, 0\n\t"
            "s_cbranch_scc1 .Lzd2\n\t"
            "s_sub_u32 s73, s73, 1\n\t"
            "global_load_dword v192, %[po], %[ws] sc1\n\t"
            "s_waitcnt vmcnt(0)\n\t"
            "s_branch .Lzc2\n\t"
            ".Lzd2:\n\t"
            "s_add_u32 m0, %[ldsw], 0\n\t"
            "s_mul_i32 s76, s43, 458752\n\t"
            "s_add_u32 s70, s84, s76\n\t"
            "s_addc_u32 s71, s85, 0\n\t"
            "global_load_lds_dwordx4 %[wv], s[70:71] offset:0 sc1\n\t"
            "global_load_lds_dwordx4 %[wv], s[70:71] offset:1024 sc1\n\t"
            "global_load_lds_dwordx4 %[wv], s[70:71] offset:2048 sc1\n\t"
            "global_load_lds_dwordx4 %[wv], s[70:71] offset:3072 sc1\n\t"
            "s_waitcnt vmcnt(6)\n\t"
            "s_barrier\n\t"
            ".Lwp33:\n\t"
            "s_cbranch_execz .Lwp98\n\t"
            "s_lshl_b32 s76, s44, 2\n\t"
            ".Lzc3:\n\t"
            "v_cmp_eq_u32 vcc, v192, %[nvl]\n\t"
            "s_lshr_b64 s[74:75], vcc, s76\n\t"
            "s_and_b32 s74, s74, 15\n\t"
            "s_cmp_eq_u32 s74, 15\n\t"
            "s_cbranch_scc1 .Lzd3\n\t"
            "s_cmp_eq_u32 s73, 0\n\t"
            "s_cbranch_scc1 .Lzd3\n\t"
            "s_sub_u32 s73, s73, 1\n\t"
            "global_load_dword v192, %[po], %[ws] sc1\n\t"
            "s_waitcnt vmcnt(0)\n\t"
            "s_branch .Lzc3\n\t"
            ".Lzd3:\n\t"
            "s_add_u32 m0, %[ldsw], 16384\n\t"
            "s_mul_i32 s76, s44, 458752\n\t"
            "s_add_u32 s70, s84, s76\n\t"
            "s_addc_u32 s71, s85, 0\n\t"
            "global_load_lds_dwordx4 %[wv], s[70:71] offset:0 sc1\n\t"
            "global_load_lds_dwordx4 %[wv], s[70:71] offset:1024 sc1\n\t"
            "global_load_lds_dwordx4 %[wv], s[70:71] offset:2048 sc1\n\t"
            "global_load_lds_dwordx4 %[wv], s[70:71] offset:3072 sc1\n\t"
            "s_waitcnt vmcnt(8)\n\t"
            "s_mov_b64 exec, 3\n\t"
            "global_store_dword %[fo0], %[nvl], %[ws] offset:64 sc1\n\t"
            "s_mov_b64 exec, -1\n\t"
            "v_mbcnt_lo_u32_b32 v193, -1, 0\n\t"
            "v_mbcnt_hi_u32_b32 v193, -1, v193\n\t"
            "v_lshrrev_b32 v193, 5, v193\n\t"
            "v_mul_u32_u24 v194, 57344, v193\n\t"
            "v_sub_u32 %[qo], %[qo], v194\n\t"
            "v_mul_u32_u24 v194, 1792, v193\n\t"
            "v_sub_u32 %[wo], %[wo], v194\n\t"
            "s_mov_b64 s[88:89], %[qbg]\n\t"
            "global_load_dwordx4 v[204:207], %[qo], s[88:89]\n\t"
            "s_add_u32 s88, s88, 14336\n\t"
            "s_addc_u32 s89, s89, 0\n\t"
            "global_load_dwordx4 v[208:211], %[qo], s[88:89]\n\t"
            "s_add_u32 s88, s88, 14336\n\t"
            "s_addc_u32 s89, s89, 0\n\t"
            "global_load_dwordx4 v[212:215], %[qo], s[88:89]\n\t"
            "s_add_u32 s88, s88, 14336\n\t"
            "s_addc_u32 s89, s89, 0\n\t"
            "global_load_dwordx4 v[216:219], %[qo], s[88:89]\n\t"
            "s_add_u32 m0, %[ldsx], 32768\n\t"
            "s_lshl_b32 s76, s44, 7\n\t"
            "s_add_u32 s68, %[xpl], s76\n\t"
            "s_addc_u32 s69, %[xph], 0\n\t"
            "global_load_lds_dwordx4 %[x0], s[68:69] offset:0\n\t"
            "global_load_lds_dwordx4 %[x1], s[68:69] offset:1024\n\t"
            "global_load_lds_dwordx4 %[x2], s[68:69] offset:2048\n\t"
            "global_load_lds_dwordx4 %[x3], s[68:69] offset:3072\n\t"
            "s_add_u32 m0, %[ldsx], 36864\n\t"
            "s_lshl_b32 s76, s44, 7\n\t"
            "s_add_u32 s76, s76, 65536\n\t"
            "s_add_u32 s68, %[xpl], s76\n\t"
            "s_addc_u32 s69, %[xph], 0\n\t"
            "global_load_lds_dwordx4 %[x0], s[68:69] offset:0\n\t"
            "global_load_lds_dwordx4 %[x1], s[68:69] offset:1024\n\t"
            "global_load_lds_dwordx4 %[x2], s[68:69] offset:2048\n\t"
            "global_load_lds_dwordx4 %[x3], s[68:69] offset:3072\n\t"
            "s_waitcnt vmcnt(17)\n\t"
            "s_barrier\n\t"
            ".Lwp34:\n\t"
            "s_cbranch_execz .Lwp35\n\t"
            "global_load_dword v192, %[po], %[ws] sc1\n\t"
            "s_waitcnt vmcnt(9)\n\t"
            "s_setprio 0\n\t"
            "s_add_u32 m0, %[ldsx], 65536\n\t"
            "s_lshl_b32 s76, s45, 7\n\t"
            "s_add_u32 s68, %[xpl], s76\n\t"
            "s_addc_u32 s69, %[xph], 0\n\t"
            "global_load_lds_dwordx4 %[x0], s[68:69] offset:0\n\t"
            "v_lshl_add_u32 v160, v204, 8, %[lutl]\n\t"
            "ds_read_u16 v160, v160 offset:0\n\t"
            "v_lshl_add_u32 v161, v208, 8, %[lutl]\n\t"
            "ds_read_u16 v161, v161 offset:0\n\t"
            "global_load_lds_dwordx4 %[x1], s[68:69] offset:1024\n\t"
            "v_lshl_add_u32 v162, v212, 8, %[lutl]\n\t"
            "ds_read_u16 v162, v162 offset:0\n\t"
            "v_lshl_add_u32 v163, v216, 8, %[lutl]\n\t"
            "ds_read_u16 v163, v163 offset:0\n\t"
            "global_load_lds_dwordx4 %[x2], s[68:69] offset:2048\n\t"
            "v_lshl_add_u32 v168, v205, 8, %[lutl]\n\t"
            "ds_read_u16 v168, v168 offset:2\n\t"
            "v_lshl_add_u32 v169, v209, 8, %[lutl]\n\t"
            "ds_read_u16 v169, v169 offset:2\n\t"
            "global_load_lds_dwordx4 %[x3], s[68:69] offset:3072\n\t"
            "v_lshl_add_u32 v170, v213, 8, %[lutl]\n\t"
            "ds_read_u16 v170, v170 offset:2\n\t"
            "v_lshl_add_u32 v171, v217, 8, %[lutl]\n\t"
            "ds_read_u16 v171, v171 offset:2\n\t"
            "s_add_u32 m0, %[ldsx], 69632\n\t"
            "s_lshl_b32 s76, s45, 7\n\t"
            "s_add_u32 s76, s76, 65536\n\t"
            "s_add_u32 s68, %[xpl], s76\n\t"
            "s_addc_u32 s69, %[xph], 0\n\t"
            "global_load_lds_dwordx4 %[x0], s[68:69] offset:0\n\t"
            "v_lshl_add_u32 v176, v206, 8, %[lutl]\n\t"
            "ds_read_u16 v176, v176 offset:128\n\t"
            "v_lshl_add_u32 v177, v210, 8, %[lutl]\n\t"
            "ds_read_u16 v177, v177 offset:128\n\t"
            "global_load_lds_dwordx4 %[x1], s[68:69] offset:1024\n\t"
            "v_lshl_add_u32 v178, v214, 8, %[lutl]\n\t"
            "ds_read_u16 v178, v178 offset:128\n\t"
            "v_lshl_add_u32 v179, v218, 8, %[lutl]\n\t"
            "ds_read_u16 v179, v179 offset:128\n\t"
            "global_load_lds_dwordx4 %[x2], s[68:69] offset:2048\n\t"
            "v_lshl_add_u32 v184, v207, 8, %[lutl]\n\t"
            "ds_read_u16 v184, v184 offset:130\n\t"
            "v_lshl_add_u32 v185, v211, 8, %[lutl]\n\t"
            "ds_read_u16 v185, v185 offset:130\n\t"
            "global_load_lds_dwordx4 %[x3], s[68:69] offset:3072\n\t"
            "v_lshl_add_u32 v186, v215, 8, %[lutl]\n\t"
            "ds_read_u16 v186, v186 offset:130\n\t"
            "v_lshl_add_u32 v187, v219, 8, %[lutl]\n\t"
            "ds_read_u16 v187, v187 offset:130\n\t"
            "s_setprio 0\n\t"
            "s_waitcnt vmcnt(8)\n\t"
            "s_lshl_b32 s76, s45, 2\n\t"
            ".Lzc4:\n\t"
            "v_cmp_eq_u32 vcc, v192, %[nvl]\n\t"
            "s_lshr_b64 s[74:75], vcc, s76\n\t"
            "s_and_b32 s74, s74, 15\n\t"
            "s_cmp_eq_u32 s74, 15\n\t"
            "s_cbranch_scc1 .Lzd4\n\t"
            "s_cmp_eq_u32 s73, 0\n\t"
            "s_cbranch_scc1 .Lzd4\n\t"
            "s_sub_u32 s73, s73, 1\n\t"
            "global_load_dword v192, %[po], %[ws] sc1\n\t"
            "s_waitcnt vmcnt(0)\n\t"
            "s_branch .Lzc4\n\t"
            ".Lzd4:\n\t"
            "s_add_u32 m0, %[ldsw], 32768\n\t"
            "s_mul_i32 s76, s45, 458752\n\t"
            "s_add_u32 s70, s84, s76\n\t"
            "s_addc_u32 s71, s85, 0\n\t"
            "global_load_lds_dwordx4 %[wv], s[70:71] offset:0 sc1\n\t"
            "global_load_lds_dwordx4 %[wv], s[70:71] offset:1024 sc1\n\t"
            "global_load_lds_dwordx4 %[wv], s[70:71] offset:2048 sc1\n\t"
            "global_load_lds_dwordx4 %[wv], s[70:71] offset:3072 sc1\n\t"
            "s_waitcnt lgkmcnt(8)\n\t"
            "v_lshl_or_b32 v220, v161, 16, v160\n\t"
            "v_lshl_or_b32 v221, v163, 16, v162\n\t"
            "v_lshl_or_b32 v222, v169, 16, v168\n\t"
            "v_lshl_or_b32 v223, v171, 16, v170\n\t"
            "s_waitcnt lgkmcnt(0)\n\t"
            "v_lshl_or_b32 v224, v177, 16, v176\n\t"
            "v_lshl_or_b32 v225, v179, 16, v178\n\t"
            "v_lshl_or_b32 v226, v185, 16, v184\n\t"
            "v_lshl_or_b32 v227, v187, 16, v186\n\t"
            "s_nop 0\n\t"
            "v_permlane32_swap_b32 v220, v222\n\t"
            "v_permlane32_swap_b32 v221, v223\n\t"
            "v_permlane32_swap_b32 v224, v226\n\t"
            "v_permlane32_swap_b32 v225, v227\n\t"
            "s_mov_b64 s[88:89], %[stb]\n\t"
            "s_nop 0\n\t"
            "global_store_dwordx4 %[wo], v[220:223], s[88:89] offset:0 sc1\n\t"
            "global_store_dwordx4 %[wo], v[224:227], s[88:89] offset:512 sc1\n\t"
            "s_barrier\n\t"
            ".Lwp35:\n\t"
            "s_cbranch_execz .Lwp36\n\t"
            "s_lshl_b32 s76, s46, 2\n\t"
            ".Lzc5:\n\t"
            "v_cmp_eq_u32 vcc, v192, %[nvl]\n\t"
            "s_lshr_b64 s[74:75], vcc, s76\n\t"
            "s_and_b32 s74, s74, 15\n\t"
            "s_cmp_eq_u32 s74, 15\n\t"
            "s_cbranch_scc1 .Lzd5\n\t"
            "s_cmp_eq_u32 s73, 0\n\t"
            "s_cbranch_scc1 .Lzd5\n\t"
            "s_sub_u32 s73, s73, 1\n\t"
            "global_load_dword v192, %[po], %[ws] sc1\n\t"
            "s_waitcnt vmcnt(0)\n\t"
            "s_branch .Lzc5\n\t"
            ".Lzd5:\n\t"
            "s_add_u32 m0, %[ldsw], 0\n\t"
            "s_mul_i32 s76, s46, 458752\n\t"
            "s_add_u32 s70, s84, s76\n\t"
            "s_addc_u32 s71, s85, 0\n\t"
            "global_load_lds_dwordx4 %[wv], s[70:71] offset:0 sc1\n\t"
            "global_load_lds_dwordx4 %[wv], s[70:71] offset:1024 sc1\n\t"
            "global_load_lds_dwordx4 %[wv], s[70:71] offset:2048 sc1\n\t"
            "global_load_lds_dwordx4 %[wv], s[70:71] offset:3072 sc1\n\t"
            "s_add_u32 m0, %[ldsx], 0\n\t"
            "s_lshl_b32 s76, s46, 7\n\t"
            "s_add_u32 s68, %[xpl], s76\n\t"
            "s_addc_u32 s69, %[xph], 0\n\t"
            "global_load_lds_dwordx4 %[x0], s[68:69] offset:0\n\t"
            "global_load_lds_dwordx4 %[x1], s[68:69] offset:1024\n\t"
            "global_load_lds_dwordx4 %[x2], s[68:69] offset:2048\n\t"
            "global_load_lds_dwordx4 %[x3], s[68:69] offset:3072\n\t"
            "s_add_u32 m0, %[ldsx], 4096\n\t"
            "s_lshl_b32 s76, s46, 7\n\t"
            "s_add_u32 s76, s76, 65536\n\t"
            "s_add_u32 s68, %[xpl], s76\n\t"
            "s_addc_u32 s69, %[xph], 0\n\t"
            "global_load_lds_dwordx4 %[x0], s[68:69] offset:0\n\t"
            "global_load_lds_dwordx4 %[x1], s[68:69] offset:1024\n\t"
            "global_load_lds_dwordx4 %[x2], s[68:69] offset:2048\n\t"
            "global_load_lds_dwordx4 %[x3], s[68:69] offset:3072\n\t"
            "s_waitcnt vmcnt(12)\n\t"
            "s_mov_b64 exec, 3\n\t"
            "global_store_dword %[fob], %[nvl], %[ws] offset:0 sc1\n\t"
            "s_mov_b64 exec, -1\n\t"
            "s_barrier\n\t"
            "s_lshl_b32 s76, s47, 2\n\t"
            ".Lzc6:\n\t"
            "v_cmp_eq_u32 vcc, v192, %[nvl]\n\t"
            "s_lshr_b64 s[74:75], vcc, s76\n\t"
            "s_and_b32 s74, s74, 15\n\t"
            "s_cmp_eq_u32 s74, 15\n\t"
            "s_cbranch_scc1 .Lzd6\n\t"
            "s_cmp_eq_u32 s73, 0\n\t"
            "s_cbranch_scc1 .Lzd6\n\t"
            "s_sub_u32 s73, s73, 1\n\t"
            "global_load_dword v192, %[po], %[ws] sc1\n\t"
            "s_waitcnt vmcnt(0)\n\t"
            "s_branch .Lzc6\n\t"
            ".Lzd6:\n\t"
            "s_add_u32 m0, %[ldsw], 16384\n\t"
            "s_mul_i32 s76, s47, 458752\n\t"
            "s_add_u32 s70, s84, s76\n\t"
            "s_addc_u32 s71, s85, 0\n\t"
            "global_load_lds_dwordx4 %[wv], s[70:71] offset:0 sc1\n\t"
            "global_load_lds_dwordx4 %[wv], s[70:71] offset:1024 sc1\n\t"
            "global_load_lds_dwordx4 %[wv], s[70:71] offset:2048 sc1\n\t"
            "global_load_lds_dwordx4 %[wv], s[70:71] offset:3072 sc1\n\t"
            "s_add_u32 m0, %[ldsx], 32768\n\t"
            "s_lshl_b32 s76, s47, 7\n\t"
            "s_add_u32 s68, %[xpl], s76\n\t"
            "s_addc_u32 s69, %[xph], 0\n\t"
            "global_load_lds_dwordx4 %[x0], s[68:69] offset:0\n\t"
            "global_load_lds_dwordx4 %[x1], s[68:69] offset:1024\n\t"
            "global_load_lds_dwordx4 %[x2], s[68:69] offset:2048\n\t"
            "global_load_lds_dwordx4 %[x3], s[68:69] offset:3072\n\t"
            "s_add_u32 m0, %[ldsx], 36864\n\t"
            "s_lshl_b32 s76, s47, 7\n\t"
            "s_add_u32 s76, s76, 65536\n\t"
            "s_add_u32 s68, %[xpl], s76\n\t"
            "s_addc_u32 s69, %[xph], 0\n\t"
            "global_load_lds_dwordx4 %[x0], s[68:69] offset:0\n\t"
            "global_load_lds_dwordx4 %[x1], s[68:69] offset:1024\n\t"
            "global_load_lds_dwordx4 %[x2], s[68:69] offset:2048\n\t"
            "global_load_lds_dwordx4 %[x3], s[68:69] offset:3072\n\t"
            "s_mov_b64 s[88:89], %[qbg]\n\t"
            "s_add_u32 s88, s88, 3670016\n\t"
            "s_addc_u32 s89, s89, 0\n\t"
            "global_load_dwordx4 v[204:207], %[qo], s[88:89]\n\t"
            "s_add_u32 s88, s88, 14336\n\t"
            "s_addc_u32 s89, s89, 0\n\t"
            "global_load_dwordx4 v[208:211], %[qo], s[88:89]\n\t"
            "s_add_u32 s88, s88, 14336\n\t"
            "s_addc_u32 s89, s89, 0\n\t"
            "global_load_dwordx4 v[212:215], %[qo], s[88:89]\n\t"
            "s_add_u32 s88, s88, 14336\n\t"
            "s_addc_u32 s89, s89, 0\n\t"
            "global_load_dwordx4 v[216:219], %[qo], s[88:89]\n\t"
            "s_waitcnt vmcnt(17)\n\t"
            "s_barrier\n\t"
            ".Lwp36:\n\t"
            "s_cbranch_execz .Lwp37\n\t"
            "global_load_dword v192, %[po], %[ws] sc1\n\t"
            "s_add_u32 m0, %[ldsx], 65536\n\t"
            "s_lshl_b32 s76, s48, 7\n\t"
            "s_add_u32 s68, %[xpl], s76\n\t"
            "s_addc_u32 s69, %[xph], 0\n\t"
            "global_load_lds_dwordx4 %[x0], s[68:69] offset:0\n\t"
            "global_load_lds_dwordx4 %[x1], s[68:69] offset:1024\n\t"
            "global_load_lds_dwordx4 %[x2], s[68:69] offset:2048\n\t"
            "global_load_lds_dwordx4 %[x3], s[68:69] offset:3072\n\t"
            "s_add_u32 m0, %[ldsx], 69632\n\t"
            "s_lshl_b32 s76, s48, 7\n\t"
            "s_add_u32 s76, s76, 65536\n\t"
            "s_add_u32 s68, %[xpl], s76\n\t"
            "s_addc_u32 s69, %[xph], 0\n\t"
            "global_load_lds_dwordx4 %[x0], s[68:69] offset:0\n\t"
            "global_load_lds_dwordx4 %[x1], s[68:69] offset:1024\n\t"
            "global_load_lds_dwordx4 %[x2], s[68:69] offset:2048\n\t"
            "global_load_lds_dwordx4 %[x3], s[68:69] offset:3072\n\t"
            "s_waitcnt vmcnt(8)\n\t"
            "s_lshl_b32 s76, s48, 2\n\t"
            ".Lzc7:\n\t"
            "v_cmp_eq_u32 vcc, v192, %[nvl]\n\t"
            "s_lshr_b64 s[74:75], vcc, s76\n\t"
            "s_and_b32 s74, s74, 15\n\t"
            "s_cmp_eq_u32 s74, 15\n\t"
            "s_cbranch_scc1 .Lzd7\n\t"
            "s_cmp_eq_u32 s73, 0\n\t"
            "s_cbranch_scc1 .Lzd7\n\t"
            "s_sub_u32 s73, s73, 1\n\t"
            "global_load_dword v192, %[po], %[ws] sc1\n\t"
            "s_waitcnt vmcnt(0)\n\t"
            "s_branch .Lzc7\n\t"
            ".Lzd7:\n\t"
            "s_add_u32 m0, %[ldsw], 32768\n\t"
            "s_mul_i32 s76, s48, 458752\n\t"
            "s_add_u32 s70, s84, s76\n\t"
            "s_addc_u32 s71, s85, 0\n\t"
            "global_load_lds_dwordx4 %[wv], s[70:71] offset:0 sc1\n\t"
            "global_load_lds_dwordx4 %[wv], s[70:71] offset:1024 sc1\n\t"
            "global_load_lds_dwordx4 %[wv], s[70:71] offset:2048 sc1\n\t"
            "global_load_lds_dwordx4 %[wv], s[70:71] offset:3072 sc1\n\t"
            "s_barrier\n\t"
            ".Lwp37:\n\t"
            "s_cbranch_execz .Lwp97\n\t"
            "s_lshl_b32 s76, s49, 2\n\t"
            ".Lzc8:\n\t"
            "v_cmp_eq_u32 vcc, v192, %[nvl]\n\t"
            "s_lshr_b64 s[74:75], vcc, s76\n\t"
            "s_and_b32 s74, s74, 15\n\t"
            "s_cmp_eq_u32 s74, 15\n\t"
            "s_cbranch_scc1 .Lzd8\n\t"
            "s_cmp_eq_u32 s73, 0\n\t"
            "s_cbranch_scc1 .Lzd8\n\t"
            "s_sub_u32 s73, s73, 1\n\t"
            "global_load_dword v192, %[po], %[ws] sc1\n\t"
            "s_waitcnt vmcnt(0)\n\t"
            "s_branch .Lzc8\n\t"
            ".Lzd8:\n\t"
            "s_add_u32 m0, %[ldsw], 0\n\t"
            "s_mul_i32 s76, s49, 458752\n\t"
            "s_add_u32 s70, s84, s76\n\t"
            "s_addc_u32 s71, s85, 0\n\t"
            "global_load_lds_dwordx4 %[wv], s[70:71] offset:0 sc1\n\t"
            "global_load_lds_dwordx4 %[wv], s[70:71] offset:1024 sc1\n\t"
            "global_load_lds_dwordx4 %[wv], s[70:71] offset:2048 sc1\n\t"
            "global_load_lds_dwordx4 %[wv], s[70:71] offset:3072 sc1\n\t"
            "s_setprio 0\n\t"
            "s_add_u32 m0, %[ldsx], 0\n\t"
            "s_lshl_b32 s76, s49, 7\n\t"
            "s_add_u32 s68, %[xpl], s76\n\t"
            "s_addc_u32 s69, %[xph], 0\n\t"
            "global_load_lds_dwordx4 %[x0], s[68:69] offset:0\n\t"
            "v_lshl_add_u32 v160, v204, 8, %[lutl]\n\t"
            "ds_read_u16 v160, v160 offset:0\n\t"
            "v_lshl_add_u32 v161, v208, 8, %[lutl]\n\t"
            "ds_read_u16 v161, v161 offset:0\n\t"
            "global_load_lds_dwordx4 %[x1], s[68:69] offset:1024\n\t"
            "v_lshl_add_u32 v162, v212, 8, %[lutl]\n\t"
            "ds_read_u16 v162, v162 offset:0\n\t"
            "v_lshl_add_u32 v163, v216, 8, %[lutl]\n\t"
            "ds_read_u16 v163, v163 offset:0\n\t"
            "global_load_lds_dwordx4 %[x2], s[68:69] offset:2048\n\t"
            "v_lshl_add_u32 v168, v205, 8, %[lutl]\n\t"
            "ds_read_u16 v168, v168 offset:2\n\t"
            "v_lshl_add_u32 v169, v209, 8, %[lutl]\n\t"
            "ds_read_u16 v169, v169 offset:2\n\t"
            "global_load_lds_dwordx4 %[x3], s[68:69] offset:3072\n\t"
            "v_lshl_add_u32 v170, v213, 8, %[lutl]\n\t"
            "ds_read_u16 v170, v170 offset:2\n\t"
            "v_lshl_add_u32 v171, v217, 8, %[lutl]\n\t"
            "ds_read_u16 v171, v171 offset:2\n\t"
            "s_add_u32 m0, %[ldsx], 4096\n\t"
            "s_lshl_b32 s76, s49, 7\n\t"
            "s_add_u32 s76, s76, 65536\n\t"
            "s_add_u32 s68, %[xpl], s76\n\t"
            "s_addc_u32 s69, %[xph], 0\n\t"
            "global_load_lds_dwordx4 %[x0], s[68:69] offset:0\n\t"
            "v_lshl_add_u32 v176, v206, 8, %[lutl]\n\t"
            "ds_read_u16 v176, v176 offset:128\n\t"
            "v_lshl_add_u32 v177, v210, 8, %[lutl]\n\t"
            "ds_read_u16 v177, v177 offset:128\n\t"
            "global_load_lds_dwordx4 %[x1], s[68:69] offset:1024\n\t"
            "v_lshl_add_u32 v178, v214, 8, %[lutl]\n\t"
            "ds_read_u16 v178, v178 offset:128\n\t"
            "v_lshl_add_u32 v179, v218, 8, %[lutl]\n\t"
            "ds_read_u16 v179, v179 offset:128\n\t"
            "global_load_lds_dwordx4 %[x2], s[68:69] offset:2048\n\t"
            "v_lshl_add_u32 v184, v207, 8, %[lutl]\n\t"
            "ds_read_u16 v184, v184 offset:130\n\t"
            "v_lshl_add_u32 v185, v211, 8, %[lutl]\n\t"
            "ds_read_u16 v185, v185 offset:130\n\t"
            "global_load_lds_dwordx4 %[x3], s[68:69] offset:3072\n\t"
            "v_lshl_add_u32 v186, v215, 8, %[lutl]\n\t"
            "ds_read_u16 v186, v186 offset:130\n\t"
            "v_lshl_add_u32 v187, v219, 8, %[lutl]\n\t"
            "ds_read_u16 v187, v187 offset:130\n\t"
            "s_setprio 0\n\t"
            "s_waitcnt lgkmcnt(8)\n\t"
            "v_lshl_or_b32 v220, v161, 16, v160\n\t"
            "v_lshl_or_b32 v221, v163, 16, v162\n\t"
            "v_lshl_or_b32 v222, v169, 16, v168\n\t"
            "v_lshl_or_b32 v223, v171, 16, v170\n\t"
            "s_waitcnt lgkmcnt(0)\n\t"
            "v_lshl_or_b32 v224, v177, 16, v176\n\t"
            "v_lshl_or_b32 v225, v179, 16, v178\n\t"
            "v_lshl_or_b32 v226, v185, 16, v184\n\t"
            "v_lshl_or_b32 v227, v187, 16, v186\n\t"
            "s_nop 0\n\t"
            "v_permlane32_swap_b32 v220, v222\n\t"
            "v_permlane32_swap_b32 v221, v223\n\t"
            "v_permlane32_swap_b32 v224, v226\n\t"
            "v_permlane32_swap_b32 v225, v227\n\t"
            "s_mov_b64 s[88:89], %[stb]\n\t"
            "s_add_u32 s88, s88, 1835008\n\t"
            "s_addc_u32 s89, s89, 0\n\t"
            "global_store_dwordx4 %[wo], v[220:223], s[88:89] offset:0 sc1\n\t"
            "global_store_dwordx4 %[wo], v[224:227], s[88:89] offset:512 sc1\n\t"
            "s_waitcnt vmcnt(14)\n\t"
            "s_barrier\n\t"
            ".Lwp38:\n\t"
            "s_cbranch_execz .Lwp39\n\t"
            "s_lshl_b32 s76, s50, 2\n\t"
            ".Lzc9:\n\t"
            "v_cmp_eq_u32 vcc, v192, %[nvl]\n\t"
            "s_lshr_b64 s[74:75], vcc, s76\n\t"
            "s_and_b32 s74, s74, 15\n\t"
            "s_cmp_eq_u32 s74, 15\n\t"
            "s_cbranch_scc1 .Lzd9\n\t"
            "s_cmp_eq_u32 s73, 0\n\t"
            "s_cbranch_scc1 .Lzd9\n\t"
            "s_sub_u32 s73, s73, 1\n\t"
            "global_load_dword v192, %[po], %[ws] sc1\n\t"
            "s_waitcnt vmcnt(0)\n\t"
            "s_branch .Lzc9\n\t"
            ".Lzd9:\n\t"
            "s_add_u32 m0, %[ldsw], 16384\n\t"
            "s_mul_i32 s76, s50, 458752\n\t"
            "s_add_u32 s70, s84, s76\n\t"
            "s_addc_u32 s71, s85, 0\n\t"
            "global_load_lds_dwordx4 %[wv], s[70:71] offset:0 sc1\n\t"
            "global_load_lds_dwordx4 %[wv], s[70:71] offset:1024 sc1\n\t"
            "global_load_lds_dwordx4 %[wv], s[70:71] offset:2048 sc1\n\t"
            "global_load_lds_dwordx4 %[wv], s[70:71] offset:3072 sc1\n\t"
            "s_add_u32 m0, %[ldsx], 32768\n\t"
            "s_lshl_b32 s76, s50, 7\n\t"
            "s_add_u32 s68, %[xpl], s76\n\t"
            "s_addc_u32 s69, %[xph], 0\n\t"
            "global_load_lds_dwordx4 %[x0], s[68:69] offset:0\n\t"
            "global_load_lds_dwordx4 %[x1], s[68:69] offset:1024\n\t"
            "global_load_lds_dwordx4 %[x2], s[68:69] offset:2048\n\t"
            "global_load_lds_dwordx4 %[x3], s[68:69] offset:3072\n\t"
            "s_add_u32 m0, %[ldsx], 36864\n\t"
            "s_lshl_b32 s76, s50, 7\n\t"
            "s_add_u32 s76, s76, 65536\n\t"
            "s_add_u32 s68, %[xpl], s76\n\t"
            "s_addc_u32 s69, %[xph], 0\n\t"
            "global_load_lds_dwordx4 %[x0], s[68:69] offset:0\n\t"
            "global_load_lds_dwordx4 %[x1], s[68:69] offset:1024\n\t"
            "global_load_lds_dwordx4 %[x2], s[68:69] offset:2048\n\t"
            "global_load_lds_dwordx4 %[x3], s[68:69] offset:3072\n\t"
            "s_waitcnt vmcnt(12)\n\t"
            "s_mov_b64 exec, 3\n\t"
            "global_store_dword %[fob], %[nvl], %[ws] offset:64 sc1\n\t"
            "s_mov_b64 exec, -1\n\t"
            "s_barrier\n\t"
            "s_lshl_b32 s76, s51, 2\n\t"
            ".Lzc10:\n\t"
            "v_cmp_eq_u32 vcc, v192, %[nvl]\n\t"
            "s_lshr_b64 s[74:75], vcc, s76\n\t"
            "s_and_b32 s74, s74, 15\n\t"
            "s_cmp_eq_u32 s74, 15\n\t"
            "s_cbranch_scc1 .Lzd10\n\t"
            "s_cmp_eq_u32 s73, 0\n\t"
            "s_cbranch_scc1 .Lzd10\n\t"
            "s_sub_u32 s73, s73, 1\n\t"
            "global_load_dword v192, %[po], %[ws] sc1\n\t"
            "s_waitcnt vmcnt(0)\n\t"
            "s_branch .Lzc10\n\t"
            ".Lzd10:\n\t"
            "s_add_u32 m0, %[ldsw], 32768\n\t"
            "s_mul_i32 s76, s51, 458752\n\t"
            "s_add_u32 s70, s84, s76\n\t"
            "s_addc_u32 s71, s85, 0\n\t"
            "global_load_lds_dwordx4 %[wv], s[70:71] offset:0 sc1\n\t"
            "global_load_lds_dwordx4 %[wv], s[70:71] offset:1024 sc1\n\t"
            "global_load_lds_dwordx4 %[wv], s[70:71] offset:2048 sc1\n\t"
            "global_load_lds_dwordx4 %[wv], s[70:71] offset:3072 sc1\n\t"
            "s_add_u32 m0, %[ldsx], 65536\n\t"
            "s_lshl_b32 s76, s51, 7\n\t"
            "s_add_u32 s68, %[xpl], s76\n\t"
            "s_addc_u32 s69, %[xph], 0\n\t"
            "global_load_lds_dwordx4 %[x0], s[68:69] offset:0\n\t"
            "global_load_lds_dwordx4 %[x1], s[68:69] offset:1024\n\t"
            "global_load_lds_dwordx4 %[x2], s[68:69] offset:2048\n\t"
            "global_load_lds_dwordx4 %[x3], s[68:69] offset:3072\n\t"
            "s_add_u32 m0, %[ldsx], 69632\n\t"
            "s_lshl_b32 s76, s51, 7\n\t"
            "s_add_u32 s76, s76, 65536\n\t"
            "s_add_u32 s68, %[xpl], s76\n\t"
            "s_addc_u32 s69, %[xph], 0\n\t"
            "global_load_lds_dwordx4 %[x0], s[68:69] offset:0\n\t"
            "global_load_lds_dwordx4 %[x1], s[68:69] offset:1024\n\t"
            "global_load_lds_dwordx4 %[x2], s[68:69] offset:2048\n\t"
            "global_load_lds_dwordx4 %[x3], s[68:69] offset:3072\n\t"
            "s_waitcnt vmcnt(13)\n\t"
            "s_barrier\n\t"
            ".Lwp39:\n\t"
            "s_cbranch_execz .Lwp40\n\t"
            "global_load_dword v192, %[po], %[ws] sc1\n\t"
            "s_add_u32 m0, %[ldsx], 0\n\t"
            "s_lshl_b32 s76, s52, 7\n\t"
            "s_add_u32 s68, %[xpl], s76\n\t"
            "s_addc_u32 s69, %[xph], 0\n\t"
            "global_load_lds_dwordx4 %[x0], s[68:69] offset:0\n\t"
            "global_load_lds_dwordx4 %[x1], s[68:69] offset:1024\n\t"
            "global_load_lds_dwordx4 %[x2], s[68:69] offset:2048\n\t"
            "global_load_lds_dwordx4 %[x3], s[68:69] offset:3072\n\t"
            "s_add_u32 m0, %[ldsx], 4096\n\t"
            "s_lshl_b32 s76, s52, 7\n\t"
            "s_add_u32 s76, s76, 65536\n\t"
            "s_add_u32 s68, %[xpl], s76\n\t"
            "s_addc_u32 s69, %[xph], 0\n\t"
            "global_load_lds_dwordx4 %[x0], s[68:69] offset:0\n\t"
            "global_load_lds_dwordx4 %[x1], s[68:69] offset:1024\n\t"
            "global_load_lds_dwordx4 %[x2], s[68:69] offset:2048\n\t"
            "global_load_lds_dwordx4 %[x3], s[68:69] offset:3072\n\t"
            "s_waitcnt vmcnt(8)\n\t"
            "s_lshl_b32 s76, s52, 2\n\t"
            ".Lzc11:\n\t"
            "v_cmp_eq_u32 vcc, v192, %[nvl]\n\t"
            "s_lshr_b64 s[74:75], vcc, s76\n\t"
            "s_and_b32 s74, s74, 15\n\t"
            "s_cmp_eq_u32 s74, 15\n\t"
            "s_cbranch_scc1 .Lzd11\n\t"
            "s_cmp_eq_u32 s73, 0\n\t"
            "s_cbranch_scc1 .Lzd11\n\t"
            "s_sub_u32 s73, s73, 1\n\t"
            "global_load_dword v192, %[po], %[ws] sc1\n\t"
            "s_waitcnt vmcnt(0)\n\t"
            "s_branch .Lzc11\n\t"
            ".Lzd11:\n\t"
            "s_add_u32 m0, %[ldsw], 0\n\t"
            "s_mul_i32 s76, s52, 458752\n\t"
            "s_add_u32 s70, s84, s76\n\t"
            "s_addc_u32 s71, s85, 0\n\t"
            "global_load_lds_dwordx4 %[wv], s[70:71] offset:0 sc1\n\t"
            "global_load_lds_dwordx4 %[wv], s[70:71] offset:1024 sc1\n\t"
            "global_load_lds_dwordx4 %[wv], s[70:71] offset:2048 sc1\n\t"
            "global_load_lds_dwordx4 %[wv], s[70:71] offset:3072 sc1\n\t"
            "s_barrier\n\t"
            "s_mov_b32 s76, 48\n\t"
            ".Lzc12:\n\t"
            "v_cmp_eq_u32 vcc, v192, %[nvl]\n\t"
            "s_lshr_b64 s[74:75], vcc, s76\n\t"
            "s_and_b32 s74, s74, 65535\n\t"
            "s_cmp_eq_u32 s74, 65535\n\t"
            "s_cbranch_scc1 .Lzd12\n\t"
            "s_cmp_eq_u32 s73, 0\n\t"
            "s_cbranch_scc1 .Lzd12\n\t"
            "s_sub_u32 s73, s73, 1\n\t"
            "global_load_dword v192, %[po], %[ws] sc1\n\t"
            "s_waitcnt vmcnt(0)\n\t"
            "s_branch .Lzc12\n\t"
            ".Lzd12:\n\t"
            "s_add_u32 m0, %[ldsw], 16384\n\t"
            "s_mul_i32 s76, s53, 458752\n\t"
            "s_add_u32 s70, s84, s76\n\t"
            "s_addc_u32 s71, s85, 0\n\t"
            "s_mul_i32 s76, s54, 458752\n\t"
            "s_sub_u32 s76, s76, 1024\n\t"
            "s_add_u32 s86, s84, s76\n\t"
            "s_addc_u32 s87, s85, 0\n\t"
            "global_load_lds_dwordx4 %[wv], s[70:71] offset:0 sc1\n\t"
            "global_load_lds_dwordx4 %[wv], s[86:87] offset:1024 sc1\n\t"
            "global_load_lds_dwordx4 %[wv], s[70:71] offset:2048 sc1\n\t"
            "global_load_lds_dwordx4 %[wv], s[86:87] offset:3072 sc1\n\t"
            "s_add_u32 m0, %[ldsx], 32768\n\t"
            "s_lshl_b32 s76, s53, 7\n\t"
            "s_add_u32 s68, %[xpl], s76\n\t"
            "s_addc_u32 s69, %[xph], 0\n\t"
            "global_load_lds_dwordx4 %[x0], s[68:69] offset:0\n\t"
            "global_load_lds_dwordx4 %[x1], s[68:69] offset:1024\n\t"
            "global_load_lds_dwordx4 %[x2], s[68:69] offset:2048\n\t"
            "global_load_lds_dwordx4 %[x3], s[68:69] offset:3072\n\t"
            "s_add_u32 m0, %[ldsx], 36864\n\t"
            "s_lshl_b32 s76, s53, 7\n\t"
            "s_add_u32 s76, s76, 65536\n\t"
            "s_add_u32 s68, %[xpl], s76\n\t"
            "s_addc_u32 s69, %[xph], 0\n\t"
            "global_load_lds_dwordx4 %[x0], s[68:69] offset:0\n\t"
            "global_load_lds_dwordx4 %[x1], s[68:69] offset:1024\n\t"
            "global_load_lds_dwordx4 %[x2], s[68:69] offset:2048\n\t"
            "global_load_lds_dwordx4 %[x3], s[68:69] offset:3072\n\t"
            "s_waitcnt vmcnt(12)\n\t"
            "s_barrier\n\t"
            ".Lwp40:\n\t"
            "s_cbranch_execz .Lwp97\n\t"
            "s_add_u32 m0, %[ldsx], 65536\n\t"
            "s_lshl_b32 s76, s54, 7\n\t"
            "s_add_u32 s68, %[xpl], s76\n\t"
            "s_addc_u32 s69, %[xph], 0\n\t"
            "global_load_lds_dwordx4 %[x0], s[68:69] offset:0\n\t"
            "global_load_lds_dwordx4 %[x1], s[68:69] offset:1024\n\t"
            "global_load_lds_dwordx4 %[x2], s[68:69] offset:2048\n\t"
            "global_load_lds_dwordx4 %[x3], s[68:69] offset:3072\n\t"
            "s_add_u32 m0, %[ldsx], 69632\n\t"
            "s_lshl_b32 s76, s54, 7\n\t"
            "s_add_u32 s76, s76, 65536\n\t"
            "s_add_u32 s68, %[xpl], s76\n\t"
            "s_addc_u32 s69, %[xph], 0\n\t"
            "global_load_lds_dwordx4 %[x0], s[68:69] offset:0\n\t"
            "global_load_lds_dwordx4 %[x1], s[68:69] offset:1024\n\t"
            "global_load_lds_dwordx4 %[x2], s[68:69] offset:2048\n\t"
            "global_load_lds_dwordx4 %[x3], s[68:69] offset:3072\n\t"
            "s_add_u32 m0, %[ldsw], 32768\n\t"
            "s_mul_i32 s76, s55, 458752\n\t"
            "s_add_u32 s70, s84, s76\n\t"
            "s_addc_u32 s71, s85, 0\n\t"
            "s_mul_i32 s76, s53, 458752\n\t"
            "s_add_u32 s86, s84, s76\n\t"
            "s_addc_u32 s87, s85, 0\n\t"
            "global_load_lds_dwordx4 %[wv], s[70:71] offset:0 sc1\n\t"
            "global_load_lds_dwordx4 %[wv], s[86:87] offset:1024 sc1\n\t"
            "global_load_lds_dwordx4 %[wv], s[70:71] offset:2048 sc1\n\t"
            "global_load_lds_dwordx4 %[wv], s[86:87] offset:3072 sc1\n\t"
            "s_waitcnt vmcnt(4)\n\t"
            "s_barrier\n\t"
            "s_add_u32 m0, %[ldsx], 0\n\t"
            "s_lshl_b32 s76, s55, 7\n\t"
            "s_add_u32 s68, %[xpl], s76\n\t"
            "s_addc_u32 s69, %[xph], 0\n\t"
            "global_load_lds_dwordx4 %[x0], s[68:69] offset:0\n\t"
            "global_load_lds_dwordx4 %[x1], s[68:69] offset:1024\n\t"
            "global_load_lds_dwordx4 %[x2], s[68:69] offset:2048\n\t"
            "global_load_lds_dwordx4 %[x3], s[68:69] offset:3072\n\t"
            "s_add_u32 m0, %[ldsx], 4096\n\t"
            "s_lshl_b32 s76, s55, 7\n\t"
            "s_add_u32 s76, s76, 65536\n\t"
            "s_add_u32 s68, %[xpl], s76\n\t"
            "s_addc_u32 s69, %[xph], 0\n\t"
            "global_load_lds_dwordx4 %[x0], s[68:69] offset:0\n\t"
            "global_load_lds_dwordx4 %[x1], s[68:69] offset:1024\n\t"
            "global_load_lds_dwordx4 %[x2], s[68:69] offset:2048\n\t"
            "global_load_lds_dwordx4 %[x3], s[68:69] offset:3072\n\t"
            "s_add_u32 m0, %[ldsw], 0\n\t"
            "s_mul_i32 s76, s54, 458752\n\t"
            "s_add_u32 s76, s76, 1024\n\t"
            "s_add_u32 s70, s84, s76\n\t"
            "s_addc_u32 s71, s85, 0\n\t"
            "s_mul_i32 s76, s55, 458752\n\t"
            "s_add_u32 s86, s84, s76\n\t"
            "s_addc_u32 s87, s85, 0\n\t"
            "global_load_lds_dwordx4 %[wv], s[70:71] offset:0 sc1\n\t"
            "global_load_lds_dwordx4 %[wv], s[86:87] offset:1024 sc1\n\t"
            "global_load_lds_dwordx4 %[wv], s[70:71] offset:2048 sc1\n\t"
            "global_load_lds_dwordx4 %[wv], s[86:87] offset:3072 sc1\n\t"
            "s_waitcnt vmcnt(4)\n\t"
            "s_barrier\n\t"
            ".Lwp41:\n\t"
            "s_cbranch_execz .Lwp14\n\t"
            "s_mov_b32 s76, 16\n\t"
            ".Lzc13:\n\t"
            "v_cmp_eq_u32 vcc, v192, %[nvl]\n\t"
            "s_lshr_b64 s[74:75], vcc, s76\n\t"
            "s_and_b32 s74, s74, 65535\n\t"
            "s_cmp_eq_u32 s74, 65535\n\t"
            "s_cbranch_scc1 .Lzd13\n\t"
            "s_cmp_eq_u32 s73, 0\n\t"
            "s_cbranch_scc1 .Lzd13\n\t"
            "s_sub_u32 s73, s73, 1\n\t"
            "global_load_dword v192, %[po], %[ws] sc1\n\t"
            "s_waitcnt vmcnt(0)\n\t"
            "s_branch .Lzc13\n\t"
            ".Lzd13:\n\t"
            "s_waitcnt vmcnt(0)\n\t"
            "s_barrier\n\t"
            : [nvl] "+v"(nvl), [qo] "+v"(qo), [wo] "+v"(wo)
            : [x0] "v"(xoff[0]), [x1] "v"(xoff[1]), [x2] "v"(xoff[2]), [x3] "v"(xoff[3]), [wv] "v"(wv), [po] "v"(po),
              [lutl] "v"(lutl), [fo0] "v"(fo0), [fob] "v"(fob), [bd] "v"(bdst),
              [xpl] "s"((u32)KPTR(0)), [xph] "s"((u32)(KPTR(0) >> 32)), [ws] "s"(KPTR(4)),
              [qbg] "s"(qbg), [qa1] "s"(qa1), [tseq] "s"(tseq), [p] "s"(p), [h2] "s"(h2), [ldsx] "s"(ldsx), [ldsw] "s"(ldsw), [st1] "s"(st1), [stb] "s"(stb)
            : "vcc", "scc", "memory", "s40", "s41", "s42", "s43", "s44", "s45", "s46", "s47", "s48", "s49", "s50", "s51", "s52", "s53", "s54", "s55",
              "s56", "s57", "s58", "s59", "s60", "s61", "s62", "s63", "s64", "s65", "s66", "s67", "s68", "s69", "s70", "s71", "s72", "s73", "s74",
              "s75", "s76", "s77", "s78", "s79", "s80", "s81", "s82", "s83", "s84", "s85", "s86", "s87", "s88", "s89", "v128", "v129", "v130", "v131",
              "v132", "v133", "v134", "v135", "v136", "v137", "v138", "v139", "v140", "v141", "v142", "v143", "v144", "v145", "v146", "v147", "v148",
              "v149", "v150", "v151", "v152", "v153", "v154", "v155", "v156", "v157", "v158", "v159", "v160", "v161", "v162", "v163", "v164", "v165",
              "v166", "v167", "v168", "v169", "v170", "v171", "v172", "v173", "v174", "v175", "v176", "v177", "v178", "v179", "v180", "v181", "v182",
              "v183", "v184", "v185", "v186", "v187", "v188", "v189", "v190", "v191", "v192", "v193", "v194", "v195", "v196", "v197", "v198", "v199",
              "v200", "v201", "v202", "v203", "v204", "v205", "v206", "v207", "v208", "v209", "v210", "v211", "v212", "v213", "v214", "v215", "v216",
              "v217", "v218", "v219", "v220", "v221", "v222", "v223", "v224", "v225", "v226", "v227", "v228", "v229", "v230", "v231", "v232", "v233",
              "v234", "v235");
        if (mt == 0 && p == 0 && lane < 2) asm volatile("global_store_dword %0, %1, %2 sc1" :: "v"(no), "v"(nvl), "s"(KPTR(4)) : "memory");
        return;
    }

    u32 oo[4];
    unsigned long ob[4];

    int xb0, xb1, ba;
    {
        u32 V[4][8];
        i32x4 Q0[8];
#define LUT1(Q, j, r) asm volatile("v_lshl_add_u32 %0, %1, 8, %2\n\tds_read_u16 %0, %0 offset:%3" : "=&v"(V[j][r]) \
                                   : "v"(Q[r][j]), "v"(lutl), "i"(((j) >> 1) * 128 + ((j) & 1) * 2) : "memory")
#define PK(b_, j, n) asm volatile("s_waitcnt lgkmcnt(" #n ")\n\t" \
                                    "v_lshl_or_b32 %0, %5, 16, %4\n\tv_lshl_or_b32 %1, %7, 16, %6\n\t" \
                                    "v_lshl_or_b32 %2, %9, 16, %8\n\tv_lshl_or_b32 %3, %11, 16, %10" \
                                    : "=&v"(b_[0]), "=&v"(b_[1]), "=&v"(b_[2]), "=&v"(b_[3]) \
                                    : "v"(V[j][0]), "v"(V[j][1]), "v"(V[j][2]), "v"(V[j][3]), "v"(V[j][4]), "v"(V[j][5]), "v"(V[j][6]), "v"(V[j][7]) : "memory")
#define WR(b_, j) asm volatile("ds_write_b128 %0, %1 offset:%2" :: "v"(bdst), "v"(b_), "i"((j) * 256) : "memory")
        {
            const u32 l = tid & 31, h = (tid >> 5) & 1;
            const u32 lo = l * 8 + h * 4, qv = h * (8 * N_ * 4) + l * 16;
            const u32 ls = (u32)((w * 4 * N_ + col0) * 2), qs = (u32)((((mt >> 1) * 64 + w * 16) * N_ + col0) * 4);
            const u32 ld = (u32)(size_t)(smem) + OFF_L + w * 1024;
            asm volatile("s_mov_b32 m0, %[ld]\n\t"
                         "s_waitcnt lgkmcnt(0)\n\t"
                         "s_mov_b64 s[60:61], %[lp]\n\ts_add_u32 s60, s60, %[ls]\n\ts_addc_u32 s61, s61, 0\n\t"
                         "s_mov_b64 s[62:63], %[qp]\n\ts_add_u32 s62, s62, %[qs]\n\ts_addc_u32 s63, s63, 0\n\t"
                         "global_load_lds_dword %[lo], s[60:61] offset:0\n\t"
                         "s_add_u32 s60, s60, 6912\n\ts_addc_u32 s61, s61, 0\n\t"
                         "global_load_lds_dword %[lo], s[60:61] offset:256\n\t"
                         "s_add_u32 s60, s60, 6912\n\ts_addc_u32 s61, s61, 0\n\t"
                         "global_load_lds_dword %[lo], s[60:61] offset:512\n\t"
                         "s_add_u32 s60, s60, 6912\n\ts_addc_u32 s61, s61, 0\n\t"
                         "global_load_lds_dword %[lo], s[60:61] offset:768\n\t"
                         "global_load_dwordx4 %[q0], %[qv], s[62:63]\n\t"
                         "s_add_u32 s62, s62, 14336\n\ts_addc_u32 s63, s63, 0\n\t"
                         "global_load_dwordx4 %[q1], %[qv], s[62:63]\n\t"
                         "s_add_u32 s62, s62, 14336\n\ts_addc_u32 s63, s63, 0\n\t"
                         "global_load_dwordx4 %[q2], %[qv], s[62:63]\n\t"
                         "s_add_u32 s62, s62, 14336\n\ts_addc_u32 s63, s63, 0\n\t"
                         "global_load_dwordx4 %[q3], %[qv], s[62:63]\n\t"
                         "s_add_u32 s62, s62, 14336\n\ts_addc_u32 s63, s63, 0\n\t"
                         "global_load_dwordx4 %[q4], %[qv], s[62:63]\n\t"
                         "s_add_u32 s62, s62, 14336\n\ts_addc_u32 s63, s63, 0\n\t"
                         "global_load_dwordx4 %[q5], %[qv], s[62:63]\n\t"
                         "s_add_u32 s62, s62, 14336\n\ts_addc_u32 s63, s63, 0\n\t"
                         "global_load_dwordx4 %[q6], %[qv], s[62:63]\n\t"
                         "s_add_u32 s62, s62, 14336\n\ts_addc_u32 s63, s63, 0\n\t"
                         "global_load_dwordx4 %[q7], %[qv], s[62:63]\n\t"
                         : [q0] "=&v"(Q0[0]), [q1] "=&v"(Q0[1]), [q2] "=&v"(Q0[2]), [q3] "=&v"(Q0[3]),
                           [q4] "=&v"(Q0[4]), [q5] "=&v"(Q0[5]), [q6] "=&v"(Q0[6]), [q7] "=&v"(Q0[7])
                         : [lo] "v"(lo), [qv] "v"(qv), [ls] "s"(ls), [qs] "s"(qs), [ld] "s"(ld), [lp] "s"(ka[2]), [qp] "s"(ka[1])
                         : "s60", "s61", "s62", "s63", "scc", "memory");
            SB();
        }
        KWAIT();
        int ln = lane;
        asm volatile("" : "+v"(ln));
        const int c = ln & 15, g = ln >> 4, l = ln & 31, h = ln >> 5;
        xb0 = (w * 64 + c) * 128 + ((g ^ (c >> 1)) * 16);
        xb1 = xb0 ^ 64;
        ba = OFF_B + g * 2048 + c * 16;
        u32 lutl = (u32)(size_t)(smem) + OFF_L + l * 4;
        u32 bdst = (u32)(size_t)(smem) + OFF_B + w * 4096 + h * 2048 + ((l >> 4) * 64 + (l & 15)) * 16;
#pragma unroll
        for (int r = 0; r < 4; ++r) {
            oo[r] = (u32)(((row0 + w * 64 + g * 4 + r) * N_ + col0 + c * 4) * 2);
            ob[r] = KPTR(3) + (unsigned long)(r * 16 * N_ * 2);
            asm volatile("" : "+v"(oo[r]), "+s"(ob[r]));
        }
        asm volatile("" : "+v"(xb0), "+v"(xb1), "+v"(ba));
        asm volatile("" : "+v"(bdst), "+v"(lutl));
        {
            unsigned long ex_;
            asm volatile("s_mov_b64 %0, exec\n\ts_mov_b64 exec, 0\n\t"
                         "s_cmp_eq_u32 %1, 0\n\ts_cbranch_scc1 .Lwp20\n\t"
                         "s_cmp_eq_u32 %1, 1\n\ts_cbranch_scc1 .Lwp10\n\t"
                         "s_cmp_eq_u32 %1, 2\n\ts_cbranch_scc1 .Lwp12\n\t"
                         "s_branch .Lwp99\n\t"
                         ".Lwp99:\n\ts_mov_b64 exec, %0" : "=&s"(ex_) : "s"(w) : "scc", "memory");
        }
        asm volatile("s_waitcnt vmcnt(8)" ::: "memory");
        BAR();
        PAD(20, 10);
        asm volatile("s_waitcnt vmcnt(7)" : "+v"(Q0[0]) :: "memory"); LUT1(Q0, 0, 0); LUT1(Q0, 1, 0); LUT1(Q0, 2, 0); LUT1(Q0, 3, 0); SB();
        asm volatile("s_waitcnt vmcnt(6)" : "+v"(Q0[1]) :: "memory"); LUT1(Q0, 0, 1); LUT1(Q0, 1, 1); LUT1(Q0, 2, 1); LUT1(Q0, 3, 1); SB();
        asm volatile("s_waitcnt vmcnt(5)" : "+v"(Q0[2]) :: "memory"); LUT1(Q0, 0, 2); LUT1(Q0, 1, 2); LUT1(Q0, 2, 2); LUT1(Q0, 3, 2); SB();
        asm volatile("s_waitcnt vmcnt(4)" : "+v"(Q0[3]) :: "memory"); LUT1(Q0, 0, 3); LUT1(Q0, 1, 3); LUT1(Q0, 2, 3); LUT1(Q0, 3, 3); SB();
        asm volatile("s_waitcnt vmcnt(3)" : "+v"(Q0[4]) :: "memory"); LUT1(Q0, 0, 4); LUT1(Q0, 1, 4); LUT1(Q0, 2, 4); LUT1(Q0, 3, 4); SB();
        asm volatile("s_waitcnt vmcnt(2)" : "+v"(Q0[5]) :: "memory"); LUT1(Q0, 0, 5); LUT1(Q0, 1, 5); LUT1(Q0, 2, 5); LUT1(Q0, 3, 5); SB();
        asm volatile("s_waitcnt vmcnt(1)" : "+v"(Q0[6]) :: "memory"); LUT1(Q0, 0, 6); LUT1(Q0, 1, 6); LUT1(Q0, 2, 6); LUT1(Q0, 3, 6); SB();
        asm volatile("s_waitcnt vmcnt(0)" : "+v"(Q0[7]) :: "memory"); LUT1(Q0, 0, 7); LUT1(Q0, 1, 7); LUT1(Q0, 2, 7); LUT1(Q0, 3, 7); SB();
        {   u32x4 b0, b1, b2, b3;
            PK(b0, 0, 3); WR(b0, 0); PK(b1, 1, 3); WR(b1, 1); PK(b2, 2, 3); WR(b2, 2); PK(b3, 3, 3); WR(b3, 3);
            SB();
        }
        asm volatile("s_waitcnt lgkmcnt(0)" ::: "memory");
        BAR();
    }
    asm volatile(
        "v_mov_b32 v232, %[xb0]\n\t"
        "v_mov_b32 v233, %[xb1]\n\t"
        "v_mov_b32 v234, %[ba]\n\t"
        ".Lwp10:\n\t"
        "s_cbranch_execz .Lwp99\n\t"
        "ds_read_b128 v[192:195], v234 offset:0\n\t"
        "ds_read_b128 v[128:131], v232 offset:0\n\t"
        "ds_read_b128 v[196:199], v234 offset:8192\n\t"
        "ds_read_b128 v[144:147], v233 offset:0\n\t"
        "ds_read_b128 v[204:207], v234 offset:8448\n\t"
        "ds_read_b128 v[200:203], v234 offset:256\n\t"
        "ds_read_b128 v[132:135], v232 offset:2048\n\t"
        "ds_read_b128 v[148:151], v233 offset:2048\n\t"
        "s_waitcnt lgkmcnt(6)\n\t"
        "v_mfma_f32_16x16x32_bf16 v[0:3], v[128:131], v[192:195], 0\n\t"
        "s_waitcnt lgkmcnt(4)\n\t"
        "v_mfma_f32_16x16x32_bf16 v[0:3], v[144:147], v[196:199], v[0:3]\n\t"
        "ds_read_b128 v[136:139], v232 offset:4096\n\t"
        "s_waitcnt lgkmcnt(4)\n\t"
        "v_mfma_f32_16x16x32_bf16 v[4:7], v[144:147], v[204:207], 0\n\t"
        "s_waitcnt lgkmcnt(3)\n\t"
        "v_mfma_f32_16x16x32_bf16 v[4:7], v[128:131], v[200:203], v[4:7]\n\t"
        "ds_read_b128 v[152:155], v233 offset:4096\n\t"
        "s_waitcnt lgkmcnt(3)\n\t"
        "v_mfma_f32_16x16x32_bf16 v[36:39], v[132:135], v[200:203], 0\n\t"
        "s_waitcnt lgkmcnt(2)\n\t"
        "v_mfma_f32_16x16x32_bf16 v[36:39], v[148:151], v[204:207], v[36:39]\n\t"
        "ds_read_b128 v[140:143], v232 offset:6144\n\t"
        "v_mfma_f32_16x16x32_bf16 v[32:35], v[148:151], v[196:199], 0\n\t"
        "v_mfma_f32_16x16x32_bf16 v[32:35], v[132:135], v[192:195], v[32:35]\n\t"
        "ds_read_b128 v[156:159], v233 offset:6144\n\t"
        "s_waitcnt lgkmcnt(3)\n\t"
        "v_mfma_f32_16x16x32_bf16 v[64:67], v[136:139], v[192:195], 0\n\t"
        "s_waitcnt lgkmcnt(2)\n\t"
        "v_mfma_f32_16x16x32_bf16 v[64:67], v[152:155], v[196:199], v[64:67]\n\t"
        "ds_read_b128 v[208:211], v234 offset:512\n\t"
        "v_mfma_f32_16x16x32_bf16 v[68:71], v[152:155], v[204:207], 0\n\t"
        "v_mfma_f32_16x16x32_bf16 v[68:71], v[136:139], v[200:203], v[68:71]\n\t"
        "ds_read_b128 v[212:215], v234 offset:8704\n\t"
        "s_waitcnt lgkmcnt(3)\n\t"
        "v_mfma_f32_16x16x32_bf16 v[100:103], v[140:143], v[200:203], 0\n\t"
        "s_waitcnt lgkmcnt(2)\n\t"
        "v_mfma_f32_16x16x32_bf16 v[100:103], v[156:159], v[204:207], v[100:103]\n\t"
        "ds_read_b128 v[220:223], v234 offset:8960\n\t"
        "v_mfma_f32_16x16x32_bf16 v[96:99], v[156:159], v[196:199], 0\n\t"
        "v_mfma_f32_16x16x32_bf16 v[96:99], v[140:143], v[192:195], v[96:99]\n\t"
        "ds_read_b128 v[216:219], v234 offset:768\n\t"
        "s_waitcnt lgkmcnt(3)\n\t"
        "v_mfma_f32_16x16x32_bf16 v[8:11], v[128:131], v[208:211], 0\n\t"
        "s_waitcnt lgkmcnt(2)\n\t"
        "v_mfma_f32_16x16x32_bf16 v[8:11], v[144:147], v[212:215], v[8:11]\n\t"
        "ds_read_b128 v[192:195], v234 offset:1024\n\t"
        "s_waitcnt lgkmcnt(2)\n\t"
        "v_mfma_f32_16x16x32_bf16 v[12:15], v[144:147], v[220:223], 0\n\t"
        "s_waitcnt lgkmcnt(1)\n\t"
        "v_mfma_f32_16x16x32_bf16 v[12:15], v[128:131], v[216:219], v[12:15]\n\t"
        "ds_read_b128 v[196:199], v234 offset:9216\n\t"
        "v_mfma_f32_16x16x32_bf16 v[44:47], v[132:135], v[216:219], 0\n\t"
        "v_mfma_f32_16x16x32_bf16 v[44:47], v[148:151], v[220:223], v[44:47]\n\t"
        "ds_read_b128 v[204:207], v234 offset:9472\n\t"
        "v_mfma_f32_16x16x32_bf16 v[40:43], v[148:151], v[212:215], 0\n\t"
        "v_mfma_f32_16x16x32_bf16 v[40:43], v[132:135], v[208:211], v[40:43]\n\t"
        "ds_read_b128 v[200:203], v234 offset:1280\n\t"
        "v_mfma_f32_16x16x32_bf16 v[72:75], v[136:139], v[208:211], 0\n\t"
        "v_mfma_f32_16x16x32_bf16 v[72:75], v[152:155], v[212:215], v[72:75]\n\t"
        "v_mfma_f32_16x16x32_bf16 v[76:79], v[152:155], v[220:223], 0\n\t"
        "v_mfma_f32_16x16x32_bf16 v[76:79], v[136:139], v[216:219], v[76:79]\n\t"
        "v_mfma_f32_16x16x32_bf16 v[108:111], v[140:143], v[216:219], 0\n\t"
        "v_mfma_f32_16x16x32_bf16 v[108:111], v[156:159], v[220:223], v[108:111]\n\t"
        "v_mfma_f32_16x16x32_bf16 v[104:107], v[156:159], v[212:215], 0\n\t"
        "v_mfma_f32_16x16x32_bf16 v[104:107], v[140:143], v[208:211], v[104:107]\n\t"
        "s_waitcnt lgkmcnt(0)\n\t"
        "v_mfma_f32_16x16x32_bf16 v[16:19], v[128:131], v[192:195], 0\n\t"
        "v_mfma_f32_16x16x32_bf16 v[16:19], v[144:147], v[196:199], v[16:19]\n\t"
        "ds_read_b128 v[208:211], v234 offset:1536\n\t"
        "v_mfma_f32_16x16x32_bf16 v[20:23], v[144:147], v[204:207], 0\n\t"
        "v_mfma_f32_16x16x32_bf16 v[20:23], v[128:131], v[200:203], v[20:23]\n\t"
        "ds_read_b128 v[212:215], v234 offset:9728\n\t"
        "v_mfma_f32_16x16x32_bf16 v[52:55], v[132:135], v[200:203], 0\n\t"
        "v_mfma_f32_16x16x32_bf16 v[52:55], v[148:151], v[204:207], v[52:55]\n\t"
        "ds_read_b128 v[220:223], v234 offset:9984\n\t"
        "v_mfma_f32_16x16x32_bf16 v[48:51], v[148:151], v[196:199], 0\n\t"
        "v_mfma_f32_16x16x32_bf16 v[48:51], v[132:135], v[192:195], v[48:51]\n\t"
        "ds_read_b128 v[216:219], v234 offset:1792\n\t"
        "v_mfma_f32_16x16x32_bf16 v[80:83], v[136:139], v[192:195], 0\n\t"
        "v_mfma_f32_16x16x32_bf16 v[80:83], v[152:155], v[196:199], v[80:83]\n\t"
        "v_mfma_f32_16x16x32_bf16 v[84:87], v[152:155], v[204:207], 0\n\t"
        "v_mfma_f32_16x16x32_bf16 v[84:87], v[136:139], v[200:203], v[84:87]\n\t"
        "s_mov_b32 s60, 32768\n\t"
        "s_mov_b32 s62, 5\n\t"
        "v_add_u32 v232, 32768, %[xb0]\n\t"
        "v_add_u32 v233, 32768, %[xb1]\n\t"
        "v_add_u32 v234, 16384, %[ba]\n\t"
        "v_mov_b64 v[24:25], 0\n\t"
        "v_mov_b64 v[26:27], 0\n\t"
        "v_mov_b64 v[28:29], 0\n\t"
        "v_mov_b64 v[30:31], 0\n\t"
        "v_mov_b64 v[56:57], 0\n\t"
        "v_mov_b64 v[58:59], 0\n\t"
        "v_mov_b64 v[60:61], 0\n\t"
        "v_mov_b64 v[62:63], 0\n\t"
        "v_mov_b64 v[88:89], 0\n\t"
        "v_mov_b64 v[90:91], 0\n\t"
        "v_mov_b64 v[92:93], 0\n\t"
        "v_mov_b64 v[94:95], 0\n\t"
        "v_mov_b64 v[120:121], 0\n\t"
        "v_mov_b64 v[122:123], 0\n\t"
        "v_mov_b64 v[124:125], 0\n\t"
        "v_mov_b64 v[126:127], 0\n\t"
        "v_mov_b64 v[116:117], 0\n\t"
        "v_mov_b64 v[118:119], 0\n\t"
        "v_mov_b64 v[112:113], 0\n\t"
        "v_mov_b64 v[114:115], 0\n\t"
        "s_nop 1\n\t"
        "s_mov_b64 exec, 0\n\t"
        "s_cmp_eq_u32 %[w], 0\n\t"
        "s_cbranch_scc1 .Lwp34\n\t"
        "s_cmp_eq_u32 %[w], 1\n\t"
        "s_cbranch_scc1 .Lwp38\n\t"
        "s_cmp_eq_u32 %[w], 2\n\t"
        "s_cbranch_scc1 .Lwp41\n\t"
        "s_branch .Lwp16\n\t"
        ".Lwp97:\n\t"
        "s_mov_b64 exec, -1\n\t"
        ".Lcloop:\n\t"
        "v_mfma_f32_16x16x32_bf16 v[116:119], v[140:143], v[200:203], v[116:119]\n\t"
        "s_sub_u32 s62, s62, 1\n\t"
        "v_mfma_f32_16x16x32_bf16 v[116:119], v[156:159], v[204:207], v[116:119]\n\t"
        "s_waitcnt lgkmcnt(0)\n\t"
        "v_mfma_f32_16x16x32_bf16 v[112:115], v[156:159], v[196:199], v[112:115]\n\t"
        "v_mfma_f32_16x16x32_bf16 v[112:115], v[140:143], v[192:195], v[112:115]\n\t"
        "s_barrier\n\t"
        "v_mfma_f32_16x16x32_bf16 v[24:27], v[128:131], v[208:211], v[24:27]\n\t"
        "v_mfma_f32_16x16x32_bf16 v[24:27], v[144:147], v[212:215], v[24:27]\n\t"
        "ds_read_b128 v[192:195], v234 offset:0\n\t"
        "ds_read_b128 v[160:163], v232 offset:0\n\t"
        "v_mfma_f32_16x16x32_bf16 v[28:31], v[144:147], v[220:223], v[28:31]\n\t"
        "v_mfma_f32_16x16x32_bf16 v[28:31], v[128:131], v[216:219], v[28:31]\n\t"
        "ds_read_b128 v[196:199], v234 offset:8192\n\t"
        "ds_read_b128 v[176:179], v233 offset:0\n\t"
        "v_mfma_f32_16x16x32_bf16 v[60:63], v[132:135], v[216:219], v[60:63]\n\t"
        "v_mfma_f32_16x16x32_bf16 v[60:63], v[148:151], v[220:223], v[60:63]\n\t"
        "ds_read_b128 v[204:207], v234 offset:8448\n\t"
        "v_mfma_f32_16x16x32_bf16 v[56:59], v[148:151], v[212:215], v[56:59]\n\t"
        "v_mfma_f32_16x16x32_bf16 v[56:59], v[132:135], v[208:211], v[56:59]\n\t"
        "ds_read_b128 v[200:203], v234 offset:256\n\t"
        "v_mfma_f32_16x16x32_bf16 v[88:91], v[136:139], v[208:211], v[88:91]\n\t"
        "v_mfma_f32_16x16x32_bf16 v[88:91], v[152:155], v[212:215], v[88:91]\n\t"
        "ds_read_b128 v[164:167], v232 offset:2048\n\t"
        "v_mfma_f32_16x16x32_bf16 v[92:95], v[152:155], v[220:223], v[92:95]\n\t"
        "v_mfma_f32_16x16x32_bf16 v[92:95], v[136:139], v[216:219], v[92:95]\n\t"
        "ds_read_b128 v[180:183], v233 offset:2048\n\t"
        "v_mfma_f32_16x16x32_bf16 v[124:127], v[140:143], v[216:219], v[124:127]\n\t"
        "v_mfma_f32_16x16x32_bf16 v[124:127], v[156:159], v[220:223], v[124:127]\n\t"
        "ds_read_b128 v[168:171], v232 offset:4096\n\t"
        "v_mfma_f32_16x16x32_bf16 v[120:123], v[156:159], v[212:215], v[120:123]\n\t"
        "v_mfma_f32_16x16x32_bf16 v[120:123], v[140:143], v[208:211], v[120:123]\n\t"
        "ds_read_b128 v[184:187], v233 offset:4096\n\t"
        "s_waitcnt lgkmcnt(4)\n\t"
        "v_mfma_f32_16x16x32_bf16 v[0:3], v[160:163], v[192:195], v[0:3]\n\t"
        "v_mfma_f32_16x16x32_bf16 v[0:3], v[176:179], v[196:199], v[0:3]\n\t"
        "ds_read_b128 v[172:175], v232 offset:6144\n\t"
        "ds_read_b128 v[208:211], v234 offset:512\n\t"
        "v_mfma_f32_16x16x32_bf16 v[4:7], v[176:179], v[204:207], v[4:7]\n\t"
        "v_mfma_f32_16x16x32_bf16 v[4:7], v[160:163], v[200:203], v[4:7]\n\t"
        "ds_read_b128 v[188:191], v233 offset:6144\n\t"
        "s_waitcnt lgkmcnt(5)\n\t"
        "v_mfma_f32_16x16x32_bf16 v[36:39], v[164:167], v[200:203], v[36:39]\n\t"
        "ds_read_b128 v[212:215], v234 offset:8704\n\t"
        "v_mfma_f32_16x16x32_bf16 v[36:39], v[180:183], v[204:207], v[36:39]\n\t"
        "ds_read_b128 v[220:223], v234 offset:8960\n\t"
        "v_mfma_f32_16x16x32_bf16 v[32:35], v[180:183], v[196:199], v[32:35]\n\t"
        "v_mfma_f32_16x16x32_bf16 v[32:35], v[164:167], v[192:195], v[32:35]\n\t"
        "ds_read_b128 v[216:219], v234 offset:768\n\t"
        "s_waitcnt lgkmcnt(6)\n\t"
        "v_mfma_f32_16x16x32_bf16 v[64:67], v[168:171], v[192:195], v[64:67]\n\t"
        "v_mfma_f32_16x16x32_bf16 v[64:67], v[184:187], v[196:199], v[64:67]\n\t"
        "v_mfma_f32_16x16x32_bf16 v[68:71], v[184:187], v[204:207], v[68:71]\n\t"
        "v_mfma_f32_16x16x32_bf16 v[68:71], v[168:171], v[200:203], v[68:71]\n\t"
        "s_waitcnt lgkmcnt(3)\n\t"
        "v_mfma_f32_16x16x32_bf16 v[100:103], v[172:175], v[200:203], v[100:103]\n\t"
        "v_mfma_f32_16x16x32_bf16 v[100:103], v[188:191], v[204:207], v[100:103]\n\t"
        "v_mfma_f32_16x16x32_bf16 v[96:99], v[188:191], v[196:199], v[96:99]\n\t"
        "v_mfma_f32_16x16x32_bf16 v[96:99], v[172:175], v[192:195], v[96:99]\n\t"
        "v_mfma_f32_16x16x32_bf16 v[8:11], v[160:163], v[208:211], v[8:11]\n\t"
        "s_waitcnt lgkmcnt(0)\n\t"
        "v_mfma_f32_16x16x32_bf16 v[8:11], v[176:179], v[212:215], v[8:11]\n\t"
        "ds_read_b128 v[192:195], v234 offset:1024\n\t"
        "v_mfma_f32_16x16x32_bf16 v[12:15], v[176:179], v[220:223], v[12:15]\n\t"
        "v_mfma_f32_16x16x32_bf16 v[12:15], v[160:163], v[216:219], v[12:15]\n\t"
        "ds_read_b128 v[196:199], v234 offset:9216\n\t"
        "v_mfma_f32_16x16x32_bf16 v[44:47], v[164:167], v[216:219], v[44:47]\n\t"
        "v_mfma_f32_16x16x32_bf16 v[44:47], v[180:183], v[220:223], v[44:47]\n\t"
        "ds_read_b128 v[204:207], v234 offset:9472\n\t"
        "v_mfma_f32_16x16x32_bf16 v[40:43], v[180:183], v[212:215], v[40:43]\n\t"
        "v_mfma_f32_16x16x32_bf16 v[40:43], v[164:167], v[208:211], v[40:43]\n\t"
        "ds_read_b128 v[200:203], v234 offset:1280\n\t"
        "v_mfma_f32_16x16x32_bf16 v[72:75], v[168:171], v[208:211], v[72:75]\n\t"
        "v_mfma_f32_16x16x32_bf16 v[72:75], v[184:187], v[212:215], v[72:75]\n\t"
        "v_mfma_f32_16x16x32_bf16 v[76:79], v[184:187], v[220:223], v[76:79]\n\t"
        "v_mfma_f32_16x16x32_bf16 v[76:79], v[168:171], v[216:219], v[76:79]\n\t"
        ".Lwp12:\n\t"
        "s_cbranch_execz .Lwp99\n\t"
        "v_mfma_f32_16x16x32_bf16 v[108:111], v[172:175], v[216:219], v[108:111]\n\t"
        "v_mfma_f32_16x16x32_bf16 v[108:111], v[188:191], v[220:223], v[108:111]\n\t"
        "v_mfma_f32_16x16x32_bf16 v[104:107], v[188:191], v[212:215], v[104:107]\n\t"
        "v_mfma_f32_16x16x32_bf16 v[104:107], v[172:175], v[208:211], v[104:107]\n\t"
        "s_waitcnt lgkmcnt(0)\n\t"
        "v_mfma_f32_16x16x32_bf16 v[16:19], v[160:163], v[192:195], v[16:19]\n\t"
        "v_mfma_f32_16x16x32_bf16 v[16:19], v[176:179], v[196:199], v[16:19]\n\t"
        "ds_read_b128 v[208:211], v234 offset:1536\n\t"
        "v_mfma_f32_16x16x32_bf16 v[20:23], v[176:179], v[204:207], v[20:23]\n\t"
        "v_mfma_f32_16x16x32_bf16 v[20:23], v[160:163], v[200:203], v[20:23]\n\t"
        "ds_read_b128 v[212:215], v234 offset:9728\n\t"
        "v_mfma_f32_16x16x32_bf16 v[52:55], v[164:167], v[200:203], v[52:55]\n\t"
        "v_mfma_f32_16x16x32_bf16 v[52:55], v[180:183], v[204:207], v[52:55]\n\t"
        "ds_read_b128 v[220:223], v234 offset:9984\n\t"
        "v_mfma_f32_16x16x32_bf16 v[48:51], v[180:183], v[196:199], v[48:51]\n\t"
        "v_mfma_f32_16x16x32_bf16 v[48:51], v[164:167], v[192:195], v[48:51]\n\t"
        "ds_read_b128 v[216:219], v234 offset:1792\n\t"
        "s_add_u32 s60, s60, 32768\n\t"
        "v_mfma_f32_16x16x32_bf16 v[80:83], v[168:171], v[192:195], v[80:83]\n\t"
        "s_cmp_eq_u32 s60, 98304\n\t"
        "s_cselect_b32 s60, 0, s60\n\t"
        "v_mfma_f32_16x16x32_bf16 v[80:83], v[184:187], v[196:199], v[80:83]\n\t"
        "v_add_u32 v232, s60, %[xb0]\n\t"
        "s_lshr_b32 s61, s60, 1\n\t"
        "v_mfma_f32_16x16x32_bf16 v[84:87], v[184:187], v[204:207], v[84:87]\n\t"
        "v_add_u32 v233, s60, %[xb1]\n\t"
        "v_add_u32 v234, s61, %[ba]\n\t"
        "v_mfma_f32_16x16x32_bf16 v[84:87], v[168:171], v[200:203], v[84:87]\n\t"
        "v_mfma_f32_16x16x32_bf16 v[116:119], v[172:175], v[200:203], v[116:119]\n\t"
        "v_mfma_f32_16x16x32_bf16 v[116:119], v[188:191], v[204:207], v[116:119]\n\t"
        "s_waitcnt lgkmcnt(0)\n\t"
        "v_mfma_f32_16x16x32_bf16 v[112:115], v[188:191], v[196:199], v[112:115]\n\t"
        "v_mfma_f32_16x16x32_bf16 v[112:115], v[172:175], v[192:195], v[112:115]\n\t"
        "s_barrier\n\t"
        "v_mfma_f32_16x16x32_bf16 v[24:27], v[160:163], v[208:211], v[24:27]\n\t"
        "v_mfma_f32_16x16x32_bf16 v[24:27], v[176:179], v[212:215], v[24:27]\n\t"
        "ds_read_b128 v[192:195], v234 offset:0\n\t"
        "ds_read_b128 v[128:131], v232 offset:0\n\t"
        "v_mfma_f32_16x16x32_bf16 v[28:31], v[176:179], v[220:223], v[28:31]\n\t"
        "v_mfma_f32_16x16x32_bf16 v[28:31], v[160:163], v[216:219], v[28:31]\n\t"
        "ds_read_b128 v[196:199], v234 offset:8192\n\t"
        "ds_read_b128 v[144:147], v233 offset:0\n\t"
        "v_mfma_f32_16x16x32_bf16 v[60:63], v[164:167], v[216:219], v[60:63]\n\t"
        "v_mfma_f32_16x16x32_bf16 v[60:63], v[180:183], v[220:223], v[60:63]\n\t"
        "ds_read_b128 v[204:207], v234 offset:8448\n\t"
        "v_mfma_f32_16x16x32_bf16 v[56:59], v[180:183], v[212:215], v[56:59]\n\t"
        "v_mfma_f32_16x16x32_bf16 v[56:59], v[164:167], v[208:211], v[56:59]\n\t"
        "ds_read_b128 v[200:203], v234 offset:256\n\t"
        "v_mfma_f32_16x16x32_bf16 v[88:91], v[168:171], v[208:211], v[88:91]\n\t"
        "v_mfma_f32_16x16x32_bf16 v[88:91], v[184:187], v[212:215], v[88:91]\n\t"
        "ds_read_b128 v[132:135], v232 offset:2048\n\t"
        "v_mfma_f32_16x16x32_bf16 v[92:95], v[184:187], v[220:223], v[92:95]\n\t"
        "v_mfma_f32_16x16x32_bf16 v[92:95], v[168:171], v[216:219], v[92:95]\n\t"
        "ds_read_b128 v[148:151], v233 offset:2048\n\t"
        "v_mfma_f32_16x16x32_bf16 v[124:127], v[172:175], v[216:219], v[124:127]\n\t"
        "v_mfma_f32_16x16x32_bf16 v[124:127], v[188:191], v[220:223], v[124:127]\n\t"
        "ds_read_b128 v[136:139], v232 offset:4096\n\t"
        "v_mfma_f32_16x16x32_bf16 v[120:123], v[188:191], v[212:215], v[120:123]\n\t"
        "v_mfma_f32_16x16x32_bf16 v[120:123], v[172:175], v[208:211], v[120:123]\n\t"
        "ds_read_b128 v[152:155], v233 offset:4096\n\t"
        "s_waitcnt lgkmcnt(4)\n\t"
        "v_mfma_f32_16x16x32_bf16 v[0:3], v[128:131], v[192:195], v[0:3]\n\t"
        "v_mfma_f32_16x16x32_bf16 v[0:3], v[144:147], v[196:199], v[0:3]\n\t"
        "ds_read_b128 v[140:143], v232 offset:6144\n\t"
        "ds_read_b128 v[208:211], v234 offset:512\n\t"
        "v_mfma_f32_16x16x32_bf16 v[4:7], v[144:147], v[204:207], v[4:7]\n\t"
        "v_mfma_f32_16x16x32_bf16 v[4:7], v[128:131], v[200:203], v[4:7]\n\t"
        "ds_read_b128 v[156:159], v233 offset:6144\n\t"
        "s_waitcnt lgkmcnt(5)\n\t"
        "v_mfma_f32_16x16x32_bf16 v[36:39], v[132:135], v[200:203], v[36:39]\n\t"
        "ds_read_b128 v[212:215], v234 offset:8704\n\t"
        "v_mfma_f32_16x16x32_bf16 v[36:39], v[148:151], v[204:207], v[36:39]\n\t"
        "ds_read_b128 v[220:223], v234 offset:8960\n\t"
        "v_mfma_f32_16x16x32_bf16 v[32:35], v[148:151], v[196:199], v[32:35]\n\t"
        "v_mfma_f32_16x16x32_bf16 v[32:35], v[132:135], v[192:195], v[32:35]\n\t"
        "ds_read_b128 v[216:219], v234 offset:768\n\t"
        "s_waitcnt lgkmcnt(6)\n\t"
        "v_mfma_f32_16x16x32_bf16 v[64:67], v[136:139], v[192:195], v[64:67]\n\t"
        "v_mfma_f32_16x16x32_bf16 v[64:67], v[152:155], v[196:199], v[64:67]\n\t"
        "v_mfma_f32_16x16x32_bf16 v[68:71], v[152:155], v[204:207], v[68:71]\n\t"
        "v_mfma_f32_16x16x32_bf16 v[68:71], v[136:139], v[200:203], v[68:71]\n\t"
        "s_waitcnt lgkmcnt(3)\n\t"
        "v_mfma_f32_16x16x32_bf16 v[100:103], v[140:143], v[200:203], v[100:103]\n\t"
        "v_mfma_f32_16x16x32_bf16 v[100:103], v[156:159], v[204:207], v[100:103]\n\t"
        "v_mfma_f32_16x16x32_bf16 v[96:99], v[156:159], v[196:199], v[96:99]\n\t"
        "v_mfma_f32_16x16x32_bf16 v[96:99], v[140:143], v[192:195], v[96:99]\n\t"
        "v_mfma_f32_16x16x32_bf16 v[8:11], v[128:131], v[208:211], v[8:11]\n\t"
        "s_waitcnt lgkmcnt(0)\n\t"
        "v_mfma_f32_16x16x32_bf16 v[8:11], v[144:147], v[212:215], v[8:11]\n\t"
        "ds_read_b128 v[192:195], v234 offset:1024\n\t"
        "v_mfma_f32_16x16x32_bf16 v[12:15], v[144:147], v[220:223], v[12:15]\n\t"
        "v_mfma_f32_16x16x32_bf16 v[12:15], v[128:131], v[216:219], v[12:15]\n\t"
        "ds_read_b128 v[196:199], v234 offset:9216\n\t"
        "v_mfma_f32_16x16x32_bf16 v[44:47], v[132:135], v[216:219], v[44:47]\n\t"
        "v_mfma_f32_16x16x32_bf16 v[44:47], v[148:151], v[220:223], v[44:47]\n\t"
        "ds_read_b128 v[204:207], v234 offset:9472\n\t"
        "v_mfma_f32_16x16x32_bf16 v[40:43], v[148:151], v[212:215], v[40:43]\n\t"
        "v_mfma_f32_16x16x32_bf16 v[40:43], v[132:135], v[208:211], v[40:43]\n\t"
        "ds_read_b128 v[200:203], v234 offset:1280\n\t"
        "v_mfma_f32_16x16x32_bf16 v[72:75], v[136:139], v[208:211], v[72:75]\n\t"
        "v_mfma_f32_16x16x32_bf16 v[72:75], v[152:155], v[212:215], v[72:75]\n\t"
        "v_mfma_f32_16x16x32_bf16 v[76:79], v[152:155], v[220:223], v[76:79]\n\t"
        "v_mfma_f32_16x16x32_bf16 v[76:79], v[136:139], v[216:219], v[76:79]\n\t"
        "v_mfma_f32_16x16x32_bf16 v[108:111], v[140:143], v[216:219], v[108:111]\n\t"
        "v_mfma_f32_16x16x32_bf16 v[108:111], v[156:159], v[220:223], v[108:111]\n\t"
        "v_mfma_f32_16x16x32_bf16 v[104:107], v[156:159], v[212:215], v[104:107]\n\t"
        "v_mfma_f32_16x16x32_bf16 v[104:107], v[140:143], v[208:211], v[104:107]\n\t"
        "s_waitcnt lgkmcnt(0)\n\t"
        "v_mfma_f32_16x16x32_bf16 v[16:19], v[128:131], v[192:195], v[16:19]\n\t"
        "v_mfma_f32_16x16x32_bf16 v[16:19], v[144:147], v[196:199], v[16:19]\n\t"
        "ds_read_b128 v[208:211], v234 offset:1536\n\t"
        "v_mfma_f32_16x16x32_bf16 v[20:23], v[144:147], v[204:207], v[20:23]\n\t"
        "v_mfma_f32_16x16x32_bf16 v[20:23], v[128:131], v[200:203], v[20:23]\n\t"
        "ds_read_b128 v[212:215], v234 offset:9728\n\t"
        "v_mfma_f32_16x16x32_bf16 v[52:55], v[132:135], v[200:203], v[52:55]\n\t"
        "v_mfma_f32_16x16x32_bf16 v[52:55], v[148:151], v[204:207], v[52:55]\n\t"
        "ds_read_b128 v[220:223], v234 offset:9984\n\t"
        "v_mfma_f32_16x16x32_bf16 v[48:51], v[148:151], v[196:199], v[48:51]\n\t"
        "v_mfma_f32_16x16x32_bf16 v[48:51], v[132:135], v[192:195], v[48:51]\n\t"
        "ds_read_b128 v[216:219], v234 offset:1792\n\t"
        "s_add_u32 s60, s60, 32768\n\t"
        "v_mfma_f32_16x16x32_bf16 v[80:83], v[136:139], v[192:195], v[80:83]\n\t"
        "s_cmp_eq_u32 s60, 98304\n\t"
        "s_cselect_b32 s60, 0, s60\n\t"
        "v_mfma_f32_16x16x32_bf16 v[80:83], v[152:155], v[196:199], v[80:83]\n\t"
        "v_add_u32 v232, s60, %[xb0]\n\t"
        "s_lshr_b32 s61, s60, 1\n\t"
        "v_mfma_f32_16x16x32_bf16 v[84:87], v[152:155], v[204:207], v[84:87]\n\t"
        "v_add_u32 v233, s60, %[xb1]\n\t"
        "v_add_u32 v234, s61, %[ba]\n\t"
        "v_mfma_f32_16x16x32_bf16 v[84:87], v[136:139], v[200:203], v[84:87]\n\t"
        "s_cmp_lg_u32 s62, -1\n\t"
        "s_cbranch_scc1 .Lcloop\n\t"
        "v_mfma_f32_16x16x32_bf16 v[116:119], v[140:143], v[200:203], v[116:119]\n\t"
        "v_mfma_f32_16x16x32_bf16 v[116:119], v[156:159], v[204:207], v[116:119]\n\t"
        "s_waitcnt lgkmcnt(0)\n\t"
        "v_mfma_f32_16x16x32_bf16 v[112:115], v[156:159], v[196:199], v[112:115]\n\t"
        "v_mfma_f32_16x16x32_bf16 v[112:115], v[140:143], v[192:195], v[112:115]\n\t"
        "s_barrier\n\t"
        "v_mfma_f32_16x16x32_bf16 v[24:27], v[128:131], v[208:211], v[24:27]\n\t"
        "s_setprio 2\n\t"
        ".Lwp14:\n\t"
        "s_cbranch_execz .Lwp15\n\t"
        "v_mfma_f32_16x16x32_bf16 v[24:27], v[144:147], v[212:215], v[24:27]\n\t"
        "ds_read_b128 v[160:163], %[ba] offset:16384\n\t"
        "ds_read_b128 v[164:167], v232 offset:0\n\t"
        "v_mfma_f32_16x16x32_bf16 v[28:31], v[144:147], v[220:223], v[28:31]\n\t"
        "v_mfma_f32_16x16x32_bf16 v[28:31], v[128:131], v[216:219], v[28:31]\n\t"
        "ds_read_b128 v[144:147], %[ba] offset:24576\n\t"
        "ds_read_b128 v[168:171], v233 offset:0\n\t"
        "v_mfma_f32_16x16x32_bf16 v[60:63], v[132:135], v[216:219], v[60:63]\n\t"
        "v_mfma_f32_16x16x32_bf16 v[60:63], v[148:151], v[220:223], v[60:63]\n\t"
        "ds_read_b128 v[128:131], %[ba] offset:24832\n\t"
        "ds_read_b128 v[172:175], %[ba] offset:16640\n\t"
        "v_mfma_f32_16x16x32_bf16 v[56:59], v[148:151], v[212:215], v[56:59]\n\t"
        "ds_read_b128 v[176:179], v232 offset:2048\n\t"
        "v_mfma_f32_16x16x32_bf16 v[56:59], v[132:135], v[208:211], v[56:59]\n\t"
        "ds_read_b128 v[148:151], v233 offset:2048\n\t"
        "v_mfma_f32_16x16x32_bf16 v[88:91], v[136:139], v[208:211], v[88:91]\n\t"
        "v_mfma_f32_16x16x32_bf16 v[88:91], v[152:155], v[212:215], v[88:91]\n\t"
        "ds_read_b128 v[132:135], v232 offset:4096\n\t"
        "v_mfma_f32_16x16x32_bf16 v[92:95], v[152:155], v[220:223], v[92:95]\n\t"
        "v_mfma_f32_16x16x32_bf16 v[92:95], v[136:139], v[216:219], v[92:95]\n\t"
        "ds_read_b128 v[152:155], v233 offset:4096\n\t"
        "v_mfma_f32_16x16x32_bf16 v[124:127], v[140:143], v[216:219], v[124:127]\n\t"
        "v_mfma_f32_16x16x32_bf16 v[124:127], v[156:159], v[220:223], v[124:127]\n\t"
        "ds_read_b128 v[136:139], v232 offset:6144\n\t"
        "v_mfma_f32_16x16x32_bf16 v[120:123], v[156:159], v[212:215], v[120:123]\n\t"
        "v_mfma_f32_16x16x32_bf16 v[120:123], v[140:143], v[208:211], v[120:123]\n\t"
        "ds_read_b128 v[156:159], v233 offset:6144\n\t"
        "s_waitcnt lgkmcnt(4)\n\t"
        "v_mfma_f32_16x16x32_bf16 v[0:3], v[164:167], v[160:163], v[0:3]\n\t"
        "v_mfma_f32_16x16x32_bf16 v[0:3], v[168:171], v[144:147], v[0:3]\n\t"
        "ds_read_b128 v[140:143], %[ba] offset:16896\n\t"
        "v_mfma_f32_16x16x32_bf16 v[4:7], v[168:171], v[128:131], v[4:7]\n\t"
        "v_mfma_f32_16x16x32_bf16 v[4:7], v[164:167], v[172:175], v[4:7]\n\t"
        "ds_read_b128 v[180:183], %[ba] offset:25088\n\t"
        "v_mfma_f32_16x16x32_bf16 v[36:39], v[176:179], v[172:175], v[36:39]\n\t"
        "v_mfma_f32_16x16x32_bf16 v[36:39], v[148:151], v[128:131], v[36:39]\n\t"
        "ds_read_b128 v[184:187], %[ba] offset:25344\n\t"
        "v_mfma_f32_16x16x32_bf16 v[32:35], v[148:151], v[144:147], v[32:35]\n\t"
        "v_mfma_f32_16x16x32_bf16 v[32:35], v[176:179], v[160:163], v[32:35]\n\t"
        "ds_read_b128 v[188:191], %[ba] offset:17152\n\t"
        "s_waitcnt lgkmcnt(4)\n\t"
        "v_mfma_f32_16x16x32_bf16 v[64:67], v[132:135], v[160:163], v[64:67]\n\t"
        "v_mfma_f32_16x16x32_bf16 v[64:67], v[152:155], v[144:147], v[64:67]\n\t"
        "ds_read_b128 v[192:195], %[ba] offset:17408\n\t"
        "v_mfma_f32_16x16x32_bf16 v[68:71], v[152:155], v[128:131], v[68:71]\n\t"
        "v_mfma_f32_16x16x32_bf16 v[68:71], v[132:135], v[172:175], v[68:71]\n\t"
        "ds_read_b128 v[196:199], v232 offset:32768\n\t"
        "v_mfma_f32_16x16x32_bf16 v[100:103], v[136:139], v[172:175], v[100:103]\n\t"
        "v_mfma_f32_16x16x32_bf16 v[100:103], v[156:159], v[128:131], v[100:103]\n\t"
        "ds_read_b128 v[172:175], %[ba] offset:25600\n\t"
        "v_mfma_f32_16x16x32_bf16 v[96:99], v[156:159], v[144:147], v[96:99]\n\t"
        "v_mfma_f32_16x16x32_bf16 v[96:99], v[136:139], v[160:163], v[96:99]\n\t"
        "ds_read_b128 v[128:131], v233 offset:32768\n\t"
        "s_waitcnt lgkmcnt(4)\n\t"
        "v_mfma_f32_16x16x32_bf16 v[8:11], v[164:167], v[140:143], v[8:11]\n\t"
        "v_mfma_f32_16x16x32_bf16 v[8:11], v[168:171], v[180:183], v[8:11]\n\t"
        "ds_read_b128 v[144:147], %[ba] offset:25856\n\t"
        "v_mfma_f32_16x16x32_bf16 v[12:15], v[168:171], v[184:187], v[12:15]\n\t"
        "v_mfma_f32_16x16x32_bf16 v[12:15], v[164:167], v[188:191], v[12:15]\n\t"
        "ds_read_b128 v[160:163], %[ba] offset:17664\n\t"
        "v_mfma_f32_16x16x32_bf16 v[44:47], v[176:179], v[188:191], v[44:47]\n\t"
        "v_mfma_f32_16x16x32_bf16 v[44:47], v[148:151], v[184:187], v[44:47]\n\t"
        "ds_read_b128 v[164:167], %[ba] offset:17920\n\t"
        "ds_read_b128 v[168:171], %[ba] offset:26112\n\t"
        "v_mfma_f32_16x16x32_bf16 v[40:43], v[148:151], v[180:183], v[40:43]\n\t"
        "v_mfma_f32_16x16x32_bf16 v[40:43], v[176:179], v[140:143], v[40:43]\n\t"
        "ds_read_b128 v[148:151], %[ba] offset:26368\n\t"
        "v_mfma_f32_16x16x32_bf16 v[72:75], v[132:135], v[140:143], v[72:75]\n\t"
        "v_mfma_f32_16x16x32_bf16 v[72:75], v[152:155], v[180:183], v[72:75]\n\t"
        "ds_read_b128 v[176:179], %[ba] offset:18176\n\t"
        "v_mfma_f32_16x16x32_bf16 v[76:79], v[152:155], v[184:187], v[76:79]\n\t"
        "v_mfma_f32_16x16x32_bf16 v[76:79], v[132:135], v[188:191], v[76:79]\n\t"
        "ds_read_b128 v[152:155], v232 offset:34816\n\t"
        "v_mfma_f32_16x16x32_bf16 v[108:111], v[136:139], v[188:191], v[108:111]\n\t"
        "v_mfma_f32_16x16x32_bf16 v[108:111], v[156:159], v[184:187], v[108:111]\n\t"
        "ds_read_b128 v[132:135], v233 offset:34816\n\t"
        "v_mfma_f32_16x16x32_bf16 v[104:107], v[156:159], v[180:183], v[104:107]\n\t"
        "v_mfma_f32_16x16x32_bf16 v[104:107], v[136:139], v[140:143], v[104:107]\n\t"
        "s_waitcnt lgkmcnt(3)\n\t"
        "v_mfma_f32_16x16x32_bf16 v[0:3], v[196:199], v[192:195], v[0:3]\n\t"
        "v_mfma_f32_16x16x32_bf16 v[0:3], v[128:131], v[172:175], v[0:3]\n\t"
        "v_mfma_f32_16x16x32_bf16 v[4:7], v[128:131], v[144:147], v[4:7]\n\t"
        "v_mfma_f32_16x16x32_bf16 v[4:7], v[196:199], v[160:163], v[4:7]\n\t"
        "ds_read_b128 v[136:139], v232 offset:36864\n\t"
        "v_mfma_f32_16x16x32_bf16 v[8:11], v[196:199], v[164:167], v[8:11]\n\t"
        "v_mfma_f32_16x16x32_bf16 v[8:11], v[128:131], v[168:171], v[8:11]\n\t"
        "ds_read_b128 v[140:143], v233 offset:36864\n\t"
        "v_mfma_f32_16x16x32_bf16 v[12:15], v[128:131], v[148:151], v[12:15]\n\t"
        "s_waitcnt lgkmcnt(2)\n\t"
        "v_mfma_f32_16x16x32_bf16 v[12:15], v[196:199], v[176:179], v[12:15]\n\t"
        "v_mfma_f32_16x16x32_bf16 v[32:35], v[152:155], v[192:195], v[32:35]\n\t"
        "v_mfma_f32_16x16x32_bf16 v[32:35], v[132:135], v[172:175], v[32:35]\n\t"
        "ds_read_b128 v[128:131], v232 offset:38912\n\t"
        "v_mfma_f32_16x16x32_bf16 v[36:39], v[132:135], v[144:147], v[36:39]\n\t"
        "v_mfma_f32_16x16x32_bf16 v[36:39], v[152:155], v[160:163], v[36:39]\n\t"
        "ds_read_b128 v[156:159], v233 offset:38912\n\t"
        "v_mfma_f32_16x16x32_bf16 v[40:43], v[152:155], v[164:167], v[40:43]\n\t"
        "v_mfma_f32_16x16x32_bf16 v[40:43], v[132:135], v[168:171], v[40:43]\n\t"
        "v_mfma_f32_16x16x32_bf16 v[44:47], v[132:135], v[148:151], v[44:47]\n\t"
        "v_mfma_f32_16x16x32_bf16 v[44:47], v[152:155], v[176:179], v[44:47]\n\t"
        "s_waitcnt lgkmcnt(0)\n\t"
        "s_barrier\n\t"
        "v_mfma_f32_16x16x32_bf16 v[64:67], v[136:139], v[192:195], v[64:67]\n\t"
        ".Lwp15:\n\t"
        "s_cbranch_execz .Lwp97\n\t"
        "v_mfma_f32_16x16x32_bf16 v[64:67], v[140:143], v[172:175], v[64:67]\n\t"
        "ds_read_b128 v[132:135], %[ba] offset:32768\n\t"
        "ds_read_b128 v[152:155], %[xb0] offset:0\n\t"
        "v_mfma_f32_16x16x32_bf16 v[68:71], v[140:143], v[144:147], v[68:71]\n\t"
        "v_mfma_f32_16x16x32_bf16 v[68:71], v[136:139], v[160:163], v[68:71]\n\t"
        "ds_read_b128 v[180:183], %[ba] offset:40960\n\t"
        "ds_read_b128 v[184:187], %[xb1] offset:0\n\t"
        "v_mfma_f32_16x16x32_bf16 v[72:75], v[136:139], v[164:167], v[72:75]\n\t"
        "v_mfma_f32_16x16x32_bf16 v[72:75], v[140:143], v[168:171], v[72:75]\n\t"
        "ds_read_b128 v[188:191], %[ba] offset:41216\n\t"
        "ds_read_b128 v[196:199], %[ba] offset:33024\n\t"
        "v_mfma_f32_16x16x32_bf16 v[76:79], v[140:143], v[148:151], v[76:79]\n\t"
        "v_mfma_f32_16x16x32_bf16 v[76:79], v[136:139], v[176:179], v[76:79]\n\t"
        "ds_read_b128 v[140:143], %[ba] offset:33280\n\t"
        "s_waitcnt lgkmcnt(5)\n\t"
        "v_mfma_f32_16x16x32_bf16 v[0:3], v[152:155], v[132:135], v[0:3]\n\t"
        "ds_read_b128 v[136:139], %[ba] offset:41472\n\t"
        "s_waitcnt lgkmcnt(4)\n\t"
        "v_mfma_f32_16x16x32_bf16 v[0:3], v[184:187], v[180:183], v[0:3]\n\t"
        "ds_read_b128 v[200:203], %[ba] offset:41728\n\t"
        "s_waitcnt lgkmcnt(4)\n\t"
        "v_mfma_f32_16x16x32_bf16 v[4:7], v[184:187], v[188:191], v[4:7]\n\t"
        "ds_read_b128 v[204:207], %[ba] offset:33536\n\t"
        "s_waitcnt lgkmcnt(4)\n\t"
        "v_mfma_f32_16x16x32_bf16 v[4:7], v[152:155], v[196:199], v[4:7]\n\t"
        "ds_read_b128 v[208:211], %[xb0] offset:2048\n\t"
        "s_waitcnt lgkmcnt(4)\n\t"
        "v_mfma_f32_16x16x32_bf16 v[8:11], v[152:155], v[140:143], v[8:11]\n\t"
        "s_waitcnt lgkmcnt(3)\n\t"
        "v_mfma_f32_16x16x32_bf16 v[8:11], v[184:187], v[136:139], v[8:11]\n\t"
        "ds_read_b128 v[212:215], %[xb1] offset:2048\n\t"
        "s_waitcnt lgkmcnt(3)\n\t"
        "v_mfma_f32_16x16x32_bf16 v[12:15], v[184:187], v[200:203], v[12:15]\n\t"
        "s_waitcnt lgkmcnt(2)\n\t"
        "v_mfma_f32_16x16x32_bf16 v[12:15], v[152:155], v[204:207], v[12:15]\n\t"
        "v_mfma_f32_16x16x32_bf16 v[96:99], v[128:131], v[192:195], v[96:99]\n\t"
        "v_mfma_f32_16x16x32_bf16 v[96:99], v[156:159], v[172:175], v[96:99]\n\t"
        "ds_read_b128 v[152:155], %[xb0] offset:4096\n\t"
        "v_cvt_pk_bf16_f32 v224, v0, v4\n\t"
        "v_mfma_f32_16x16x32_bf16 v[100:103], v[156:159], v[144:147], v[100:103]\n\t"
        "v_mfma_f32_16x16x32_bf16 v[100:103], v[128:131], v[160:163], v[100:103]\n\t"
        "ds_read_b128 v[144:147], %[xb1] offset:4096\n\t"
        "v_cvt_pk_bf16_f32 v225, v8, v12\n\t"
        "v_mfma_f32_16x16x32_bf16 v[104:107], v[128:131], v[164:167], v[104:107]\n\t"
        "global_store_dwordx2 %[oo0], v[224:225], %[ob0] offset:0 nt\n\t"
        "v_mfma_f32_16x16x32_bf16 v[104:107], v[156:159], v[168:171], v[104:107]\n\t"
        "ds_read_b128 v[160:163], %[xb0] offset:6144\n\t"
        "v_cvt_pk_bf16_f32 v227, v9, v13\n\t"
        "v_mfma_f32_16x16x32_bf16 v[108:111], v[156:159], v[148:151], v[108:111]\n\t"
        "v_cvt_pk_bf16_f32 v226, v1, v5\n\t"
        "v_mfma_f32_16x16x32_bf16 v[108:111], v[128:131], v[176:179], v[108:111]\n\t"
        "ds_read_b128 v[148:151], %[xb1] offset:6144\n\t"
        "s_waitcnt lgkmcnt(4)\n\t"
        "v_mfma_f32_16x16x32_bf16 v[32:35], v[208:211], v[132:135], v[32:35]\n\t"
        "global_store_dwordx2 %[oo1], v[226:227], %[ob0] offset:0 nt\n\t"
        "v_mfma_f32_16x16x32_bf16 v[32:35], v[212:215], v[180:183], v[32:35]\n\t"
        "ds_read_b128 v[128:131], v232 offset:0\n\t"
        "v_cvt_pk_bf16_f32 v229, v10, v14\n\t"
        "v_mfma_f32_16x16x32_bf16 v[36:39], v[212:215], v[188:191], v[36:39]\n\t"
        "v_cvt_pk_bf16_f32 v228, v2, v6\n\t"
        "v_mfma_f32_16x16x32_bf16 v[36:39], v[208:211], v[196:199], v[36:39]\n\t"
        "ds_read_b128 v[156:159], %[ba] offset:33792\n\t"
        "global_store_dwordx2 %[oo2], v[228:229], %[ob0] offset:0 nt\n\t"
        "v_mfma_f32_16x16x32_bf16 v[40:43], v[208:211], v[140:143], v[40:43]\n\t"
        "v_mfma_f32_16x16x32_bf16 v[40:43], v[212:215], v[136:139], v[40:43]\n\t"
        "ds_read_b128 v[164:167], v233 offset:0\n\t"
        "v_cvt_pk_bf16_f32 v231, v11, v15\n\t"
        "v_mfma_f32_16x16x32_bf16 v[44:47], v[212:215], v[200:203], v[44:47]\n\t"
        "v_cvt_pk_bf16_f32 v230, v3, v7\n\t"
        "v_mfma_f32_16x16x32_bf16 v[44:47], v[208:211], v[204:207], v[44:47]\n\t"
        "ds_read_b128 v[168:171], %[ba] offset:41984\n\t"
        "s_waitcnt lgkmcnt(4)\n\t"
        "v_mfma_f32_16x16x32_bf16 v[64:67], v[152:155], v[132:135], v[64:67]\n\t"
        "global_store_dwordx2 %[oo3], v[230:231], %[ob0] offset:0 nt\n\t"
        "v_mfma_f32_16x16x32_bf16 v[64:67], v[144:147], v[180:183], v[64:67]\n\t"
        "ds_read_b128 v[172:175], %[ba] offset:42240\n\t"
        "v_cvt_pk_bf16_f32 v224, v32, v36\n\t"
        "v_mfma_f32_16x16x32_bf16 v[68:71], v[144:147], v[188:191], v[68:71]\n\t"
        "v_mfma_f32_16x16x32_bf16 v[68:71], v[152:155], v[196:199], v[68:71]\n\t"
        "ds_read_b128 v[176:179], %[ba] offset:34048\n\t"
        "v_cvt_pk_bf16_f32 v225, v40, v44\n\t"
        "v_mfma_f32_16x16x32_bf16 v[72:75], v[152:155], v[140:143], v[72:75]\n\t"
        "global_store_dwordx2 %[oo0], v[224:225], %[ob1] offset:0 nt\n\t"
        "v_mfma_f32_16x16x32_bf16 v[72:75], v[144:147], v[136:139], v[72:75]\n\t"
        "ds_read_b128 v[184:187], %[ba] offset:34304\n\t"
        "ds_read_b128 v[192:195], %[ba] offset:42496\n\t"
        "v_mfma_f32_16x16x32_bf16 v[76:79], v[144:147], v[200:203], v[76:79]\n\t"
        "v_cvt_pk_bf16_f32 v227, v41, v45\n\t"
        "v_cvt_pk_bf16_f32 v226, v33, v37\n\t"
        "v_mfma_f32_16x16x32_bf16 v[76:79], v[152:155], v[204:207], v[76:79]\n\t"
        "ds_read_b128 v[144:147], %[ba] offset:42752\n\t"
        "global_store_dwordx2 %[oo1], v[226:227], %[ob1] offset:0 nt\n\t"
        "v_mfma_f32_16x16x32_bf16 v[96:99], v[160:163], v[132:135], v[96:99]\n\t"
        ".Lwp16:\n\t"
        "s_cbranch_execz .Lwp17\n\t"
        "v_mfma_f32_16x16x32_bf16 v[96:99], v[148:151], v[180:183], v[96:99]\n\t"
        "ds_read_b128 v[132:135], %[ba] offset:34560\n\t"
        "v_cvt_pk_bf16_f32 v229, v42, v46\n\t"
        "v_mfma_f32_16x16x32_bf16 v[100:103], v[148:151], v[188:191], v[100:103]\n\t"
        "v_cvt_pk_bf16_f32 v228, v34, v38\n\t"
        "v_mfma_f32_16x16x32_bf16 v[100:103], v[160:163], v[196:199], v[100:103]\n\t"
        "ds_read_b128 v[152:155], v232 offset:2048\n\t"
        "global_store_dwordx2 %[oo2], v[228:229], %[ob1] offset:0 nt\n\t"
        "v_mfma_f32_16x16x32_bf16 v[104:107], v[160:163], v[140:143], v[104:107]\n\t"
        "v_mfma_f32_16x16x32_bf16 v[104:107], v[148:151], v[136:139], v[104:107]\n\t"
        "ds_read_b128 v[140:143], v233 offset:2048\n\t"
        "v_cvt_pk_bf16_f32 v231, v43, v47\n\t"
        "v_mfma_f32_16x16x32_bf16 v[108:111], v[148:151], v[200:203], v[108:111]\n\t"
        "v_cvt_pk_bf16_f32 v230, v35, v39\n\t"
        "v_mfma_f32_16x16x32_bf16 v[108:111], v[160:163], v[204:207], v[108:111]\n\t"
        "global_store_dwordx2 %[oo3], v[230:231], %[ob1] offset:0 nt\n\t"
        "s_waitcnt lgkmcnt(3)\n\t"
        "v_mfma_f32_16x16x32_bf16 v[16:19], v[128:131], v[156:159], v[16:19]\n\t"
        "v_mfma_f32_16x16x32_bf16 v[16:19], v[164:167], v[168:171], v[16:19]\n\t"
        "ds_read_b128 v[136:139], v232 offset:4096\n\t"
        "v_cvt_pk_bf16_f32 v225, v72, v76\n\t"
        "v_mfma_f32_16x16x32_bf16 v[20:23], v[164:167], v[172:175], v[20:23]\n\t"
        "v_cvt_pk_bf16_f32 v224, v64, v68\n\t"
        "v_mfma_f32_16x16x32_bf16 v[20:23], v[128:131], v[176:179], v[20:23]\n\t"
        "ds_read_b128 v[148:151], v233 offset:4096\n\t"
        "global_store_dwordx2 %[oo0], v[224:225], %[ob2] offset:0 nt\n\t"
        "v_mfma_f32_16x16x32_bf16 v[24:27], v[128:131], v[184:187], v[24:27]\n\t"
        "v_mfma_f32_16x16x32_bf16 v[24:27], v[164:167], v[192:195], v[24:27]\n\t"
        "ds_read_b128 v[160:163], v232 offset:6144\n\t"
        "v_cvt_pk_bf16_f32 v227, v73, v77\n\t"
        "v_mfma_f32_16x16x32_bf16 v[28:31], v[164:167], v[144:147], v[28:31]\n\t"
        "v_cvt_pk_bf16_f32 v226, v65, v69\n\t"
        "s_waitcnt lgkmcnt(3)\n\t"
        "v_mfma_f32_16x16x32_bf16 v[28:31], v[128:131], v[132:135], v[28:31]\n\t"
        "ds_read_b128 v[164:167], v233 offset:6144\n\t"
        "global_store_dwordx2 %[oo1], v[226:227], %[ob2] offset:0 nt\n\t"
        "v_mfma_f32_16x16x32_bf16 v[48:51], v[152:155], v[156:159], v[48:51]\n\t"
        "v_mfma_f32_16x16x32_bf16 v[48:51], v[140:143], v[168:171], v[48:51]\n\t"
        "ds_read_b128 v[128:131], v232 offset:32768\n\t"
        "v_cvt_pk_bf16_f32 v229, v74, v78\n\t"
        "v_mfma_f32_16x16x32_bf16 v[52:55], v[140:143], v[172:175], v[52:55]\n\t"
        "v_cvt_pk_bf16_f32 v228, v66, v70\n\t"
        "v_mfma_f32_16x16x32_bf16 v[52:55], v[152:155], v[176:179], v[52:55]\n\t"
        "ds_read_b128 v[180:183], v233 offset:32768\n\t"
        "ds_read_b128 v[188:191], %[xb0] offset:0\n\t"
        "v_mfma_f32_16x16x32_bf16 v[56:59], v[152:155], v[184:187], v[56:59]\n\t"
        "global_store_dwordx2 %[oo2], v[228:229], %[ob2] offset:0 nt\n\t"
        "v_mfma_f32_16x16x32_bf16 v[56:59], v[140:143], v[192:195], v[56:59]\n\t"
        "v_cvt_pk_bf16_f32 v231, v75, v79\n\t"
        "v_cvt_pk_bf16_f32 v230, v67, v71\n\t"
        "v_mfma_f32_16x16x32_bf16 v[60:63], v[140:143], v[144:147], v[60:63]\n\t"
        "v_mfma_f32_16x16x32_bf16 v[60:63], v[152:155], v[132:135], v[60:63]\n\t"
        "global_store_dwordx2 %[oo3], v[230:231], %[ob2] offset:0 nt\n\t"
        "s_barrier\n\t"
        "s_waitcnt lgkmcnt(3)\n\t"
        "v_mfma_f32_16x16x32_bf16 v[80:83], v[136:139], v[156:159], v[80:83]\n\t"
        "v_mfma_f32_16x16x32_bf16 v[80:83], v[148:151], v[168:171], v[80:83]\n\t"
        "ds_read_b128 v[140:143], %[ba] offset:0\n\t"
        "ds_read_b128 v[152:155], %[xb1] offset:0\n\t"
        "v_mfma_f32_16x16x32_bf16 v[84:87], v[148:151], v[172:175], v[84:87]\n\t"
        "v_cvt_pk_bf16_f32 v225, v104, v108\n\t"
        "v_cvt_pk_bf16_f32 v224, v96, v100\n\t"
        "v_mfma_f32_16x16x32_bf16 v[84:87], v[136:139], v[176:179], v[84:87]\n\t"
        ".Lwp17:\n\t"
        "s_cbranch_execz .Lwp18\n\t"
        "ds_read_b128 v[196:199], %[ba] offset:8192\n\t"
        "v_mfma_f32_16x16x32_bf16 v[88:91], v[136:139], v[184:187], v[88:91]\n\t"
        "ds_read_b128 v[200:203], %[ba] offset:1024\n\t"
        "ds_read_b128 v[204:207], %[ba] offset:9216\n\t"
        "v_mfma_f32_16x16x32_bf16 v[88:91], v[148:151], v[192:195], v[88:91]\n\t"
        "global_store_dwordx2 %[oo0], v[224:225], %[ob3] offset:0 nt\n\t"
        "ds_read_b128 v[208:211], %[ba] offset:9472\n\t"
        "v_mfma_f32_16x16x32_bf16 v[92:95], v[148:151], v[144:147], v[92:95]\n\t"
        "ds_read_b128 v[212:215], %[ba] offset:1280\n\t"
        "ds_read_b128 v[216:219], %[ba] offset:8448\n\t"
        "v_mfma_f32_16x16x32_bf16 v[92:95], v[136:139], v[132:135], v[92:95]\n\t"
        "v_cvt_pk_bf16_f32 v227, v105, v109\n\t"
        "v_cvt_pk_bf16_f32 v226, v97, v101\n\t"
        "v_mfma_f32_16x16x32_bf16 v[112:115], v[160:163], v[156:159], v[112:115]\n\t"
        "ds_read_b128 v[136:139], %[ba] offset:256\n\t"
        "ds_read_b128 v[148:151], %[ba] offset:512\n\t"
        "v_mfma_f32_16x16x32_bf16 v[112:115], v[164:167], v[168:171], v[112:115]\n\t"
        "ds_read_b128 v[156:159], %[ba] offset:8704\n\t"
        "global_store_dwordx2 %[oo1], v[226:227], %[ob3] offset:0 nt\n\t"
        "v_mfma_f32_16x16x32_bf16 v[116:119], v[164:167], v[172:175], v[116:119]\n\t"
        "ds_read_b128 v[168:171], %[ba] offset:1536\n\t"
        "ds_read_b128 v[220:223], %[ba] offset:9728\n\t"
        "v_mfma_f32_16x16x32_bf16 v[116:119], v[160:163], v[176:179], v[116:119]\n\t"
        "v_cvt_pk_bf16_f32 v229, v106, v110\n\t"
        "v_cvt_pk_bf16_f32 v228, v98, v102\n\t"
        "v_mfma_f32_16x16x32_bf16 v[120:123], v[160:163], v[184:187], v[120:123]\n\t"
        "ds_read_b128 v[172:175], %[ba] offset:9984\n\t"
        "ds_read_b128 v[176:179], %[ba] offset:1792\n\t"
        "v_mfma_f32_16x16x32_bf16 v[120:123], v[164:167], v[192:195], v[120:123]\n\t"
        "global_store_dwordx2 %[oo2], v[228:229], %[ob3] offset:0 nt\n\t"
        "ds_read_b128 v[184:187], %[ba] offset:8960\n\t"
        "v_mfma_f32_16x16x32_bf16 v[124:127], v[164:167], v[144:147], v[124:127]\n\t"
        "v_cvt_pk_bf16_f32 v231, v107, v111\n\t"
        "v_cvt_pk_bf16_f32 v230, v99, v103\n\t"
        "v_mfma_f32_16x16x32_bf16 v[124:127], v[160:163], v[132:135], v[124:127]\n\t"
        "ds_read_b128 v[144:147], %[ba] offset:768\n\t"
        "s_waitcnt lgkmcnt(9)\n\t"
        "v_mfma_f32_16x16x32_bf16 v[16:19], v[128:131], v[140:143], v[16:19]\n\t"
        "global_store_dwordx2 %[oo3], v[230:231], %[ob3] offset:0 nt\n\t"
        "v_mfma_f32_16x16x32_bf16 v[16:19], v[180:183], v[196:199], v[16:19]\n\t"
        "ds_read_b128 v[132:135], v232 offset:34816\n\t"
        "v_mfma_f32_16x16x32_bf16 v[16:19], v[188:191], v[200:203], v[16:19]\n\t"
        "v_mfma_f32_16x16x32_bf16 v[16:19], v[152:155], v[204:207], v[16:19]\n\t"
        "ds_read_b128 v[160:163], v233 offset:34816\n\t"
        "v_mfma_f32_16x16x32_bf16 v[20:23], v[152:155], v[208:211], v[20:23]\n\t"
        "v_mfma_f32_16x16x32_bf16 v[20:23], v[188:191], v[212:215], v[20:23]\n\t"
        "ds_read_b128 v[164:167], %[xb0] offset:2048\n\t"
        "v_mfma_f32_16x16x32_bf16 v[20:23], v[180:183], v[216:219], v[20:23]\n\t"
        "s_waitcnt lgkmcnt(4)\n\t"
        "v_mfma_f32_16x16x32_bf16 v[20:23], v[128:131], v[136:139], v[20:23]\n\t"
        "ds_read_b128 v[192:195], %[xb1] offset:2048\n\t"
        "v_mfma_f32_16x16x32_bf16 v[24:27], v[128:131], v[148:151], v[24:27]\n\t"
        "v_mfma_f32_16x16x32_bf16 v[24:27], v[180:183], v[156:159], v[24:27]\n\t"
        "v_mfma_f32_16x16x32_bf16 v[24:27], v[188:191], v[168:171], v[24:27]\n\t"
        "v_mfma_f32_16x16x32_bf16 v[24:27], v[152:155], v[220:223], v[24:27]\n\t"
        "v_mfma_f32_16x16x32_bf16 v[28:31], v[152:155], v[172:175], v[28:31]\n\t"
        "v_mfma_f32_16x16x32_bf16 v[28:31], v[188:191], v[176:179], v[28:31]\n\t"
        "v_mfma_f32_16x16x32_bf16 v[28:31], v[180:183], v[184:187], v[28:31]\n\t"
        "s_waitcnt lgkmcnt(1)\n\t"
        "v_mfma_f32_16x16x32_bf16 v[28:31], v[128:131], v[144:147], v[28:31]\n\t"
        "v_mfma_f32_16x16x32_bf16 v[48:51], v[132:135], v[140:143], v[48:51]\n\t"
        "v_mfma_f32_16x16x32_bf16 v[48:51], v[160:163], v[196:199], v[48:51]\n\t"
        "ds_read_b128 v[128:131], v232 offset:36864\n\t"
        "v_cvt_pk_bf16_f32 v224, v16, v20\n\t"
        "v_mfma_f32_16x16x32_bf16 v[48:51], v[164:167], v[200:203], v[48:51]\n\t"
        "s_waitcnt lgkmcnt(1)\n\t"
        "v_mfma_f32_16x16x32_bf16 v[48:51], v[192:195], v[204:207], v[48:51]\n\t"
        "ds_read_b128 v[152:155], v233 offset:36864\n\t"
        "v_cvt_pk_bf16_f32 v225, v24, v28\n\t"
        "v_mfma_f32_16x16x32_bf16 v[52:55], v[192:195], v[208:211], v[52:55]\n\t"
        "global_store_dwordx2 %[oo0], v[224:225], %[ob0] offset:128 nt\n\t"
        "v_mfma_f32_16x16x32_bf16 v[52:55], v[164:167], v[212:215], v[52:55]\n\t"
        "ds_read_b128 v[180:183], %[xb0] offset:4096\n\t"
        "v_cvt_pk_bf16_f32 v227, v25, v29\n\t"
        "v_mfma_f32_16x16x32_bf16 v[52:55], v[160:163], v[216:219], v[52:55]\n\t"
        "v_cvt_pk_bf16_f32 v226, v17, v21\n\t"
        "v_mfma_f32_16x16x32_bf16 v[52:55], v[132:135], v[136:139], v[52:55]\n\t"
        "ds_read_b128 v[188:191], %[xb1] offset:4096\n\t"
        "global_store_dwordx2 %[oo1], v[226:227], %[ob0] offset:128 nt\n\t"
        "v_mfma_f32_16x16x32_bf16 v[56:59], v[132:135], v[148:151], v[56:59]\n\t"
        "v_mfma_f32_16x16x32_bf16 v[56:59], v[160:163], v[156:159], v[56:59]\n\t"
        "v_cvt_pk_bf16_f32 v229, v26, v30\n\t"
        "v_cvt_pk_bf16_f32 v228, v18, v22\n\t"
        "v_mfma_f32_16x16x32_bf16 v[56:59], v[164:167], v[168:171], v[56:59]\n\t"
        "v_mfma_f32_16x16x32_bf16 v[56:59], v[192:195], v[220:223], v[56:59]\n\t"
        "global_store_dwordx2 %[oo2], v[228:229], %[ob0] offset:128 nt\n\t"
        "v_mfma_f32_16x16x32_bf16 v[60:63], v[192:195], v[172:175], v[60:63]\n\t"
        "v_mfma_f32_16x16x32_bf16 v[60:63], v[164:167], v[176:179], v[60:63]\n\t"
        "v_cvt_pk_bf16_f32 v231, v27, v31\n\t"
        "v_cvt_pk_bf16_f32 v230, v19, v23\n\t"
        "v_mfma_f32_16x16x32_bf16 v[60:63], v[160:163], v[184:187], v[60:63]\n\t"
        ".Lwp18:\n\t"
        "s_cbranch_execz .Lwp97\n\t"
        "v_mfma_f32_16x16x32_bf16 v[60:63], v[132:135], v[144:147], v[60:63]\n\t"
        "global_store_dwordx2 %[oo3], v[230:231], %[ob0] offset:128 nt\n\t"
        "s_waitcnt lgkmcnt(0)\n\t"
        "v_mfma_f32_16x16x32_bf16 v[80:83], v[128:131], v[140:143], v[80:83]\n\t"
        "v_mfma_f32_16x16x32_bf16 v[80:83], v[152:155], v[196:199], v[80:83]\n\t"
        "ds_read_b128 v[132:135], v232 offset:38912\n\t"
        "v_cvt_pk_bf16_f32 v224, v48, v52\n\t"
        "v_mfma_f32_16x16x32_bf16 v[80:83], v[180:183], v[200:203], v[80:83]\n\t"
        "v_mfma_f32_16x16x32_bf16 v[80:83], v[188:191], v[204:207], v[80:83]\n\t"
        "ds_read_b128 v[160:163], v233 offset:38912\n\t"
        "v_cvt_pk_bf16_f32 v225, v56, v60\n\t"
        "v_mfma_f32_16x16x32_bf16 v[84:87], v[188:191], v[208:211], v[84:87]\n\t"
        "global_store_dwordx2 %[oo0], v[224:225], %[ob1] offset:128 nt\n\t"
        "v_mfma_f32_16x16x32_bf16 v[84:87], v[180:183], v[212:215], v[84:87]\n\t"
        "ds_read_b128 v[164:167], %[xb0] offset:6144\n\t"
        "v_cvt_pk_bf16_f32 v227, v57, v61\n\t"
        "v_mfma_f32_16x16x32_bf16 v[84:87], v[152:155], v[216:219], v[84:87]\n\t"
        "v_cvt_pk_bf16_f32 v226, v49, v53\n\t"
        "v_mfma_f32_16x16x32_bf16 v[84:87], v[128:131], v[136:139], v[84:87]\n\t"
        "ds_read_b128 v[192:195], %[xb1] offset:6144\n\t"
        "global_store_dwordx2 %[oo1], v[226:227], %[ob1] offset:128 nt\n\t"
        "v_mfma_f32_16x16x32_bf16 v[88:91], v[128:131], v[148:151], v[88:91]\n\t"
        "v_mfma_f32_16x16x32_bf16 v[88:91], v[152:155], v[156:159], v[88:91]\n\t"
        "v_cvt_pk_bf16_f32 v229, v58, v62\n\t"
        "v_cvt_pk_bf16_f32 v228, v50, v54\n\t"
        "v_mfma_f32_16x16x32_bf16 v[88:91], v[180:183], v[168:171], v[88:91]\n\t"
        "v_mfma_f32_16x16x32_bf16 v[88:91], v[188:191], v[220:223], v[88:91]\n\t"
        "global_store_dwordx2 %[oo2], v[228:229], %[ob1] offset:128 nt\n\t"
        "v_mfma_f32_16x16x32_bf16 v[92:95], v[188:191], v[172:175], v[92:95]\n\t"
        "v_mfma_f32_16x16x32_bf16 v[92:95], v[180:183], v[176:179], v[92:95]\n\t"
        "v_cvt_pk_bf16_f32 v231, v59, v63\n\t"
        "v_cvt_pk_bf16_f32 v230, v51, v55\n\t"
        "v_mfma_f32_16x16x32_bf16 v[92:95], v[152:155], v[184:187], v[92:95]\n\t"
        "v_mfma_f32_16x16x32_bf16 v[92:95], v[128:131], v[144:147], v[92:95]\n\t"
        "global_store_dwordx2 %[oo3], v[230:231], %[ob1] offset:128 nt\n\t"
        "s_waitcnt lgkmcnt(0)\n\t"
        "v_mfma_f32_16x16x32_bf16 v[112:115], v[132:135], v[140:143], v[112:115]\n\t"
        "v_mfma_f32_16x16x32_bf16 v[112:115], v[160:163], v[196:199], v[112:115]\n\t"
        "v_cvt_pk_bf16_f32 v224, v80, v84\n\t"
        "v_mfma_f32_16x16x32_bf16 v[112:115], v[164:167], v[200:203], v[112:115]\n\t"
        "v_mfma_f32_16x16x32_bf16 v[112:115], v[192:195], v[204:207], v[112:115]\n\t"
        "v_cvt_pk_bf16_f32 v225, v88, v92\n\t"
        "global_store_dwordx2 %[oo0], v[224:225], %[ob2] offset:128 nt\n\t"
        "v_mfma_f32_16x16x32_bf16 v[116:119], v[192:195], v[208:211], v[116:119]\n\t"
        "v_mfma_f32_16x16x32_bf16 v[116:119], v[164:167], v[212:215], v[116:119]\n\t"
        "v_cvt_pk_bf16_f32 v227, v89, v93\n\t"
        "v_cvt_pk_bf16_f32 v226, v81, v85\n\t"
        "v_mfma_f32_16x16x32_bf16 v[116:119], v[160:163], v[216:219], v[116:119]\n\t"
        "v_mfma_f32_16x16x32_bf16 v[116:119], v[132:135], v[136:139], v[116:119]\n\t"
        "global_store_dwordx2 %[oo1], v[226:227], %[ob2] offset:128 nt\n\t"
        "v_mfma_f32_16x16x32_bf16 v[120:123], v[132:135], v[148:151], v[120:123]\n\t"
        "v_mfma_f32_16x16x32_bf16 v[120:123], v[160:163], v[156:159], v[120:123]\n\t"
        "v_cvt_pk_bf16_f32 v229, v90, v94\n\t"
        "v_cvt_pk_bf16_f32 v228, v82, v86\n\t"
        "v_mfma_f32_16x16x32_bf16 v[120:123], v[164:167], v[168:171], v[120:123]\n\t"
        "v_mfma_f32_16x16x32_bf16 v[120:123], v[192:195], v[220:223], v[120:123]\n\t"
        "global_store_dwordx2 %[oo2], v[228:229], %[ob2] offset:128 nt\n\t"
        "v_mfma_f32_16x16x32_bf16 v[124:127], v[192:195], v[172:175], v[124:127]\n\t"
        "v_mfma_f32_16x16x32_bf16 v[124:127], v[164:167], v[176:179], v[124:127]\n\t"
        "v_cvt_pk_bf16_f32 v230, v113, v117\n\t"
        "v_cvt_pk_bf16_f32 v224, v112, v116\n\t"
        "v_mfma_f32_16x16x32_bf16 v[124:127], v[160:163], v[184:187], v[124:127]\n\t"
        "v_cvt_pk_bf16_f32 v227, v91, v95\n\t"
        "v_cvt_pk_bf16_f32 v226, v83, v87\n\t"
        "v_mfma_f32_16x16x32_bf16 v[124:127], v[132:135], v[144:147], v[124:127]\n\t"
        "v_cvt_pk_bf16_f32 v228, v115, v119\n\t"
        "v_cvt_pk_bf16_f32 v128, v114, v118\n\t"
        "global_store_dwordx2 %[oo3], v[226:227], %[ob2] offset:128 nt\n\t"
        "s_nop 7\n\t"
        "v_cvt_pk_bf16_f32 v225, v120, v124\n\t"
        "global_store_dwordx2 %[oo0], v[224:225], %[ob3] offset:128 nt\n\t"
        "v_cvt_pk_bf16_f32 v231, v121, v125\n\t"
        "global_store_dwordx2 %[oo1], v[230:231], %[ob3] offset:128 nt\n\t"
        "v_cvt_pk_bf16_f32 v129, v122, v126\n\t"
        "global_store_dwordx2 %[oo2], v[128:129], %[ob3] offset:128 nt\n\t"
        "v_cvt_pk_bf16_f32 v229, v123, v127\n\t"
        "global_store_dwordx2 %[oo3], v[228:229], %[ob3] offset:128 nt\n\t"
        :
        : [xb0] "v"(xb0), [xb1] "v"(xb1), [ba] "v"(ba), [oo0] "v"(oo[0]), [oo1] "v"(oo[1]), [oo2] "v"(oo[2]), [oo3] "v"(oo[3]),
          [ob0] "s"(ob[0]), [ob1] "s"(ob[1]), [ob2] "s"(ob[2]), [ob3] "s"(ob[3]), [w] "s"(w)
        : "scc", "memory", "s60", "s61", "s62", "s63", "s64", "s65", "s66", "s67", "v0", "v1", "v2", "v3", "v4", "v5", "v6", "v7", "v8", "v9", "v10",
              "v11", "v12", "v13", "v14", "v15", "v16", "v17", "v18", "v19", "v20", "v21", "v22", "v23", "v24", "v25", "v26", "v27", "v28", "v29",
              "v30", "v31", "v32", "v33", "v34", "v35", "v36", "v37", "v38", "v39", "v40", "v41", "v42", "v43", "v44", "v45", "v46", "v47", "v48",
              "v49", "v50", "v51", "v52", "v53", "v54", "v55", "v56", "v57", "v58", "v59", "v60", "v61", "v62", "v63", "v64", "v65", "v66", "v67",
              "v68", "v69", "v70", "v71", "v72", "v73", "v74", "v75", "v76", "v77", "v78", "v79", "v80", "v81", "v82", "v83", "v84", "v85", "v86",
              "v87", "v88", "v89", "v90", "v91", "v92", "v93", "v94", "v95", "v96", "v97", "v98", "v99", "v100", "v101", "v102", "v103", "v104",
              "v105", "v106", "v107", "v108", "v109", "v110", "v111", "v112", "v113", "v114", "v115", "v116", "v117", "v118", "v119", "v120", "v121",
              "v122", "v123", "v124", "v125", "v126", "v127", "v128", "v129", "v130", "v131", "v132", "v133", "v134", "v135", "v136", "v137", "v138",
              "v139", "v140", "v141", "v142", "v143", "v144", "v145", "v146", "v147", "v148", "v149", "v150", "v151", "v152", "v153", "v154", "v155",
              "v156", "v157", "v158", "v159", "v160", "v161", "v162", "v163", "v164", "v165", "v166", "v167", "v168", "v169", "v170", "v171", "v172",
              "v173", "v174", "v175", "v176", "v177", "v178", "v179", "v180", "v181", "v182", "v183", "v184", "v185", "v186", "v187", "v188", "v189",
              "v190", "v191", "v192", "v193", "v194", "v195", "v196", "v197", "v198", "v199", "v200", "v201", "v202", "v203", "v204", "v205", "v206",
              "v207", "v208", "v209", "v210", "v211", "v212", "v213", "v214", "v215", "v216", "v217", "v218", "v219", "v220", "v221", "v222", "v223",
              "v224", "v225", "v226", "v227", "v228", "v229", "v230", "v231", "v232", "v233", "v234", "v235");
}

extern "C" void kernel_launch(void* const* d_in, const int* in_sizes, int n_in,
                              void* d_out, int out_size, void* d_ws, size_t ws_size,
                              hipStream_t stream) {
    const u16* x = (const u16*)d_in[0];
    const int* qw = (const int*)d_in[1];
    const u16* lut = (const u16*)d_in[2];
    u16* out = (u16*)d_out;
    (void)hipFuncSetAttribute((const void*)fused_kernel, hipFuncAttributeMaxDynamicSharedMemorySize, LDS_BYTES);
    fused_kernel<<<(M_ / BM) * (N_ / BN) + NPF, 512, LDS_BYTES, stream>>>(x, qw, lut, out, (char*)d_ws);
}
```

```cpp
#include <hip/hip_runtime.h>
#include <hip/hip_bf16.h>

typedef unsigned short u16;
typedef unsigned int u32;
using i32x4  = __attribute__((ext_vector_type(4))) int;
using u32x4  = __attribute__((ext_vector_type(4))) unsigned int;

constexpr int M_ = 2048, K_ = 1024, N_ = 3584;
constexpr int BM = 256, BN = 128;
constexpr int NPF = 32;
#define CPF 116
#define CPFB 32
#define PACE 0
constexpr int XS = BM * 64 * 2;
constexpr int BS = BN * 32 * 2;
constexpr int OFF_B = 3 * XS;
constexpr int WSL = 2 * BS;
constexpr int OFF_L = OFF_B + 3 * WSL;
constexpr int OFF_D = OFF_L + 16 * BN * 2;
constexpr int WS_F = 28 * 256, WS_W = 65536;
constexpr unsigned TW = 28 * 16384;
constexpr int LDS_BYTES = OFF_D + 1024;

#define TSEQ(mt) (0x76532140u ^ ((u32)((mt) >> 1) * 0x11111111u))
#define BAR() __builtin_amdgcn_s_barrier()
#define SB() __builtin_amdgcn_sched_barrier(0)
#define PAD(id, nx) asm volatile(".Lwp%c0:\n\ts_cbranch_execz .Lwp%c1" :: "i"(id), "i"(nx))

__global__ __launch_bounds__(512, 2) void fused_kernel(const u16* __restrict__ x_, const int* __restrict__ qw_,
                                                       const u16* __restrict__ lut_, u16* __restrict__ out_, char* ws_) {
    extern __shared__ __attribute__((aligned(16))) char smem[];
    typedef unsigned long u64x4 __attribute__((ext_vector_type(4)));
    u64x4 ka;
    unsigned long kp[5], kw;
    asm volatile("s_load_dwordx8 %0, %2, 0x0\n\ts_load_dwordx2 %1, %2, 0x20" : "=&s"(ka), "=&s"(kw) : "s"(__builtin_amdgcn_kernarg_segment_ptr()));
    {
        unsigned long pc0, ex;
        u32 co;
        asm volatile("s_getpc_b64 %0\n\t"
                     "s_cmp_lt_u32 %4, %5\n\t"
                     "s_cselect_b32 m0, %6, 0\n\t"
                     "v_cmp_gt_u32 vcc, m0, %3\n\t"
                     "s_and_b32 m0, %4, 24\n\t"
                     "s_lshl_b32 m0, m0, 4\n\t"
                     "v_lshl_add_u32 %2, %3, 9, m0\n\t"
                     "s_mov_b32 m0, %7\n\t"
                     "s_and_saveexec_b64 %1, vcc\n\t"
                     "global_load_lds_dword %2, %0\n\t"
                     "s_mov_b64 exec, %1"
                     : "=&s"(pc0), "=&s"(ex), "=&v"(co) : "v"(threadIdx.x), "s"(blockIdx.x), "i"(CPFB), "i"(CPF / 4), "i"(OFF_D) : "vcc", "scc", "memory");
    }
#define KWAIT() do { SB(); asm volatile("s_waitcnt lgkmcnt(0)\n\ts_mov_b64 %0, %5\n\ts_mov_b64 %1, %6\n\ts_mov_b64 %2, %7\n\ts_mov_b64 %3, %8\n\ts_mov_b64 %4, %9" \
                     : "=&s"(kp[0]), "=&s"(kp[1]), "=&s"(kp[2]), "=&s"(kp[3]), "=&s"(kp[4]) : "s"(ka[0]), "s"(ka[1]), "s"(ka[2]), "s"(ka[3]), "s"(kw) : "memory"); SB(); } while (0)
#define KPTR(i) (kp[i])
    const int tid = threadIdx.x;
    const int w = __builtin_amdgcn_readfirstlane(tid >> 6);
    const int lane = tid & 63;
    const int bid = (int)blockIdx.x - NPF;
    const int xcd = blockIdx.x & 7, idx = bid >> 3;
    if (bid < 0) {
        const int pw = (blockIdx.x >> 3) * 8 + w;
        KWAIT();
        const char* qb = (const char*)KPTR(1) + (xcd >> 1) * 896 * 4 + (long)((xcd & 1) * 2) * (64 * N_ * 4);
        const char* xb = (const char*)KPTR(0) + (long)(xcd & 1) * 1024 * 2048;
        const u32 dst = (u32)(size_t)(smem) + w * 256;
        {
            const int m = (xcd & 1) * 4 + (w < 4 ? blockIdx.x >> 3 : xcd >> 1);
            const u32 off = (u32)((m * 256 + (w < 4 ? w : blockIdx.x >> 3) * 64 + lane) * 2048 + ((TSEQ(m) >> (w < 4 ? 0 : 4 * w - 12)) & 15) * 128);
            if (w >= 4) __builtin_amdgcn_s_sleep(1);
            asm volatile("s_mov_b32 m0, %2\n\ts_nop 0\n\tglobal_load_lds_dword %0, %1" :: "v"(off), "s"(KPTR(0)), "s"(dst) : "memory");
        }
#pragma unroll 1
        for (int G = pw; G < 192; G += 32) {
            int kind = 0, arg = 0, e = 0;
            if (G >= 0) { kind = 1; arg = 8; e = G - 0; }
            if (G >= 32) { kind = 0; arg = 5; e = G - 32 + (xcd >> 1) * 4; }
            if (G >= 36) { kind = 1; arg = 8; e = G - 4; }
            if (G >= 60) { kind = 0; arg = 6; e = G - 60 + (xcd >> 1) * 4; }
            if (G >= 64) { kind = 0; arg = 7; e = G - 64 + (xcd >> 1) * 4; }
            if (G >= 68) { kind = 0; arg = 8; e = G - 68 + (xcd >> 1) * 4; }
            if (G >= 72) { kind = 1; arg = 12; e = G - 72; }
            if (G >= 96) { kind = 0; arg = 9; e = G - 96 + (xcd >> 1) * 4; }
            if (G >= 100) { kind = 1; arg = 12; e = G - 76; }
            if (G >= 128) { kind = 1; arg = 12; e = G - 76; }
            if (G >= 132) { kind = 0; arg = 10; e = G - 132 + (xcd >> 1) * 4; }
            if (G >= 136) { kind = 0; arg = 11; e = G - 136 + (xcd >> 1) * 4; }
            if (G >= 140) { kind = 0; arg = 12; e = G - 140 + (xcd >> 1) * 4; }
            if (G >= 144) { kind = 0; arg = 13; e = G - 144; }
            if (G >= 160) { kind = 0; arg = 14; e = G - 160; }
            if (G >= 176) { kind = 0; arg = 15; e = G - 176; }
            asm volatile("s_waitcnt vmcnt(%0)" :: "i"(PACE) : "memory");
            if (kind) {
                const int L = e * 64 + lane;
                const int r = L / 28, c = L - r * 28;
                const u32 off = (u32)((arg * 64 + r) * (N_ * 4) + c * 128);
                asm volatile("s_mov_b32 m0, %2\n\ts_nop 0\n\tglobal_load_lds_dword %0, %1" :: "v"(off), "s"(qb), "s"(dst) : "memory");
            } else {
                const int t = arg >= 8 ? arg : (int)((TSEQ((xcd & 1) * 4 + (e >> 2)) >> (4 * arg)) & 15);
                const u32 off = (u32)((e * 64 + lane) * 2048 + t * 128);
                asm volatile("s_mov_b32 m0, %2\n\ts_nop 0\n\tglobal_load_lds_dword %0, %1" :: "v"(off), "s"(xb), "s"(dst) : "memory");
            }
        }
        asm volatile("s_waitcnt vmcnt(0)" ::: "memory");
        return;
    }
    const int mt = (xcd & 1) * 4 + (idx & 3);
    const int nt = (xcd >> 1) * 7 + (idx >> 2);
    const int row0 = mt * BM, col0 = nt * BN;

    if (__builtin_expect(w >= 4, 0)) {
        int lane;
        asm volatile("v_mbcnt_lo_u32_b32 %0, -1, 0\n\tv_mbcnt_hi_u32_b32 %0, -1, %0" : "=v"(lane));
        const int p = w - 4;
        const int l = lane & 31, h = lane >> 5;
        const int g = lane >> 4;
        u32 xoff[4];
#pragma unroll
        for (int i = 0; i < 4; ++i)
            xoff[i] = (u32)(((row0 + p * 64 + i * 8 + (lane >> 3)) * K_ + (((lane & 7) ^ ((i & 1) * 4 + g)) * 8)) * 2 - i * 1024);
        const u32 ldsx = (u32)(size_t)(smem) + p * 8192;
        u32 qo = (u32)(((p * 16 + h * 8) * N_ + col0 + l * 4) * 4);
        u32 lutl = (u32)(size_t)(smem) + OFF_L + l * 4;
        u32 bdst = (u32)(size_t)(smem) + OFF_B + (p >> 1) * BS + ((2 * p + h) & 3) * 2048 + ((l >> 4) * 64 + (l & 15)) * 16;
        const int i2 = (mt >> 1), h2 = mt & 1;
        const unsigned long tseq = 0xFEDCBA9800000000ul | TSEQ(mt);
        const u32 ldsw = (u32)(size_t)(smem) + OFF_B + p * 4096;
        u32 wv = (u32)(nt * 16384 + p * 4096 + lane * 16);
        u32 wo = (u32)(nt * 16384 + p * 4096 + h * 2048 + (l >> 4) * 1024 + (l & 15) * 16);
        u32 po = (u32)(WS_F + (nt * 4 + p) * 256 + lane * 4);
        u32 fo0 = po + (u32)(i2 * 16 + h2 * 8);
        u32 no = (u32)(nt * 256 + (lane & 1) * 4);
        u32 nvl = no;
        KWAIT();
        const char* st1 = (const char*)KPTR(4) + WS_W + (long)(i2 + 4) * TW + h2 * 512;
        const char* qa1 = (const char*)KPTR(1);
        const int tb = 8 + (mt >> 1), kg = (mt & 1) * 4 + p;
        const char* qbg = (const char*)KPTR(1) + (long)(tb * 64 + kg * 8 - p * 16) * (N_ * 4);
        const char* stb = (const char*)KPTR(4) + WS_W + (long)tb * TW + (kg * 2048 - p * 4096);
        u32 fob = po + (u32)(((kg >> 1) - p) * 256 + tb * 16 + (kg & 1) * 8);
        asm volatile("" : "+v"(wo), "+v"(fo0), "+v"(fob), "+v"(wv), "+v"(po), "+v"(bdst), "+v"(lutl), "+v"(qo));
        asm volatile("" : "+v"(xoff[0]), "+v"(xoff[1]), "+v"(xoff[2]), "+v"(xoff[3]));
        asm volatile("" : "+s"(st1), "+s"(qa1), "+s"(qbg), "+s"(stb));
        {
            unsigned long ex_;
            asm volatile("s_mov_b64 %0, exec\n\ts_mov_b64 exec, 0\n\t"
                         "s_cmp_eq_u32 %1, 0\n\ts_cbranch_scc1 .Lwp30\n\t"
                         "s_cmp_eq_u32 %1, 1\n\ts_cbranch_scc1 .Lwp31\n\t"
                         "s_cmp_eq_u32 %1, 2\n\ts_cbranch_scc1 .Lwp32\n\t"
                         "s_branch .Lwp33\n\t"
                         ".Lwp98:\n\ts_mov_b64 exec, %0" : "=&s"(ex_) : "s"(p) : "scc", "memory");
        }
        asm volatile(
            "s_mov_b32 s73, 65536\n\t"
            "s_mov_b32 s72, 0\n\t"
            "s_mov_b64 s[84:85], %[ws]\n\t"
            "s_add_u32 s84, s84, 65536\n\t"
            "s_addc_u32 s85, s85, 0\n\t"
            "s_mov_b64 s[56:57], %[tseq]\n\t"
            "s_bfe_u32 s40, s56, 262144\n\t"
            "s_bfe_u32 s41, s56, 262148\n\t"
            "s_bfe_u32 s42, s56, 262152\n\t"
            "s_bfe_u32 s43, s56, 262156\n\t"
            "s_bfe_u32 s44, s56, 262160\n\t"
            "s_bfe_u32 s45, s56, 262164\n\t"
            "s_bfe_u32 s46, s56, 262168\n\t"
            "s_bfe_u32 s47, s56, 262172\n\t"
            "s_bfe_u32 s48, s57, 262144\n\t"
            "s_bfe_u32 s49, s57, 262148\n\t"
            "s_bfe_u32 s50, s57, 262152\n\t"
            "s_bfe_u32 s51, s57, 262156\n\t"
            "s_bfe_u32 s52, s57, 262160\n\t"
            "s_bfe_u32 s53, s57, 262164\n\t"
            "s_bfe_u32 s54, s57, 262168\n\t"
            "s_bfe_u32 s55, s57, 262172\n\t"
            "global_load_dword %[nvl], %[nvl], %[ws] sc1\n\t"
            "global_load_dword v192, %[po], %[ws] sc1\n\t"
            "s_barrier\n\t"
            ".Lwp30:\n\t"
            "s_cbranch_execz .Lwp98\n\t"
            "s_add_u32 m0, %[ldsx], 0\n\t"
            "s_lshl_b32 s76, s40, 7\n\t"
            "s_add_u32 s68, %[xpl], s76\n\t"
            "s_addc_u32 s69, %[xph], 0\n\t"
            "global_load_lds_dwordx4 %[x0], s[68:69] offset:0\n\t"
            "global_load_lds_dwordx4 %[x1], s[68:69] offset:1024\n\t"
            "global_load_lds_dwordx4 %[x2], s[68:69] offset:2048\n\t"
            "global_load_lds_dwordx4 %[x3], s[68:69] offset:3072\n\t"
            "s_add_u32 m0, %[ldsx], 4096\n\t"
            "s_lshl_b32 s76, s40, 7\n\t"
            "s_add_u32 s76, s76, 65536\n\t"
            "s_add_u32 s68, %[xpl], s76\n\t"
            "s_addc_u32 s69, %[xph], 0\n\t"
            "global_load_lds_dwordx4 %[x0], s[68:69] offset:0\n\t"
            "global_load_lds_dwordx4 %[x1], s[68:69] offset:1024\n\t"
            "global_load_lds_dwordx4 %[x2], s[68:69] offset:2048\n\t"
            "global_load_lds_dwordx4 %[x3], s[68:69] offset:3072\n\t"
            "s_mul_i32 s76, s41, 917504\n\t"
            "s_mov_b64 s[88:89], %[qa1]\n\t"
            "s_add_u32 s88, s88, s76\n\t"
            "s_addc_u32 s89, s89, 0\n\t"
            "global_load_dwordx4 v[128:131], %[qo], s[88:89]\n\t"
            "s_add_u32 s88, s88, 14336\n\t"
            "s_addc_u32 s89, s89, 0\n\t"
            "global_load_dwordx4 v[132:135], %[qo], s[88:89]\n\t"
            "s_add_u32 s88, s88, 14336\n\t"
            "s_addc_u32 s89, s89, 0\n\t"
            "global_load_dwordx4 v[136:139], %[qo], s[88:89]\n\t"
            "s_add_u32 s88, s88, 14336\n\t"
            "s_addc_u32 s89, s89, 0\n\t"
            "global_load_dwordx4 v[140:143], %[qo], s[88:89]\n\t"
            "s_add_u32 s88, s88, 14336\n\t"
            "s_addc_u32 s89, s89, 0\n\t"
            "global_load_dwordx4 v[144:147], %[qo], s[88:89]\n\t"
            "s_add_u32 s88, s88, 14336\n\t"
            "s_addc_u32 s89, s89, 0\n\t"
            "global_load_dwordx4 v[148:151], %[qo], s[88:89]\n\t"
            "s_add_u32 s88, s88, 14336\n\t"
            "s_addc_u32 s89, s89, 0\n\t"
            "global_load_dwordx4 v[152:155], %[qo], s[88:89]\n\t"
            "s_add_u32 s88, s88, 14336\n\t"
            "s_addc_u32 s89, s89, 0\n\t"
            "global_load_dwordx4 v[156:159], %[qo], s[88:89]\n\t"
            "s_waitcnt vmcnt(8)\n\t"
            "s_barrier\n\t"
            ".Lwp31:\n\t"
            "s_cbranch_execz .Lwp98\n\t"
            "s_lshl_b32 s76, %[h2], 9\n\t"
            "v_add_u32 v193, s76, %[bd]\n\t"
            "ds_read_b128 v[196:199], v193\n\t"
            "ds_read_b128 v[200:203], v193 offset:256\n\t"
            "s_mov_b64 s[70:71], %[st1]\n\t"
            "s_sub_u32 s70, s70, 1835008\n\t"
            "s_subb_u32 s71, s71, 0\n\t"
            "s_waitcnt lgkmcnt(0)\n\t"
            "global_store_dwordx4 %[wo], v[196:199], s[70:71] sc1\n\t"
            "global_store_dwordx4 %[wo], v[200:203], s[70:71] offset:256 sc1\n\t"
            "s_mov_b32 s76, 1058696237\n\t"
            "v_mul_lo_u32 %[nvl], %[nvl], s76\n\t"
            "v_add_u32 %[nvl], 1535021219, %[nvl]\n\t"
            "s_setprio 1\n\t"
            "s_waitcnt vmcnt(9)\n\t"
            "v_lshl_add_u32 v160, v128, 8, %[lutl]\n\t"
            "ds_read_u16 v160, v160 offset:0\n\t"
            "v_lshl_add_u32 v168, v129, 8, %[lutl]\n\t"
            "ds_read_u16 v168, v168 offset:2\n\t"
            "v_lshl_add_u32 v176, v130, 8, %[lutl]\n\t"
            "ds_read_u16 v176, v176 offset:128\n\t"
            "v_lshl_add_u32 v184, v131, 8, %[lutl]\n\t"
            "ds_read_u16 v184, v184 offset:130\n\t"
            "s_add_u32 m0, %[ldsx], 32768\n\t"
            "s_lshl_b32 s76, s41, 7\n\t"
            "s_add_u32 s68, %[xpl], s76\n\t"
            "s_addc_u32 s69, %[xph], 0\n\t"
            "global_load_lds_dwordx4 %[x0], s[68:69] offset:0\n\t"
            "s_waitcnt vmcnt(9)\n\t"
            "v_lshl_add_u32 v161, v132, 8, %[lutl]\n\t"
            "ds_read_u16 v161, v161 offset:0\n\t"
            "v_lshl_add_u32 v169, v133, 8, %[lutl]\n\t"
            "ds_read_u16 v169, v169 offset:2\n\t"
            "v_lshl_add_u32 v177, v134, 8, %[lutl]\n\t"
            "ds_read_u16 v177, v177 offset:128\n\t"
            "v_lshl_add_u32 v185, v135, 8, %[lutl]\n\t"
            "ds_read_u16 v185, v185 offset:130\n\t"
            "global_load_lds_dwordx4 %[x1], s[68:69] offset:1024\n\t"
            "s_waitcnt vmcnt(9)\n\t"
            "v_lshl_add_u32 v162, v136, 8, %[lutl]\n\t"
            "ds_read_u16 v162, v162 offset:0\n\t"
            "v_lshl_add_u32 v170, v137, 8, %[lutl]\n\t"
            "ds_read_u16 v170, v170 offset:2\n\t"
            "v_lshl_add_u32 v178, v138, 8, %[lutl]\n\t"
            "ds_read_u16 v178, v178 offset:128\n\t"
            "v_lshl_add_u32 v186, v139, 8, %[lutl]\n\t"
            "ds_read_u16 v186, v186 offset:130\n\t"
            "global_load_lds_dwordx4 %[x2], s[68:69] offset:2048\n\t"
            "s_waitcnt vmcnt(9)\n\t"
            "v_lshl_add_u32 v163, v140, 8, %[lutl]\n\t"
            "ds_read_u16 v163, v163 offset:0\n\t"
            "v_lshl_add_u32 v171, v141, 8, %[lutl]\n\t"
            "ds_read_u16 v171, v171 offset:2\n\t"
            "v_lshl_add_u32 v179, v142, 8, %[lutl]\n\t"
            "ds_read_u16 v179, v179 offset:128\n\t"
            "v_lshl_add_u32 v187, v143, 8, %[lutl]\n\t"
            "ds_read_u16 v187, v187 offset:130\n\t"
            "global_load_lds_dwordx4 %[x3], s[68:69] offset:3072\n\t"
            "s_waitcnt vmcnt(9)\n\t"
            "v_lshl_add_u32 v164, v144, 8, %[lutl]\n\t"
            "ds_read_u16 v164, v164 offset:0\n\t"
            "v_lshl_add_u32 v172, v145, 8, %[lutl]\n\t"
            "ds_read_u16 v172, v172 offset:2\n\t"
            "v_lshl_add_u32 v180, v146, 8, %[lutl]\n\t"
            "ds_read_u16 v180, v180 offset:128\n\t"
            "v_lshl_add_u32 v188, v147, 8, %[lutl]\n\t"
            "ds_read_u16 v188, v188 offset:130\n\t"
            "s_add_u32 m0, %[ldsx], 36864\n\t"
            "s_lshl_b32 s76, s41, 7\n\t"
            "s_add_u32 s76, s76, 65536\n\t"
            "s_add_u32 s68, %[xpl], s76\n\t"
            "s_addc_u32 s69, %[xph], 0\n\t"
            "global_load_lds_dwordx4 %[x0], s[68:69] offset:0\n\t"
            "s_waitcnt vmcnt(9)\n\t"
            "v_lshl_add_u32 v165, v148, 8, %[lutl]\n\t"
            "ds_read_u16 v165, v165 offset:0\n\t"
            "v_lshl_add_u32 v173, v149, 8, %[lutl]\n\t"
            "ds_read_u16 v173, v173 offset:2\n\t"
            "v_lshl_add_u32 v181, v150, 8, %[lutl]\n\t"
            "ds_read_u16 v181, v181 offset:128\n\t"
            "v_lshl_add_u32 v189, v151, 8, %[lutl]\n\t"
            "ds_read_u16 v189, v189 offset:130\n\t"
            "global_load_lds_dwordx4 %[x1], s[68:69] offset:1024\n\t"
            "s_waitcnt vmcnt(9)\n\t"
            "v_lshl_add_u32 v166, v152, 8, %[lutl]\n\t"
            "ds_read_u16 v166, v166 offset:0\n\t"
            "v_lshl_add_u32 v174, v153, 8, %[lutl]\n\t"
            "ds_read_u16 v174, v174 offset:2\n\t"
            "v_lshl_add_u32 v182, v154, 8, %[lutl]\n\t"
            "ds_read_u16 v182, v182 offset:128\n\t"
            "v_lshl_add_u32 v190, v155, 8, %[lutl]\n\t"
            "ds_read_u16 v190, v190 offset:130\n\t"
            "global_load_lds_dwordx4 %[x2], s[68:69] offset:2048\n\t"
            "s_waitcnt vmcnt(9)\n\t"
            "v_lshl_add_u32 v167, v156, 8, %[lutl]\n\t"
            "ds_read_u16 v167, v167 offset:0\n\t"
            "v_lshl_add_u32 v175, v157, 8, %[lutl]\n\t"
            "ds_read_u16 v175, v175 offset:2\n\t"
            "v_lshl_add_u32 v183, v158, 8, %[lutl]\n\t"
            "ds_read_u16 v183, v183 offset:128\n\t"
            "v_lshl_add_u32 v191, v159, 8, %[lutl]\n\t"
            "ds_read_u16 v191, v191 offset:130\n\t"
            "global_load_lds_dwordx4 %[x3], s[68:69] offset:3072\n\t"
            "s_setprio 0\n\t"
            "s_waitcnt vmcnt(8)\n\t"
            "s_mov_b64 exec, 3\n\t"
            "global_store_dword %[fo0], %[nvl], %[ws] offset:0 sc1\n\t"
            "s_mov_b64 exec, -1\n\t"
            "s_waitcnt lgkmcnt(3)\n\t"
            "v_lshl_or_b32 v128, v161, 16, v160\n\t"
            "v_lshl_or_b32 v129, v163, 16, v162\n\t"
            "v_lshl_or_b32 v130, v165, 16, v164\n\t"
            "v_lshl_or_b32 v131, v167, 16, v166\n\t"
            "ds_write_b128 %[bd], v[128:131] offset:16384\n\t"
            "s_waitcnt lgkmcnt(3)\n\t"
            "v_lshl_or_b32 v132, v169, 16, v168\n\t"
            "v_lshl_or_b32 v133, v171, 16, v170\n\t"
            "v_lshl_or_b32 v134, v173, 16, v172\n\t"
            "v_lshl_or_b32 v135, v175, 16, v174\n\t"
            "ds_write_b128 %[bd], v[132:135] offset:16640\n\t"
            "s_waitcnt lgkmcnt(3)\n\t"
            "v_lshl_or_b32 v136, v177, 16, v176\n\t"
            "v_lshl_or_b32 v137, v179, 16, v178\n\t"
            "v_lshl_or_b32 v138, v181, 16, v180\n\t"
            "v_lshl_or_b32 v139, v183, 16, v182\n\t"
            "ds_write_b128 %[bd], v[136:139] offset:16896\n\t"
            "s_waitcnt lgkmcnt(3)\n\t"
            "v_lshl_or_b32 v140, v185, 16, v184\n\t"
            "v_lshl_or_b32 v141, v187, 16, v186\n\t"
            "v_lshl_or_b32 v142, v189, 16, v188\n\t"
            "v_lshl_or_b32 v143, v191, 16, v190\n\t"
            "ds_write_b128 %[bd], v[140:143] offset:17152\n\t"
            "s_add_u32 m0, %[ldsx], 65536\n\t"
            "s_lshl_b32 s76, s42, 7\n\t"
            "s_add_u32 s68, %[xpl], s76\n\t"
            "s_addc_u32 s69, %[xph], 0\n\t"
            "global_load_lds_dwordx4 %[x0], s[68:69] offset:0\n\t"
            "global_load_lds_dwordx4 %[x1], s[68:69] offset:1024\n\t"
            "global_load_lds_dwordx4 %[x2], s[68:69] offset:2048\n\t"
            "global_load_lds_dwordx4 %[x3], s[68:69] offset:3072\n\t"
            "s_add_u32 m0, %[ldsx], 69632\n\t"
            "s_lshl_b32 s76, s42, 7\n\t"
            "s_add_u32 s76, s76, 65536\n\t"
            "s_add_u32 s68, %[xpl], s76\n\t"
            "s_addc_u32 s69, %[xph], 0\n\t"
            "global_load_lds_dwordx4 %[x0], s[68:69] offset:0\n\t"
            "global_load_lds_dwordx4 %[x1], s[68:69] offset:1024\n\t"
            "global_load_lds_dwordx4 %[x2], s[68:69] offset:2048\n\t"
            "global_load_lds_dwordx4 %[x3], s[68:69] offset:3072\n\t"
            "s_waitcnt vmcnt(9) lgkmcnt(0)\n\t"
            "s_barrier\n\t"
            ".Lwp32:\n\t"
            "s_cbranch_execz .Lwp98\n\t"
            "global_load_dword v192, %[po], %[ws] sc1\n\t"
            "s_add_u32 m0, %[ldsx], 0\n\t"
            "s_lshl_b32 s76, s43, 7\n\t"
            "s_add_u32 s68, %[xpl], s76\n\t"
            "s_addc_u32 s69, %[xph], 0\n\t"
            "global_load_lds_dwordx4 %[x0], s[68:69] offset:0\n\t"
            "global_load_lds_dwordx4 %[x1], s[68:69] offset:1024\n\t"
            "global_load_lds_dwordx4 %[x2], s[68:69] offset:2048\n\t"
            "global_load_lds_dwordx4 %[x3], s[68:69] offset:3072\n\t"
            "s_add_u32 m0, %[ldsx], 4096\n\t"
            "s_lshl_b32 s76, s43, 7\n\t"
            "s_add_u32 s76, s76, 65536\n\t"
            "s_add_u32 s68, %[xpl], s76\n\t"
            "s_addc_u32 s69, %[xph], 0\n\t"
            "global_load_lds_dwordx4 %[x0], s[68:69] offset:0\n\t"
            "global_load_lds_dwordx4 %[x1], s[68:69] offset:1024\n\t"
            "global_load_lds_dwordx4 %[x2], s[68:69] offset:2048\n\t"
            "global_load_lds_dwordx4 %[x3], s[68:69] offset:3072\n\t"
            "global_load_dword v195, %[po], %[ws] sc1\n\t"
            "s_waitcnt vmcnt(9)\n\t"
            "s_lshl_b32 s76, s42, 2\n\t"
            ".Lzc0:\n\t"
            "v_cmp_eq_u32 vcc, v192, %[nvl]\n\t"
            "s_lshr_b64 s[74:75], vcc, s76\n\t"
            "s_and_b32 s74, s74, 15\n\t"
            "s_cmp_eq_u32 s74, 15\n\t"
            "s_cbranch_scc1 .Lzd0\n\t"
            "s_cmp_eq_u32 s73, 0\n\t"
            "s_cbranch_scc1 .Lzd0\n\t"
            "s_sub_u32 s73, s73, 1\n\t"
            "s_cmp_eq_u32 s72, 1\n\t"
            "s_cbranch_scc1 .Lze0\n\t"
            "s_mov_b32 s72, 1\n\t"
            "s_waitcnt vmcnt(0)\n\t"
            "v_mov_b32 v192, v195\n\t"
            "s_branch .Lzc0\n\t"
            ".Lze0:\n\t"
            "global_load_dword v192, %[po], %[ws] sc1\n\t"
            "s_waitcnt vmcnt(0)\n\t"
            "v_mov_b32 v195, v192\n\t"
            "s_branch .Lzc0\n\t"
            ".Lzd0:\n\t"
            "s_add_u32 m0, %[ldsw], 32768\n\t"
            "s_mul_i32 s76, s42, 458752\n\t"
            "s_add_u32 s70, s84, s76\n\t"
            "s_addc_u32 s71, s85, 0\n\t"
            "global_load_lds_dwordx4 %[wv], s[70:71] offset:0 sc1\n\t"
            "global_load_lds_dwordx4 %[wv], s[70:71] offset:1024 sc1\n\t"
            "global_load_lds_dwordx4 %[wv], s[70:71] offset:2048 sc1\n\t"
            "global_load_lds_dwordx4 %[wv], s[70:71] offset:3072 sc1\n\t"
            "s_cmp_lg_u32 %[h2], 0\n\t"
            "s_cbranch_scc1 .Lzs1\n\t"
            "global_store_dwordx4 %[wo], v[128:131], %[st1] sc1\n\t"
            "global_store_dwordx4 %[wo], v[132:135], %[st1] offset:256 sc1\n\t"
            "s_branch .Lzt1\n\t"
            ".Lzs1:\n\t"
            "global_store_dwordx4 %[wo], v[136:139], %[st1] sc1\n\t"
            "global_store_dwordx4 %[wo], v[140:143], %[st1] offset:256 sc1\n\t"
            ".Lzt1:\n\t"
            "s_waitcnt vmcnt(6)\n\t"
            "v_mov_b32 v192, v195\n\t"
            "s_lshl_b32 s76, s43, 2\n\t"
            ".Lzc2:\n\t"
            "v_cmp_eq_u32 vcc, v192, %[nvl]\n\t"
            "s_lshr_b64 s[74:75], vcc, s76\n\t"
            "s_and_b32 s74, s74, 15\n\t"
            "s_cmp_eq_u32 s74, 15\n\t"
            "s_cbranch_scc1 .Lzd2\n\t"
            "s_cmp_eq_u32 s73, 0\n\t"
            "s_cbranch_scc1 .Lzd2\n\t"
            "s_sub_u32 s73, s73, 1\n\t"
            "global_load_dword v192, %[po], %[ws] sc1\n\t"
            "s_waitcnt vmcnt(0)\n\t"
            "s_branch .Lzc2\n\t"
            ".Lzd2:\n\t"
            "s_add_u32 m0, %[ldsw], 0\n\t"
            "s_mul_i32 s76, s43, 458752\n\t"
            "s_add_u32 s70, s84, s76\n\t"
            "s_addc_u32 s71, s85, 0\n\t"
            "global_load_lds_dwordx4 %[wv], s[70:71] offset:0 sc1\n\t"
            "global_load_lds_dwordx4 %[wv], s[70:71] offset:1024 sc1\n\t"
            "global_load_lds_dwordx4 %[wv], s[70:71] offset:2048 sc1\n\t"
            "global_load_lds_dwordx4 %[wv], s[70:71] offset:3072 sc1\n\t"
            "s_waitcnt vmcnt(6)\n\t"
            "s_barrier\n\t"
            ".Lwp33:\n\t"
            "s_cbranch_execz .Lwp98\n\t"
            "s_lshl_b32 s76, s44, 2\n\t"
            ".Lzc3:\n\t"
            "v_cmp_eq_u32 vcc, v192, %[nvl]\n\t"
            "s_lshr_b64 s[74:75], vcc, s76\n\t"
            "s_and_b32 s74, s74, 15\n\t"
            "s_cmp_eq_u32 s74, 15\n\t"
            "s_cbranch_scc1 .Lzd3\n\t"
            "s_cmp_eq_u32 s73, 0\n\t"
            "s_cbranch_scc1 .Lzd3\n\t"
            "s_sub_u32 s73, s73, 1\n\t"
            "global_load_dword v192, %[po], %[ws] sc1\n\t"
            "s_waitcnt vmcnt(0)\n\t"
            "s_branch .Lzc3\n\t"
            ".Lzd3:\n\t"
            "s_add_u32 m0, %[ldsw], 16384\n\t"
            "s_mul_i32 s76, s44, 458752\n\t"
            "s_add_u32 s70, s84, s76\n\t"
            "s_addc_u32 s71, s85, 0\n\t"
            "global_load_lds_dwordx4 %[wv], s[70:71] offset:0 sc1\n\t"
            "global_load_lds_dwordx4 %[wv], s[70:71] offset:1024 sc1\n\t"
            "global_load_lds_dwordx4 %[wv], s[70:71] offset:2048 sc1\n\t"
            "global_load_lds_dwordx4 %[wv], s[70:71] offset:3072 sc1\n\t"
            "s_waitcnt vmcnt(8)\n\t"
            "s_mov_b64 exec, 3\n\t"
            "global_store_dword %[fo0], %[nvl], %[ws] offset:64 sc1\n\t"
            "s_mov_b64 exec, -1\n\t"
            "v_mbcnt_lo_u32_b32 v193, -1, 0\n\t"
            "v_mbcnt_hi_u32_b32 v193, -1, v193\n\t"
            "v_lshrrev_b32 v193, 5, v193\n\t"
            "v_mul_u32_u24 v194, 57344, v193\n\t"
            "v_sub_u32 %[qo], %[qo], v194\n\t"
            "v_mul_u32_u24 v194, 1792, v193\n\t"
            "v_sub_u32 %[wo], %[wo], v194\n\t"
            "s_mov_b64 s[88:89], %[qbg]\n\t"
            "global_load_dwordx4 v[204:207], %[qo], s[88:89]\n\t"
            "s_add_u32 s88, s88, 14336\n\t"
            "s_addc_u32 s89, s89, 0\n\t"
            "global_load_dwordx4 v[208:211], %[qo], s[88:89]\n\t"
            "s_add_u32 s88, s88, 14336\n\t"
            "s_addc_u32 s89, s89, 0\n\t"
            "global_load_dwordx4 v[212:215], %[qo], s[88:89]\n\t"
            "s_add_u32 s88, s88, 14336\n\t"
            "s_addc_u32 s89, s89, 0\n\t"
            "global_load_dwordx4 v[216:219], %[qo], s[88:89]\n\t"
            "s_add_u32 m0, %[ldsx], 32768\n\t"
            "s_lshl_b32 s76, s44, 7\n\t"
            "s_add_u32 s68, %[xpl], s76\n\t"
            "s_addc_u32 s69, %[xph], 0\n\t"
            "global_load_lds_dwordx4 %[x0], s[68:69] offset:0\n\t"
            "global_load_lds_dwordx4 %[x1], s[68:69] offset:1024\n\t"
            "global_load_lds_dwordx4 %[x2], s[68:69] offset:2048\n\t"
            "global_load_lds_dwordx4 %[x3], s[68:69] offset:3072\n\t"
            "s_add_u32 m0, %[ldsx], 36864\n\t"
            "s_lshl_b32 s76, s44, 7\n\t"
            "s_add_u32 s76, s76, 65536\n\t"
            "s_add_u32 s68, %[xpl], s76\n\t"
            "s_addc_u32 s69, %[xph], 0\n\t"
            "global_load_lds_dwordx4 %[x0], s[68:69] offset:0\n\t"
            "global_load_lds_dwordx4 %[x1], s[68:69] offset:1024\n\t"
            "global_load_lds_dwordx4 %[x2], s[68:69] offset:2048\n\t"
            "global_load_lds_dwordx4 %[x3], s[68:69] offset:3072\n\t"
            "s_waitcnt vmcnt(17)\n\t"
            "s_barrier\n\t"
            ".Lwp34:\n\t"
            "s_cbranch_execz .Lwp35\n\t"
            "global_load_dword v192, %[po], %[ws] sc1\n\t"
            "s_waitcnt vmcnt(9)\n\t"
            "s_setprio 0\n\t"
            "s_add_u32 m0, %[ldsx], 65536\n\t"
            "s_lshl_b32 s76, s45, 7\n\t"
            "s_add_u32 s68, %[xpl], s76\n\t"
            "s_addc_u32 s69, %[xph], 0\n\t"
            "global_load_lds_dwordx4 %[x0], s[68:69] offset:0\n\t"
            "v_lshl_add_u32 v160, v204, 8, %[lutl]\n\t"
            "ds_read_u16 v160, v160 offset:0\n\t"
            "v_lshl_add_u32 v161, v208, 8, %[lutl]\n\t"
            "ds_read_u16 v161, v161 offset:0\n\t"
            "global_load_lds_dwordx4 %[x1], s[68:69] offset:1024\n\t"
            "v_lshl_add_u32 v162, v212, 8, %[lutl]\n\t"
            "ds_read_u16 v162, v162 offset:0\n\t"
            "v_lshl_add_u32 v163, v216, 8, %[lutl]\n\t"
            "ds_read_u16 v163, v163 offset:0\n\t"
            "global_load_lds_dwordx4 %[x2], s[68:69] offset:2048\n\t"
            "v_lshl_add_u32 v168, v205, 8, %[lutl]\n\t"
            "ds_read_u16 v168, v168 offset:2\n\t"
            "v_lshl_add_u32 v169, v209, 8, %[lutl]\n\t"
            "ds_read_u16 v169, v169 offset:2\n\t"
            "global_load_lds_dwordx4 %[x3], s[68:69] offset:3072\n\t"
            "v_lshl_add_u32 v170, v213, 8, %[lutl]\n\t"
            "ds_read_u16 v170, v170 offset:2\n\t"
            "v_lshl_add_u32 v171, v217, 8, %[lutl]\n\t"
            "ds_read_u16 v171, v171 offset:2\n\t"
            "s_add_u32 m0, %[ldsx], 69632\n\t"
            "s_lshl_b32 s76, s45, 7\n\t"
            "s_add_u32 s76, s76, 65536\n\t"
            "s_add_u32 s68, %[xpl], s76\n\t"
            "s_addc_u32 s69, %[xph], 0\n\t"
            "global_load_lds_dwordx4 %[x0], s[68:69] offset:0\n\t"
            "v_lshl_add_u32 v176, v206, 8, %[lutl]\n\t"
            "ds_read_u16 v176, v176 offset:128\n\t"
            "v_lshl_add_u32 v177, v210, 8, %[lutl]\n\t"
            "ds_read_u16 v177, v177 offset:128\n\t"
            "global_load_lds_dwordx4 %[x1], s[68:69] offset:1024\n\t"
            "v_lshl_add_u32 v178, v214, 8, %[lutl]\n\t"
            "ds_read_u16 v178, v178 offset:128\n\t"
            "v_lshl_add_u32 v179, v218, 8, %[lutl]\n\t"
            "ds_read_u16 v179, v179 offset:128\n\t"
            "global_load_lds_dwordx4 %[x2], s[68:69] offset:2048\n\t"
            "v_lshl_add_u32 v184, v207, 8, %[lutl]\n\t"
            "ds_read_u16 v184, v184 offset:130\n\t"
            "v_lshl_add_u32 v185, v211, 8, %[lutl]\n\t"
            "ds_read_u16 v185, v185 offset:130\n\t"
            "global_load_lds_dwordx4 %[x3], s[68:69] offset:3072\n\t"
            "v_lshl_add_u32 v186, v215, 8, %[lutl]\n\t"
            "ds_read_u16 v186, v186 offset:130\n\t"
            "v_lshl_add_u32 v187, v219, 8, %[lutl]\n\t"
            "ds_read_u16 v187, v187 offset:130\n\t"
            "s_setprio 0\n\t"
            "s_waitcnt vmcnt(8)\n\t"
            "s_lshl_b32 s76, s45, 2\n\t"
            ".Lzc4:\n\t"
            "v_cmp_eq_u32 vcc, v192, %[nvl]\n\t"
            "s_lshr_b64 s[74:75], vcc, s76\n\t"
            "s_and_b32 s74, s74, 15\n\t"
            "s_cmp_eq_u32 s74, 15\n\t"
            "s_cbranch_scc1 .Lzd4\n\t"
            "s_cmp_eq_u32 s73, 0\n\t"
            "s_cbranch_scc1 .Lzd4\n\t"
            "s_sub_u32 s73, s73, 1\n\t"
            "global_load_dword v192, %[po], %[ws] sc1\n\t"
            "s_waitcnt vmcnt(0)\n\t"
            "s_branch .Lzc4\n\t"
            ".Lzd4:\n\t"
            "s_add_u32 m0, %[ldsw], 32768\n\t"
            "s_mul_i32 s76, s45, 458752\n\t"
            "s_add_u32 s70, s84, s76\n\t"
            "s_addc_u32 s71, s85, 0\n\t"
            "global_load_lds_dwordx4 %[wv], s[70:71] offset:0 sc1\n\t"
            "global_load_lds_dwordx4 %[wv], s[70:71] offset:1024 sc1\n\t"
            "global_load_lds_dwordx4 %[wv], s[70:71] offset:2048 sc1\n\t"
            "global_load_lds_dwordx4 %[wv], s[70:71] offset:3072 sc1\n\t"
            "s_waitcnt lgkmcnt(8)\n\t"
            "v_lshl_or_b32 v220, v161, 16, v160\n\t"
            "v_lshl_or_b32 v221, v163, 16, v162\n\t"
            "v_lshl_or_b32 v222, v169, 16, v168\n\t"
            "v_lshl_or_b32 v223, v171, 16, v170\n\t"
            "s_waitcnt lgkmcnt(0)\n\t"
            "v_lshl_or_b32 v224, v177, 16, v176\n\t"
            "v_lshl_or_b32 v225, v179, 16, v178\n\t"
            "v_lshl_or_b32 v226, v185, 16, v184\n\t"
            "v_lshl_or_b32 v227, v187, 16, v186\n\t"
            "s_nop 0\n\t"
            "v_permlane32_swap_b32 v220, v222\n\t"
            "v_permlane32_swap_b32 v221, v223\n\t"
            "v_permlane32_swap_b32 v224, v226\n\t"
            "v_permlane32_swap_b32 v225, v227\n\t"
            "s_mov_b64 s[88:89], %[stb]\n\t"
            "s_nop 0\n\t"
            "global_store_dwordx4 %[wo], v[220:223], s[88:89] offset:0 sc1\n\t"
            "global_store_dwordx4 %[wo], v[224:227], s[88:89] offset:512 sc1\n\t"
            "s_barrier\n\t"
            ".Lwp35:\n\t"
            "s_cbranch_execz .Lwp36\n\t"
            "s_lshl_b32 s76, s46, 2\n\t"
            ".Lzc5:\n\t"
            "v_cmp_eq_u32 vcc, v192, %[nvl]\n\t"
            "s_lshr_b64 s[74:75], vcc, s76\n\t"
            "s_and_b32 s74, s74, 15\n\t"
            "s_cmp_eq_u32 s74, 15\n\t"
            "s_cbranch_scc1 .Lzd5\n\t"
            "s_cmp_eq_u32 s73, 0\n\t"
            "s_cbranch_scc1 .Lzd5\n\t"
            "s_sub_u32 s73, s73, 1\n\t"
            "global_load_dword v192, %[po], %[ws] sc1\n\t"
            "s_waitcnt vmcnt(0)\n\t"
            "s_branch .Lzc5\n\t"
            ".Lzd5:\n\t"
            "s_add_u32 m0, %[ldsw], 0\n\t"
            "s_mul_i32 s76, s46, 458752\n\t"
            "s_add_u32 s70, s84, s76\n\t"
            "s_addc_u32 s71, s85, 0\n\t"
            "global_load_lds_dwordx4 %[wv], s[70:71] offset:0 sc1\n\t"
            "global_load_lds_dwordx4 %[wv], s[70:71] offset:1024 sc1\n\t"
            "global_load_lds_dwordx4 %[wv], s[70:71] offset:2048 sc1\n\t"
            "global_load_lds_dwordx4 %[wv], s[70:71] offset:3072 sc1\n\t"
            "s_add_u32 m0, %[ldsx], 0\n\t"
            "s_lshl_b32 s76, s46, 7\n\t"
            "s_add_u32 s68, %[xpl], s76\n\t"
            "s_addc_u32 s69, %[xph], 0\n\t"
            "global_load_lds_dwordx4 %[x0], s[68:69] offset:0\n\t"
            "global_load_lds_dwordx4 %[x1], s[68:69] offset:1024\n\t"
            "global_load_lds_dwordx4 %[x2], s[68:69] offset:2048\n\t"
            "global_load_lds_dwordx4 %[x3], s[68:69] offset:3072\n\t"
            "s_add_u32 m0, %[ldsx], 4096\n\t"
            "s_lshl_b32 s76, s46, 7\n\t"
            "s_add_u32 s76, s76, 65536\n\t"
            "s_add_u32 s68, %[xpl], s76\n\t"
            "s_addc_u32 s69, %[xph], 0\n\t"
            "global_load_lds_dwordx4 %[x0], s[68:69] offset:0\n\t"
            "global_load_lds_dwordx4 %[x1], s[68:69] offset:1024\n\t"
            "global_load_lds_dwordx4 %[x2], s[68:69] offset:2048\n\t"
            "global_load_lds_dwordx4 %[x3], s[68:69] offset:3072\n\t"
            "s_waitcnt vmcnt(12)\n\t"
            "s_mov_b64 exec, 3\n\t"
            "global_store_dword %[fob], %[nvl], %[ws] offset:0 sc1\n\t"
            "s_mov_b64 exec, -1\n\t"
            "s_barrier\n\t"
            "s_lshl_b32 s76, s47, 2\n\t"
            ".Lzc6:\n\t"
            "v_cmp_eq_u32 vcc, v192, %[nvl]\n\t"
            "s_lshr_b64 s[74:75], vcc, s76\n\t"
            "s_and_b32 s74, s74, 15\n\t"
            "s_cmp_eq_u32 s74, 15\n\t"
            "s_cbranch_scc1 .Lzd6\n\t"
            "s_cmp_eq_u32 s73, 0\n\t"
            "s_cbranch_scc1 .Lzd6\n\t"
            "s_sub_u32 s73, s73, 1\n\t"
            "global_load_dword v192, %[po], %[ws] sc1\n\t"
            "s_waitcnt vmcnt(0)\n\t"
            "s_branch .Lzc6\n\t"
            ".Lzd6:\n\t"
            "s_add_u32 m0, %[ldsw], 16384\n\t"
            "s_mul_i32 s76, s47, 458752\n\t"
            "s_add_u32 s70, s84, s76\n\t"
            "s_addc_u32 s71, s85, 0\n\t"
            "global_load_lds_dwordx4 %[wv], s[70:71] offset:0 sc1\n\t"
            "global_load_lds_dwordx4 %[wv], s[70:71] offset:1024 sc1\n\t"
            "global_load_lds_dwordx4 %[wv], s[70:71] offset:2048 sc1\n\t"
            "global_load_lds_dwordx4 %[wv], s[70:71] offset:3072 sc1\n\t"
            "s_add_u32 m0, %[ldsx], 32768\n\t"
            "s_lshl_b32 s76, s47, 7\n\t"
            "s_add_u32 s68, %[xpl], s76\n\t"
            "s_addc_u32 s69, %[xph], 0\n\t"
            "global_load_lds_dwordx4 %[x0], s[68:69] offset:0\n\t"
            "global_load_lds_dwordx4 %[x1], s[68:69] offset:1024\n\t"
            "global_load_lds_dwordx4 %[x2], s[68:69] offset:2048\n\t"
            "global_load_lds_dwordx4 %[x3], s[68:69] offset:3072\n\t"
            "s_add_u32 m0, %[ldsx], 36864\n\t"
            "s_lshl_b32 s76, s47, 7\n\t"
            "s_add_u32 s76, s76, 65536\n\t"
            "s_add_u32 s68, %[xpl], s76\n\t"
            "s_addc_u32 s69, %[xph], 0\n\t"
            "global_load_lds_dwordx4 %[x0], s[68:69] offset:0\n\t"
            "global_load_lds_dwordx4 %[x1], s[68:69] offset:1024\n\t"
            "global_load_lds_dwordx4 %[x2], s[68:69] offset:2048\n\t"
            "global_load_lds_dwordx4 %[x3], s[68:69] offset:3072\n\t"
            "s_mov_b64 s[88:89], %[qbg]\n\t"
            "s_add_u32 s88, s88, 3670016\n\t"
            "s_addc_u32 s89, s89, 0\n\t"
            "global_load_dwordx4 v[204:207], %[qo], s[88:89]\n\t"
            "s_add_u32 s88, s88, 14336\n\t"
            "s_addc_u32 s89, s89, 0\n\t"
            "global_load_dwordx4 v[208:211], %[qo], s[88:89]\n\t"
            "s_add_u32 s88, s88, 14336\n\t"
            "s_addc_u32 s89, s89, 0\n\t"
            "global_load_dwordx4 v[212:215], %[qo], s[88:89]\n\t"
            "s_add_u32 s88, s88, 14336\n\t"
            "s_addc_u32 s89, s89, 0\n\t"
            "global_load_dwordx4 v[216:219], %[qo], s[88:89]\n\t"
            "s_waitcnt vmcnt(17)\n\t"
            "s_barrier\n\t"
            ".Lwp36:\n\t"
            "s_cbranch_execz .Lwp37\n\t"
            "global_load_dword v192, %[po], %[ws] sc1\n\t"
            "s_add_u32 m0, %[ldsx], 65536\n\t"
            "s_lshl_b32 s76, s48, 7\n\t"
            "s_add_u32 s68, %[xpl], s76\n\t"
            "s_addc_u32 s69, %[xph], 0\n\t"
            "global_load_lds_dwordx4 %[x0], s[68:69] offset:0\n\t"
            "global_load_lds_dwordx4 %[x1], s[68:69] offset:1024\n\t"
            "global_load_lds_dwordx4 %[x2], s[68:69] offset:2048\n\t"
            "global_load_lds_dwordx4 %[x3], s[68:69] offset:3072\n\t"
            "s_add_u32 m0, %[ldsx], 69632\n\t"
            "s_lshl_b32 s76, s48, 7\n\t"
            "s_add_u32 s76, s76, 65536\n\t"
            "s_add_u32 s68, %[xpl], s76\n\t"
            "s_addc_u32 s69, %[xph], 0\n\t"
            "global_load_lds_dwordx4 %[x0], s[68:69] offset:0\n\t"
            "global_load_lds_dwordx4 %[x1], s[68:69] offset:1024\n\t"
            "global_load_lds_dwordx4 %[x2], s[68:69] offset:2048\n\t"
            "global_load_lds_dwordx4 %[x3], s[68:69] offset:3072\n\t"
            "s_waitcnt vmcnt(8)\n\t"
            "s_lshl_b32 s76, s48, 2\n\t"
            ".Lzc7:\n\t"
            "v_cmp_eq_u32 vcc, v192, %[nvl]\n\t"
            "s_lshr_b64 s[74:75], vcc, s76\n\t"
            "s_and_b32 s74, s74, 15\n\t"
            "s_cmp_eq_u32 s74, 15\n\t"
            "s_cbranch_scc1 .Lzd7\n\t"
            "s_cmp_eq_u32 s73, 0\n\t"
            "s_cbranch_scc1 .Lzd7\n\t"
            "s_sub_u32 s73, s73, 1\n\t"
            "global_load_dword v192, %[po], %[ws] sc1\n\t"
            "s_waitcnt vmcnt(0)\n\t"
            "s_branch .Lzc7\n\t"
            ".Lzd7:\n\t"
            "s_add_u32 m0, %[ldsw], 32768\n\t"
            "s_mul_i32 s76, s48, 458752\n\t"
            "s_add_u32 s70, s84, s76\n\t"
            "s_addc_u32 s71, s85, 0\n\t"
            "global_load_lds_dwordx4 %[wv], s[70:71] offset:0 sc1\n\t"
            "global_load_lds_dwordx4 %[wv], s[70:71] offset:1024 sc1\n\t"
            "global_load_lds_dwordx4 %[wv], s[70:71] offset:2048 sc1\n\t"
            "global_load_lds_dwordx4 %[wv], s[70:71] offset:3072 sc1\n\t"
            "s_barrier\n\t"
            ".Lwp37:\n\t"
            "s_cbranch_execz .Lwp97\n\t"
            "s_lshl_b32 s76, s49, 2\n\t"
            ".Lzc8:\n\t"
            "v_cmp_eq_u32 vcc, v192, %[nvl]\n\t"
            "s_lshr_b64 s[74:75], vcc, s76\n\t"
            "s_and_b32 s74, s74, 15\n\t"
            "s_cmp_eq_u32 s74, 15\n\t"
            "s_cbranch_scc1 .Lzd8\n\t"
            "s_cmp_eq_u32 s73, 0\n\t"
            "s_cbranch_scc1 .Lzd8\n\t"
            "s_sub_u32 s73, s73, 1\n\t"
            "global_load_dword v192, %[po], %[ws] sc1\n\t"
            "s_waitcnt vmcnt(0)\n\t"
            "s_branch .Lzc8\n\t"
            ".Lzd8:\n\t"
            "s_add_u32 m0, %[ldsw], 0\n\t"
            "s_mul_i32 s76, s49, 458752\n\t"
            "s_add_u32 s70, s84, s76\n\t"
            "s_addc_u32 s71, s85, 0\n\t"
            "global_load_lds_dwordx4 %[wv], s[70:71] offset:0 sc1\n\t"
            "global_load_lds_dwordx4 %[wv], s[70:71] offset:1024 sc1\n\t"
            "global_load_lds_dwordx4 %[wv], s[70:71] offset:2048 sc1\n\t"
            "global_load_lds_dwordx4 %[wv], s[70:71] offset:3072 sc1\n\t"
            "s_setprio 0\n\t"
            "s_add_u32 m0, %[ldsx], 0\n\t"
            "s_lshl_b32 s76, s49, 7\n\t"
            "s_add_u32 s68, %[xpl], s76\n\t"
            "s_addc_u32 s69, %[xph], 0\n\t"
            "global_load_lds_dwordx4 %[x0], s[68:69] offset:0\n\t"
            "v_lshl_add_u32 v160, v204, 8, %[lutl]\n\t"
            "ds_read_u16 v160, v160 offset:0\n\t"
            "v_lshl_add_u32 v161, v208, 8, %[lutl]\n\t"
            "ds_read_u16 v161, v161 offset:0\n\t"
            "global_load_lds_dwordx4 %[x1], s[68:69] offset:1024\n\t"
            "v_lshl_add_u32 v162, v212, 8, %[lutl]\n\t"
            "ds_read_u16 v162, v162 offset:0\n\t"
            "v_lshl_add_u32 v163, v216, 8, %[lutl]\n\t"
            "ds_read_u16 v163, v163 offset:0\n\t"
            "global_load_lds_dwordx4 %[x2], s[68:69] offset:2048\n\t"
            "v_lshl_add_u32 v168, v205, 8, %[lutl]\n\t"
            "ds_read_u16 v168, v168 offset:2\n\t"
            "v_lshl_add_u32 v169, v209, 8, %[lutl]\n\t"
            "ds_read_u16 v169, v169 offset:2\n\t"
            "global_load_lds_dwordx4 %[x3], s[68:69] offset:3072\n\t"
            "v_lshl_add_u32 v170, v213, 8, %[lutl]\n\t"
            "ds_read_u16 v170, v170 offset:2\n\t"
            "v_lshl_add_u32 v171, v217, 8, %[lutl]\n\t"
            "ds_read_u16 v171, v171 offset:2\n\t"
            "s_add_u32 m0, %[ldsx], 4096\n\t"
            "s_lshl_b32 s76, s49, 7\n\t"
            "s_add_u32 s76, s76, 65536\n\t"
            "s_add_u32 s68, %[xpl], s76\n\t"
            "s_addc_u32 s69, %[xph], 0\n\t"
            "global_load_lds_dwordx4 %[x0], s[68:69] offset:0\n\t"
            "v_lshl_add_u32 v176, v206, 8, %[lutl]\n\t"
            "ds_read_u16 v176, v176 offset:128\n\t"
            "v_lshl_add_u32 v177, v210, 8, %[lutl]\n\t"
            "ds_read_u16 v177, v177 offset:128\n\t"
            "global_load_lds_dwordx4 %[x1], s[68:69] offset:1024\n\t"
            "v_lshl_add_u32 v178, v214, 8, %[lutl]\n\t"
            "ds_read_u16 v178, v178 offset:128\n\t"
            "v_lshl_add_u32 v179, v218, 8, %[lutl]\n\t"
            "ds_read_u16 v179, v179 offset:128\n\t"
            "global_load_lds_dwordx4 %[x2], s[68:69] offset:2048\n\t"
            "v_lshl_add_u32 v184, v207, 8, %[lutl]\n\t"
            "ds_read_u16 v184, v184 offset:130\n\t"
            "v_lshl_add_u32 v185, v211, 8, %[lutl]\n\t"
            "ds_read_u16 v185, v185 offset:130\n\t"
            "global_load_lds_dwordx4 %[x3], s[68:69] offset:3072\n\t"
            "v_lshl_add_u32 v186, v215, 8, %[lutl]\n\t"
            "ds_read_u16 v186, v186 offset:130\n\t"
            "v_lshl_add_u32 v187, v219, 8, %[lutl]\n\t"
            "ds_read_u16 v187, v187 offset:130\n\t"
            "s_setprio 0\n\t"
            "s_waitcnt lgkmcnt(8)\n\t"
            "v_lshl_or_b32 v220, v161, 16, v160\n\t"
            "v_lshl_or_b32 v221, v163, 16, v162\n\t"
            "v_lshl_or_b32 v222, v169, 16, v168\n\t"
            "v_lshl_or_b32 v223, v171, 16, v170\n\t"
            "s_waitcnt lgkmcnt(0)\n\t"
            "v_lshl_or_b32 v224, v177, 16, v176\n\t"
            "v_lshl_or_b32 v225, v179, 16, v178\n\t"
            "v_lshl_or_b32 v226, v185, 16, v184\n\t"
            "v_lshl_or_b32 v227, v187, 16, v186\n\t"
            "s_nop 0\n\t"
            "v_permlane32_swap_b32 v220, v222\n\t"
            "v_permlane32_swap_b32 v221, v223\n\t"
            "v_permlane32_swap_b32 v224, v226\n\t"
            "v_permlane32_swap_b32 v225, v227\n\t"
            "s_mov_b64 s[88:89], %[stb]\n\t"
            "s_add_u32 s88, s88, 1835008\n\t"
            "s_addc_u32 s89, s89, 0\n\t"
            "global_store_dwordx4 %[wo], v[220:223], s[88:89] offset:0 sc1\n\t"
            "global_store_dwordx4 %[wo], v[224:227], s[88:89] offset:512 sc1\n\t"
            "s_waitcnt vmcnt(14)\n\t"
            "s_barrier\n\t"
            ".Lwp38:\n\t"
            "s_cbranch_execz .Lwp39\n\t"
            "s_lshl_b32 s76, s50, 2\n\t"
            ".Lzc9:\n\t"
            "v_cmp_eq_u32 vcc, v192, %[nvl]\n\t"
            "s_lshr_b64 s[74:75], vcc, s76\n\t"
            "s_and_b32 s74, s74, 15\n\t"
            "s_cmp_eq_u32 s74, 15\n\t"
            "s_cbranch_scc1 .Lzd9\n\t"
            "s_cmp_eq_u32 s73, 0\n\t"
            "s_cbranch_scc1 .Lzd9\n\t"
            "s_sub_u32 s73, s73, 1\n\t"
            "global_load_dword v192, %[po], %[ws] sc1\n\t"
            "s_waitcnt vmcnt(0)\n\t"
            "s_branch .Lzc9\n\t"
            ".Lzd9:\n\t"
            "s_add_u32 m0, %[ldsw], 16384\n\t"
            "s_mul_i32 s76, s50, 458752\n\t"
            "s_add_u32 s70, s84, s76\n\t"
            "s_addc_u32 s71, s85, 0\n\t"
            "global_load_lds_dwordx4 %[wv], s[70:71] offset:0 sc1\n\t"
            "global_load_lds_dwordx4 %[wv], s[70:71] offset:1024 sc1\n\t"
            "global_load_lds_dwordx4 %[wv], s[70:71] offset:2048 sc1\n\t"
            "global_load_lds_dwordx4 %[wv], s[70:71] offset:3072 sc1\n\t"
            "s_add_u32 m0, %[ldsx], 32768\n\t"
            "s_lshl_b32 s76, s50, 7\n\t"
            "s_add_u32 s68, %[xpl], s76\n\t"
            "s_addc_u32 s69, %[xph], 0\n\t"
            "global_load_lds_dwordx4 %[x0], s[68:69] offset:0\n\t"
            "global_load_lds_dwordx4 %[x1], s[68:69] offset:1024\n\t"
            "global_load_lds_dwordx4 %[x2], s[68:69] offset:2048\n\t"
            "global_load_lds_dwordx4 %[x3], s[68:69] offset:3072\n\t"
            "s_add_u32 m0, %[ldsx], 36864\n\t"
            "s_lshl_b32 s76, s50, 7\n\t"
            "s_add_u32 s76, s76, 65536\n\t"
            "s_add_u32 s68, %[xpl], s76\n\t"
            "s_addc_u32 s69, %[xph], 0\n\t"
            "global_load_lds_dwordx4 %[x0], s[68:69] offset:0\n\t"
            "global_load_lds_dwordx4 %[x1], s[68:69] offset:1024\n\t"
            "global_load_lds_dwordx4 %[x2], s[68:69] offset:2048\n\t"
            "global_load_lds_dwordx4 %[x3], s[68:69] offset:3072\n\t"
            "s_waitcnt vmcnt(12)\n\t"
            "s_mov_b64 exec, 3\n\t"
            "global_store_dword %[fob], %[nvl], %[ws] offset:64 sc1\n\t"
            "s_mov_b64 exec, -1\n\t"
            "s_barrier\n\t"
            "s_lshl_b32 s76, s51, 2\n\t"
            ".Lzc10:\n\t"
            "v_cmp_eq_u32 vcc, v192, %[nvl]\n\t"
            "s_lshr_b64 s[74:75], vcc, s76\n\t"
            "s_and_b32 s74, s74, 15\n\t"
            "s_cmp_eq_u32 s74, 15\n\t"
            "s_cbranch_scc1 .Lzd10\n\t"
            "s_cmp_eq_u32 s73, 0\n\t"
            "s_cbranch_scc1 .Lzd10\n\t"
            "s_sub_u32 s73, s73, 1\n\t"
            "global_load_dword v192, %[po], %[ws] sc1\n\t"
            "s_waitcnt vmcnt(0)\n\t"
            "s_branch .Lzc10\n\t"
            ".Lzd10:\n\t"
            "s_add_u32 m0, %[ldsw], 32768\n\t"
            "s_mul_i32 s76, s51, 458752\n\t"
            "s_add_u32 s70, s84, s76\n\t"
            "s_addc_u32 s71, s85, 0\n\t"
            "global_load_lds_dwordx4 %[wv], s[70:71] offset:0 sc1\n\t"
            "global_load_lds_dwordx4 %[wv], s[70:71] offset:1024 sc1\n\t"
            "global_load_lds_dwordx4 %[wv], s[70:71] offset:2048 sc1\n\t"
            "global_load_lds_dwordx4 %[wv], s[70:71] offset:3072 sc1\n\t"
            "s_add_u32 m0, %[ldsx], 65536\n\t"
            "s_lshl_b32 s76, s51, 7\n\t"
            "s_add_u32 s68, %[xpl], s76\n\t"
            "s_addc_u32 s69, %[xph], 0\n\t"
            "global_load_lds_dwordx4 %[x0], s[68:69] offset:0\n\t"
            "global_load_lds_dwordx4 %[x1], s[68:69] offset:1024\n\t"
            "global_load_lds_dwordx4 %[x2], s[68:69] offset:2048\n\t"
            "global_load_lds_dwordx4 %[x3], s[68:69] offset:3072\n\t"
            "s_add_u32 m0, %[ldsx], 69632\n\t"
            "s_lshl_b32 s76, s51, 7\n\t"
            "s_add_u32 s76, s76, 65536\n\t"
            "s_add_u32 s68, %[xpl], s76\n\t"
            "s_addc_u32 s69, %[xph], 0\n\t"
            "global_load_lds_dwordx4 %[x0], s[68:69] offset:0\n\t"
            "global_load_lds_dwordx4 %[x1], s[68:69] offset:1024\n\t"
            "global_load_lds_dwordx4 %[x2], s[68:69] offset:2048\n\t"
            "global_load_lds_dwordx4 %[x3], s[68:69] offset:3072\n\t"
            "s_waitcnt vmcnt(13)\n\t"
            "s_barrier\n\t"
            ".Lwp39:\n\t"
            "s_cbranch_execz .Lwp40\n\t"
            "global_load_dword v192, %[po], %[ws] sc1\n\t"
            "s_add_u32 m0, %[ldsx], 0\n\t"
            "s_lshl_b32 s76, s52, 7\n\t"
            "s_add_u32 s68, %[xpl], s76\n\t"
            "s_addc_u32 s69, %[xph], 0\n\t"
            "global_load_lds_dwordx4 %[x0], s[68:69] offset:0\n\t"
            "global_load_lds_dwordx4 %[x1], s[68:69] offset:1024\n\t"
            "global_load_lds_dwordx4 %[x2], s[68:69] offset:2048\n\t"
            "global_load_lds_dwordx4 %[x3], s[68:69] offset:3072\n\t"
            "s_add_u32 m0, %[ldsx], 4096\n\t"
            "s_lshl_b32 s76, s52, 7\n\t"
            "s_add_u32 s76, s76, 65536\n\t"
            "s_add_u32 s68, %[xpl], s76\n\t"
            "s_addc_u32 s69, %[xph], 0\n\t"
            "global_load_lds_dwordx4 %[x0], s[68:69] offset:0\n\t"
            "global_load_lds_dwordx4 %[x1], s[68:69] offset:1024\n\t"
            "global_load_lds_dwordx4 %[x2], s[68:69] offset:2048\n\t"
            "global_load_lds_dwordx4 %[x3], s[68:69] offset:3072\n\t"
            "s_waitcnt vmcnt(8)\n\t"
            "s_lshl_b32 s76, s52, 2\n\t"
            ".Lzc11:\n\t"
            "v_cmp_eq_u32 vcc, v192, %[nvl]\n\t"
            "s_lshr_b64 s[74:75], vcc, s76\n\t"
            "s_and_b32 s74, s74, 15\n\t"
            "s_cmp_eq_u32 s74, 15\n\t"
            "s_cbranch_scc1 .Lzd11\n\t"
            "s_cmp_eq_u32 s73, 0\n\t"
            "s_cbranch_scc1 .Lzd11\n\t"
            "s_sub_u32 s73, s73, 1\n\t"
            "global_load_dword v192, %[po], %[ws] sc1\n\t"
            "s_waitcnt vmcnt(0)\n\t"
            "s_branch .Lzc11\n\t"
            ".Lzd11:\n\t"
            "s_add_u32 m0, %[ldsw], 0\n\t"
            "s_mul_i32 s76, s52, 458752\n\t"
            "s_add_u32 s70, s84, s76\n\t"
            "s_addc_u32 s71, s85, 0\n\t"
            "global_load_lds_dwordx4 %[wv], s[70:71] offset:0 sc1\n\t"
            "global_load_lds_dwordx4 %[wv], s[70:71] offset:1024 sc1\n\t"
            "global_load_lds_dwordx4 %[wv], s[70:71] offset:2048 sc1\n\t"
            "global_load_lds_dwordx4 %[wv], s[70:71] offset:3072 sc1\n\t"
            "s_barrier\n\t"
            "s_mov_b32 s76, 48\n\t"
            ".Lzc12:\n\t"
            "v_cmp_eq_u32 vcc, v192, %[nvl]\n\t"
            "s_lshr_b64 s[74:75], vcc, s76\n\t"
            "s_and_b32 s74, s74, 65535\n\t"
            "s_cmp_eq_u32 s74, 65535\n\t"
            "s_cbranch_scc1 .Lzd12\n\t"
            "s_cmp_eq_u32 s73, 0\n\t"
            "s_cbranch_scc1 .Lzd12\n\t"
            "s_sub_u32 s73, s73, 1\n\t"
            "global_load_dword v192, %[po], %[ws] sc1\n\t"
            "s_waitcnt vmcnt(0)\n\t"
            "s_branch .Lzc12\n\t"
            ".Lzd12:\n\t"
            "s_add_u32 m0, %[ldsw], 16384\n\t"
            "s_mul_i32 s76, s53, 458752\n\t"
            "s_add_u32 s70, s84, s76\n\t"
            "s_addc_u32 s71, s85, 0\n\t"
            "s_mul_i32 s76, s54, 458752\n\t"
            "s_sub_u32 s76, s76, 1024\n\t"
            "s_add_u32 s86, s84, s76\n\t"
            "s_addc_u32 s87, s85, 0\n\t"
            "global_load_lds_dwordx4 %[wv], s[70:71] offset:0 sc1\n\t"
            "global_load_lds_dwordx4 %[wv], s[86:87] offset:1024 sc1\n\t"
            "global_load_lds_dwordx4 %[wv], s[70:71] offset:2048 sc1\n\t"
            "global_load_lds_dwordx4 %[wv], s[86:87] offset:3072 sc1\n\t"
            "s_add_u32 m0, %[ldsx], 32768\n\t"
            "s_lshl_b32 s76, s53, 7\n\t"
            "s_add_u32 s68, %[xpl], s76\n\t"
            "s_addc_u32 s69, %[xph], 0\n\t"
            "global_load_lds_dwordx4 %[x0], s[68:69] offset:0\n\t"
            "global_load_lds_dwordx4 %[x1], s[68:69] offset:1024\n\t"
            "global_load_lds_dwordx4 %[x2], s[68:69] offset:2048\n\t"
            "global_load_lds_dwordx4 %[x3], s[68:69] offset:3072\n\t"
            "s_add_u32 m0, %[ldsx], 36864\n\t"
            "s_lshl_b32 s76, s53, 7\n\t"
            "s_add_u32 s76, s76, 65536\n\t"
            "s_add_u32 s68, %[xpl], s76\n\t"
            "s_addc_u32 s69, %[xph], 0\n\t"
            "global_load_lds_dwordx4 %[x0], s[68:69] offset:0\n\t"
            "global_load_lds_dwordx4 %[x1], s[68:69] offset:1024\n\t"
            "global_load_lds_dwordx4 %[x2], s[68:69] offset:2048\n\t"
            "global_load_lds_dwordx4 %[x3], s[68:69] offset:3072\n\t"
            "s_waitcnt vmcnt(12)\n\t"
            "s_barrier\n\t"
            ".Lwp40:\n\t"
            "s_cbranch_execz .Lwp97\n\t"
            "s_add_u32 m0, %[ldsx], 65536\n\t"
            "s_lshl_b32 s76, s54, 7\n\t"
            "s_add_u32 s68, %[xpl], s76\n\t"
            "s_addc_u32 s69, %[xph], 0\n\t"
            "global_load_lds_dwordx4 %[x0], s[68:69] offset:0\n\t"
            "global_load_lds_dwordx4 %[x1], s[68:69] offset:1024\n\t"
            "global_load_lds_dwordx4 %[x2], s[68:69] offset:2048\n\t"
            "global_load_lds_dwordx4 %[x3], s[68:69] offset:3072\n\t"
            "s_add_u32 m0, %[ldsx], 69632\n\t"
            "s_lshl_b32 s76, s54, 7\n\t"
            "s_add_u32 s76, s76, 65536\n\t"
            "s_add_u32 s68, %[xpl], s76\n\t"
            "s_addc_u32 s69, %[xph], 0\n\t"
            "global_load_lds_dwordx4 %[x0], s[68:69] offset:0\n\t"
            "global_load_lds_dwordx4 %[x1], s[68:69] offset:1024\n\t"
            "global_load_lds_dwordx4 %[x2], s[68:69] offset:2048\n\t"
            "global_load_lds_dwordx4 %[x3], s[68:69] offset:3072\n\t"
            "s_add_u32 m0, %[ldsw], 32768\n\t"
            "s_mul_i32 s76, s55, 458752\n\t"
            "s_add_u32 s70, s84, s76\n\t"
            "s_addc_u32 s71, s85, 0\n\t"
            "s_mul_i32 s76, s53, 458752\n\t"
            "s_add_u32 s86, s84, s76\n\t"
            "s_addc_u32 s87, s85, 0\n\t"
            "global_load_lds_dwordx4 %[wv], s[70:71] offset:0 sc1\n\t"
            "global_load_lds_dwordx4 %[wv], s[86:87] offset:1024 sc1\n\t"
            "global_load_lds_dwordx4 %[wv], s[70:71] offset:2048 sc1\n\t"
            "global_load_lds_dwordx4 %[wv], s[86:87] offset:3072 sc1\n\t"
            "s_waitcnt vmcnt(4)\n\t"
            "s_barrier\n\t"
            "s_add_u32 m0, %[ldsx], 0\n\t"
            "s_lshl_b32 s76, s55, 7\n\t"
            "s_add_u32 s68, %[xpl], s76\n\t"
            "s_addc_u32 s69, %[xph], 0\n\t"
            "global_load_lds_dwordx4 %[x0], s[68:69] offset:0\n\t"
            "global_load_lds_dwordx4 %[x1], s[68:69] offset:1024\n\t"
            "global_load_lds_dwordx4 %[x2], s[68:69] offset:2048\n\t"
            "global_load_lds_dwordx4 %[x3], s[68:69] offset:3072\n\t"
            "s_add_u32 m0, %[ldsx], 4096\n\t"
            "s_lshl_b32 s76, s55, 7\n\t"
            "s_add_u32 s76, s76, 65536\n\t"
            "s_add_u32 s68, %[xpl], s76\n\t"
            "s_addc_u32 s69, %[xph], 0\n\t"
            "global_load_lds_dwordx4 %[x0], s[68:69] offset:0\n\t"
            "global_load_lds_dwordx4 %[x1], s[68:69] offset:1024\n\t"
            "global_load_lds_dwordx4 %[x2], s[68:69] offset:2048\n\t"
            "global_load_lds_dwordx4 %[x3], s[68:69] offset:3072\n\t"
            "s_add_u32 m0, %[ldsw], 0\n\t"
            "s_mul_i32 s76, s54, 458752\n\t"
            "s_add_u32 s76, s76, 1024\n\t"
            "s_add_u32 s70, s84, s76\n\t"
            "s_addc_u32 s71, s85, 0\n\t"
            "s_mul_i32 s76, s55, 458752\n\t"
            "s_add_u32 s86, s84, s76\n\t"
            "s_addc_u32 s87, s85, 0\n\t"
            "global_load_lds_dwordx4 %[wv], s[70:71] offset:0 sc1\n\t"
            "global_load_lds_dwordx4 %[wv], s[86:87] offset:1024 sc1\n\t"
            "global_load_lds_dwordx4 %[wv], s[70:71] offset:2048 sc1\n\t"
            "global_load_lds_dwordx4 %[wv], s[86:87] offset:3072 sc1\n\t"
            "s_waitcnt vmcnt(4)\n\t"
            "s_barrier\n\t"
            ".Lwp41:\n\t"
            "s_cbranch_execz .Lwp100\n\t"
            "s_mov_b32 s76, 16\n\t"
            ".Lzc13:\n\t"
            "v_cmp_eq_u32 vcc, v192, %[nvl]\n\t"
            "s_lshr_b64 s[74:75], vcc, s76\n\t"
            "s_and_b32 s74, s74, 65535\n\t"
            "s_cmp_eq_u32 s74, 65535\n\t"
            "s_cbranch_scc1 .Lzd13\n\t"
            "s_cmp_eq_u32 s73, 0\n\t"
            "s_cbranch_scc1 .Lzd13\n\t"
            "s_sub_u32 s73, s73, 1\n\t"
            "global_load_dword v192, %[po], %[ws] sc1\n\t"
            "s_waitcnt vmcnt(0)\n\t"
            "s_branch .Lzc13\n\t"
            ".Lzd13:\n\t"
            "s_waitcnt vmcnt(0)\n\t"
            "s_barrier\n\t"
            : [nvl] "+v"(nvl), [qo] "+v"(qo), [wo] "+v"(wo)
            : [x0] "v"(xoff[0]), [x1] "v"(xoff[1]), [x2] "v"(xoff[2]), [x3] "v"(xoff[3]), [wv] "v"(wv), [po] "v"(po),
              [lutl] "v"(lutl), [fo0] "v"(fo0), [fob] "v"(fob), [bd] "v"(bdst),
              [xpl] "s"((u32)KPTR(0)), [xph] "s"((u32)(KPTR(0) >> 32)), [ws] "s"(KPTR(4)),
              [qbg] "s"(qbg), [qa1] "s"(qa1), [tseq] "s"(tseq), [p] "s"(p), [h2] "s"(h2), [ldsx] "s"(ldsx), [ldsw] "s"(ldsw), [st1] "s"(st1), [stb] "s"(stb)
            : "vcc", "scc", "memory", "s40", "s41", "s42", "s43", "s44", "s45", "s46", "s47", "s48", "s49", "s50", "s51", "s52", "s53", "s54", "s55",
              "s56", "s57", "s58", "s59", "s60", "s61", "s62", "s63", "s64", "s65", "s66", "s67", "s68", "s69", "s70", "s71", "s72", "s73", "s74",
              "s75", "s76", "s77", "s78", "s79", "s80", "s81", "s82", "s83", "s84", "s85", "s86", "s87", "s88", "s89", "v128", "v129", "v130", "v131",
              "v132", "v133", "v134", "v135", "v136", "v137", "v138", "v139", "v140", "v141", "v142", "v143", "v144", "v145", "v146", "v147", "v148",
              "v149", "v150", "v151", "v152", "v153", "v154", "v155", "v156", "v157", "v158", "v159", "v160", "v161", "v162", "v163", "v164", "v165",
              "v166", "v167", "v168", "v169", "v170", "v171", "v172", "v173", "v174", "v175", "v176", "v177", "v178", "v179", "v180", "v181", "v182",
              "v183", "v184", "v185", "v186", "v187", "v188", "v189", "v190", "v191", "v192", "v193", "v194", "v195", "v196", "v197", "v198", "v199",
              "v200", "v201", "v202", "v203", "v204", "v205", "v206", "v207", "v208", "v209", "v210", "v211", "v212", "v213", "v214", "v215", "v216",
              "v217", "v218", "v219", "v220", "v221", "v222", "v223", "v224", "v225", "v226", "v227", "v228", "v229", "v230", "v231", "v232", "v233",
              "v234", "v235");
        if (mt == 0 && p == 0 && lane < 2) asm volatile("global_store_dword %0, %1, %2 sc1" :: "v"(no), "v"(nvl), "s"(KPTR(4)) : "memory");
        return;
    }

    u32 oo[4];
    unsigned long ob[4];

    int xb0, xb1, ba;
    {
        u32 V[4][8];
        i32x4 Q0[8];
#define LUT1(Q, j, r) asm volatile("v_lshl_add_u32 %0, %1, 8, %2\n\tds_read_u16 %0, %0 offset:%3" : "=&v"(V[j][r]) \
                                   : "v"(Q[r][j]), "v"(lutl), "i"(((j) >> 1) * 128 + ((j) & 1) * 2) : "memory")
#define PK(b_, j, n) asm volatile("s_waitcnt lgkmcnt(" #n ")\n\t" \
                                    "v_lshl_or_b32 %0, %5, 16, %4\n\tv_lshl_or_b32 %1, %7, 16, %6\n\t" \
                                    "v_lshl_or_b32 %2, %9, 16, %8\n\tv_lshl_or_b32 %3, %11, 16, %10" \
                                    : "=&v"(b_[0]), "=&v"(b_[1]), "=&v"(b_[2]), "=&v"(b_[3]) \
                                    : "v"(V[j][0]), "v"(V[j][1]), "v"(V[j][2]), "v"(V[j][3]), "v"(V[j][4]), "v"(V[j][5]), "v"(V[j][6]), "v"(V[j][7]) : "memory")
#define WR(b_, j) asm volatile("ds_write_b128 %0, %1 offset:%2" :: "v"(bdst), "v"(b_), "i"((j) * 256) : "memory")
        {
            const u32 l = tid & 31, h = (tid >> 5) & 1;
            const u32 lo = l * 8 + h * 4, qv = h * (8 * N_ * 4) + l * 16;
            const u32 ls = (u32)((w * 4 * N_ + col0) * 2), qs = (u32)((((mt >> 1) * 64 + w * 16) * N_ + col0) * 4);
            const u32 ld = (u32)(size_t)(smem) + OFF_L + w * 1024;
            asm volatile("s_mov_b32 m0, %[ld]\n\t"
                         "s_waitcnt lgkmcnt(0)\n\t"
                         "s_mov_b64 s[60:61], %[lp]\n\ts_add_u32 s60, s60, %[ls]\n\ts_addc_u32 s61, s61, 0\n\t"
                         "s_mov_b64 s[62:63], %[qp]\n\ts_add_u32 s62, s62, %[qs]\n\ts_addc_u32 s63, s63, 0\n\t"
                         "global_load_lds_dword %[lo], s[60:61] offset:0\n\t"
                         "s_add_u32 s60, s60, 6912\n\ts_addc_u32 s61, s61, 0\n\t"
                         "global_load_lds_dword %[lo], s[60:61] offset:256\n\t"
                         "s_add_u32 s60, s60, 6912\n\ts_addc_u32 s61, s61, 0\n\t"
                         "global_load_lds_dword %[lo], s[60:61] offset:512\n\t"
                         "s_add_u32 s60, s60, 6912\n\ts_addc_u32 s61, s61, 0\n\t"
                         "global_load_lds_dword %[lo], s[60:61] offset:768\n\t"
                         "global_load_dwordx4 %[q0], %[qv], s[62:63]\n\t"
                         "s_add_u32 s62, s62, 14336\n\ts_addc_u32 s63, s63, 0\n\t"
                         "global_load_dwordx4 %[q1], %[qv], s[62:63]\n\t"
                         "s_add_u32 s62, s62, 14336\n\ts_addc_u32 s63, s63, 0\n\t"
                         "global_load_dwordx4 %[q2], %[qv], s[62:63]\n\t"
                         "s_add_u32 s62, s62, 14336\n\ts_addc_u32 s63, s63, 0\n\t"
                         "global_load_dwordx4 %[q3], %[qv], s[62:63]\n\t"
                         "s_add_u32 s62, s62, 14336\n\ts_addc_u32 s63, s63, 0\n\t"
                         "global_load_dwordx4 %[q4], %[qv], s[62:63]\n\t"
                         "s_add_u32 s62, s62, 14336\n\ts_addc_u32 s63, s63, 0\n\t"
                         "global_load_dwordx4 %[q5], %[qv], s[62:63]\n\t"
                         "s_add_u32 s62, s62, 14336\n\ts_addc_u32 s63, s63, 0\n\t"
                         "global_load_dwordx4 %[q6], %[qv], s[62:63]\n\t"
                         "s_add_u32 s62, s62, 14336\n\ts_addc_u32 s63, s63, 0\n\t"
                         "global_load_dwordx4 %[q7], %[qv], s[62:63]\n\t"
                         : [q0] "=&v"(Q0[0]), [q1] "=&v"(Q0[1]), [q2] "=&v"(Q0[2]), [q3] "=&v"(Q0[3]),
                           [q4] "=&v"(Q0[4]), [q5] "=&v"(Q0[5]), [q6] "=&v"(Q0[6]), [q7] "=&v"(Q0[7])
                         : [lo] "v"(lo), [qv] "v"(qv), [ls] "s"(ls), [qs] "s"(qs), [ld] "s"(ld), [lp] "s"(ka[2]), [qp] "s"(ka[1])
                         : "s60", "s61", "s62", "s63", "scc", "memory");
            SB();
        }
        KWAIT();
        int ln = lane;
        asm volatile("" : "+v"(ln));
        const int c = ln & 15, g = ln >> 4, l = ln & 31, h = ln >> 5;
        xb0 = (w * 64 + c) * 128 + ((g ^ (c >> 1)) * 16);
        xb1 = xb0 ^ 64;
        ba = OFF_B + g * 2048 + c * 16;
        u32 lutl = (u32)(size_t)(smem) + OFF_L + l * 4;
        u32 bdst = (u32)(size_t)(smem) + OFF_B + w * 4096 + h * 2048 + ((l >> 4) * 64 + (l & 15)) * 16;
#pragma unroll
        for (int r = 0; r < 4; ++r) {
            oo[r] = (u32)(((row0 + w * 64 + g * 4 + r) * N_ + col0 + c * 4) * 2);
            ob[r] = KPTR(3) + (unsigned long)(r * 16 * N_ * 2);
            asm volatile("" : "+v"(oo[r]), "+s"(ob[r]));
        }
        asm volatile("" : "+v"(xb0), "+v"(xb1), "+v"(ba));
        asm volatile("" : "+v"(bdst), "+v"(lutl));
        {
            unsigned long ex_;
            asm volatile("s_mov_b64 %0, exec\n\ts_mov_b64 exec, 0\n\t"
                         "s_cmp_eq_u32 %1, 0\n\ts_cbranch_scc1 .Lwp20\n\t"
                         "s_cmp_eq_u32 %1, 1\n\ts_cbranch_scc1 .Lwp10\n\t"
                         "s_cmp_eq_u32 %1, 2\n\ts_cbranch_scc1 .Lwp12\n\t"
                         "s_branch .Lwp99\n\t"
                         ".Lwp99:\n\ts_mov_b64 exec, %0" : "=&s"(ex_) : "s"(w) : "scc", "memory");
        }
        asm volatile("s_waitcnt vmcnt(8)" ::: "memory");
        BAR();
        PAD(20, 10);
        asm volatile("s_waitcnt vmcnt(7)" : "+v"(Q0[0]) :: "memory"); LUT1(Q0, 0, 0); LUT1(Q0, 1, 0); LUT1(Q0, 2, 0); LUT1(Q0, 3, 0); SB();
        asm volatile("s_waitcnt vmcnt(6)" : "+v"(Q0[1]) :: "memory"); LUT1(Q0, 0, 1); LUT1(Q0, 1, 1); LUT1(Q0, 2, 1); LUT1(Q0, 3, 1); SB();
        asm volatile("s_waitcnt vmcnt(5)" : "+v"(Q0[2]) :: "memory"); LUT1(Q0, 0, 2); LUT1(Q0, 1, 2); LUT1(Q0, 2, 2); LUT1(Q0, 3, 2); SB();
        asm volatile("s_waitcnt vmcnt(4)" : "+v"(Q0[3]) :: "memory"); LUT1(Q0, 0, 3); LUT1(Q0, 1, 3); LUT1(Q0, 2, 3); LUT1(Q0, 3, 3); SB();
        asm volatile("s_waitcnt vmcnt(3)" : "+v"(Q0[4]) :: "memory"); LUT1(Q0, 0, 4); LUT1(Q0, 1, 4); LUT1(Q0, 2, 4); LUT1(Q0, 3, 4); SB();
        asm volatile("s_waitcnt vmcnt(2)" : "+v"(Q0[5]) :: "memory"); LUT1(Q0, 0, 5); LUT1(Q0, 1, 5); LUT1(Q0, 2, 5); LUT1(Q0, 3, 5); SB();
        asm volatile("s_waitcnt vmcnt(1)" : "+v"(Q0[6]) :: "memory"); LUT1(Q0, 0, 6); LUT1(Q0, 1, 6); LUT1(Q0, 2, 6); LUT1(Q0, 3, 6); SB();
        asm volatile("s_waitcnt vmcnt(0)" : "+v"(Q0[7]) :: "memory"); LUT1(Q0, 0, 7); LUT1(Q0, 1, 7); LUT1(Q0, 2, 7); LUT1(Q0, 3, 7); SB();
        {   u32x4 b0, b1, b2, b3;
            PK(b0, 0, 3); WR(b0, 0); PK(b1, 1, 3); WR(b1, 1); PK(b2, 2, 3); WR(b2, 2); PK(b3, 3, 3); WR(b3, 3);
            SB();
        }
        asm volatile("s_waitcnt lgkmcnt(0)" ::: "memory");
        BAR();
    }
    asm volatile(
        "v_mov_b32 v232, %[xb0]\n\t"
        "v_mov_b32 v233, %[xb1]\n\t"
        "v_mov_b32 v234, %[ba]\n\t"
        ".Lwp10:\n\t"
        "s_cbranch_execz .Lwp99\n\t"
        "ds_read_b128 v[192:195], v234 offset:0\n\t"
        "ds_read_b128 v[128:131], v232 offset:0\n\t"
        "ds_read_b128 v[196:199], v234 offset:8192\n\t"
        "ds_read_b128 v[144:147], v233 offset:0\n\t"
        "ds_read_b128 v[204:207], v234 offset:8448\n\t"
        "ds_read_b128 v[200:203], v234 offset:256\n\t"
        "ds_read_b128 v[132:135], v232 offset:2048\n\t"
        "ds_read_b128 v[148:151], v233 offset:2048\n\t"
        "s_waitcnt lgkmcnt(6)\n\t"
        "v_mfma_f32_16x16x32_bf16 v[0:3], v[128:131], v[192:195], 0\n\t"
        "s_waitcnt lgkmcnt(4)\n\t"
        "v_mfma_f32_16x16x32_bf16 v[0:3], v[144:147], v[196:199], v[0:3]\n\t"
        "ds_read_b128 v[136:139], v232 offset:4096\n\t"
        "s_waitcnt lgkmcnt(4)\n\t"
        "v_mfma_f32_16x16x32_bf16 v[4:7], v[144:147], v[204:207], 0\n\t"
        "s_waitcnt lgkmcnt(3)\n\t"
        "v_mfma_f32_16x16x32_bf16 v[4:7], v[128:131], v[200:203], v[4:7]\n\t"
        "ds_read_b128 v[152:155], v233 offset:4096\n\t"
        "s_waitcnt lgkmcnt(3)\n\t"
        "v_mfma_f32_16x16x32_bf16 v[36:39], v[132:135], v[200:203], 0\n\t"
        "s_waitcnt lgkmcnt(2)\n\t"
        "v_mfma_f32_16x16x32_bf16 v[36:39], v[148:151], v[204:207], v[36:39]\n\t"
        "ds_read_b128 v[140:143], v232 offset:6144\n\t"
        "v_mfma_f32_16x16x32_bf16 v[32:35], v[148:151], v[196:199], 0\n\t"
        "v_mfma_f32_16x16x32_bf16 v[32:35], v[132:135], v[192:195], v[32:35]\n\t"
        "ds_read_b128 v[156:159], v233 offset:6144\n\t"
        "s_waitcnt lgkmcnt(3)\n\t"
        "v_mfma_f32_16x16x32_bf16 v[64:67], v[136:139], v[192:195], 0\n\t"
        "s_waitcnt lgkmcnt(2)\n\t"
        "v_mfma_f32_16x16x32_bf16 v[64:67], v[152:155], v[196:199], v[64:67]\n\t"
        "ds_read_b128 v[208:211], v234 offset:512\n\t"
        "v_mfma_f32_16x16x32_bf16 v[68:71], v[152:155], v[204:207], 0\n\t"
        "v_mfma_f32_16x16x32_bf16 v[68:71], v[136:139], v[200:203], v[68:71]\n\t"
        "ds_read_b128 v[212:215], v234 offset:8704\n\t"
        "s_waitcnt lgkmcnt(3)\n\t"
        "v_mfma_f32_16x16x32_bf16 v[100:103], v[140:143], v[200:203], 0\n\t"
        "s_waitcnt lgkmcnt(2)\n\t"
        "v_mfma_f32_16x16x32_bf16 v[100:103], v[156:159], v[204:207], v[100:103]\n\t"
        "ds_read_b128 v[220:223], v234 offset:8960\n\t"
        "v_mfma_f32_16x16x32_bf16 v[96:99], v[156:159], v[196:199], 0\n\t"
        "v_mfma_f32_16x16x32_bf16 v[96:99], v[140:143], v[192:195], v[96:99]\n\t"
        "ds_read_b128 v[216:219], v234 offset:768\n\t"
        "s_waitcnt lgkmcnt(3)\n\t"
        "v_mfma_f32_16x16x32_bf16 v[8:11], v[128:131], v[208:211], 0\n\t"
        "s_waitcnt lgkmcnt(2)\n\t"
        "v_mfma_f32_16x16x32_bf16 v[8:11], v[144:147], v[212:215], v[8:11]\n\t"
        "ds_read_b128 v[192:195], v234 offset:1024\n\t"
        "s_waitcnt lgkmcnt(2)\n\t"
        "v_mfma_f32_16x16x32_bf16 v[12:15], v[144:147], v[220:223], 0\n\t"
        "s_waitcnt lgkmcnt(1)\n\t"
        "v_mfma_f32_16x16x32_bf16 v[12:15], v[128:131], v[216:219], v[12:15]\n\t"
        "ds_read_b128 v[196:199], v234 offset:9216\n\t"
        "v_mfma_f32_16x16x32_bf16 v[44:47], v[132:135], v[216:219], 0\n\t"
        "v_mfma_f32_16x16x32_bf16 v[44:47], v[148:151], v[220:223], v[44:47]\n\t"
        "ds_read_b128 v[204:207], v234 offset:9472\n\t"
        "v_mfma_f32_16x16x32_bf16 v[40:43], v[148:151], v[212:215], 0\n\t"
        "v_mfma_f32_16x16x32_bf16 v[40:43], v[132:135], v[208:211], v[40:43]\n\t"
        "ds_read_b128 v[200:203], v234 offset:1280\n\t"
        "v_mfma_f32_16x16x32_bf16 v[72:75], v[136:139], v[208:211], 0\n\t"
        "v_mfma_f32_16x16x32_bf16 v[72:75], v[152:155], v[212:215], v[72:75]\n\t"
        "v_mfma_f32_16x16x32_bf16 v[76:79], v[152:155], v[220:223], 0\n\t"
        "v_mfma_f32_16x16x32_bf16 v[76:79], v[136:139], v[216:219], v[76:79]\n\t"
        "v_mfma_f32_16x16x32_bf16 v[108:111], v[140:143], v[216:219], 0\n\t"
        "v_mfma_f32_16x16x32_bf16 v[108:111], v[156:159], v[220:223], v[108:111]\n\t"
        "v_mfma_f32_16x16x32_bf16 v[104:107], v[156:159], v[212:215], 0\n\t"
        "v_mfma_f32_16x16x32_bf16 v[104:107], v[140:143], v[208:211], v[104:107]\n\t"
        "s_waitcnt lgkmcnt(0)\n\t"
        "v_mfma_f32_16x16x32_bf16 v[16:19], v[128:131], v[192:195], 0\n\t"
        "v_mfma_f32_16x16x32_bf16 v[16:19], v[144:147], v[196:199], v[16:19]\n\t"
        "ds_read_b128 v[208:211], v234 offset:1536\n\t"
        "v_mfma_f32_16x16x32_bf16 v[20:23], v[144:147], v[204:207], 0\n\t"
        "v_mfma_f32_16x16x32_bf16 v[20:23], v[128:131], v[200:203], v[20:23]\n\t"
        "ds_read_b128 v[212:215], v234 offset:9728\n\t"
        "v_mfma_f32_16x16x32_bf16 v[52:55], v[132:135], v[200:203], 0\n\t"
        "v_mfma_f32_16x16x32_bf16 v[52:55], v[148:151], v[204:207], v[52:55]\n\t"
        "ds_read_b128 v[220:223], v234 offset:9984\n\t"
        "v_mfma_f32_16x16x32_bf16 v[48:51], v[148:151], v[196:199], 0\n\t"
        "v_mfma_f32_16x16x32_bf16 v[48:51], v[132:135], v[192:195], v[48:51]\n\t"
        "ds_read_b128 v[216:219], v234 offset:1792\n\t"
        "v_mfma_f32_16x16x32_bf16 v[80:83], v[136:139], v[192:195], 0\n\t"
        "v_mfma_f32_16x16x32_bf16 v[80:83], v[152:155], v[196:199], v[80:83]\n\t"
        "v_mfma_f32_16x16x32_bf16 v[84:87], v[152:155], v[204:207], 0\n\t"
        "v_mfma_f32_16x16x32_bf16 v[84:87], v[136:139], v[200:203], v[84:87]\n\t"
        "s_mov_b32 s60, 32768\n\t"
        "s_mov_b32 s62, 5\n\t"
        "v_add_u32 v232, 32768, %[xb0]\n\t"
        "v_add_u32 v233, 32768, %[xb1]\n\t"
        "v_add_u32 v234, 16384, %[ba]\n\t"
        "v_mov_b64 v[24:25], 0\n\t"
        "v_mov_b64 v[26:27], 0\n\t"
        "v_mov_b64 v[28:29], 0\n\t"
        "v_mov_b64 v[30:31], 0\n\t"
        "v_mov_b64 v[56:57], 0\n\t"
        "v_mov_b64 v[58:59], 0\n\t"
        "v_mov_b64 v[60:61], 0\n\t"
        "v_mov_b64 v[62:63], 0\n\t"
        "v_mov_b64 v[88:89], 0\n\t"
        "v_mov_b64 v[90:91], 0\n\t"
        "v_mov_b64 v[92:93], 0\n\t"
        "v_mov_b64 v[94:95], 0\n\t"
        "v_mov_b64 v[120:121], 0\n\t"
        "v_mov_b64 v[122:123], 0\n\t"
        "v_mov_b64 v[124:125], 0\n\t"
        "v_mov_b64 v[126:127], 0\n\t"
        "v_mov_b64 v[116:117], 0\n\t"
        "v_mov_b64 v[118:119], 0\n\t"
        "v_mov_b64 v[112:113], 0\n\t"
        "v_mov_b64 v[114:115], 0\n\t"
        "s_nop 1\n\t"
        "s_mov_b64 exec, 0\n\t"
        "s_cmp_eq_u32 %[w], 0\n\t"
        "s_cbranch_scc1 .Lwp34\n\t"
        "s_cmp_eq_u32 %[w], 1\n\t"
        "s_cbranch_scc1 .Lwp38\n\t"
        "s_cmp_eq_u32 %[w], 2\n\t"
        "s_cbranch_scc1 .Lwp41\n\t"
        "s_branch .Lwp102\n\t"
        ".Lwp97:\n\t"
        "s_mov_b64 exec, -1\n\t"
        ".Lcloop:\n\t"
        "v_mfma_f32_16x16x32_bf16 v[116:119], v[140:143], v[200:203], v[116:119]\n\t"
        "s_sub_u32 s62, s62, 1\n\t"
        "v_mfma_f32_16x16x32_bf16 v[116:119], v[156:159], v[204:207], v[116:119]\n\t"
        "s_waitcnt lgkmcnt(0)\n\t"
        "v_mfma_f32_16x16x32_bf16 v[112:115], v[156:159], v[196:199], v[112:115]\n\t"
        "v_mfma_f32_16x16x32_bf16 v[112:115], v[140:143], v[192:195], v[112:115]\n\t"
        "s_barrier\n\t"
        "v_mfma_f32_16x16x32_bf16 v[24:27], v[128:131], v[208:211], v[24:27]\n\t"
        "v_mfma_f32_16x16x32_bf16 v[24:27], v[144:147], v[212:215], v[24:27]\n\t"
        "ds_read_b128 v[192:195], v234 offset:0\n\t"
        "ds_read_b128 v[160:163], v232 offset:0\n\t"
        "v_mfma_f32_16x16x32_bf16 v[28:31], v[144:147], v[220:223], v[28:31]\n\t"
        "v_mfma_f32_16x16x32_bf16 v[28:31], v[128:131], v[216:219], v[28:31]\n\t"
        "ds_read_b128 v[196:199], v234 offset:8192\n\t"
        "ds_read_b128 v[176:179], v233 offset:0\n\t"
        "v_mfma_f32_16x16x32_bf16 v[60:63], v[132:135], v[216:219], v[60:63]\n\t"
        "v_mfma_f32_16x16x32_bf16 v[60:63], v[148:151], v[220:223], v[60:63]\n\t"
        "ds_read_b128 v[204:207], v234 offset:8448\n\t"
        "v_mfma_f32_16x16x32_bf16 v[56:59], v[148:151], v[212:215], v[56:59]\n\t"
        "v_mfma_f32_16x16x32_bf16 v[56:59], v[132:135], v[208:211], v[56:59]\n\t"
        "ds_read_b128 v[200:203], v234 offset:256\n\t"
        "v_mfma_f32_16x16x32_bf16 v[88:91], v[136:139], v[208:211], v[88:91]\n\t"
        "v_mfma_f32_16x16x32_bf16 v[88:91], v[152:155], v[212:215], v[88:91]\n\t"
        "ds_read_b128 v[164:167], v232 offset:2048\n\t"
        "v_mfma_f32_16x16x32_bf16 v[92:95], v[152:155], v[220:223], v[92:95]\n\t"
        "v_mfma_f32_16x16x32_bf16 v[92:95], v[136:139], v[216:219], v[92:95]\n\t"
        "ds_read_b128 v[180:183], v233 offset:2048\n\t"
        "v_mfma_f32_16x16x32_bf16 v[124:127], v[140:143], v[216:219], v[124:127]\n\t"
        "v_mfma_f32_16x16x32_bf16 v[124:127], v[156:159], v[220:223], v[124:127]\n\t"
        "ds_read_b128 v[168:171], v232 offset:4096\n\t"
        "v_mfma_f32_16x16x32_bf16 v[120:123], v[156:159], v[212:215], v[120:123]\n\t"
        "v_mfma_f32_16x16x32_bf16 v[120:123], v[140:143], v[208:211], v[120:123]\n\t"
        "ds_read_b128 v[184:187], v233 offset:4096\n\t"
        "s_waitcnt lgkmcnt(4)\n\t"
        "v_mfma_f32_16x16x32_bf16 v[0:3], v[160:163], v[192:195], v[0:3]\n\t"
        "v_mfma_f32_16x16x32_bf16 v[0:3], v[176:179], v[196:199], v[0:3]\n\t"
        "ds_read_b128 v[172:175], v232 offset:6144\n\t"
        "ds_read_b128 v[208:211], v234 offset:512\n\t"
        "v_mfma_f32_16x16x32_bf16 v[4:7], v[176:179], v[204:207], v[4:7]\n\t"
        "v_mfma_f32_16x16x32_bf16 v[4:7], v[160:163], v[200:203], v[4:7]\n\t"
        "ds_read_b128 v[188:191], v233 offset:6144\n\t"
        "s_waitcnt lgkmcnt(5)\n\t"
        "v_mfma_f32_16x16x32_bf16 v[36:39], v[164:167], v[200:203], v[36:39]\n\t"
        "ds_read_b128 v[212:215], v234 offset:8704\n\t"
        "v_mfma_f32_16x16x32_bf16 v[36:39], v[180:183], v[204:207], v[36:39]\n\t"
        "ds_read_b128 v[220:223], v234 offset:8960\n\t"
        "v_mfma_f32_16x16x32_bf16 v[32:35], v[180:183], v[196:199], v[32:35]\n\t"
        "v_mfma_f32_16x16x32_bf16 v[32:35], v[164:167], v[192:195], v[32:35]\n\t"
        "ds_read_b128 v[216:219], v234 offset:768\n\t"
        "s_waitcnt lgkmcnt(6)\n\t"
        "v_mfma_f32_16x16x32_bf16 v[64:67], v[168:171], v[192:195], v[64:67]\n\t"
        "v_mfma_f32_16x16x32_bf16 v[64:67], v[184:187], v[196:199], v[64:67]\n\t"
        "v_mfma_f32_16x16x32_bf16 v[68:71], v[184:187], v[204:207], v[68:71]\n\t"
        "v_mfma_f32_16x16x32_bf16 v[68:71], v[168:171], v[200:203], v[68:71]\n\t"
        "s_waitcnt lgkmcnt(3)\n\t"
        "v_mfma_f32_16x16x32_bf16 v[100:103], v[172:175], v[200:203], v[100:103]\n\t"
        "v_mfma_f32_16x16x32_bf16 v[100:103], v[188:191], v[204:207], v[100:103]\n\t"
        "v_mfma_f32_16x16x32_bf16 v[96:99], v[188:191], v[196:199], v[96:99]\n\t"
        "v_mfma_f32_16x16x32_bf16 v[96:99], v[172:175], v[192:195], v[96:99]\n\t"
        "v_mfma_f32_16x16x32_bf16 v[8:11], v[160:163], v[208:211], v[8:11]\n\t"
        "s_waitcnt lgkmcnt(0)\n\t"
        "v_mfma_f32_16x16x32_bf16 v[8:11], v[176:179], v[212:215], v[8:11]\n\t"
        "ds_read_b128 v[192:195], v234 offset:1024\n\t"
        "v_mfma_f32_16x16x32_bf16 v[12:15], v[176:179], v[220:223], v[12:15]\n\t"
        "v_mfma_f32_16x16x32_bf16 v[12:15], v[160:163], v[216:219], v[12:15]\n\t"
        "ds_read_b128 v[196:199], v234 offset:9216\n\t"
        "v_mfma_f32_16x16x32_bf16 v[44:47], v[164:167], v[216:219], v[44:47]\n\t"
        "v_mfma_f32_16x16x32_bf16 v[44:47], v[180:183], v[220:223], v[44:47]\n\t"
        "ds_read_b128 v[204:207], v234 offset:9472\n\t"
        "v_mfma_f32_16x16x32_bf16 v[40:43], v[180:183], v[212:215], v[40:43]\n\t"
        "v_mfma_f32_16x16x32_bf16 v[40:43], v[164:167], v[208:211], v[40:43]\n\t"
        "ds_read_b128 v[200:203], v234 offset:1280\n\t"
        "v_mfma_f32_16x16x32_bf16 v[72:75], v[168:171], v[208:211], v[72:75]\n\t"
        "v_mfma_f32_16x16x32_bf16 v[72:75], v[184:187], v[212:215], v[72:75]\n\t"
        "v_mfma_f32_16x16x32_bf16 v[76:79], v[184:187], v[220:223], v[76:79]\n\t"
        "v_mfma_f32_16x16x32_bf16 v[76:79], v[168:171], v[216:219], v[76:79]\n\t"
        ".Lwp12:\n\t"
        "s_cbranch_execz .Lwp99\n\t"
        "v_mfma_f32_16x16x32_bf16 v[108:111], v[172:175], v[216:219], v[108:111]\n\t"
        "v_mfma_f32_16x16x32_bf16 v[108:111], v[188:191], v[220:223], v[108:111]\n\t"
        "v_mfma_f32_16x16x32_bf16 v[104:107], v[188:191], v[212:215], v[104:107]\n\t"
        "v_mfma_f32_16x16x32_bf16 v[104:107], v[172:175], v[208:211], v[104:107]\n\t"
        "s_waitcnt lgkmcnt(0)\n\t"
        "v_mfma_f32_16x16x32_bf16 v[16:19], v[160:163], v[192:195], v[16:19]\n\t"
        "v_mfma_f32_16x16x32_bf16 v[16:19], v[176:179], v[196:199], v[16:19]\n\t"
        "ds_read_b128 v[208:211], v234 offset:1536\n\t"
        "v_mfma_f32_16x16x32_bf16 v[20:23], v[176:179], v[204:207], v[20:23]\n\t"
        "v_mfma_f32_16x16x32_bf16 v[20:23], v[160:163], v[200:203], v[20:23]\n\t"
        "ds_read_b128 v[212:215], v234 offset:9728\n\t"
        "v_mfma_f32_16x16x32_bf16 v[52:55], v[164:167], v[200:203], v[52:55]\n\t"
        "v_mfma_f32_16x16x32_bf16 v[52:55], v[180:183], v[204:207], v[52:55]\n\t"
        "ds_read_b128 v[220:223], v234 offset:9984\n\t"
        "v_mfma_f32_16x16x32_bf16 v[48:51], v[180:183], v[196:199], v[48:51]\n\t"
        "v_mfma_f32_16x16x32_bf16 v[48:51], v[164:167], v[192:195], v[48:51]\n\t"
        "ds_read_b128 v[216:219], v234 offset:1792\n\t"
        "s_add_u32 s60, s60, 32768\n\t"
        "v_mfma_f32_16x16x32_bf16 v[80:83], v[168:171], v[192:195], v[80:83]\n\t"
        "s_cmp_eq_u32 s60, 98304\n\t"
        "s_cselect_b32 s60, 0, s60\n\t"
        "v_mfma_f32_16x16x32_bf16 v[80:83], v[184:187], v[196:199], v[80:83]\n\t"
        "v_add_u32 v232, s60, %[xb0]\n\t"
        "s_lshr_b32 s61, s60, 1\n\t"
        "v_mfma_f32_16x16x32_bf16 v[84:87], v[184:187], v[204:207], v[84:87]\n\t"
        "v_add_u32 v233, s60, %[xb1]\n\t"
        "v_add_u32 v234, s61, %[ba]\n\t"
        "v_mfma_f32_16x16x32_bf16 v[84:87], v[168:171], v[200:203], v[84:87]\n\t"
        "v_mfma_f32_16x16x32_bf16 v[116:119], v[172:175], v[200:203], v[116:119]\n\t"
        "v_mfma_f32_16x16x32_bf16 v[116:119], v[188:191], v[204:207], v[116:119]\n\t"
        "s_waitcnt lgkmcnt(0)\n\t"
        "v_mfma_f32_16x16x32_bf16 v[112:115], v[188:191], v[196:199], v[112:115]\n\t"
        "v_mfma_f32_16x16x32_bf16 v[112:115], v[172:175], v[192:195], v[112:115]\n\t"
        "s_barrier\n\t"
        "v_mfma_f32_16x16x32_bf16 v[24:27], v[160:163], v[208:211], v[24:27]\n\t"
        "v_mfma_f32_16x16x32_bf16 v[24:27], v[176:179], v[212:215], v[24:27]\n\t"
        "ds_read_b128 v[192:195], v234 offset:0\n\t"
        "ds_read_b128 v[128:131], v232 offset:0\n\t"
        "v_mfma_f32_16x16x32_bf16 v[28:31], v[176:179], v[220:223], v[28:31]\n\t"
        "v_mfma_f32_16x16x32_bf16 v[28:31], v[160:163], v[216:219], v[28:31]\n\t"
        "ds_read_b128 v[196:199], v234 offset:8192\n\t"
        "ds_read_b128 v[144:147], v233 offset:0\n\t"
        "v_mfma_f32_16x16x32_bf16 v[60:63], v[164:167], v[216:219], v[60:63]\n\t"
        "v_mfma_f32_16x16x32_bf16 v[60:63], v[180:183], v[220:223], v[60:63]\n\t"
        "ds_read_b128 v[204:207], v234 offset:8448\n\t"
        "v_mfma_f32_16x16x32_bf16 v[56:59], v[180:183], v[212:215], v[56:59]\n\t"
        "v_mfma_f32_16x16x32_bf16 v[56:59], v[164:167], v[208:211], v[56:59]\n\t"
        "ds_read_b128 v[200:203], v234 offset:256\n\t"
        "v_mfma_f32_16x16x32_bf16 v[88:91], v[168:171], v[208:211], v[88:91]\n\t"
        "v_mfma_f32_16x16x32_bf16 v[88:91], v[184:187], v[212:215], v[88:91]\n\t"
        "ds_read_b128 v[132:135], v232 offset:2048\n\t"
        "v_mfma_f32_16x16x32_bf16 v[92:95], v[184:187], v[220:223], v[92:95]\n\t"
        "v_mfma_f32_16x16x32_bf16 v[92:95], v[168:171], v[216:219], v[92:95]\n\t"
        "ds_read_b128 v[148:151], v233 offset:2048\n\t"
        "v_mfma_f32_16x16x32_bf16 v[124:127], v[172:175], v[216:219], v[124:127]\n\t"
        "v_mfma_f32_16x16x32_bf16 v[124:127], v[188:191], v[220:223], v[124:127]\n\t"
        "ds_read_b128 v[136:139], v232 offset:4096\n\t"
        "v_mfma_f32_16x16x32_bf16 v[120:123], v[188:191], v[212:215], v[120:123]\n\t"
        "v_mfma_f32_16x16x32_bf16 v[120:123], v[172:175], v[208:211], v[120:123]\n\t"
        "ds_read_b128 v[152:155], v233 offset:4096\n\t"
        "s_waitcnt lgkmcnt(4)\n\t"
        "v_mfma_f32_16x16x32_bf16 v[0:3], v[128:131], v[192:195], v[0:3]\n\t"
        "v_mfma_f32_16x16x32_bf16 v[0:3], v[144:147], v[196:199], v[0:3]\n\t"
        "ds_read_b128 v[140:143], v232 offset:6144\n\t"
        "ds_read_b128 v[208:211], v234 offset:512\n\t"
        "v_mfma_f32_16x16x32_bf16 v[4:7], v[144:147], v[204:207], v[4:7]\n\t"
        "v_mfma_f32_16x16x32_bf16 v[4:7], v[128:131], v[200:203], v[4:7]\n\t"
        "ds_read_b128 v[156:159], v233 offset:6144\n\t"
        "s_waitcnt lgkmcnt(5)\n\t"
        "v_mfma_f32_16x16x32_bf16 v[36:39], v[132:135], v[200:203], v[36:39]\n\t"
        "ds_read_b128 v[212:215], v234 offset:8704\n\t"
        "v_mfma_f32_16x16x32_bf16 v[36:39], v[148:151], v[204:207], v[36:39]\n\t"
        "ds_read_b128 v[220:223], v234 offset:8960\n\t"
        "v_mfma_f32_16x16x32_bf16 v[32:35], v[148:151], v[196:199], v[32:35]\n\t"
        "v_mfma_f32_16x16x32_bf16 v[32:35], v[132:135], v[192:195], v[32:35]\n\t"
        "ds_read_b128 v[216:219], v234 offset:768\n\t"
        "s_waitcnt lgkmcnt(6)\n\t"
        "v_mfma_f32_16x16x32_bf16 v[64:67], v[136:139], v[192:195], v[64:67]\n\t"
        "v_mfma_f32_16x16x32_bf16 v[64:67], v[152:155], v[196:199], v[64:67]\n\t"
        "v_mfma_f32_16x16x32_bf16 v[68:71], v[152:155], v[204:207], v[68:71]\n\t"
        "v_mfma_f32_16x16x32_bf16 v[68:71], v[136:139], v[200:203], v[68:71]\n\t"
        "s_waitcnt lgkmcnt(3)\n\t"
        "v_mfma_f32_16x16x32_bf16 v[100:103], v[140:143], v[200:203], v[100:103]\n\t"
        "v_mfma_f32_16x16x32_bf16 v[100:103], v[156:159], v[204:207], v[100:103]\n\t"
        "v_mfma_f32_16x16x32_bf16 v[96:99], v[156:159], v[196:199], v[96:99]\n\t"
        "v_mfma_f32_16x16x32_bf16 v[96:99], v[140:143], v[192:195], v[96:99]\n\t"
        "v_mfma_f32_16x16x32_bf16 v[8:11], v[128:131], v[208:211], v[8:11]\n\t"
        "s_waitcnt lgkmcnt(0)\n\t"
        "v_mfma_f32_16x16x32_bf16 v[8:11], v[144:147], v[212:215], v[8:11]\n\t"
        "ds_read_b128 v[192:195], v234 offset:1024\n\t"
        "v_mfma_f32_16x16x32_bf16 v[12:15], v[144:147], v[220:223], v[12:15]\n\t"
        "v_mfma_f32_16x16x32_bf16 v[12:15], v[128:131], v[216:219], v[12:15]\n\t"
        "ds_read_b128 v[196:199], v234 offset:9216\n\t"
        "v_mfma_f32_16x16x32_bf16 v[44:47], v[132:135], v[216:219], v[44:47]\n\t"
        "v_mfma_f32_16x16x32_bf16 v[44:47], v[148:151], v[220:223], v[44:47]\n\t"
        "ds_read_b128 v[204:207], v234 offset:9472\n\t"
        "v_mfma_f32_16x16x32_bf16 v[40:43], v[148:151], v[212:215], v[40:43]\n\t"
        "v_mfma_f32_16x16x32_bf16 v[40:43], v[132:135], v[208:211], v[40:43]\n\t"
        "ds_read_b128 v[200:203], v234 offset:1280\n\t"
        "v_mfma_f32_16x16x32_bf16 v[72:75], v[136:139], v[208:211], v[72:75]\n\t"
        "v_mfma_f32_16x16x32_bf16 v[72:75], v[152:155], v[212:215], v[72:75]\n\t"
        "v_mfma_f32_16x16x32_bf16 v[76:79], v[152:155], v[220:223], v[76:79]\n\t"
        "v_mfma_f32_16x16x32_bf16 v[76:79], v[136:139], v[216:219], v[76:79]\n\t"
        "v_mfma_f32_16x16x32_bf16 v[108:111], v[140:143], v[216:219], v[108:111]\n\t"
        "v_mfma_f32_16x16x32_bf16 v[108:111], v[156:159], v[220:223], v[108:111]\n\t"
        "v_mfma_f32_16x16x32_bf16 v[104:107], v[156:159], v[212:215], v[104:107]\n\t"
        "v_mfma_f32_16x16x32_bf16 v[104:107], v[140:143], v[208:211], v[104:107]\n\t"
        "s_waitcnt lgkmcnt(0)\n\t"
        "v_mfma_f32_16x16x32_bf16 v[16:19], v[128:131], v[192:195], v[16:19]\n\t"
        "v_mfma_f32_16x16x32_bf16 v[16:19], v[144:147], v[196:199], v[16:19]\n\t"
        "ds_read_b128 v[208:211], v234 offset:1536\n\t"
        "v_mfma_f32_16x16x32_bf16 v[20:23], v[144:147], v[204:207], v[20:23]\n\t"
        "v_mfma_f32_16x16x32_bf16 v[20:23], v[128:131], v[200:203], v[20:23]\n\t"
        "ds_read_b128 v[212:215], v234 offset:9728\n\t"
        "v_mfma_f32_16x16x32_bf16 v[52:55], v[132:135], v[200:203], v[52:55]\n\t"
        "v_mfma_f32_16x16x32_bf16 v[52:55], v[148:151], v[204:207], v[52:55]\n\t"
        "ds_read_b128 v[220:223], v234 offset:9984\n\t"
        "v_mfma_f32_16x16x32_bf16 v[48:51], v[148:151], v[196:199], v[48:51]\n\t"
        "v_mfma_f32_16x16x32_bf16 v[48:51], v[132:135], v[192:195], v[48:51]\n\t"
        "ds_read_b128 v[216:219], v234 offset:1792\n\t"
        "s_add_u32 s60, s60, 32768\n\t"
        "v_mfma_f32_16x16x32_bf16 v[80:83], v[136:139], v[192:195], v[80:83]\n\t"
        "s_cmp_eq_u32 s60, 98304\n\t"
        "s_cselect_b32 s60, 0, s60\n\t"
        "v_mfma_f32_16x16x32_bf16 v[80:83], v[152:155], v[196:199], v[80:83]\n\t"
        "v_add_u32 v232, s60, %[xb0]\n\t"
        "s_lshr_b32 s61, s60, 1\n\t"
        "v_mfma_f32_16x16x32_bf16 v[84:87], v[152:155], v[204:207], v[84:87]\n\t"
        "v_add_u32 v233, s60, %[xb1]\n\t"
        "v_add_u32 v234, s61, %[ba]\n\t"
        "v_mfma_f32_16x16x32_bf16 v[84:87], v[136:139], v[200:203], v[84:87]\n\t"
        "s_cmp_lg_u32 s62, -1\n\t"
        "s_cbranch_scc1 .Lcloop\n\t"
        "v_mfma_f32_16x16x32_bf16 v[116:119], v[140:143], v[200:203], v[116:119]\n\t"
        "v_mfma_f32_16x16x32_bf16 v[116:119], v[156:159], v[204:207], v[116:119]\n\t"
        "s_waitcnt lgkmcnt(0)\n\t"
        "v_mfma_f32_16x16x32_bf16 v[112:115], v[156:159], v[196:199], v[112:115]\n\t"
        "v_mfma_f32_16x16x32_bf16 v[112:115], v[140:143], v[192:195], v[112:115]\n\t"
        "s_barrier\n\t"
        "v_mfma_f32_16x16x32_bf16 v[24:27], v[128:131], v[208:211], v[24:27]\n\t"
        "s_setprio 2\n\t"
        "v_mfma_f32_16x16x32_bf16 v[24:27], v[144:147], v[212:215], v[24:27]\n\t"
        ".Lwp100:\n\t"
        "s_cbranch_execz .Lwp101\n\t"
        "ds_read_b128 v[160:163], %[ba] offset:16384\n\t"
        "v_mfma_f32_16x16x32_bf16 v[28:31], v[144:147], v[220:223], v[28:31]\n\t"
        "ds_read_b128 v[164:167], v232 offset:0\n\t"
        "v_mfma_f32_16x16x32_bf16 v[28:31], v[128:131], v[216:219], v[28:31]\n\t"
        "ds_read_b128 v[144:147], %[ba] offset:24576\n\t"
        "ds_read_b128 v[168:171], v233 offset:0\n\t"
        "v_mfma_f32_16x16x32_bf16 v[60:63], v[132:135], v[216:219], v[60:63]\n\t"
        "v_mfma_f32_16x16x32_bf16 v[60:63], v[148:151], v[220:223], v[60:63]\n\t"
        "ds_read_b128 v[128:131], %[ba] offset:24832\n\t"
        "ds_read_b128 v[172:175], %[ba] offset:16640\n\t"
        "v_mfma_f32_16x16x32_bf16 v[56:59], v[148:151], v[212:215], v[56:59]\n\t"
        "ds_read_b128 v[176:179], v232 offset:2048\n\t"
        "v_mfma_f32_16x16x32_bf16 v[56:59], v[132:135], v[208:211], v[56:59]\n\t"
        "ds_read_b128 v[148:151], v233 offset:2048\n\t"
        "v_mfma_f32_16x16x32_bf16 v[88:91], v[136:139], v[208:211], v[88:91]\n\t"
        "v_mfma_f32_16x16x32_bf16 v[88:91], v[152:155], v[212:215], v[88:91]\n\t"
        "ds_read_b128 v[132:135], v232 offset:4096\n\t"
        "v_mfma_f32_16x16x32_bf16 v[92:95], v[152:155], v[220:223], v[92:95]\n\t"
        "v_mfma_f32_16x16x32_bf16 v[92:95], v[136:139], v[216:219], v[92:95]\n\t"
        "ds_read_b128 v[152:155], v233 offset:4096\n\t"
        "v_mfma_f32_16x16x32_bf16 v[124:127], v[140:143], v[216:219], v[124:127]\n\t"
        "v_mfma_f32_16x16x32_bf16 v[124:127], v[156:159], v[220:223], v[124:127]\n\t"
        "ds_read_b128 v[136:139], v232 offset:6144\n\t"
        "v_mfma_f32_16x16x32_bf16 v[120:123], v[156:159], v[212:215], v[120:123]\n\t"
        "v_mfma_f32_16x16x32_bf16 v[120:123], v[140:143], v[208:211], v[120:123]\n\t"
        "ds_read_b128 v[156:159], v233 offset:6144\n\t"
        "s_waitcnt lgkmcnt(4)\n\t"
        "v_mfma_f32_16x16x32_bf16 v[0:3], v[164:167], v[160:163], v[0:3]\n\t"
        "v_mfma_f32_16x16x32_bf16 v[0:3], v[168:171], v[144:147], v[0:3]\n\t"
        "ds_read_b128 v[140:143], %[ba] offset:16896\n\t"
        "v_mfma_f32_16x16x32_bf16 v[4:7], v[168:171], v[128:131], v[4:7]\n\t"
        "v_mfma_f32_16x16x32_bf16 v[4:7], v[164:167], v[172:175], v[4:7]\n\t"
        "ds_read_b128 v[180:183], %[ba] offset:25088\n\t"
        "v_mfma_f32_16x16x32_bf16 v[36:39], v[176:179], v[172:175], v[36:39]\n\t"
        "v_mfma_f32_16x16x32_bf16 v[36:39], v[148:151], v[128:131], v[36:39]\n\t"
        "ds_read_b128 v[184:187], %[ba] offset:25344\n\t"
        "v_mfma_f32_16x16x32_bf16 v[32:35], v[148:151], v[144:147], v[32:35]\n\t"
        "v_mfma_f32_16x16x32_bf16 v[32:35], v[176:179], v[160:163], v[32:35]\n\t"
        "ds_read_b128 v[188:191], %[ba] offset:17152\n\t"
        "s_waitcnt lgkmcnt(4)\n\t"
        "v_mfma_f32_16x16x32_bf16 v[64:67], v[132:135], v[160:163], v[64:67]\n\t"
        "v_mfma_f32_16x16x32_bf16 v[64:67], v[152:155], v[144:147], v[64:67]\n\t"
        "ds_read_b128 v[192:195], %[ba] offset:17408\n\t"
        "v_mfma_f32_16x16x32_bf16 v[68:71], v[152:155], v[128:131], v[68:71]\n\t"
        "v_mfma_f32_16x16x32_bf16 v[68:71], v[132:135], v[172:175], v[68:71]\n\t"
        "ds_read_b128 v[196:199], v232 offset:32768\n\t"
        "v_mfma_f32_16x16x32_bf16 v[100:103], v[136:139], v[172:175], v[100:103]\n\t"
        "v_mfma_f32_16x16x32_bf16 v[100:103], v[156:159], v[128:131], v[100:103]\n\t"
        "ds_read_b128 v[172:175], %[ba] offset:25600\n\t"
        "v_mfma_f32_16x16x32_bf16 v[96:99], v[156:159], v[144:147], v[96:99]\n\t"
        "v_mfma_f32_16x16x32_bf16 v[96:99], v[136:139], v[160:163], v[96:99]\n\t"
        "ds_read_b128 v[128:131], v233 offset:32768\n\t"
        "s_waitcnt lgkmcnt(4)\n\t"
        "v_mfma_f32_16x16x32_bf16 v[8:11], v[164:167], v[140:143], v[8:11]\n\t"
        "v_mfma_f32_16x16x32_bf16 v[8:11], v[168:171], v[180:183], v[8:11]\n\t"
        "ds_read_b128 v[144:147], %[ba] offset:25856\n\t"
        "v_mfma_f32_16x16x32_bf16 v[12:15], v[168:171], v[184:187], v[12:15]\n\t"
        "v_mfma_f32_16x16x32_bf16 v[12:15], v[164:167], v[188:191], v[12:15]\n\t"
        "ds_read_b128 v[160:163], %[ba] offset:17664\n\t"
        "v_mfma_f32_16x16x32_bf16 v[44:47], v[176:179], v[188:191], v[44:47]\n\t"
        "v_mfma_f32_16x16x32_bf16 v[44:47], v[148:151], v[184:187], v[44:47]\n\t"
        "ds_read_b128 v[164:167], %[ba] offset:17920\n\t"
        "ds_read_b128 v[168:171], %[ba] offset:26112\n\t"
        "v_mfma_f32_16x16x32_bf16 v[40:43], v[148:151], v[180:183], v[40:43]\n\t"
        "v_mfma_f32_16x16x32_bf16 v[40:43], v[176:179], v[140:143], v[40:43]\n\t"
        "ds_read_b128 v[148:151], %[ba] offset:26368\n\t"
        "v_mfma_f32_16x16x32_bf16 v[72:75], v[132:135], v[140:143], v[72:75]\n\t"
        "v_mfma_f32_16x16x32_bf16 v[72:75], v[152:155], v[180:183], v[72:75]\n\t"
        "ds_read_b128 v[176:179], %[ba] offset:18176\n\t"
        "v_mfma_f32_16x16x32_bf16 v[76:79], v[152:155], v[184:187], v[76:79]\n\t"
        "v_mfma_f32_16x16x32_bf16 v[76:79], v[132:135], v[188:191], v[76:79]\n\t"
        "ds_read_b128 v[152:155], v232 offset:34816\n\t"
        "v_mfma_f32_16x16x32_bf16 v[108:111], v[136:139], v[188:191], v[108:111]\n\t"
        "v_mfma_f32_16x16x32_bf16 v[108:111], v[156:159], v[184:187], v[108:111]\n\t"
        "ds_read_b128 v[132:135], v233 offset:34816\n\t"
        "v_mfma_f32_16x16x32_bf16 v[104:107], v[156:159], v[180:183], v[104:107]\n\t"
        "v_mfma_f32_16x16x32_bf16 v[104:107], v[136:139], v[140:143], v[104:107]\n\t"
        "s_waitcnt lgkmcnt(3)\n\t"
        "v_mfma_f32_16x16x32_bf16 v[0:3], v[196:199], v[192:195], v[0:3]\n\t"
        "v_mfma_f32_16x16x32_bf16 v[0:3], v[128:131], v[172:175], v[0:3]\n\t"
        "v_mfma_f32_16x16x32_bf16 v[4:7], v[128:131], v[144:147], v[4:7]\n\t"
        "v_mfma_f32_16x16x32_bf16 v[4:7], v[196:199], v[160:163], v[4:7]\n\t"
        "ds_read_b128 v[136:139], v232 offset:36864\n\t"
        "v_mfma_f32_16x16x32_bf16 v[8:11], v[196:199], v[164:167], v[8:11]\n\t"
        "v_mfma_f32_16x16x32_bf16 v[8:11], v[128:131], v[168:171], v[8:11]\n\t"
        "ds_read_b128 v[140:143], v233 offset:36864\n\t"
        "v_mfma_f32_16x16x32_bf16 v[12:15], v[128:131], v[148:151], v[12:15]\n\t"
        "s_waitcnt lgkmcnt(2)\n\t"
        "v_mfma_f32_16x16x32_bf16 v[12:15], v[196:199], v[176:179], v[12:15]\n\t"
        "v_mfma_f32_16x16x32_bf16 v[32:35], v[152:155], v[192:195], v[32:35]\n\t"
        "v_mfma_f32_16x16x32_bf16 v[32:35], v[132:135], v[172:175], v[32:35]\n\t"
        "ds_read_b128 v[128:131], v232 offset:38912\n\t"
        "v_mfma_f32_16x16x32_bf16 v[36:39], v[132:135], v[144:147], v[36:39]\n\t"
        "v_mfma_f32_16x16x32_bf16 v[36:39], v[152:155], v[160:163], v[36:39]\n\t"
        "ds_read_b128 v[156:159], v233 offset:38912\n\t"
        "v_mfma_f32_16x16x32_bf16 v[40:43], v[152:155], v[164:167], v[40:43]\n\t"
        "v_mfma_f32_16x16x32_bf16 v[40:43], v[132:135], v[168:171], v[40:43]\n\t"
        "v_mfma_f32_16x16x32_bf16 v[44:47], v[132:135], v[148:151], v[44:47]\n\t"
        "v_mfma_f32_16x16x32_bf16 v[44:47], v[152:155], v[176:179], v[44:47]\n\t"
        "s_waitcnt lgkmcnt(0)\n\t"
        "s_barrier\n\t"
        "v_mfma_f32_16x16x32_bf16 v[64:67], v[136:139], v[192:195], v[64:67]\n\t"
        "v_mfma_f32_16x16x32_bf16 v[64:67], v[140:143], v[172:175], v[64:67]\n\t"
        ".Lwp101:\n\t"
        "s_cbranch_execz .Lwp97\n\t"
        "ds_read_b128 v[132:135], %[ba] offset:32768\n\t"
        "v_mfma_f32_16x16x32_bf16 v[68:71], v[140:143], v[144:147], v[68:71]\n\t"
        "ds_read_b128 v[152:155], %[xb0] offset:0\n\t"
        "v_mfma_f32_16x16x32_bf16 v[68:71], v[136:139], v[160:163], v[68:71]\n\t"
        "ds_read_b128 v[180:183], %[ba] offset:40960\n\t"
        "ds_read_b128 v[184:187], %[xb1] offset:0\n\t"
        "v_mfma_f32_16x16x32_bf16 v[72:75], v[136:139], v[164:167], v[72:75]\n\t"
        "v_mfma_f32_16x16x32_bf16 v[72:75], v[140:143], v[168:171], v[72:75]\n\t"
        "ds_read_b128 v[188:191], %[ba] offset:41216\n\t"
        "ds_read_b128 v[196:199], %[ba] offset:33024\n\t"
        "v_mfma_f32_16x16x32_bf16 v[76:79], v[140:143], v[148:151], v[76:79]\n\t"
        "v_mfma_f32_16x16x32_bf16 v[76:79], v[136:139], v[176:179], v[76:79]\n\t"
        "ds_read_b128 v[140:143], %[ba] offset:33280\n\t"
        "s_waitcnt lgkmcnt(5)\n\t"
        "v_mfma_f32_16x16x32_bf16 v[0:3], v[152:155], v[132:135], v[0:3]\n\t"
        "ds_read_b128 v[136:139], %[ba] offset:41472\n\t"
        "s_waitcnt lgkmcnt(4)\n\t"
        "v_mfma_f32_16x16x32_bf16 v[0:3], v[184:187], v[180:183], v[0:3]\n\t"
        "ds_read_b128 v[200:203], %[ba] offset:41728\n\t"
        "s_waitcnt lgkmcnt(4)\n\t"
        "v_mfma_f32_16x16x32_bf16 v[4:7], v[184:187], v[188:191], v[4:7]\n\t"
        "ds_read_b128 v[204:207], %[ba] offset:33536\n\t"
        "s_waitcnt lgkmcnt(4)\n\t"
        "v_mfma_f32_16x16x32_bf16 v[4:7], v[152:155], v[196:199], v[4:7]\n\t"
        "ds_read_b128 v[208:211], %[xb0] offset:2048\n\t"
        "s_waitcnt lgkmcnt(4)\n\t"
        "v_mfma_f32_16x16x32_bf16 v[8:11], v[152:155], v[140:143], v[8:11]\n\t"
        "s_waitcnt lgkmcnt(3)\n\t"
        "v_mfma_f32_16x16x32_bf16 v[8:11], v[184:187], v[136:139], v[8:11]\n\t"
        "ds_read_b128 v[212:215], %[xb1] offset:2048\n\t"
        "s_waitcnt lgkmcnt(3)\n\t"
        "v_mfma_f32_16x16x32_bf16 v[12:15], v[184:187], v[200:203], v[12:15]\n\t"
        "s_waitcnt lgkmcnt(2)\n\t"
        "v_mfma_f32_16x16x32_bf16 v[12:15], v[152:155], v[204:207], v[12:15]\n\t"
        "v_mfma_f32_16x16x32_bf16 v[96:99], v[128:131], v[192:195], v[96:99]\n\t"
        "v_mfma_f32_16x16x32_bf16 v[96:99], v[156:159], v[172:175], v[96:99]\n\t"
        "ds_read_b128 v[152:155], %[xb0] offset:4096\n\t"
        "v_cvt_pk_bf16_f32 v224, v0, v4\n\t"
        "v_mfma_f32_16x16x32_bf16 v[100:103], v[156:159], v[144:147], v[100:103]\n\t"
        "v_mfma_f32_16x16x32_bf16 v[100:103], v[128:131], v[160:163], v[100:103]\n\t"
        "ds_read_b128 v[144:147], %[xb1] offset:4096\n\t"
        "v_cvt_pk_bf16_f32 v225, v8, v12\n\t"
        "v_mfma_f32_16x16x32_bf16 v[104:107], v[128:131], v[164:167], v[104:107]\n\t"
        "global_store_dwordx2 %[oo0], v[224:225], %[ob0] offset:0 nt\n\t"
        "v_mfma_f32_16x16x32_bf16 v[104:107], v[156:159], v[168:171], v[104:107]\n\t"
        "ds_read_b128 v[160:163], %[xb0] offset:6144\n\t"
        "v_cvt_pk_bf16_f32 v227, v9, v13\n\t"
        "v_mfma_f32_16x16x32_bf16 v[108:111], v[156:159], v[148:151], v[108:111]\n\t"
        "v_cvt_pk_bf16_f32 v226, v1, v5\n\t"
        "v_mfma_f32_16x16x32_bf16 v[108:111], v[128:131], v[176:179], v[108:111]\n\t"
        "ds_read_b128 v[148:151], %[xb1] offset:6144\n\t"
        "s_waitcnt lgkmcnt(4)\n\t"
        "v_mfma_f32_16x16x32_bf16 v[32:35], v[208:211], v[132:135], v[32:35]\n\t"
        "global_store_dwordx2 %[oo1], v[226:227], %[ob0] offset:0 nt\n\t"
        "v_mfma_f32_16x16x32_bf16 v[32:35], v[212:215], v[180:183], v[32:35]\n\t"
        "ds_read_b128 v[128:131], v232 offset:0\n\t"
        "v_cvt_pk_bf16_f32 v229, v10, v14\n\t"
        "v_mfma_f32_16x16x32_bf16 v[36:39], v[212:215], v[188:191], v[36:39]\n\t"
        "v_cvt_pk_bf16_f32 v228, v2, v6\n\t"
        "v_mfma_f32_16x16x32_bf16 v[36:39], v[208:211], v[196:199], v[36:39]\n\t"
        "ds_read_b128 v[156:159], %[ba] offset:33792\n\t"
        "global_store_dwordx2 %[oo2], v[228:229], %[ob0] offset:0 nt\n\t"
        "v_mfma_f32_16x16x32_bf16 v[40:43], v[208:211], v[140:143], v[40:43]\n\t"
        "v_mfma_f32_16x16x32_bf16 v[40:43], v[212:215], v[136:139], v[40:43]\n\t"
        "ds_read_b128 v[164:167], v233 offset:0\n\t"
        "v_cvt_pk_bf16_f32 v231, v11, v15\n\t"
        "v_mfma_f32_16x16x32_bf16 v[44:47], v[212:215], v[200:203], v[44:47]\n\t"
        "v_cvt_pk_bf16_f32 v230, v3, v7\n\t"
        "v_mfma_f32_16x16x32_bf16 v[44:47], v[208:211], v[204:207], v[44:47]\n\t"
        "ds_read_b128 v[168:171], %[ba] offset:41984\n\t"
        "s_waitcnt lgkmcnt(4)\n\t"
        "v_mfma_f32_16x16x32_bf16 v[64:67], v[152:155], v[132:135], v[64:67]\n\t"
        "global_store_dwordx2 %[oo3], v[230:231], %[ob0] offset:0 nt\n\t"
        "v_mfma_f32_16x16x32_bf16 v[64:67], v[144:147], v[180:183], v[64:67]\n\t"
        "ds_read_b128 v[172:175], %[ba] offset:42240\n\t"
        "v_cvt_pk_bf16_f32 v224, v32, v36\n\t"
        "v_mfma_f32_16x16x32_bf16 v[68:71], v[144:147], v[188:191], v[68:71]\n\t"
        "v_mfma_f32_16x16x32_bf16 v[68:71], v[152:155], v[196:199], v[68:71]\n\t"
        "ds_read_b128 v[176:179], %[ba] offset:34048\n\t"
        "v_cvt_pk_bf16_f32 v225, v40, v44\n\t"
        "v_mfma_f32_16x16x32_bf16 v[72:75], v[152:155], v[140:143], v[72:75]\n\t"
        "global_store_dwordx2 %[oo0], v[224:225], %[ob1] offset:0 nt\n\t"
        "v_mfma_f32_16x16x32_bf16 v[72:75], v[144:147], v[136:139], v[72:75]\n\t"
        "ds_read_b128 v[184:187], %[ba] offset:34304\n\t"
        "ds_read_b128 v[192:195], %[ba] offset:42496\n\t"
        "v_mfma_f32_16x16x32_bf16 v[76:79], v[144:147], v[200:203], v[76:79]\n\t"
        "v_cvt_pk_bf16_f32 v227, v41, v45\n\t"
        "v_cvt_pk_bf16_f32 v226, v33, v37\n\t"
        "v_mfma_f32_16x16x32_bf16 v[76:79], v[152:155], v[204:207], v[76:79]\n\t"
        ".Lwp102:\n\t"
        "s_cbranch_execz .Lwp103\n\t"
        "ds_read_b128 v[144:147], %[ba] offset:42752\n\t"
        "v_mfma_f32_16x16x32_bf16 v[96:99], v[160:163], v[132:135], v[96:99]\n\t"
        "global_store_dwordx2 %[oo1], v[226:227], %[ob1] offset:0 nt\n\t"
        "v_mfma_f32_16x16x32_bf16 v[96:99], v[148:151], v[180:183], v[96:99]\n\t"
        "ds_read_b128 v[132:135], %[ba] offset:34560\n\t"
        "v_cvt_pk_bf16_f32 v229, v42, v46\n\t"
        "v_mfma_f32_16x16x32_bf16 v[100:103], v[148:151], v[188:191], v[100:103]\n\t"
        "v_cvt_pk_bf16_f32 v228, v34, v38\n\t"
        "v_mfma_f32_16x16x32_bf16 v[100:103], v[160:163], v[196:199], v[100:103]\n\t"
        "ds_read_b128 v[152:155], v232 offset:2048\n\t"
        "global_store_dwordx2 %[oo2], v[228:229], %[ob1] offset:0 nt\n\t"
        "v_mfma_f32_16x16x32_bf16 v[104:107], v[160:163], v[140:143], v[104:107]\n\t"
        "v_mfma_f32_16x16x32_bf16 v[104:107], v[148:151], v[136:139], v[104:107]\n\t"
        "ds_read_b128 v[140:143], v233 offset:2048\n\t"
        "v_cvt_pk_bf16_f32 v231, v43, v47\n\t"
        "v_mfma_f32_16x16x32_bf16 v[108:111], v[148:151], v[200:203], v[108:111]\n\t"
        "v_cvt_pk_bf16_f32 v230, v35, v39\n\t"
        "v_mfma_f32_16x16x32_bf16 v[108:111], v[160:163], v[204:207], v[108:111]\n\t"
        "global_store_dwordx2 %[oo3], v[230:231], %[ob1] offset:0 nt\n\t"
        "s_waitcnt lgkmcnt(3)\n\t"
        "v_mfma_f32_16x16x32_bf16 v[16:19], v[128:131], v[156:159], v[16:19]\n\t"
        "v_mfma_f32_16x16x32_bf16 v[16:19], v[164:167], v[168:171], v[16:19]\n\t"
        "ds_read_b128 v[136:139], v232 offset:4096\n\t"
        "v_cvt_pk_bf16_f32 v225, v72, v76\n\t"
        "v_mfma_f32_16x16x32_bf16 v[20:23], v[164:167], v[172:175], v[20:23]\n\t"
        "v_cvt_pk_bf16_f32 v224, v64, v68\n\t"
        "v_mfma_f32_16x16x32_bf16 v[20:23], v[128:131], v[176:179], v[20:23]\n\t"
        "ds_read_b128 v[148:151], v233 offset:4096\n\t"
        "global_store_dwordx2 %[oo0], v[224:225], %[ob2] offset:0 nt\n\t"
        "v_mfma_f32_16x16x32_bf16 v[24:27], v[128:131], v[184:187], v[24:27]\n\t"
        "v_mfma_f32_16x16x32_bf16 v[24:27], v[164:167], v[192:195], v[24:27]\n\t"
        "ds_read_b128 v[160:163], v232 offset:6144\n\t"
        "v_cvt_pk_bf16_f32 v227, v73, v77\n\t"
        "v_mfma_f32_16x16x32_bf16 v[28:31], v[164:167], v[144:147], v[28:31]\n\t"
        "v_cvt_pk_bf16_f32 v226, v65, v69\n\t"
        "s_waitcnt lgkmcnt(3)\n\t"
        "v_mfma_f32_16x16x32_bf16 v[28:31], v[128:131], v[132:135], v[28:31]\n\t"
        "ds_read_b128 v[164:167], v233 offset:6144\n\t"
        "global_store_dwordx2 %[oo1], v[226:227], %[ob2] offset:0 nt\n\t"
        "v_mfma_f32_16x16x32_bf16 v[48:51], v[152:155], v[156:159], v[48:51]\n\t"
        "v_mfma_f32_16x16x32_bf16 v[48:51], v[140:143], v[168:171], v[48:51]\n\t"
        "ds_read_b128 v[128:131], v232 offset:32768\n\t"
        "v_cvt_pk_bf16_f32 v229, v74, v78\n\t"
        "v_mfma_f32_16x16x32_bf16 v[52:55], v[140:143], v[172:175], v[52:55]\n\t"
        "v_cvt_pk_bf16_f32 v228, v66, v70\n\t"
        "v_mfma_f32_16x16x32_bf16 v[52:55], v[152:155], v[176:179], v[52:55]\n\t"
        "ds_read_b128 v[180:183], v233 offset:32768\n\t"
        "ds_read_b128 v[188:191], %[xb0] offset:0\n\t"
        "v_mfma_f32_16x16x32_bf16 v[56:59], v[152:155], v[184:187], v[56:59]\n\t"
        "global_store_dwordx2 %[oo2], v[228:229], %[ob2] offset:0 nt\n\t"
        "v_mfma_f32_16x16x32_bf16 v[56:59], v[140:143], v[192:195], v[56:59]\n\t"
        "v_cvt_pk_bf16_f32 v231, v75, v79\n\t"
        "v_cvt_pk_bf16_f32 v230, v67, v71\n\t"
        "v_mfma_f32_16x16x32_bf16 v[60:63], v[140:143], v[144:147], v[60:63]\n\t"
        "v_mfma_f32_16x16x32_bf16 v[60:63], v[152:155], v[132:135], v[60:63]\n\t"
        "global_store_dwordx2 %[oo3], v[230:231], %[ob2] offset:0 nt\n\t"
        "s_barrier\n\t"
        "s_waitcnt lgkmcnt(3)\n\t"
        "v_mfma_f32_16x16x32_bf16 v[80:83], v[136:139], v[156:159], v[80:83]\n\t"
        "v_mfma_f32_16x16x32_bf16 v[80:83], v[148:151], v[168:171], v[80:83]\n\t"
        "ds_read_b128 v[140:143], %[ba] offset:0\n\t"
        "ds_read_b128 v[152:155], %[xb1] offset:0\n\t"
        "v_mfma_f32_16x16x32_bf16 v[84:87], v[148:151], v[172:175], v[84:87]\n\t"
        "v_cvt_pk_bf16_f32 v225, v104, v108\n\t"
        "v_cvt_pk_bf16_f32 v224, v96, v100\n\t"
        "v_mfma_f32_16x16x32_bf16 v[84:87], v[136:139], v[176:179], v[84:87]\n\t"
        "ds_read_b128 v[196:199], %[ba] offset:8192\n\t"
        "ds_read_b128 v[200:203], %[ba] offset:1024\n\t"
        "v_mfma_f32_16x16x32_bf16 v[88:91], v[136:139], v[184:187], v[88:91]\n\t"
        "ds_read_b128 v[204:207], %[ba] offset:9216\n\t"
        "global_store_dwordx2 %[oo0], v[224:225], %[ob3] offset:0 nt\n\t"
        "v_mfma_f32_16x16x32_bf16 v[88:91], v[148:151], v[192:195], v[88:91]\n\t"
        "ds_read_b128 v[208:211], %[ba] offset:9472\n\t"
        "ds_read_b128 v[212:215], %[ba] offset:1280\n\t"
        "v_mfma_f32_16x16x32_bf16 v[92:95], v[148:151], v[144:147], v[92:95]\n\t"
        "ds_read_b128 v[216:219], %[ba] offset:8448\n\t"
        "v_cvt_pk_bf16_f32 v227, v105, v109\n\t"
        "v_mfma_f32_16x16x32_bf16 v[92:95], v[136:139], v[132:135], v[92:95]\n\t"
        "v_cvt_pk_bf16_f32 v226, v97, v101\n\t"
        "ds_read_b128 v[148:151], %[ba] offset:256\n\t"
        "v_mfma_f32_16x16x32_bf16 v[112:115], v[160:163], v[156:159], v[112:115]\n\t"
        "ds_read_b128 v[136:139], %[ba] offset:512\n\t"
        "ds_read_b128 v[220:223], %[ba] offset:8704\n\t"
        "v_mfma_f32_16x16x32_bf16 v[112:115], v[164:167], v[168:171], v[112:115]\n\t"
        "global_store_dwordx2 %[oo1], v[226:227], %[ob3] offset:0 nt\n\t"
        ".Lwp103:\n\t"
        "s_cbranch_execz .Lwp104\n\t"
        "v_mfma_f32_16x16x32_bf16 v[116:119], v[164:167], v[172:175], v[116:119]\n\t"
        "ds_read_b128 v[156:159], %[ba] offset:1536\n\t"
        "ds_read_b128 v[168:171], %[ba] offset:9728\n\t"
        "v_mfma_f32_16x16x32_bf16 v[116:119], v[160:163], v[176:179], v[116:119]\n\t"
        "v_cvt_pk_bf16_f32 v229, v106, v110\n\t"
        "v_cvt_pk_bf16_f32 v228, v98, v102\n\t"
        "v_mfma_f32_16x16x32_bf16 v[120:123], v[160:163], v[184:187], v[120:123]\n\t"
        "ds_read_b128 v[172:175], %[ba] offset:9984\n\t"
        "ds_read_b128 v[176:179], %[ba] offset:1792\n\t"
        "v_mfma_f32_16x16x32_bf16 v[120:123], v[164:167], v[192:195], v[120:123]\n\t"
        "global_store_dwordx2 %[oo2], v[228:229], %[ob3] offset:0 nt\n\t"
        "ds_read_b128 v[184:187], %[ba] offset:8960\n\t"
        "v_mfma_f32_16x16x32_bf16 v[124:127], v[164:167], v[144:147], v[124:127]\n\t"
        "v_cvt_pk_bf16_f32 v231, v107, v111\n\t"
        "v_cvt_pk_bf16_f32 v230, v99, v103\n\t"
        "v_mfma_f32_16x16x32_bf16 v[124:127], v[160:163], v[132:135], v[124:127]\n\t"
        "ds_read_b128 v[144:147], %[ba] offset:768\n\t"
        "s_waitcnt lgkmcnt(8)\n\t"
        "v_mfma_f32_16x16x32_bf16 v[16:19], v[128:131], v[140:143], v[16:19]\n\t"
        "global_store_dwordx2 %[oo3], v[230:231], %[ob3] offset:0 nt\n\t"
        "v_mfma_f32_16x16x32_bf16 v[16:19], v[180:183], v[196:199], v[16:19]\n\t"
        "ds_read_b128 v[132:135], v232 offset:34816\n\t"
        "v_mfma_f32_16x16x32_bf16 v[16:19], v[188:191], v[200:203], v[16:19]\n\t"
        "v_mfma_f32_16x16x32_bf16 v[16:19], v[152:155], v[204:207], v[16:19]\n\t"
        "ds_read_b128 v[160:163], v233 offset:34816\n\t"
        "v_mfma_f32_16x16x32_bf16 v[20:23], v[152:155], v[208:211], v[20:23]\n\t"
        "v_mfma_f32_16x16x32_bf16 v[20:23], v[188:191], v[212:215], v[20:23]\n\t"
        "ds_read_b128 v[164:167], %[xb0] offset:2048\n\t"
        "v_mfma_f32_16x16x32_bf16 v[20:23], v[180:183], v[216:219], v[20:23]\n\t"
        "v_mfma_f32_16x16x32_bf16 v[20:23], v[128:131], v[148:151], v[20:23]\n\t"
        "ds_read_b128 v[192:195], %[xb1] offset:2048\n\t"
        "s_waitcnt lgkmcnt(4)\n\t"
        "v_mfma_f32_16x16x32_bf16 v[24:27], v[128:131], v[136:139], v[24:27]\n\t"
        "v_mfma_f32_16x16x32_bf16 v[24:27], v[180:183], v[220:223], v[24:27]\n\t"
        "v_mfma_f32_16x16x32_bf16 v[24:27], v[188:191], v[156:159], v[24:27]\n\t"
        "v_mfma_f32_16x16x32_bf16 v[24:27], v[152:155], v[168:171], v[24:27]\n\t"
        "v_mfma_f32_16x16x32_bf16 v[28:31], v[152:155], v[172:175], v[28:31]\n\t"
        "v_mfma_f32_16x16x32_bf16 v[28:31], v[188:191], v[176:179], v[28:31]\n\t"
        "v_mfma_f32_16x16x32_bf16 v[28:31], v[180:183], v[184:187], v[28:31]\n\t"
        "v_mfma_f32_16x16x32_bf16 v[28:31], v[128:131], v[144:147], v[28:31]\n\t"
        "s_waitcnt lgkmcnt(0)\n\t"
        "v_mfma_f32_16x16x32_bf16 v[48:51], v[132:135], v[140:143], v[48:51]\n\t"
        "v_mfma_f32_16x16x32_bf16 v[48:51], v[160:163], v[196:199], v[48:51]\n\t"
        "ds_read_b128 v[128:131], v232 offset:36864\n\t"
        "v_cvt_pk_bf16_f32 v224, v16, v20\n\t"
        "v_mfma_f32_16x16x32_bf16 v[48:51], v[164:167], v[200:203], v[48:51]\n\t"
        "v_mfma_f32_16x16x32_bf16 v[48:51], v[192:195], v[204:207], v[48:51]\n\t"
        "ds_read_b128 v[152:155], v233 offset:36864\n\t"
        "v_cvt_pk_bf16_f32 v225, v24, v28\n\t"
        "v_mfma_f32_16x16x32_bf16 v[52:55], v[192:195], v[208:211], v[52:55]\n\t"
        "global_store_dwordx2 %[oo0], v[224:225], %[ob0] offset:128 nt\n\t"
        "v_mfma_f32_16x16x32_bf16 v[52:55], v[164:167], v[212:215], v[52:55]\n\t"
        "ds_read_b128 v[180:183], %[xb0] offset:4096\n\t"
        "v_cvt_pk_bf16_f32 v227, v25, v29\n\t"
        "v_mfma_f32_16x16x32_bf16 v[52:55], v[160:163], v[216:219], v[52:55]\n\t"
        "v_cvt_pk_bf16_f32 v226, v17, v21\n\t"
        "v_mfma_f32_16x16x32_bf16 v[52:55], v[132:135], v[148:151], v[52:55]\n\t"
        "ds_read_b128 v[188:191], %[xb1] offset:4096\n\t"
        "global_store_dwordx2 %[oo1], v[226:227], %[ob0] offset:128 nt\n\t"
        "v_mfma_f32_16x16x32_bf16 v[56:59], v[132:135], v[136:139], v[56:59]\n\t"
        "v_mfma_f32_16x16x32_bf16 v[56:59], v[160:163], v[220:223], v[56:59]\n\t"
        "v_cvt_pk_bf16_f32 v229, v26, v30\n\t"
        "v_cvt_pk_bf16_f32 v228, v18, v22\n\t"
        "v_mfma_f32_16x16x32_bf16 v[56:59], v[164:167], v[156:159], v[56:59]\n\t"
        "v_mfma_f32_16x16x32_bf16 v[56:59], v[192:195], v[168:171], v[56:59]\n\t"
        "global_store_dwordx2 %[oo2], v[228:229], %[ob0] offset:128 nt\n\t"
        "v_mfma_f32_16x16x32_bf16 v[60:63], v[192:195], v[172:175], v[60:63]\n\t"
        "v_mfma_f32_16x16x32_bf16 v[60:63], v[164:167], v[176:179], v[60:63]\n\t"
        "v_cvt_pk_bf16_f32 v231, v27, v31\n\t"
        "v_cvt_pk_bf16_f32 v230, v19, v23\n\t"
        "v_mfma_f32_16x16x32_bf16 v[60:63], v[160:163], v[184:187], v[60:63]\n\t"
        "v_mfma_f32_16x16x32_bf16 v[60:63], v[132:135], v[144:147], v[60:63]\n\t"
        ".Lwp104:\n\t"
        "s_cbranch_execz .Lwp97\n\t"
        "s_waitcnt lgkmcnt(0)\n\t"
        "v_mfma_f32_16x16x32_bf16 v[80:83], v[128:131], v[140:143], v[80:83]\n\t"
        "global_store_dwordx2 %[oo3], v[230:231], %[ob0] offset:128 nt\n\t"
        "v_mfma_f32_16x16x32_bf16 v[80:83], v[152:155], v[196:199], v[80:83]\n\t"
        "ds_read_b128 v[132:135], v232 offset:38912\n\t"
        "v_cvt_pk_bf16_f32 v224, v48, v52\n\t"
        "v_mfma_f32_16x16x32_bf16 v[80:83], v[180:183], v[200:203], v[80:83]\n\t"
        "v_mfma_f32_16x16x32_bf16 v[80:83], v[188:191], v[204:207], v[80:83]\n\t"
        "ds_read_b128 v[160:163], v233 offset:38912\n\t"
        "v_cvt_pk_bf16_f32 v225, v56, v60\n\t"
        "v_mfma_f32_16x16x32_bf16 v[84:87], v[188:191], v[208:211], v[84:87]\n\t"
        "global_store_dwordx2 %[oo0], v[224:225], %[ob1] offset:128 nt\n\t"
        "v_mfma_f32_16x16x32_bf16 v[84:87], v[180:183], v[212:215], v[84:87]\n\t"
        "ds_read_b128 v[164:167], %[xb0] offset:6144\n\t"
        "v_cvt_pk_bf16_f32 v227, v57, v61\n\t"
        "v_mfma_f32_16x16x32_bf16 v[84:87], v[152:155], v[216:219], v[84:87]\n\t"
        "v_cvt_pk_bf16_f32 v226, v49, v53\n\t"
        "v_mfma_f32_16x16x32_bf16 v[84:87], v[128:131], v[148:151], v[84:87]\n\t"
        "ds_read_b128 v[192:195], %[xb1] offset:6144\n\t"
        "global_store_dwordx2 %[oo1], v[226:227], %[ob1] offset:128 nt\n\t"
        "v_mfma_f32_16x16x32_bf16 v[88:91], v[128:131], v[136:139], v[88:91]\n\t"
        "v_mfma_f32_16x16x32_bf16 v[88:91], v[152:155], v[220:223], v[88:91]\n\t"
        "v_cvt_pk_bf16_f32 v229, v58, v62\n\t"
        "v_cvt_pk_bf16_f32 v228, v50, v54\n\t"
        "v_mfma_f32_16x16x32_bf16 v[88:91], v[180:183], v[156:159], v[88:91]\n\t"
        "v_mfma_f32_16x16x32_bf16 v[88:91], v[188:191], v[168:171], v[88:91]\n\t"
        "global_store_dwordx2 %[oo2], v[228:229], %[ob1] offset:128 nt\n\t"
        "v_mfma_f32_16x16x32_bf16 v[92:95], v[188:191], v[172:175], v[92:95]\n\t"
        "v_mfma_f32_16x16x32_bf16 v[92:95], v[180:183], v[176:179], v[92:95]\n\t"
        "v_cvt_pk_bf16_f32 v231, v59, v63\n\t"
        "v_cvt_pk_bf16_f32 v230, v51, v55\n\t"
        "v_mfma_f32_16x16x32_bf16 v[92:95], v[152:155], v[184:187], v[92:95]\n\t"
        "v_mfma_f32_16x16x32_bf16 v[92:95], v[128:131], v[144:147], v[92:95]\n\t"
        "global_store_dwordx2 %[oo3], v[230:231], %[ob1] offset:128 nt\n\t"
        "s_waitcnt lgkmcnt(0)\n\t"
        "v_mfma_f32_16x16x32_bf16 v[112:115], v[132:135], v[140:143], v[112:115]\n\t"
        "v_mfma_f32_16x16x32_bf16 v[112:115], v[160:163], v[196:199], v[112:115]\n\t"
        "v_cvt_pk_bf16_f32 v224, v80, v84\n\t"
        "v_mfma_f32_16x16x32_bf16 v[112:115], v[164:167], v[200:203], v[112:115]\n\t"
        "v_mfma_f32_16x16x32_bf16 v[112:115], v[192:195], v[204:207], v[112:115]\n\t"
        "v_cvt_pk_bf16_f32 v225, v88, v92\n\t"
        "global_store_dwordx2 %[oo0], v[224:225], %[ob2] offset:128 nt\n\t"
        "v_mfma_f32_16x16x32_bf16 v[116:119], v[192:195], v[208:211], v[116:119]\n\t"
        "v_mfma_f32_16x16x32_bf16 v[116:119], v[164:167], v[212:215], v[116:119]\n\t"
        "v_cvt_pk_bf16_f32 v227, v89, v93\n\t"
        "v_cvt_pk_bf16_f32 v226, v81, v85\n\t"
        "v_mfma_f32_16x16x32_bf16 v[116:119], v[160:163], v[216:219], v[116:119]\n\t"
        "v_mfma_f32_16x16x32_bf16 v[116:119], v[132:135], v[148:151], v[116:119]\n\t"
        "global_store_dwordx2 %[oo1], v[226:227], %[ob2] offset:128 nt\n\t"
        "v_mfma_f32_16x16x32_bf16 v[120:123], v[132:135], v[136:139], v[120:123]\n\t"
        "v_mfma_f32_16x16x32_bf16 v[120:123], v[160:163], v[220:223], v[120:123]\n\t"
        "v_cvt_pk_bf16_f32 v229, v90, v94\n\t"
        "v_cvt_pk_bf16_f32 v228, v82, v86\n\t"
        "v_mfma_f32_16x16x32_bf16 v[120:123], v[164:167], v[156:159], v[120:123]\n\t"
        "v_mfma_f32_16x16x32_bf16 v[120:123], v[192:195], v[168:171], v[120:123]\n\t"
        "global_store_dwordx2 %[oo2], v[228:229], %[ob2] offset:128 nt\n\t"
        "v_mfma_f32_16x16x32_bf16 v[124:127], v[192:195], v[172:175], v[124:127]\n\t"
        "v_mfma_f32_16x16x32_bf16 v[124:127], v[164:167], v[176:179], v[124:127]\n\t"
        "v_cvt_pk_bf16_f32 v230, v113, v117\n\t"
        "v_cvt_pk_bf16_f32 v224, v112, v116\n\t"
        "v_mfma_f32_16x16x32_bf16 v[124:127], v[160:163], v[184:187], v[124:127]\n\t"
        "v_cvt_pk_bf16_f32 v227, v91, v95\n\t"
        "v_cvt_pk_bf16_f32 v226, v83, v87\n\t"
        "v_mfma_f32_16x16x32_bf16 v[124:127], v[132:135], v[144:147], v[124:127]\n\t"
        "v_cvt_pk_bf16_f32 v228, v115, v119\n\t"
        "v_cvt_pk_bf16_f32 v128, v114, v118\n\t"
        "global_store_dwordx2 %[oo3], v[226:227], %[ob2] offset:128 nt\n\t"
        "s_nop 7\n\t"
        "v_cvt_pk_bf16_f32 v225, v120, v124\n\t"
        "global_store_dwordx2 %[oo0], v[224:225], %[ob3] offset:128 nt\n\t"
        "v_cvt_pk_bf16_f32 v231, v121, v125\n\t"
        "global_store_dwordx2 %[oo1], v[230:231], %[ob3] offset:128 nt\n\t"
        "v_cvt_pk_bf16_f32 v129, v122, v126\n\t"
        "global_store_dwordx2 %[oo2], v[128:129], %[ob3] offset:128 nt\n\t"
        "v_cvt_pk_bf16_f32 v229, v123, v127\n\t"
        "global_store_dwordx2 %[oo3], v[228:229], %[ob3] offset:128 nt\n\t"
        :
        : [xb0] "v"(xb0), [xb1] "v"(xb1), [ba] "v"(ba), [oo0] "v"(oo[0]), [oo1] "v"(oo[1]), [oo2] "v"(oo[2]), [oo3] "v"(oo[3]),
          [ob0] "s"(ob[0]), [ob1] "s"(ob[1]), [ob2] "s"(ob[2]), [ob3] "s"(ob[3]), [w] "s"(w)
        : "scc", "memory", "s60", "s61", "s62", "s63", "s64", "s65", "s66", "s67", "v0", "v1", "v2", "v3", "v4", "v5", "v6", "v7", "v8", "v9", "v10",
              "v11", "v12", "v13", "v14", "v15", "v16", "v17", "v18", "v19", "v20", "v21", "v22", "v23", "v24", "v25", "v26", "v27", "v28", "v29",
              "v30", "v31", "v32", "v33", "v34", "v35", "v36", "v37", "v38", "v39", "v40", "v41", "v42", "v43", "v44", "v45", "v46", "v47", "v48",
              "v49", "v50", "v51", "v52", "v53", "v54", "v55", "v56", "v57", "v58", "v59", "v60", "v61", "v62", "v63", "v64", "v65", "v66", "v67",
              "v68", "v69", "v70", "v71", "v72", "v73", "v74", "v75", "v76", "v77", "v78", "v79", "v80", "v81", "v82", "v83", "v84", "v85", "v86",
              "v87", "v88", "v89", "v90", "v91", "v92", "v93", "v94", "v95", "v96", "v97", "v98", "v99", "v100", "v101", "v102", "v103", "v104",
              "v105", "v106", "v107", "v108", "v109", "v110", "v111", "v112", "v113", "v114", "v115", "v116", "v117", "v118", "v119", "v120", "v121",
              "v122", "v123", "v124", "v125", "v126", "v127", "v128", "v129", "v130", "v131", "v132", "v133", "v134", "v135", "v136", "v137", "v138",
              "v139", "v140", "v141", "v142", "v143", "v144", "v145", "v146", "v147", "v148", "v149", "v150", "v151", "v152", "v153", "v154", "v155",
              "v156", "v157", "v158", "v159", "v160", "v161", "v162", "v163", "v164", "v165", "v166", "v167", "v168", "v169", "v170", "v171", "v172",
              "v173", "v174", "v175", "v176", "v177", "v178", "v179", "v180", "v181", "v182", "v183", "v184", "v185", "v186", "v187", "v188", "v189",
              "v190", "v191", "v192", "v193", "v194", "v195", "v196", "v197", "v198", "v199", "v200", "v201", "v202", "v203", "v204", "v205", "v206",
              "v207", "v208", "v209", "v210", "v211", "v212", "v213", "v214", "v215", "v216", "v217", "v218", "v219", "v220", "v221", "v222", "v223",
              "v224", "v225", "v226", "v227", "v228", "v229", "v230", "v231", "v232", "v233", "v234", "v235");
}

extern "C" void kernel_launch(void* const* d_in, const int* in_sizes, int n_in,
                              void* d_out, int out_size, void* d_ws, size_t ws_size,
                              hipStream_t stream) {
    const u16* x = (const u16*)d_in[0];
    const int* qw = (const int*)d_in[1];
    const u16* lut = (const u16*)d_in[2];
    u16* out = (u16*)d_out;
    (void)hipFuncSetAttribute((const void*)fused_kernel, hipFuncAttributeMaxDynamicSharedMemorySize, LDS_BYTES);
    fused_kernel<<<(M_ / BM) * (N_ / BN) + NPF, 512, LDS_BYTES, stream>>>(x, qw, lut, out, (char*)d_ws);
}
```
